```python
import torch

def _softmax(x, axis=-1):
    return torch.softmax(x, dim=axis)
def _gelu(x):
    return torch.nn.functional.gelu(x, approximate="tanh")
def _silu(x):
    return torch.nn.functional.silu(x)
def _sigmoid(x):
    return torch.sigmoid(x)
def _layernorm(x, axis=-1, eps=1e-5):
    m = x.mean(dim=axis, keepdim=True)
    v = x.var(dim=axis, keepdim=True, unbiased=False)
    return (x - m) / torch.sqrt(v + eps)
def _scatter_add(tgt, idx, src, axis=0):
    return tgt.clone().index_add_(axis, idx, src)
def _segment_sum(src, seg, n):
    out = torch.zeros((n,) + src.shape[1:], dtype=src.dtype)
    return out.index_add_(0, seg, src)
def _pad(x, pw):
    flat = tuple(v for pair in reversed(pw) for v in pair)
    return torch.nn.functional.pad(x, flat)

kernel_name = 'interleave_layers__ssm_conv_s5__ssm_conv_h3__prompt'

def setup_inputs():

    _w = (lambda a: (lambda t: t.to(torch.bfloat16) if t.is_floating_point() else t)(torch.as_tensor(a)))
    def _A_setup():
        B, S, D = 1, 2048, 640
        P = D
        Lambda_re = -0.5 * (lambda _x: torch.exp(torch.as_tensor(_x)))((lambda *s: torch.randn(*s, dtype=torch.bfloat16) * 0.2)(P))
        Lambda_im = (lambda *s: torch.randn(*s, dtype=torch.bfloat16) * 0.8)(P) * 1.0
        B_re = (lambda *s: torch.randn(*s, dtype=torch.bfloat16) * 0.2)(P, D) * (D ** -0.5)
        B_im = (lambda *s: torch.randn(*s, dtype=torch.bfloat16) * 0.2)(P, D) * (D ** -0.5)
        C_re = (lambda *s: torch.randn(*s, dtype=torch.bfloat16) * 0.2)(D, P) * (P ** -0.5)
        C_im = (lambda *s: torch.randn(*s, dtype=torch.bfloat16) * 0.2)(D, P) * (P ** -0.5)
        log_dt = (lambda *s: torch.randn(*s, dtype=torch.bfloat16) * 0.2)(P) * 0.3 - 2.0
        Dskip = (lambda *s: torch.randn(*s, dtype=torch.bfloat16) * 0.05)(D)
        return {
            "x": _w((lambda *s: torch.randn(*s, dtype=torch.bfloat16) * 0.05)(B, S, D)),
            "Lambda_re": _w(Lambda_re),
            "Lambda_im": _w(Lambda_im),
            "B_re": _w(B_re),
            "B_im": _w(B_im),
            "C_re": _w(C_re),
            "C_im": _w(C_im),
            "log_dt": _w(log_dt),
            "Dskip": _w(Dskip),
        }
    _A_in = _A_setup()

    def _B_setup():
        B, S, D = 1, 2048, 640
        ks = 4
        return {
            "q": _w((lambda *s: torch.randn(*s, dtype=torch.bfloat16) * 0.05)(B, S, D)),
            "k": _w((lambda *s: torch.randn(*s, dtype=torch.bfloat16) * 0.4)(B, S, D)),
            "v": _w((lambda *s: torch.randn(*s, dtype=torch.bfloat16) * 0.4)(B, S, D)),
            "shift_k": _w((lambda *s: torch.randn(*s, dtype=torch.bfloat16) * 0.4)(D, ks) * 0.5),
            "ssm_k": _w((lambda *s: torch.randn(*s, dtype=torch.bfloat16) * 0.4)(D, S) * (1.0 / S ** 0.5)),
            "ssm_D": _w((lambda *s: torch.randn(*s, dtype=torch.bfloat16) * 0.4)(D)),
        }
    _B_in = _B_setup()

    return {
        "A_x": _A_in["x"],
        "A_Lambda_re": _A_in["Lambda_re"],
        "A_Lambda_im": _A_in["Lambda_im"],
        "A_B_re": _A_in["B_re"],
        "A_B_im": _A_in["B_im"],
        "A_C_re": _A_in["C_re"],
        "A_C_im": _A_in["C_im"],
        "A_log_dt": _A_in["log_dt"],
        "A_Dskip": _A_in["Dskip"],
        "B_q": _B_in["q"],
        "B_k": _B_in["k"],
        "B_v": _B_in["v"],
        "B_shift_k": _B_in["shift_k"],
        "B_ssm_k": _B_in["ssm_k"],
        "B_ssm_D": _B_in["ssm_D"],
    }

def reference(A_x, A_Lambda_re, A_Lambda_im, A_B_re, A_B_im, A_C_re, A_C_im, A_log_dt, A_Dskip, B_q, B_k, B_v, B_shift_k, B_ssm_k, B_ssm_D):
    def _A_ref(x, Lambda_re, Lambda_im, B_re, B_im, C_re, C_im, log_dt, Dskip):
        Bsz, L, H = x.shape
        Lam = Lambda_re + 1j * Lambda_im
        Bmat = B_re + 1j * B_im
        Cmat = C_re + 1j * C_im
        dt = torch.exp(log_dt)
        Lam_bar = torch.exp(dt * Lam)
        B_bar = ((Lam_bar - 1.0) / Lam)[:, None] * Bmat
        Bu = torch.einsum("bld,pd->blp", x + 0j, B_bar)
        h = torch.zeros_like(Bu[:, 0, :])
        hs = []
        for t in range(L):
            h = Lam_bar[None, :] * h + Bu[:, t, :]
            hs.append(h)
        hseq = torch.stack(hs, 1)
        y = 2.0 * torch.einsum("blp,dp->bld", hseq, Cmat).real + x * Dskip[None, None, :]
        return y

    def _B_ref(q, k, v, shift_k, ssm_k, ssm_D):
        B, S, D = q.shape
        ks = shift_k.shape[-1]
        k_pad = _pad(k, ((0, 0), (ks - 1, 0), (0, 0)))
        k_shift = torch.zeros_like(k)
        for t in range(ks):
            k_shift = k_shift + k_pad[:, t:t + S, :] * shift_k[:, ks - 1 - t][None, None, :]
        kv = k_shift * v
        L = 2 * S
        kv_pad = _pad(kv, ((0, 0), (0, S), (0, 0)))
        kf_pad = _pad(ssm_k, ((0, 0), (0, S)))
        kv_f = (lambda x, axes=(-2,-1): torch.fft.rfft2(x.float(), dim=axes))(kv_pad[:, None, :, :], axes=(1, 2))
        kf_f = (lambda x, axes=(-2,-1): torch.fft.rfft2(x.float(), dim=axes))(kf_pad[:, None, :], axes=(1, 2))
        kf_f = (lambda x, *d: x.permute(*d))(kf_f, 1, 2, 0)
        y_f = kv_f * kf_f[None, :, :, :]
        y = (lambda x, s, axes=(-2,-1): torch.fft.irfft2(x, s=s, dim=axes).to(torch.bfloat16))(y_f, (1, L), axes=(1, 2))[:, 0, :S, :]
        y = y + kv * ssm_D[None, None, :]
        return q * y

    x = A_x
    x = x + _A_ref(_layernorm(x, axis=-1), A_Lambda_re, A_Lambda_im, A_B_re, A_B_im, A_C_re, A_C_im, A_log_dt, A_Dskip)
    x = x + _B_ref(_layernorm(x, axis=-1), B_k, B_v, B_shift_k, B_ssm_k, B_ssm_D)
    x = x + _A_ref(_layernorm(x, axis=-1), A_Lambda_re, A_Lambda_im, A_B_re, A_B_im, A_C_re, A_C_im, A_log_dt, A_Dskip)
    x = x + _B_ref(_layernorm(x, axis=-1), B_k, B_v, B_shift_k, B_ssm_k, B_ssm_D)
    return x
```

```cpp
#include <hip/hip_runtime.h>
#include <stdint.h>
#include <utility>

#define WSOFF 0
#define STAMPS 0

typedef unsigned short u16;
typedef unsigned long long u64;
typedef unsigned v4u __attribute__((ext_vector_type(4)));
typedef unsigned v2u __attribute__((ext_vector_type(2)));
typedef float f32x4 __attribute__((ext_vector_type(4)));
typedef float f32x2 __attribute__((ext_vector_type(2)));
typedef short s16x4 __attribute__((ext_vector_type(4)));
typedef __bf16 bf16x2v __attribute__((ext_vector_type(2)));
#define LAS __attribute__((address_space(3)))
#define DI __device__ __forceinline__

constexpr int S_ = 2048, D_ = 640;
#define KR1 5
#define KR2 12
constexpr int RING1 = KR1;
#define KCR1 4
constexpr int kCR1 = KCR1;
#define KXA 7
#define KN3E 17
#define KN3 12
constexpr int kXA = KXA;
constexpr int kN3E = KN3E, kN3 = KN3;
#define KFHK 9
#define KFHP 7
constexpr int kFHK = KFHK, kFHP = KFHP;
#define KGPI 28
#define KGCI 34
#define KFGI 12
#define G2ND 1
constexpr int kGSI = 2, kFGI = KFGI, kGPI = KGPI, kGCI = KGCI;

constexpr unsigned OFF_NONCE = 0;
constexpr unsigned OFF_ST = 4096;
constexpr unsigned OFF_FW = OFF_ST + 65536;
constexpr unsigned OFF_FK = OFF_FW + 131072;
constexpr unsigned OFF_FH = OFF_FK + 81920;
constexpr unsigned OFF_FX = OFF_FH + 262144;
constexpr unsigned OFF_FG = OFF_FX + 131072;
constexpr unsigned OFF_EG = OFF_FG + 98304;
constexpr unsigned OFF_SG = OFF_EG + 327680;
constexpr unsigned OFF_PS = OFF_SG + 524288;
constexpr unsigned OFF_KVT = OFF_PS + 131072;
constexpr unsigned OFF_GT = OFF_KVT + 2621440;
constexpr unsigned OFF_W2F = OFF_GT + 2621440;
constexpr unsigned OFF_X2F = OFF_W2F + 1638400;
constexpr unsigned OFF_DUM = OFF_X2F + 2621440;
constexpr unsigned OFF_HF = OFF_DUM + 163840;
constexpr unsigned WS_BYTES = OFF_HF + 2 * 5242880;
constexpr unsigned kNone = 0x40000000u;
constexpr unsigned OFF_DBG = 0x1800000;
#define I32M 1
#define KVS 1
#define KVU 0
#define TILES() const int xq = b & 7, j = b >> 3, mt1 = 2 * xq + (j >> 4), nt1 = j & 15, mt2 = 4 * xq + (j >> 3), nt2 = j & 7, i32 = I32M == 0 ? mt2 : I32M == 1 ? 16 * (mt2 & 1) + mt1 : I32M == 2 ? 31 - mt2 : 16 * ((mt2 ^ (j >> (I32M < 3 ? 0 : I32M - 3))) & 1) + mt1;

constexpr int SC = 102400;
constexpr int REGSZ = 13056;
constexpr int L_XR = SC;
constexpr int L_CO = SC + 12288;
constexpr int L_KB = SC + 51712;
constexpr int L_TAB = SC + 55808;
constexpr int L_CW = SC + 39168;
constexpr int INBOX = 15360;
constexpr int l_rs(int l, int w) { return l ? SC + w * INBOX : L_KB + w * 256; }
constexpr int l_sg(int l, int w) { return l ? SC + w * INBOX + 256 : SC + (w == 0 ? 43264 : w == 1 ? 46080 : w == 2 ? 52736 : 56320); }
constexpr int l_tb(int l, int w) { return l ? SC + w * INBOX + 2048 : SC + 40960 + w * 5120; }
constexpr int LDS_BYTES = 163840;
constexpr int CV_E = 4608;
constexpr int CV_O = CV_E + (1056 + 15) * 4;

struct Params {
    const u16 *x0, *Lre, *Lim, *Bre, *Bim, *Cre, *Cim, *logdt, *Dskip, *k, *v, *shk, *ssmk, *ssmD;
    char* ws;
    float* out;
};

#define MF "v_mfma_f32_16x16x32_bf16 "
DI void mf(f32x4& c, v4u a, v4u b) { asm(MF "%0, %1, %2, %0" : "+v"(c) : "v"(a), "v"(b)); }
DI void mf0(f32x4& c, v4u a, v4u b) { asm(MF "%0, %1, %2, 0" : "=&v"(c) : "v"(a), "v"(b)); }
constexpr int W2R = 56;
#define WA(n) "a[%c" #n ":%c" #n "+3]"
template <int R> DI void mfa(f32x4& c, v4u a) { asm(MF "%0, " WA(2) ", %1, %0" : "+v"(c) : "v"(a), "i"(W2R + 4 * R)); }
template <int R> DI void mfa0(f32x4& c, v4u a) { asm(MF "%0, " WA(2) ", %1, 0" : "=&v"(c) : "v"(a), "i"(W2R + 4 * R)); }
#define ACC_FENCE10(c, d) asm volatile("" : "+v"(c[0]), "+v"(c[1]), "+v"(c[2]), "+v"(c[3]), "+v"(c[4]), "+v"(d[0]), "+v"(d[1]), "+v"(d[2]), "+v"(d[3]), "+v"(d[4]))
#define ACC_TIE5(c) asm volatile("" : "+v"(c[0]), "+v"(c[1]), "+v"(c[2]), "+v"(c[3]), "+v"(c[4]))
#define QP " quad_perm:[1,0,3,2] row_mask:0xf bank_mask:0xf"
DI void cstep(float (&t)[5], const float (&h)[5], const float (&lr)[5], const float (&ls)[5], float b0, float b1, float b2, float b3, float b4) {
    asm("v_fma_f32 %0, %10, %5, %20\n\tv_fma_f32 %1, %11, %6, %21\n\tv_fma_f32 %2, %12, %7, %22\n\tv_fma_f32 %3, %13, %8, %23\n\tv_fma_f32 %4, %14, %9, %24\n\t"
        "v_fmac_f32_dpp %0, %5, %15" QP "\n\tv_fmac_f32_dpp %1, %6, %16" QP "\n\tv_fmac_f32_dpp %2, %7, %17" QP "\n\tv_fmac_f32_dpp %3, %8, %18" QP "\n\tv_fmac_f32_dpp %4, %9, %19" QP
        : "=&v"(t[0]), "=&v"(t[1]), "=&v"(t[2]), "=&v"(t[3]), "=&v"(t[4])
        : "v"(h[0]), "v"(h[1]), "v"(h[2]), "v"(h[3]), "v"(h[4]), "v"(lr[0]), "v"(lr[1]), "v"(lr[2]), "v"(lr[3]), "v"(lr[4]),
          "v"(ls[0]), "v"(ls[1]), "v"(ls[2]), "v"(ls[3]), "v"(ls[4]), "v"(b0), "v"(b1), "v"(b2), "v"(b3), "v"(b4));
}
DI void cstep3(float* t, const float* h, const float* lr, const float* ls, const float* b) {
    asm("v_fma_f32 %0, %6, %3, %12\n\tv_fma_f32 %1, %7, %4, %13\n\tv_fma_f32 %2, %8, %5, %14\n\t"
        "v_fmac_f32_dpp %0, %3, %9" QP "\n\tv_fmac_f32_dpp %1, %4, %10" QP "\n\tv_fmac_f32_dpp %2, %5, %11" QP
        : "=&v"(t[0]), "=&v"(t[1]), "=&v"(t[2])
        : "v"(h[0]), "v"(h[1]), "v"(h[2]), "v"(lr[0]), "v"(lr[1]), "v"(lr[2]), "v"(ls[0]), "v"(ls[1]), "v"(ls[2]), "v"(b[0]), "v"(b[1]), "v"(b[2]));
}
template <int o> DI void chain24(float (*t)[3], const float (*b)[5], const float* lr, const float* ls) {
    asm("v_fma_f32 %0, %18, %16, %8\n\t" "v_fma_f32 %1, %19, %17, %9\n\t" "v_fmac_f32_dpp %0, %16, %20" QP "\n\t" "v_fmac_f32_dpp %1, %17, %21" QP "\n\t"
        "v_fma_f32 %2, %18, %0, %10\n\t" "v_fma_f32 %3, %19, %1, %11\n\t" "v_fmac_f32_dpp %2, %0, %20" QP "\n\t" "v_fmac_f32_dpp %3, %1, %21" QP "\n\t"
        "v_fma_f32 %4, %18, %2, %12\n\t" "v_fma_f32 %5, %19, %3, %13\n\t" "v_fmac_f32_dpp %4, %2, %20" QP "\n\t" "v_fmac_f32_dpp %5, %3, %21" QP "\n\t"
        "v_fma_f32 %6, %18, %4, %14\n\t" "v_fma_f32 %7, %19, %5, %15\n\t" "v_fmac_f32_dpp %6, %4, %20" QP "\n\t" "v_fmac_f32_dpp %7, %5, %21" QP "\n\t"
        : "=&v"(t[1][0]), "=&v"(t[1][1]), "=&v"(t[2][0]), "=&v"(t[2][1]), "=&v"(t[3][0]), "=&v"(t[3][1]), "=&v"(t[4][0]), "=&v"(t[4][1])
        : "v"(b[0][o + 0]), "v"(b[0][o + 1]), "v"(b[1][o + 0]), "v"(b[1][o + 1]), "v"(b[2][o + 0]), "v"(b[2][o + 1]), "v"(b[3][o + 0]), "v"(b[3][o + 1]), "v"(t[0][0]), "v"(t[0][1]), "v"(lr[o + 0]), "v"(lr[o + 1]), "v"(ls[o + 0]), "v"(ls[o + 1]));
}
template <int o> DI void chain33(float (*t)[3], const float (*b)[5], const float* lr, const float* ls) {
    asm("v_fma_f32 %0, %21, %18, %9\n\t" "v_fma_f32 %1, %22, %19, %10\n\t" "v_fma_f32 %2, %23, %20, %11\n\t" "v_fmac_f32_dpp %0, %18, %24" QP "\n\t" "v_fmac_f32_dpp %1, %19, %25" QP "\n\t" "v_fmac_f32_dpp %2, %20, %26" QP "\n\t"
        "v_fma_f32 %3, %21, %0, %12\n\t" "v_fma_f32 %4, %22, %1, %13\n\t" "v_fma_f32 %5, %23, %2, %14\n\t" "v_fmac_f32_dpp %3, %0, %24" QP "\n\t" "v_fmac_f32_dpp %4, %1, %25" QP "\n\t" "v_fmac_f32_dpp %5, %2, %26" QP "\n\t"
        "v_fma_f32 %6, %21, %3, %15\n\t" "v_fma_f32 %7, %22, %4, %16\n\t" "v_fma_f32 %8, %23, %5, %17\n\t" "v_fmac_f32_dpp %6, %3, %24" QP "\n\t" "v_fmac_f32_dpp %7, %4, %25" QP "\n\t" "v_fmac_f32_dpp %8, %5, %26" QP "\n\t"
        : "=&v"(t[1][0]), "=&v"(t[1][1]), "=&v"(t[1][2]), "=&v"(t[2][0]), "=&v"(t[2][1]), "=&v"(t[2][2]), "=&v"(t[3][0]), "=&v"(t[3][1]), "=&v"(t[3][2])
        : "v"(b[0][o + 0]), "v"(b[0][o + 1]), "v"(b[0][o + 2]), "v"(b[1][o + 0]), "v"(b[1][o + 1]), "v"(b[1][o + 2]), "v"(b[2][o + 0]), "v"(b[2][o + 1]), "v"(b[2][o + 2]), "v"(t[0][0]), "v"(t[0][1]), "v"(t[0][2]), "v"(lr[o + 0]), "v"(lr[o + 1]), "v"(lr[o + 2]), "v"(ls[o + 0]), "v"(ls[o + 1]), "v"(ls[o + 2]));
}
template <int o> DI void chain32(float (*t)[3], const float (*b)[5], const float* lr, const float* ls) {
    asm("v_fma_f32 %0, %15, %12, %6\n\t" "v_fma_f32 %1, %16, %13, %7\n\t" "v_fma_f32 %2, %17, %14, %8\n\t" "v_fmac_f32_dpp %0, %12, %18" QP "\n\t" "v_fmac_f32_dpp %1, %13, %19" QP "\n\t" "v_fmac_f32_dpp %2, %14, %20" QP "\n\t"
        "v_fma_f32 %3, %15, %0, %9\n\t" "v_fma_f32 %4, %16, %1, %10\n\t" "v_fma_f32 %5, %17, %2, %11\n\t" "v_fmac_f32_dpp %3, %0, %18" QP "\n\t" "v_fmac_f32_dpp %4, %1, %19" QP "\n\t" "v_fmac_f32_dpp %5, %2, %20" QP "\n\t"
        : "=&v"(t[1][0]), "=&v"(t[1][1]), "=&v"(t[1][2]), "=&v"(t[2][0]), "=&v"(t[2][1]), "=&v"(t[2][2])
        : "v"(b[0][o + 0]), "v"(b[0][o + 1]), "v"(b[0][o + 2]), "v"(b[1][o + 0]), "v"(b[1][o + 1]), "v"(b[1][o + 2]), "v"(t[0][0]), "v"(t[0][1]), "v"(t[0][2]), "v"(lr[o + 0]), "v"(lr[o + 1]), "v"(lr[o + 2]), "v"(ls[o + 0]), "v"(ls[o + 1]), "v"(ls[o + 2]));
}
#define UNDEF8(c) asm volatile("" : "=v"(c[0]), "=v"(c[1]), "=v"(c[2]), "=v"(c[3]), "=v"(c[4]), "=v"(c[5]), "=v"(c[6]), "=v"(c[7]))
#define PIN5(t) asm volatile("s_nop 1" : "+v"(t[0]), "+v"(t[1]), "+v"(t[2]), "+v"(t[3]), "+v"(t[4]))
DI unsigned cvtpk(float lo, float hi) {
    f32x2 v = {lo, hi};
    return __builtin_bit_cast(unsigned, __builtin_convertvector(v, bf16x2v));
}
DI float lo16f(unsigned u) { return __uint_as_float(u << 16); }
DI float hi16f(unsigned u) { return __uint_as_float(u & 0xffff0000u); }
DI float rbf(float v) { return lo16f(cvtpk(v, 0.f)); }
template <int CTRL> DI float dpp(float x) {
    return __int_as_float(__builtin_amdgcn_update_dpp(0, __float_as_int(x), CTRL, 0xF, 0xF, true));
}
DI float rowsum16(float x) {
    x += dpp<0x128>(x); x += dpp<0x124>(x); x += dpp<0x122>(x); x += dpp<0x121>(x);
    return x;
}
DI float xsum32(float x) { auto r = __builtin_amdgcn_permlane32_swap(__float_as_uint(x), __float_as_uint(x), false, false); return __uint_as_float(r[0]) + __uint_as_float(r[1]); }
DI void xsum2(float& a, float& d) {
    auto r = __builtin_amdgcn_permlane16_swap(__float_as_uint(a), __float_as_uint(d), false, false);
    const float s = __uint_as_float(r[0]) + __uint_as_float(r[1]);
    const float t = xsum32(s);
    auto u = __builtin_amdgcn_permlane16_swap(__float_as_uint(t), __float_as_uint(t), false, false);
    a = __uint_as_float(u[0]); d = __uint_as_float(u[1]);
}
DI float xsum2h(float a, float d) {
    auto r = __builtin_amdgcn_permlane16_swap(__float_as_uint(a), __float_as_uint(d), false, false);
    return xsum32(__uint_as_float(r[0]) + __uint_as_float(r[1]));
}
DI float hsum4(f32x4 v) { return (v.x + v.y) + (v.z + v.w); }
typedef __amdgpu_buffer_rsrc_t rsrc_t;
constexpr int SC1 = 16;
DI v4u ld128(rsrc_t rs, unsigned off) { return __builtin_amdgcn_raw_buffer_load_b128(rs, off, 0, SC1); }
DI v4u ld128s(rsrc_t rs, unsigned off, unsigned soff) { return __builtin_amdgcn_raw_buffer_load_b128(rs, off, soff, SC1); }
DI v2u ld64(rsrc_t rs, unsigned off) { return __builtin_amdgcn_raw_buffer_load_b64(rs, off, 0, SC1); }
DI void st128(v4u v, rsrc_t rs, unsigned off) { __builtin_amdgcn_raw_buffer_store_b128(v, rs, off, 0, SC1); }
DI void st64(v2u v, rsrc_t rs, unsigned off) { __builtin_amdgcn_raw_buffer_store_b64(v, rs, off, 0, SC1); }
#define LGKM0() { __builtin_amdgcn_s_waitcnt(0xC07F); asm volatile("" ::: "memory"); }
#define DRAIN() asm volatile("s_waitcnt vmcnt(0)" ::: "memory")
#define ORDER() __builtin_amdgcn_fence(__ATOMIC_ACQUIRE, "wavefront")
#define SCHED() __builtin_amdgcn_sched_barrier(0)
constexpr unsigned kSpinCap = 1u << 17;
#define WAVE_WAIT(...)                                                      \
    {                                                                       \
        unsigned spins_ = dead ? kSpinCap : 0u;                             \
        _Pragma("nounroll") for (;;) {                                      \
            asm volatile("" ::: "memory");                                  \
            bool ok;                                                        \
            __VA_ARGS__;                                                    \
            if (__builtin_amdgcn_ballot_w64(!ok) == 0ull) break;            \
            if (++spins_ > kSpinCap) { dead = true; break; }                \
            __builtin_amdgcn_s_sleep(1);                                    \
        }                                                                   \
        ORDER();                                                            \
    }
#define KPD1 2
#define KPDS 8
#define KPDS0 2
#define KPF0 12
#define KPF1 4
constexpr int PD1 = KPD1, PDS = KPDS, PDS0 = KPDS0;
#define PL(r) "global_load_dwordx2 " r ", %5, %6 sc1\n\t"
#define PS_(r) "s_waitcnt vmcnt(1)\n\tv_cmp_ne_u64 vcc, %7, " r "\n\ts_cbranch_vccz 2f\n\t" PL(r)
DI void pipe_flag(v2u (&t)[4], unsigned off, const void* base, u64 tag, bool& dead) {
    unsigned n = dead ? 0u : kSpinCap;
    asm volatile("s_sleep %8\n\t" PL("%0") "s_sleep %9\n\t" PL("%1")
                 "1:\n\t" PS_("%0") PS_("%1") "s_sub_u32 %4, %4, 1\n\ts_cbranch_scc0 1b\n2:"
                 : "=&v"(t[0]), "=&v"(t[1]), "=&v"(t[2]), "=&v"(t[3]), "+s"(n) : "v"(off), "s"(base), "s"(tag), "i"(KPF0), "i"(KPF1) : "memory", "vcc", "scc");
    dead = __builtin_amdgcn_readfirstlane(n) == ~0u;
}
#define WAVE_RETRY(OK, ...) { if (__builtin_expect(__builtin_amdgcn_ballot_w64(!(OK)) != 0ull, 0)) WAVE_WAIT({ __VA_ARGS__; ok = OK; }) ORDER(); }
#define STAMP(i)
#define STAMP_DUMP()

#define PADS 9000, 9001, 9002, 9003, 9004, 9005, 9006, 9007, 9008
#define T1POS 27
#define TW 2
#define KLB0 0
#define KLB1 10
#define SKIP0 0
#define SKIP7 0
#define SKIP5 0
#define SKIP4 0
constexpr int kPads[] = {PADS};
constexpr int pad_next(int id) {
    for (unsigned i = 0; i + 1 < sizeof(kPads) / sizeof(int); ++i) if (kPads[i] == id) return kPads[i + 1];
    return -1;
}
template <int ID> DI void site() {
    if constexpr (pad_next(ID) >= 0) asm volatile("kp%c0:\n\ts_cbranch_execz kp%c1" ::"i"(ID), "i"(pad_next(ID)));
    else asm volatile("");
}
template <int T> DI void tour() {
    if constexpr (pad_next(9000 + T) >= 0 && pad_next(9000 + T) != 9001 + T) asm volatile("s_mov_b64 exec, 0\n\ts_branch kp%c0\nkp%c1:\n\ts_mov_b64 exec, -1" ::"i"(pad_next(9000 + T)), "i"(9001 + T));
}
template <int N> DI void skip() { if constexpr (N > 0) asm volatile("s_branch 1f\n\t.rept %c0\n\ts_nop 0\n\t.endr\n1:" ::"i"(N)); }
template <class F, int... I> DI void static_for_impl(std::integer_sequence<int, I...>, F&& f) { (f(std::integral_constant<int, I>{}), ...); }
template <int N, class F> DI void static_for(F&& f) { static_for_impl(std::make_integer_sequence<int, N>{}, f); }
#define LAMBDA(v) [&](auto v) __attribute__((always_inline))
#define PK2 " op_sel:[1,1,0] op_sel_hi:[1,0,1] neg_lo:[1,0,0]"
DI f32x2 cmul(f32x2 a, f32x2 b) {
    f32x2 t;
    asm("v_pk_mul_f32 %0, %1, %2 op_sel_hi:[0,1]\n\ts_nop 0\n\tv_pk_fma_f32 %0, %1, %2, %0" PK2 "\n\ts_nop 0" : "=&v"(t) : "v"(a), "v"(b));
    return t;
}
DI f32x2 cfma(f32x2 a, f32x2 b, f32x2 c) {
    f32x2 t;
    asm("v_pk_fma_f32 %0, %1, %2, %3 op_sel_hi:[0,1,1]\n\ts_nop 0\n\tv_pk_fma_f32 %0, %1, %2, %0" PK2 "\n\ts_nop 0" : "=&v"(t) : "v"(a), "v"(b), "v"(c));
    return t;
}
DI void cfma4(f32x2 (&t)[4], f32x2 a, f32x2 b0, f32x2 b1, f32x2 b2, f32x2 b3, f32x2 c0, f32x2 c1, f32x2 c2, f32x2 c3) {
    asm("v_pk_fma_f32 %0, %4, %5, %9 op_sel_hi:[0,1,1]\n\tv_pk_fma_f32 %1, %4, %6, %10 op_sel_hi:[0,1,1]\n\tv_pk_fma_f32 %2, %4, %7, %11 op_sel_hi:[0,1,1]\n\tv_pk_fma_f32 %3, %4, %8, %12 op_sel_hi:[0,1,1]\n\t"
        "v_pk_fma_f32 %0, %4, %5, %0" PK2 "\n\tv_pk_fma_f32 %1, %4, %6, %1" PK2 "\n\tv_pk_fma_f32 %2, %4, %7, %2" PK2 "\n\tv_pk_fma_f32 %3, %4, %8, %3" PK2 "\n\ts_nop 0"
        : "=&v"(t[0]), "=&v"(t[1]), "=&v"(t[2]), "=&v"(t[3]) : "v"(a), "v"(b0), "v"(b1), "v"(b2), "v"(b3), "v"(c0), "v"(c1), "v"(c2), "v"(c3));
}
DI void gacc2(f32x2& h0, f32x2& h1, f32x2 w0, f32x2 w1, v4u e0, v4u e1) {
    const v2u a0 = {e0.x, e0.y}, b0 = {e0.z, e0.w}, a1 = {e1.x, e1.y}, b1 = {e1.z, e1.w};
    asm("v_pk_fma_f32 %0, %4, %2, %0 op_sel_hi:[0,1,1]\n\tv_pk_fma_f32 %1, %6, %3, %1 op_sel_hi:[0,1,1]\n\t"
        "v_pk_fma_f32 %0, %5, %2, %0 op_sel:[0,1,0] op_sel_hi:[0,0,1] neg_lo:[1,0,0]\n\tv_pk_fma_f32 %1, %7, %3, %1 op_sel:[0,1,0] op_sel_hi:[0,0,1] neg_lo:[1,0,0]"
        : "+v"(h0), "+v"(h1) : "v"(w0), "v"(w1), "v"(a0), "v"(b0), "v"(a1), "v"(b1));
}
DI s16x4 trread(char* smem, int byteoff) {
    return __builtin_amdgcn_ds_read_tr16_b64_v4i16((LAS s16x4*)(smem + byteoff));
}

#define CV_LOAD_A(A, a, rr_, hf) { const unsigned* ap_ = (const unsigned*)(smem + ab[(a) >> 1] + (((a) & 1) ? 0 : 512) + 64 * (7 - (rr_)) + 8 * (hf)); A[a][2 * (hf)] = ap_[0]; A[a][2 * (hf) + 1] = ap_[1]; }
#define CV_LOAD_B(B, e, rr_) { B[e] = *(const v4u*)(smem + bb + 64 * (7 - (rr_)) + 512 * (e)); }
template <int R, bool PF, class F, class G>
DI void conv_round(f32x4 (&ca)[8], const v4u (&A)[8], const v4u (&B)[8], v4u (&An)[8], v4u (&Bn)[8], const char* smem, const int (&ab)[4], int bb, F&& fill, G&& last) {
    static_for<36>(LAMBDA(g_) {
        constexpr int g = g_.value;
        constexpr int a = g < 8 ? 0 : g < 15 ? 1 : g < 21 ? 2 : g < 26 ? 3 : g < 30 ? 4 : g < 33 ? 5 : g < 35 ? 6 : 7;
        constexpr int e = g - (a * 8 - a * (a - 1) / 2);
        if constexpr (R == 0 && a == 0) mf0(ca[e], A[0], B[e]); else mf(ca[a + e], A[a], B[e]);
        SCHED();
        if constexpr (PF && g < 32 && (g & 3) != 3) { if constexpr ((g & 3) == 2) CV_LOAD_B(Bn, g >> 2, R + 1) else CV_LOAD_A(An, g >> 2, R + 1, g & 1) SCHED(); }
        site<3000 + 36 * R + g>();
        if constexpr (R == 7) { last(g_); SCHED(); }
        if constexpr ((g & 3) == 3 && g < 28) { fill(std::integral_constant<int, 7 * R + g / 4>{}); SCHED(); }
    });
}
template <int R0, int R1, class F, class G, class M>
DI void conv_part(f32x4 (&ca)[8], const char* smem, const int (&ab)[4], int bb, F&& fill, G&& last, M&& mid) {
    v4u A[2][8], B[2][8];
    static_for<(R0 ? 1 : 8)>(LAMBDA(a_) { constexpr int a = a_.value; CV_LOAD_A(A[0], a, R0, 0) CV_LOAD_A(A[0], a, R0, 1) });
    SCHED();
    mid();
    static_for<8>(LAMBDA(a_) { constexpr int a = a_.value; CV_LOAD_B(B[0], a, R0) SCHED(); });
    if constexpr (R0 > 0) static_for<7>(LAMBDA(a_) { constexpr int a = a_.value + 1; CV_LOAD_A(A[0], a, R0, 0) CV_LOAD_A(A[0], a, R0, 1) });
    SCHED();
    static_for<R1 - R0>(LAMBDA(r_) {
        constexpr int r = r_.value;
        conv_round<R0 + r, (R0 + r + 1 < R1)>(ca, A[r & 1], B[r & 1], A[(r & 1) ^ 1], B[(r & 1) ^ 1], smem, ab, bb, fill, last);
    });
}
template <int IMM> DI void gld(v4u& dst, unsigned voff, const void* sbase) {
    asm volatile("global_load_dwordx4 %0, %1, %2 offset:%3" : "=v"(dst) : "v"(voff), "s"(sbase), "i"(IMM));
}
#define OOB 3
#define OUTP 16
#define NTM 31
template <int IMM, int BIT> DI void gldn(v4u& dst, unsigned voff, const void* sbase) {
    if constexpr ((NTM >> BIT) & 1) asm volatile("global_load_dwordx4 %0, %1, %2 offset:%3 nt" : "=v"(dst) : "v"(voff), "s"(sbase), "i"(IMM));
    else gld<IMM>(dst, voff, sbase);
}
DI void gldu16(unsigned& dst, unsigned voff, const void* sbase) { asm volatile("global_load_ushort %0, %1, %2" : "=v"(dst) : "v"(voff), "s"(sbase)); }
template <int IMM> DI void gld64(v4u& dst, const void* a) {
    if constexpr ((NTM >> 5) & 1) asm volatile("global_load_dwordx4 %0, %1, off offset:%2 nt" : "=v"(dst) : "v"(a), "i"(IMM));
    else asm volatile("global_load_dwordx4 %0, %1, off offset:%2" : "=v"(dst) : "v"(a), "i"(IMM));
}
template <int V> DI void SETM0(unsigned ldsbase) { asm volatile("s_add_u32 m0, %0, %1" ::"s"(ldsbase), "i"(V)); }
template <int GOFF> DI void GLD0(unsigned voff, const char* sbase) { asm volatile("global_load_lds_dwordx4 %0, %1 offset:%2" ::"v"(voff), "s"(sbase), "i"(GOFF) : "memory"); }
template <int GOFF, int V> DI void GLD1(unsigned voff, const char* sbase, unsigned ldsbase) {
    asm volatile("global_load_lds_dwordx4 %0, %1 offset:%3\n\ts_add_u32 m0, %2, %4" ::"v"(voff), "s"(sbase), "s"(ldsbase), "i"(GOFF), "i"(V) : "memory");
}
constexpr int dma_goff(int it) { return it < 40 ? (it >> 2) * 128 : ((it - 100) / 5) * 256; }
constexpr int dma_loff(int it) { return it < 40 ? L_XR + ((it >> 2) % 3) * 4096 + (it & 3) * 1024 : ((it - 100) % 5) * 20480 + ((it - 100) / 5) * 2048; }
#define KFS 4
#define APOS 0
#define PSA 0
#define CWP 1
#define SAF 2
#define KD0 36
#define KDS 3
#define KNW 11
constexpr int kD0 = KD0, kDS = KDS;
static_assert(kD0 + 26 * kDS < 117, "");
#define KW2S 16
#define KW2M 0x4A
#define KFWP 13
constexpr int kW2S = KW2S, kW2M = KW2M;
constexpr int NW2P = (20 - kW2S) * __builtin_popcount(kW2M);
constexpr int P1_PRE = 34;
constexpr int p1_pre(int i) { return i < 5 ? 100 + i : i < 13 ? i - 5 : i < 18 ? 105 + (i - 13) : i < 22 ? 8 + (i - 18) : i < 27 ? 110 + (i - 22) : 400; }
constexpr int p1_item(int s, int g) {
    if (s == KFS && g == 4) return 324;
    if (g == 9) return s < 10 ? 115 + s : s == 11 ? 202 : s == 12 ? 203 : -1;
    if (s == KFWP && g == 1) return 208;
    if (s >= kW2S && ((kW2M >> g) & 1)) return 500 + (s - kW2S) * __builtin_popcount(kW2M) + __builtin_popcount(kW2M & ((1 << g) - 1));
    if ((s & 1) == 0 && s <= 12) return g >= 5 ? 4 * (s / 2 + 3) + (g - 5) : -1;
    if (s == 14) return g == 5 ? 207 : g == 7 ? 206 : -1;
    return -1;
}
constexpr int p1_cnt(int s, int g) {
    int n = P1_PRE;
    for (int t = 0; t <= s; ++t)
        for (int u = 0; u < 10; ++u) {
            if (t == s && u >= g) break;
            if (p1_item(t, u) >= 0 && p1_item(t, u) != 319) ++n;
        }
    return n;
}
constexpr int p1_pos(int code) {
    for (int i = 0; i < P1_PRE; ++i) if (p1_pre(i) == code) return i;
    for (int t = 0; t < 20; ++t)
        for (int u = 0; u < 10; ++u) if (p1_item(t, u) == code) return p1_cnt(t, u);
    return -1000;
}
constexpr int p1_cap(int s, int g) {
    int u = (s < 19 && g < 5) + (g == 9 && s < 19);
    if ((s & 1) && s < 19 && g >= 5 && g < 9) u += 1 + (g == 5);
    if ((s & 1) == 0 && g == 4) u += 1;
    const int it = p1_item(s, g), nt = g < 9 ? p1_item(s, g + 1) : -1;
    u += (it >= 0 && it < 500 && it != 208) + (nt >= 0 && nt < 200) + (it == 324 ? 2 : 0);
    if ((s == 18 && g == 1) || (s == 19 && g == 2) || (g == 9 && ((s & 3) == 2 || s == 17))) u += 2;
    return u < 2 ? 2 - u : 0;
}
constexpr int p1_slot(int s0, int s, int g, int gmax) {
    int n = 0;
    for (int t = s0; t <= s; ++t)
        for (int u = 0; u < gmax && !(t == s && u >= g); ++u) n += p1_cap(t, u);
    return n;
}
constexpr int p1_lim(int n) { return n > 63 ? 63 : n; }
template <int IMM> DI void gld4s(v4u& dst, unsigned voff, const void* sbase) { asm volatile("global_load_dwordx4 %0, %1, %2 offset:%3 sc1" : "=v"(dst) : "v"(voff), "s"(sbase), "i"(IMM)); }
template <int IMM> DI void gld2s(v2u& dst, unsigned voff, const void* sbase) { asm volatile("global_load_dwordx2 %0, %1, %2 offset:%3 sc1" : "=v"(dst) : "v"(voff), "s"(sbase), "i"(IMM)); }
template <class T> DI void pin(T& v) { asm volatile("" : "+v"(v)); }
DI void mul2(float& a, float& b, float x, float y, float s) { asm volatile("v_mul_f32 %0, %2, %4\n\tv_mul_f32 %1, %3, %4" : "=&v"(a), "=&v"(b) : "v"(x), "v"(y), "v"(s)); }
DI void xor2(float& a, float& b, unsigned x, unsigned y, unsigned s) { asm volatile("v_xor_b32 %0, %2, %4\n\tv_xor_b32 %1, %3, %4" : "=&v"(a), "=&v"(b) : "v"(x), "v"(y), "v"(s)); }
DI void rot2(unsigned& a, unsigned& b, unsigned x, unsigned y) { asm volatile("v_alignbit_b32 %0, %2, %2, 16\n\tv_alignbit_b32 %1, %3, %3, 16" : "=&v"(a), "=&v"(b) : "v"(x), "v"(y)); }
DI void mix2(unsigned& a, unsigned& b, unsigned x, unsigned y, unsigned z) { asm volatile("v_bfi_b32 %0, %5, %2, %3\n\tv_bfi_b32 %1, %5, %3, %4" : "=&v"(a), "=&v"(b) : "v"(x), "v"(y), "v"(z), "s"(0xffffu)); }
template <int I> DI void shr1(unsigned& o, unsigned prev, unsigned x) {
    if constexpr (I > 0) asm volatile("v_mov_b32_dpp %0, %1 wave_ror:1 row_mask:0xf bank_mask:0xf\n\tv_mov_b32_dpp %0, %2 wave_shr:1 row_mask:0xf bank_mask:0xf" : "=&v"(o) : "v"(prev), "v"(x));
    else asm volatile("v_mov_b32_dpp %0, %1 wave_shr:1 row_mask:0xf bank_mask:0xf bound_ctrl:0" : "=&v"(o) : "v"(x));
}
template <int G> DI void bc2(float& a, float& b, unsigned x) {
    asm volatile("v_mov_b32_dpp %0, %2 row_newbcast:%3 row_mask:0xf bank_mask:0xf\n\tv_mov_b32_dpp %1, %2 row_newbcast:%4 row_mask:0xf bank_mask:0xf" : "=&v"(a), "=&v"(b) : "v"(x), "i"(G), "i"(G + 8));
}
DI void rot1(unsigned& a, unsigned x) { asm volatile("v_alignbit_b32 %0, %1, %1, 16" : "=v"(a) : "v"(x)); }
DI void mix1(unsigned& a, unsigned x, unsigned y) { asm volatile("v_bfi_b32 %0, %3, %1, %2" : "=v"(a) : "v"(x), "v"(y), "s"(0xffffu)); }
DI void ror1(unsigned& o, unsigned prev) { asm volatile("v_mov_b32_dpp %0, %1 wave_ror:1 row_mask:0xf bank_mask:0xf" : "=v"(o) : "v"(prev)); }
DI void shr1b(unsigned& o, unsigned x) { asm volatile("v_mov_b32_dpp %0, %1 wave_shr:1 row_mask:0xf bank_mask:0xf" : "+v"(o) : "v"(x)); }
DI void xor1(float& a, unsigned x, unsigned s) { asm volatile("v_xor_b32 %0, %1, %2" : "=v"(a) : "v"(x), "v"(s)); }
template <int G> DI void bc1(float& a, unsigned x) { asm volatile("v_mov_b32_dpp %0, %1 row_newbcast:%2 row_mask:0xf bank_mask:0xf" : "=v"(a) : "v"(x), "i"(G)); }
template <int G> DI void mulb(float& o, unsigned x, float y) { asm volatile("v_mul_f32_dpp %0, %1, %2 row_newbcast:%3 row_mask:0xf bank_mask:0xf" : "=v"(o) : "v"(x), "v"(y), "i"(G)); }
template <int N> DI void lgkm() { __builtin_amdgcn_s_waitcnt(0xC07F | (N << 8)); asm volatile("" ::: "memory"); }
DI void unp2(float& a, float& b, unsigned x) { asm volatile("v_lshlrev_b32 %0, 16, %2\n\tv_and_b32 %1, 0xffff0000, %2" : "=&v"(a), "=&v"(b) : "v"(x)); }
#define GL(a) "global_load_dwordx4 a[" #a ":" #a "+3], %7, %8 sc1\n\tglobal_load_dwordx4 a[" #a "+4:" #a "+7], %7, %8 offset:256 sc1\n\t"
#define AR(d, a, o) "v_accvgpr_read_b32 " d ", a[" #a "+" #o "]\n\t"
#define GS_(a) "s_waitcnt vmcnt(4)\n\t" AR("%2", a, 1) AR("%3", a, 3) AR("%4", a, 5) AR("%5", a, 7) AR("%0", a, 0) AR("%1", a, 2)                              \
    "v_xor_b32 %2, %9, %2\n\tv_bitop3_b32 %4, %4, %9, %2 bitop3:0xbe\n\tv_bitop3_b32 %4, %3, %9, %4 bitop3:0xbe\n\tv_bitop3_b32 %4, %5, %9, %4 bitop3:0xbe\n\t" \
    AR("%2", a, 4) AR("%3", a, 6) "v_cmp_ne_u32 vcc, 0, %4\n\ts_cbranch_vccz 2f\n\t" GL(a)
template <int D0> DI void pipe_gran(float& a, float& d, unsigned off, const void* base, unsigned tag, bool& dead) {
    unsigned n = dead ? 0u : kSpinCap, y, z;
    float a1, d1;
    asm volatile("s_sleep %10\n\t" GL(32) "s_sleep %11\n\t" GL(40) "s_sleep %11\n\t" GL(48)
                 "1:\n\t" GS_(32) GS_(40) GS_(48) "s_sub_u32 %6, %6, 1\n\ts_cbranch_scc0 1b\n2:"
                 : "=&v"(a), "=&v"(d), "=&v"(a1), "=&v"(d1), "=&v"(y), "=&v"(z), "+s"(n) : "v"(off), "s"(base), "s"(tag), "i"(D0), "i"(PD1) : "memory", "vcc", "scc");
    dead = __builtin_amdgcn_readfirstlane(n) == ~0u;
    a += a1; d += d1;
}
DI void fma2(float& a, float& b, float x, float y, float s, float t, float u, float v) {
    asm volatile("v_fma_f32 %0, %2, %4, %6\n\tv_fma_f32 %1, %3, %5, %7" : "=&v"(a), "=&v"(b) : "v"(x), "v"(y), "v"(s), "v"(t), "v"(u), "v"(v));
}
DI void swp2(unsigned& a, unsigned& b, unsigned& c, unsigned& d) { asm volatile("s_nop 1\n\tv_permlane16_swap_b32 %0, %1\n\tv_permlane16_swap_b32 %2, %3" : "+v"(a), "+v"(b), "+v"(c), "+v"(d)); }
template <int G> DI void bc2b(float& a, float& b, float x, float y) {
    asm volatile("v_mov_b32_dpp %0, %2 row_newbcast:%4 row_mask:0xf bank_mask:0xf\n\tv_mov_b32_dpp %1, %3 row_newbcast:%4 row_mask:0xf bank_mask:0xf" : "=&v"(a), "=&v"(b) : "v"(x), "v"(y), "i"(G));
}
template <class T> DI void pins(T& v) { asm volatile("" : "+s"(v)); }
template <int I> DI void gld_w2(unsigned voff, const void* sbase) {
    asm volatile("global_load_dwordx4 " WA(2) ", %0, %1 offset:%3 sc1" ::"v"(voff), "s"(sbase), "i"(W2R + 4 * I), "i"((I % 5) * 1024 - 2048));
}
DI void gld2t(v2u& dst, unsigned voff, const void* sbase) { asm volatile("global_load_dwordx2 %0, %1, %2 sc1" : "+v"(dst) : "v"(voff), "s"(sbase)); }
DI void wait2(v2u& v, v4u (&x)[3]) { asm volatile("s_waitcnt vmcnt(0)" : "+v"(v), "+v"(x[0]), "+v"(x[1]), "+v"(x[2])::"memory"); }
template <int IMM> DI void gld4p(v4u& dst, unsigned voff, const void* sbase) { asm volatile("global_load_dwordx4 %0, %1, %2 offset:%3" : "+v"(dst) : "v"(voff), "s"(sbase), "i"(IMM)); }
DI void undef4(v4u (&x)[4]) { asm volatile("" : "=v"(x[0]), "=v"(x[1]), "=v"(x[2]), "=v"(x[3])); }
DI void wait1(v2u& v) { asm volatile("s_waitcnt vmcnt(0)" : "+v"(v)::"memory"); }
template <int N> DI void wait_fkn(v2u& v, v2u& u, unsigned& x) { asm volatile("s_waitcnt vmcnt(%3)" : "+v"(v), "+v"(u), "+v"(x) : "i"(N) : "memory"); }
DI void wait_fk(v2u& v, v2u& u, unsigned& x) { asm volatile("s_waitcnt vmcnt(0)" : "+v"(v), "+v"(u), "+v"(x)::"memory"); }
DI void ld_ur(unsigned o, const void* base) {
    asm volatile("global_load_dwordx4 a[40:40+3], %0, %1 sc1\n\tglobal_load_dwordx4 a[44:44+3], %0, %1 offset:1024 sc1\n\t"
                 "global_load_dwordx4 a[48:48+3], %0, %1 offset:2048 sc1\n\tglobal_load_dwordx4 a[52:52+3], %0, %1 offset:3072 sc1" ::"v"(o), "s"(base));
}
DI void st_ur(unsigned a) {
    asm volatile("s_waitcnt vmcnt(%1)\n\tds_write_b128 %0, a[40:40+3] offset:480\n\tds_write_b128 %0, a[44:44+3] offset:1504\n\t"
                 "ds_write_b128 %0, a[48:48+3] offset:2528\n\tds_write_b128 %0, a[52:52+3] offset:3552" ::"v"(a), "i"(2 * __builtin_popcount(kW2M)) : "memory");
}
DI void wait4(v4u (&x)[4]) { asm volatile("s_waitcnt vmcnt(0)" : "+v"(x[0]), "+v"(x[1]), "+v"(x[2]), "+v"(x[3])::"memory"); }
template <int A, int IMM> DI void lda4(unsigned o, const void* base) { asm volatile("global_load_dwordx4 a[%c2:%c2+3], %0, %1 offset:%3" ::"v"(o), "s"(base), "i"(A), "i"(IMM)); }
template <int A> DI void rda(unsigned& v) { asm volatile("v_accvgpr_read_b32 %0, a[%c1+0]" : "=v"(v) : "i"(A)); }
DI void poll1(unsigned a, const void* base) { asm volatile("global_load_dwordx2 a[36:36+1], %0, %1 sc1" ::"v"(a), "s"(base)); }
DI void poll2(unsigned a, unsigned b, const void* base) { asm volatile("global_load_dwordx2 a[36:36+1], %0, %2 sc1\n\tglobal_load_dwordx2 a[36+2:36+3], %1, %2 sc1" ::"v"(a), "v"(b), "s"(base)); }
DI void touch_(unsigned a, const void* base) { asm volatile("global_load_dword a[32+1], %0, %1" ::"v"(a), "s"(base)); }
template <int N> DI void wait_st(v2u& a, v4u& b) { asm volatile("s_waitcnt vmcnt(%2)" : "+v"(a), "+v"(b) : "i"(N) : "memory"); }
DI void undef3(v4u (&x)[3]) { asm volatile("" : "=v"(x[0]), "=v"(x[1]), "=v"(x[2])); }
template <int IMM> DI void gld4t(v4u& dst, unsigned voff, const void* sbase) { asm volatile("global_load_dwordx4 %0, %1, %2 offset:%3 sc1" : "+v"(dst) : "v"(voff), "s"(sbase), "i"(IMM)); }
template <int N> DI void vm_wait() { asm volatile("s_waitcnt vmcnt(%0)" ::"i"(N) : "memory"); }
DI float dot2a(unsigned a, unsigned b, float c) {
    return __builtin_amdgcn_fdot2_f32_bf16(__builtin_bit_cast(bf16x2v, a), __builtin_bit_cast(bf16x2v, b), c, false);
}
DI void disc(unsigned l0, unsigned l1, unsigned l2, f32x2& lam, f32x2& co) {
    const float dt = rbf(__builtin_amdgcn_exp2f(lo16f(l2) * 1.44269504088896f));
    const float lre = lo16f(l0), lim = lo16f(l1);
    const float e_ = __builtin_amdgcn_exp2f(dt * lre * 1.44269504088896f);
    const float rv = __builtin_amdgcn_fractf(dt * lim * 0.159154943091895f);
    lam = f32x2{e_ * __builtin_amdgcn_cosf(rv), e_ * __builtin_amdgcn_sinf(rv)};
    const float nr = lam.x - 1.0f, ni = lam.y, id = __builtin_amdgcn_rcpf(lre * lre + lim * lim);
    co = f32x2{(nr * lre + ni * lim) * id, (ni * lre - nr * lim) * id};
}
DI f32x4 unpk4(v2u u) { return f32x4{lo16f(u.x), hi16f(u.x), lo16f(u.y), hi16f(u.y)}; }
#define D2 "v_dot2_f32_bf16 "
#define CP "v_cvt_pk_bf16_f32 "
DI void d2x4(unsigned* o, unsigned a0, unsigned a1, unsigned a2, unsigned a3, unsigned b0, unsigned b1, unsigned b2, unsigned b3) {
    asm volatile(D2 "%0, %4, %8, 0\n\t" D2 "%1, %5, %9, 0\n\t" D2 "%2, %6, %10, 0\n\t" D2 "%3, %7, %11, 0"
                 : "=&v"(o[0]), "=&v"(o[1]), "=&v"(o[2]), "=&v"(o[3]) : "v"(a0), "v"(a1), "v"(a2), "v"(a3), "v"(b0), "v"(b1), "v"(b2), "v"(b3));
}
DI void cpx4(unsigned* o, unsigned a0, unsigned a1, unsigned a2, unsigned a3, unsigned b0, unsigned b1, unsigned b2, unsigned b3) {
    asm volatile(CP "%0, %4, %8\n\t" CP "%1, %5, %9\n\t" CP "%2, %6, %10\n\t" CP "%3, %7, %11"
                 : "=&v"(o[0]), "=&v"(o[1]), "=&v"(o[2]), "=&v"(o[3]) : "v"(a0), "v"(a1), "v"(a2), "v"(a3), "v"(b0), "v"(b1), "v"(b2), "v"(b3));
}
DI void cpx4i(unsigned* t, const unsigned* ka) {
    asm volatile(CP "%0, %0, %4\n\t" CP "%1, %1, %5\n\t" CP "%2, %2, %6\n\t" CP "%3, %3, %7"
                 : "+v"(t[0]), "+v"(t[1]), "+v"(t[2]), "+v"(t[3]) : "v"(ka[0]), "v"(ka[1]), "v"(ka[2]), "v"(ka[3]));
}
template <int IMM> DI void bld4(v4u& dst, unsigned voff, v4u rsrc) {
    if constexpr ((NTM >> 4) & 1) asm volatile("buffer_load_dwordx4 %0, %1, %2, 0 offen offset:%3 nt" : "=v"(dst) : "v"(voff), "s"(rsrc), "i"(IMM));
    else asm volatile("buffer_load_dwordx4 %0, %1, %2, 0 offen offset:%3" : "=v"(dst) : "v"(voff), "s"(rsrc), "i"(IMM));
}

__global__ __launch_bounds__(256, 1) __attribute__((aligned(4096))) void k_main(Params p) {
    __shared__ __attribute__((aligned(16))) char smem[LDS_BYTES];
    const rsrc_t rs = __builtin_amdgcn_make_buffer_rsrc((void*)p.ws, 0, WS_BYTES, 0x00020000);
    const rsrc_t ro = __builtin_amdgcn_make_buffer_rsrc((void*)p.out, 0, S_ * D_ * 4, 0x00020000);
    const int w0 = __builtin_amdgcn_readfirstlane(threadIdx.x >> 6), b0 = blockIdx.x;
    bool dead = false;
    asm volatile("" ::: "a255");
    float SW[5];
    f32x4 xin[5];
    v2u gt[5], dsk[5];
    f32x4 acc[2][5];
    f32x4 rr[2];
    float tt[2][5][4];
    float lr[5], ls[5], lqr[5], lqs[5];
    f32x2 l8, l32, l128;
    bool kearly;
    unsigned sdq;
    int sgn_, ab[4], bb, zpa, uwa, spa, tbw, tbr, cwa, spl, dst[3];
    unsigned w2o, wro, fwo, fko, xso, fho1, fho, hso, eo, es, ho[4];
    bool cv0;
    int mt1_;
    auto addrs = [&](auto layer_, int lane, int w, int b) __attribute__((always_inline)) {
        constexpr int layer = layer_.value;
        const int c = lane & 15, q = lane >> 4;
        TILES()
        static_assert(l_sg(0, 0) == SC + 0xA900 && l_sg(0, 1) == SC + 0xB400 && l_sg(0, 2) == SC + 0xCE00 && l_sg(0, 3) == SC + 0xDC00, "");
        sgn_ = (layer ? l_sg(1, 0) + w * INBOX : SC + (int)((0xDC00CE00B400A900ull >> (16 * w)) & 0xffff)) + q * 320 + c * 4;
        const int pl = lane < 40 ? lane : 39, tbb = l_tb(layer, 0) + w * (layer ? INBOX : 5120);
        spl = SC + pl * 8;
        eo = OFF_EG + (unsigned)(layer * 163840 + nt1 * 640 + pl * 16);
        es = eo + (unsigned)(mt1 * 10240);
        spa = l_rs(layer, 0) + (2 * (mt2 & 1) + (w >> 1)) * (layer ? INBOX : 256) + (2 * (w & 1) + (c >> 3)) * 64 + (c & 7) * 4;
        tbw = tbb + c * 64 + ((q ^ ((c >> 1) & 3)) * 16);
        tbr = tbb + (2 * (c >> 2) + (q & 1)) * 64 + (((2 * (q >> 1) + ((c >> 1) & 1)) ^ (c >> 2)) * 16) + (c & 1) * 8;
        hso = OFF_HF + (unsigned)(layer * 5242880 + ((8 * mt1 + 2 * w + (q >> 1)) * 160 + 10 * nt1 + 5 * (q & 1)) * 256 + c * 16);
        cwa = L_CW + c * 4;
        fho1 = lane ? kNone : OFF_FH + (unsigned)((((layer * 32 + 2 * mt1 + (w >> 1)) * 4 + (nt1 >> 2)) * 8 + (nt1 & 3) * 2 + (w & 1)) * 128);
        fho = OFF_FH + (unsigned)(((layer * 32 + mt2) * 4 + w) * 1024 + (lane & 7) * 128);
        pin(sgn_); pin(spl); pin(eo); pin(es); pin(spa); pin(tbw); pin(tbr); pin(hso); pin(cwa); pin(fho1); pin(fho);
        const unsigned hb_ = OFF_HF + (unsigned)(layer * 5242880 + (4 * mt2 * 160 + 40 * w) * 256);
#pragma unroll
        for (int m = 0; m < 4; ++m) { ho[m] = hb_ + (unsigned)(((m + 1 + w) & 3) * 40960 + lane * 16); pin(ho[m]); }
#pragma unroll
        for (int m = 0; m < 3; ++m) { dst[m] = SC + ((w + 1 + m) & 3) * INBOX + m * 5120 + lane * 16; pin(dst[m]); }
        mt1_ = mt1;
        if constexpr (layer == 0) {
            const int r0 = SC + (w < 3 ? w : 0) * REGSZ, y0 = 2031 + 8 * q - c, l34 = lane < 33 ? lane : 33;
#pragma unroll
            for (int i = 0; i < 4; ++i) { ab[i] = r0 + ((y0 & 1) ? CV_O : CV_E) + ((y0 + 1) >> 1) * 4 - 448 - 512 - 1024 * i; pin(ab[i]); }
            bb = r0 + 32 * c + 16 * q;
            zpa = w < 3 ? r0 + l34 * 16 + (l34 >= 30 ? CV_E + 4096 - 480 : 0) + (l34 >= 32 ? CV_O + 4100 - 32 - CV_E - 4096 : 0) : SC + 47360 + lane * 16;
            uwa = r0 + lane * 16;
            xso = (unsigned)((64 * mt2 + 16 * w + c) * D_ + 80 * nt2 + 16 * (q & 1) + 8 * (q >> 1)) * 2u;
            pin(bb); pin(zpa); pin(uwa); pin(xso);
        }
        SCHED();
    };
    unsigned tag0, tag1;
#define FLAG(off) { if (lane == 0) st64(v2u{tag0, tag1}, rs, off); }
#define ISTAG(f) (((f.x ^ tag0) | (f.y ^ tag1)) == 0u)
#define KW_REQ() { if (cv) ld_ur(wro, p.ws + OFF_KVT); static_for<NW2P>(LAMBDA(i_) { gld_w2<i_.value>(w2o, p.ws + 2048 + (i_.value / 5) * 5120); }); }
#define FW_OK() { if (__builtin_expect(!kearly, 0)) { WAVE_WAIT({ const v2u fk_ = ld64(rs, fko); const v2u fw_ = ld64(rs, fwo); ok = ISTAG(fk_) && ISTAG(fw_); }) KW_REQ() } }
#define TT_GAP(n) { constexpr int mb_ = (n) / 10, nb_ = ((n) % 10) / 2, e_ = ((n) & 1) * 2; \
        mul2(tt[mb_][nb_][e_], tt[mb_][nb_][e_ + 1], rm_[4 * mb_ + e_], rm_[4 * mb_ + e_ + 1], SW[nb_]); }
    {
        const int tid = threadIdx.x, lane = tid & 63, w = w0, b = b0;
        const int c = lane & 15, q = lane >> 4;
        TILES()
        STAMP(0)
        const unsigned lds0 = (unsigned)(size_t)(LAS char*)smem;
        v2u nonce;
        unsigned cpf0;
        {
            u64 pc;
            int len, len2;
            asm volatile("s_getpc_b64 %0\nkpf_begin:\n\ts_mov_b32 %1, kpf_end-kpf_begin\n\ts_mov_b32 %2, kpf_end2-kpf_begin" : "=s"(pc), "=s"(len), "=s"(len2));
            len = len > len2 ? len : len2;
            const int o0 = (((j & 15) * 4 + w) + 64 * (lane & 7)) * 128;
            asm volatile("global_load_dword %0, %1, %2" : "=v"(cpf0) : "v"(o0 < len ? o0 : 0), "s"(pc));
            unsigned zo = 0u;
            asm volatile("global_load_dwordx2 %0, %1, %2 sc1" : "=v"(nonce) : "v"(zo), "s"(p.ws + OFF_NONCE));
        }
        const bool cv = w < 2 || (w == 2 && i32 < 16);
        const int chn = 80 * nt2 + i32 + 32 * (cv ? w : 0);
        const bool kw = KVS ? w < 2 || (w == 2 && i32 >= 16) : cv;
        const int kp = w == 2 ? 256 + 8 * nt2 + ((i32 & 15) >> 1) : 32 * nt2 + i32, kh_ = w == 2 ? i32 & 1 : w & 1;
        const int kc = KVU ? kp % 10 : (80 * nt2 + (w == 2 ? 64 + (i32 & 15) : i32 + 32 * (w & 1))) % 10, kr = KVU ? 2 * (kp / 10) + kh_ : (80 * nt2 + (w == 2 ? 64 + (i32 & 15) : i32 + 32 * (w & 1))) / 10;
        const int lk = kw ? lane : 8, lc = cv ? lane : 8;
        v4u ax[15], sa[5], cr[2], wr[4];
        unsigned uL[3];
        const int cu_ = (b < 200 ? b : 0) * 4 + w, cdb = cu_ / 20, ccg = cu_ % 20;
        {
            int ca_ = 32 * ccg, cb_ = 40 * nt1 + 10 * w - 32;
            asm volatile("" : "+s"(ca_), "+s"(cb_));
            const unsigned cpo = (unsigned)((lane < 32 ? ca_ : cb_) + (lane < 41 ? lane : 41)) * 2u;
            gldu16(uL[0], cpo, p.Lre); gldu16(uL[1], cpo, p.Lim); gldu16(uL[2], cpo, p.logdt);
        }
        const char* ssrc;
        {
            u64 pa = (u64)(p.x0 + (128 * mt1 + 8 * nt1 + 2 * w) * D_), pb = (u64)(((mt1 & 1) ? p.Bim : p.Bre) + (40 * nt1 + (mt1 >> 1)) * D_);
            asm volatile("" : "+s"(pa), "+s"(pb));
            const int nbx = 2 * w + q - 2, r = q < 2 ? q : 8 * (nbx < 5 ? nbx : 4);
            ssrc = (const char*)(q < 2 ? pa : pb) + (unsigned)(r * 1280 + c * 16);
            pin(ssrc);
        }
        {
            const unsigned kso = (unsigned)(64 * kc + 8 * (lk & 7)) * 2u, ko = (unsigned)(32 * kr + 4 * (lk >> 3)) * 1280u + kso, kh = ko - 3840u;
            const v4u rk = {(unsigned)(u64)p.k, (unsigned)((u64)p.k >> 32) & 0xffffu, S_ * D_ * 2u, 0x00020000u};
            static_for<4>(LAMBDA(i) { gld<i.value * 16>(ax[11 + i.value], kso * 4u, p.shk); });
            static_for<3>(LAMBDA(i) { bld4<i.value * 1280>(ax[i.value], kh, rk); });
            static_for<4>(LAMBDA(i) { gldn<i.value * 1280, 3>(ax[3 + i.value], ko, p.k); });
            static_for<4>(LAMBDA(i) { gldn<i.value * 1280, 0>(ax[7 + i.value], ko, p.v); });
        }
        if (SAF == 0) static_for<5>(LAMBDA(i) { gld64<256 * i.value>(sa[i.value], ssrc); });
        SCHED();
        if (w == TW && !kw) tour<0>();
        STAMP(22)
        const int row8 = lane >> 3;
        const unsigned wvo = (unsigned)(40 * nt1 + row8) * 1280u + (unsigned)(((lane & 7) ^ ((w & 1) | ((row8 >> 1) << 1))) * 16 + (w >> 1) * 128);
        const char* const wbase = (const char*)((w & 1) ? p.Bim : p.Bre);
        const unsigned wl = lds0 + (unsigned)(((w & 1) * 10 + (w >> 1)) * 1024);
        const unsigned xvo0 = (unsigned)(8 * (row8 >> 1) + 2 * (row8 & 1)) * 1280u + (unsigned)(((lane & 7) ^ ((row8 >> 1) << 1)) * 16);
        const unsigned xvo1 = (xvo0 ^ 16u) + 1280u;
        const char* const xbase = (const char*)p.x0 + (size_t)(128 * mt1 + 32 * w) * 1280;
        const unsigned xl = lds0 + (unsigned)(w * REGSZ);
        unsigned xv[4] = {xvo0, xvo1, xvo0 + 5120u, xvo1 + 5120u}, wv[5];
#pragma unroll
        for (int i = 0; i < 5; ++i) { wv[i] = wvo + i * 10240u; pin(wv[i]); if (i < 4) pin(xv[i]); }
#define DMA_M0(it) SETM0<dma_loff(it) - dma_goff(it)>((it) < 40 ? xl : wl)
#define DMA_LD1(it, nx_) GLD1<dma_goff(it), dma_loff(nx_) - dma_goff(nx_)>((it) < 40 ? xv[(it) & 3] : wv[(it) < 40 ? 0 : ((it) - 100) % 5], (it) < 40 ? xbase : wbase, (nx_) < 40 ? xl : wl)
#define DMA_LD(it) GLD0<dma_goff(it)>((it) < 40 ? xv[(it) & 3] : wv[(it) < 40 ? 0 : ((it) - 100) % 5], (it) < 40 ? xbase : wbase)
        const int swz = (c & 1) | ((c >> 2) << 1);
        int w1lane0 = (c & 1) * 10240 + (c >> 1) * 128 + ((q ^ swz) * 16);
        int w1lane1 = (c & 1) * 10240 + (c >> 1) * 128 + (((4 + q) ^ swz) * 16);
        int xlane0 = L_XR + w * REGSZ + (c & 1) * 1024 + (c >> 1) * 128 + ((q ^ swz) * 16);
        int xlane1 = L_XR + w * REGSZ + (c & 1) * 1024 + (c >> 1) * 128 + (((4 + q) ^ swz) * 16);
        unsigned cvo = (unsigned)((16 * cdb + c) * D_ + 32 * ccg + 8 * q) * 2u, for0, for1;
        {
            const int G = 4 * ccg + q, ch = 10 * (G / 5) + G % 5, ci = ch + 5;
            const unsigned fo = OFF_W2F + (unsigned)(((cdb / 5) * 200 + cdb % 5) * 1024 + c * 16);
            for0 = fo + (unsigned)((ch >> 2) * 5120 + (ch & 3) * 256); for1 = fo + (unsigned)((ci >> 2) * 5120 + (ci & 3) * 256);
        }
        unsigned sto = OFF_ST + (unsigned)((mt1 * 16 + 4 * w + q) * 256 + c * 8), swo = OFF_ST + (unsigned)(((lane >> 2) * 16 + nt1) * 256 + 128 + (lane & 3) * 16);
        unsigned kvo = OFF_KVT + (unsigned)((64 * kc + 8 * (lane >> 3) + ((lane >> 2) & 1)) * 4096 + (32 * kr + 8 * (lane & 3)) * 2);
        int kbw = L_KB + w * 1024 + (lane & 7) * 128 + (((lane >> 3) * 8) ^ ((lane & 7) * 16));
        int kbr = L_KB + w * 1024 + (lane >> 3) * 128 + (((lane & 7) * 16) ^ ((lane >> 3) * 16));
        const unsigned dum = (OOB & 1) ? kNone : OFF_DUM + (unsigned)((b * 4 + w) * 128 + (lane & 7) * 16);
        unsigned fko1 = OFF_FK + (unsigned)((10 * kr + kc) * 128);
        if (!kw) kvo = dum;
        if (b >= 200) { for0 = dum; for1 = dum; cvo = (unsigned)((b * 4 + w) * 128); }
        wro = (unsigned)(chn * 4096 + lc * 16);
        cv0 = cv;
        pin(kvo); pin(kbw); pin(kbr); pin(fko1);
        int w1hi0 = w1lane0 + 61440, w1hi1 = w1lane1 + 61440;
        pin(w1hi0); pin(w1hi1);
        pin(w1lane0); pin(w1lane1); pin(xlane0); pin(xlane1); pin(cvo); pin(for0); pin(for1); pin(sto); pin(swo); pin(wro);
        SCHED();
        auto pre = LAMBDA(i_) {
            constexpr int it = p1_pre(i_.value);
            SCHED();
            DMA_M0(it);
            asm volatile("s_nop 0");
            DMA_LD(it);
            SCHED();
        };
        fko = OFF_FK + (unsigned)(((cv ? lane : 0) * 10 + (chn >> 6)) * 128);
        fwo = OFF_FW + (unsigned)(((5 * nt2 + (lane < 25 ? lane / 5 : 0)) * 20 + 5 * w + (lane < 25 ? lane % 5 : 0)) * 128), w2o = OFF_W2F + (unsigned)((nt2 * 40 + 10 * w) * 5120 + lane * 16);
        int ime = SC + (w < 3 ? w * REGSZ + CV_E : 43264) + 1008 - 16 * lane, imo = SC + (w < 3 ? w * REGSZ + CV_O + 4 : 47360) + 1008 - 16 * lane, im3 = w < 3 ? ime + 3072 : imo;
        int tca = L_TAB + (c >> 1) * 136, tcb = tca + 8 + 32 * w + 8 * q, tcl = L_TAB + (lane < 40 ? lane : 39) * 136, coa = L_CO + w * REGSZ + q * 64, rsa = l_rs(0, 0) + w * 256 + lane * 4;
        unsigned sgn = (c & 1) ? 0u : 0x80000000u;
        fko1 = kw && lane < 32 ? fko1 : OFF_FW + (unsigned)((b * 4 + w) * 128);
        pin(fko); pin(fwo); pin(w2o); pin(fko1); pin(ime); pin(imo); pin(im3); pin(tca); pin(tcb); pin(tcl); pin(coa); pin(rsa); pin(sgn);
        SCHED();
        auto cw_ = [&]() __attribute__((always_inline)) {
            gldn<0, 2>(cr[0], cvo, p.Cre); gldn<0, 2>(cr[1], cvo, p.Cim);
            static_for<4>(LAMBDA(i) { gldn<i.value * 1024, 1>(wr[i.value], wro, p.ssmk); });
        };
        if (SAF == 1) static_for<5>(LAMBDA(i) { gld64<256 * i.value>(sa[i.value], ssrc); });
        if (CWP == 0) cw_();
        STAMP(33)
        asm volatile("s_waitcnt vmcnt(%5)" : "+v"(uL[0]), "+v"(uL[1]), "+v"(uL[2]), "+v"(nonce), "+v"(cpf0) : "i"(15 + (SAF < 2 ? 5 : 0) + (CWP ? 0 : 6)) : "memory");
        {
            f32x2 lam, co, lp[16];
            disc(uL[0], uL[1], uL[2], lam, co);
            if (lane < 32) *(f32x2*)(smem + L_CO + w * REGSZ + lane * 8) = 2.0f * co;
            lp[0] = cmul(lam, lam); lp[0] = cmul(lp[0], lp[0]); lp[1] = cmul(lp[0], lp[0]);
            lp[0] = f32x2{1.f, 0.f};
            lp[2] = cmul(lp[1], lp[1]); lp[3] = cmul(lp[2], lp[1]); lp[4] = cmul(lp[2], lp[2]);
            const f32x2 z2 = {0.f, 0.f};
            f32x2 t4[4];
            cfma4(t4, lp[4], lp[1], lp[2], lp[3], lp[4], z2, z2, z2, z2);
            lp[5] = t4[0]; lp[6] = t4[1]; lp[7] = t4[2]; lp[8] = t4[3];
            cfma4(t4, lp[8], lp[1], lp[2], lp[3], lp[4], z2, z2, z2, z2);
            lp[9] = t4[0]; lp[10] = t4[1]; lp[11] = t4[2]; lp[12] = t4[3];
            cfma4(t4, lp[8], lp[5], lp[6], lp[7], lp[7], z2, z2, z2, z2);
            lp[13] = t4[0]; lp[14] = t4[1]; lp[15] = t4[2];
            if (lane >= 32 && lane < 42) {
                f32x2* tl = (f32x2*)(smem + L_TAB + (10 * w + lane - 32) * 136);
                tl[0] = lam;
#pragma unroll
                for (int i = 0; i < 16; ++i) tl[1 + i] = lp[i];
            }
        }
        SCHED();
        STAMP(35)
        if (SAF == 2) static_for<5>(LAMBDA(i) { gld64<256 * i.value>(sa[i.value], ssrc); });
        if (CWP) cw_();
        if (APOS == 0) addrs(std::integral_constant<int, 0>{}, lane, w, b);
        v4u kvt[4];
        unsigned ka[8], kt[8], se[8], vs[8], ko4[4], one2 = 0x3F803F80u;
        pin(one2);
        auto kvop = LAMBDA(n_) {
            constexpr int n = n_.value, h = n ? (n - 1) / 29 : -1, u = n ? (n - 1) % 29 : 26;
            if constexpr (u < 2) d2x4(ka + 4 * u, ax[0][h], ax[1][h], ax[2][h], ax[3][h], se[u], se[u], se[u], se[u]);
            else if constexpr (u == 8) { const v4u sk = ax[11 + h]; se[4] = sk[0] >> 16; se[5] = sk[2] & 0xffff0000u; se[6] = sk[0] & 0xffffu; se[7] = sk[2] << 16; pin(se[4]); pin(se[5]); pin(se[6]); pin(se[7]); }
            else if constexpr (u < 21) {
                constexpr int v = u < 8 ? u - 2 : u - 3, tp = 1 + v / 6, st = v % 6, e = st & 1;
                if constexpr (st < 2) d2x4(kt + 4 * e, ax[tp][h], ax[tp + 1][h], ax[tp + 2][h], ax[tp + 3][h], se[2 * tp + e], se[2 * tp + e], se[2 * tp + e], se[2 * tp + e]);
                else if constexpr (st < 4) cpx4i(kt + 4 * e, ka + 4 * e);
                else d2x4(ka + 4 * e, kt[4 * e], kt[4 * e + 1], kt[4 * e + 2], kt[4 * e + 3], one2, one2, one2, one2);
            }
            else if constexpr (u == 21) { vs[0] = ax[7][h] & 0xffffu; vs[1] = ax[8][h] << 16; vs[2] = ax[9][h] & 0xffffu; vs[3] = ax[10][h] << 16; pin(vs[0]); pin(vs[1]); pin(vs[2]); pin(vs[3]); }
            else if constexpr (u == 22) { vs[4] = ax[7][h] >> 16; vs[5] = ax[8][h] & 0xffff0000u; vs[6] = ax[9][h] >> 16; vs[7] = ax[10][h] & 0xffff0000u; pin(vs[4]); pin(vs[5]); pin(vs[6]); pin(vs[7]); }
            else if constexpr (u == 23) cpx4(ko4, ka[0], ka[2], ka[4], ka[6], ka[1], ka[3], ka[5], ka[7]);
            else if constexpr (u < 26) { constexpr int e = u - 24; d2x4(kt + 4 * e, ko4[2 * e], ko4[2 * e], ko4[2 * e + 1], ko4[2 * e + 1], vs[4 * e], vs[4 * e + 1], vs[4 * e + 2], vs[4 * e + 3]); }
            else if constexpr (u == 26) {
                if constexpr (h < 3) { const v4u sk = ax[12 + h]; se[0] = sk[1] >> 16; se[1] = sk[3] & 0xffff0000u; se[2] = sk[1] & 0xffffu; se[3] = sk[3] << 16; pin(se[0]); pin(se[1]); pin(se[2]); pin(se[3]); }
                else asm volatile("s_nop 2");
            }
            else if constexpr (u == 27) {
                cpx4(ko4, kt[0], kt[2], kt[4], kt[6], kt[1], kt[3], kt[5], kt[7]);
                *(v2u*)(smem + kbw) = v2u{ko4[0], ko4[1]};
                *(v2u*)(smem + (kbw ^ 64)) = v2u{ko4[2], ko4[3]};
            }
            else kvt[h] = *(const v4u*)(smem + kbr);
            SCHED();
        };
        asm volatile("s_waitcnt vmcnt(%15)" : "+v"(ax[0]), "+v"(ax[1]), "+v"(ax[2]), "+v"(ax[3]), "+v"(ax[4]), "+v"(ax[5]), "+v"(ax[6]), "+v"(ax[7]), "+v"(ax[8]), "+v"(ax[9]),
                     "+v"(ax[10]), "+v"(ax[11]), "+v"(ax[12]), "+v"(ax[13]), "+v"(ax[14]) : "i"(11) : "memory");
        STAMP(30)
        unsigned tch = 0u;
        if (kw) {
            static_for<117>(LAMBDA(n_) {
                constexpr int n = n_.value;
                site<1000 + n>(); SCHED();
                kvop(n_);
                if constexpr (n >= kD0 && (n - kD0) % kDS == 0 && (n - kD0) / kDS < 27) { pre(std::integral_constant<int, ((n - kD0) / kDS + 270) % 27>{}); SCHED(); }
            });
        } else {
            if (w == 3) {
            const int l = lane, l3 = l & 3;
            unsigned a = OFF_SG + (unsigned)((((((l >> 3) & 1) * 32 + mt2) * 4 + ((l >> 1) & 3)) * 8 + nt2) * 256 + (l & 1) * 128);
            a = l >= 16 ? OFF_EG + (unsigned)((l >= 21 ? 163840 - 640 : 0) + (mt1 * 16 + nt1) * 640 + (l < 26 ? l - 16 : 5) * 128) : a;
            a = l >= 28 ? OFF_PS + (unsigned)(((mt2 * 4 + l3) * 8 + nt2) * 128) : a;
            a = l >= 32 ? OFF_FH + (unsigned)((((((l >> 2) & 1) * 32 + 2 * mt1 + (l3 >> 1)) * 4 + (nt1 >> 2)) * 8 + (nt1 & 3) * 2 + (l3 & 1)) * 128) : a;
            a = l >= 40 ? OFF_FX + (unsigned)(((mt1 * 4 + 2 * (mt2 & 1) + (l3 >> 1)) * 16 + nt2 * 2 + (l3 & 1)) * 128) : a;
            a = l >= 48 ? OFF_FG + (unsigned)((nt2 * 96 + i32) * 128) : a;
            const unsigned big = (l & 7) < 2 ? OFF_HF + (unsigned)((l & 1) * 5242880 + (8 * mt1 * 160 + 10 * nt1) * 256) : (l & 7) == 2 ? OFF_X2F + (unsigned)(2 * mt2 * 40960 + 2560 * nt2)
                               : (l & 7) < 5 ? OFF_KVT + (unsigned)((l & 1) * 2621440 + (80 * nt2 + i32) * 4096) : OFF_W2F + (unsigned)(nt2 * 204800 + ((l & 7) - 5) * 98304);
            a = l >= 56 ? big : a;
            tch = a;
        }
            vm_wait<KNW>();
            static_for<27>(LAMBDA(i_) { if constexpr (i_.value == T1POS) { if (w == TW) tour<1>(); } pre(i_); });
            if (T1POS == 27 && w == TW) tour<1>();
            skip<SKIP0>();
            undef4(kvt);
        }
        tag0 = __builtin_amdgcn_readfirstlane(nonce.x) + 1u; tag1 = __builtin_amdgcn_readfirstlane(nonce.y) + (tag0 == 0u ? 1u : 0u);
#pragma unroll
        for (int i = 0; i < 4; ++i) st128(kvt[i], rs, kvo + i * 8192);
        SCHED();
        STAMP(34)
        if (APOS == 1) addrs(std::integral_constant<int, 0>{}, lane, w, b);
        {
            asm volatile("s_waitcnt vmcnt(35)" : "+v"(cr[0]), "+v"(cr[1]), "+v"(sa[0]), "+v"(sa[1]), "+v"(sa[2]), "+v"(sa[3]), "+v"(sa[4])::"memory");
            v4u orr, oi;
#pragma unroll
            for (int e = 0; e < 4; ++e) {
                const f32x4 co = *(const f32x4*)(smem + coa + e * 16);
                const f32x2 o0 = lo16f(cr[0][e]) * f32x2{co.x, -co.y} - lo16f(cr[1][e]) * f32x2{co.y, co.x};
                const f32x2 o1 = hi16f(cr[0][e]) * f32x2{co.z, -co.w} - hi16f(cr[1][e]) * f32x2{co.w, co.z};
                orr[e] = cvtpk(o0.x, o1.x); oi[e] = cvtpk(o0.y, o1.y);
            }
            st128(orr, rs, for0); st128(oi, rs, for1);
            SCHED();
        }
        {
            float s1[2] = {0.f, 0.f}, s2[2] = {0.f, 0.f};
#pragma unroll
            for (int i = 0; i < 5; ++i)
#pragma unroll
                for (int e = 0; e < 4; ++e) { s1[e & 1] = dot2a(sa[i][e], one2, s1[e & 1]); s2[e & 1] = dot2a(sa[i][e], sa[i][e], s2[e & 1]); }
            const float a1 = rowsum16(s1[0] + s1[1]), a2 = rowsum16(s2[0] + s2[1]);
            const float mean = a1 * (1.0f / D_);
            const float var = fmaxf(a2 * (1.0f / D_) - mean * mean, 0.f);
            const float ri = __builtin_amdgcn_rcpf(rbf(__builtin_amdgcn_sqrtf(rbf(rbf(var) + 1e-5f))));
            if (c < 2) st64(v2u{__float_as_uint(q >= 2 ? a1 : c ? -ri * rbf(mean) : ri), tag0}, rs, OFF_ST + (unsigned)((mt1 * 16 + nt1) * 256 + ((q < 2 ? 8 * c : 14) + 2 * w + q) * 8));
        }
        SCHED();
        STAMP(23)
        if (w == 3) tour<7>();
        asm volatile("s_waitcnt vmcnt(%4) lgkmcnt(0)" : "+v"(wr[0]), "+v"(wr[1]), "+v"(wr[2]), "+v"(wr[3]) : "i"(P1_PRE - 9) : "memory");
        __builtin_amdgcn_s_barrier();
        asm volatile("" ::: "memory");
        STAMP(24)
        v4u wf[2][5], af[2][2][2];
#pragma unroll
        for (int nb = 0; nb < 5; ++nb) wf[0][nb] = *(const v4u*)(smem + w1lane0 + nb * 20480 + (0) * 2048);
#pragma unroll
        for (int i = 0; i < 4; ++i) af[0][i >> 1][i & 1] = *(const v4u*)(smem + ((i >> 1) ? xlane1 : xlane0) + (i & 1) * 2048);
        SCHED();
        const char *kvtb = p.ws + OFF_KVT, *w2b0 = p.ws + 2048;
        pins(kvtb); pins(w2b0);
        v2u sg, tc[10], fk, fw;
        v4u sw0;
        unsigned ie[4][4], io[4][4], iw, zq[4];
        float rr_[8];
        auto kstep = LAMBDA(s_) {
            constexpr int s = s_.value, ph = s >> 2, s4 = s & 3, cu = s & 1, nx = cu ^ 1, jj = s >> 1, pp = jj & 1;
            if constexpr (s4 == 3 && ph < 4) {
                constexpr int pw = p1_pos(104 + 5 * (ph + 1)), px = p1_pos(4 * (jj + 1) + 3), pm = pw > px ? pw : px;
                constexpr int vm = p1_lim(p1_cnt(s, 0) - 1 - pm);
                asm volatile("" ::: "memory");
                __builtin_amdgcn_s_waitcnt((vm & 15) | ((vm >> 4) << 14) | 0x70);
                __builtin_amdgcn_s_barrier();
                asm volatile("" ::: "memory");
                STAMP(25 + ph)
                SCHED();
            }
            if constexpr (s == 15) {
                wait_st<p1_cnt(s, 0) - 1 - p1_pos(203)>(sg, sw0);
                WAVE_RETRY((((sg.y ^ tag0) | (sw0.y ^ tag0) | (sw0.w ^ tag0)) == 0u), sg = ld64(rs, sto); sw0 = ld128(rs, swo))
                STAMP(7)
            }
            if constexpr (s == kW2S) { vm_wait<p1_cnt(s, 0) - 1 - p1_pos(208)>(); SCHED(); }
            if constexpr (s == 18) {
                wait_fkn<p1_cnt(s, 0) - 1 - p1_pos(206)>(fk, fw, sdq);
                kearly = __builtin_amdgcn_ballot_w64(!(ISTAG(fk) && ISTAG(fw))) == 0ull;
                ORDER();
                if (kearly && cv) ld_ur(wro, kvtb);
                SCHED();
            }
            if constexpr (s4 != 3 || ph == 4) lgkm<(cu == 0 && s > 0 ? 3 : s == 17 ? 4 : s == 19 ? 2 : 0)>();
            SCHED();
            static_for<10>(LAMBDA(g_) {
                constexpr int g = g_.value, mb = g / 5, k = g % 5, it = p1_item(s, g), nt_ = g < 9 ? p1_item(s, g + 1) : -1;
                constexpr bool d0 = it >= 0 && it < 200, d1 = nt_ >= 0 && nt_ < 200;
                if constexpr (s == 0) mf0(acc[mb][k], af[pp][cu][mb], wf[cu][k]); else mf(acc[mb][k], af[pp][cu][mb], wf[cu][k]);
                SCHED();
                if constexpr (s < 19) {
                    if constexpr (g < 5) wf[nx][g] = *(const v4u*)(smem + (g < 3 ? (nx ? w1lane1 : w1lane0) : (nx ? w1hi1 : w1hi0)) + (g < 3 ? g : g - 3) * 20480 + ((s + 1) >> 1) * 1024);
                    if constexpr (cu == 1 && g == 5) vm_wait<p1_lim(p1_cnt(s, 5) - 1 - p1_pos(4 * (jj + 1) + 3))>();
                    if constexpr (cu == 1 && g >= 5 && g < 9)
                        af[pp ^ 1][(g - 5) >> 1][(g - 5) & 1] = *(const v4u*)(smem + (((g - 5) >> 1) ? xlane1 : xlane0) + ((jj + 1) % 3) * 4096 + ((g - 5) & 1) * 2048);
                }
                if constexpr (cu == 0 && g == 4 && s > 0) lgkm<(s == 8 || s == 10 || s == 14 ? 8 : 5)>();
                static_for<p1_cap(s, g)>(LAMBDA(e_) {
                    constexpr int na = p1_slot(0, s, g, 10) + e_.value, i = (na / 10) & 3, u = na % 10;
                    static_assert(p1_slot(0, 8, 0, 10) >= 40, "");
                    if constexpr (na < 40) {
                        if constexpr (u == 0) { if constexpr (i > 0) ror1(iw, wr[i - 1].w); else { iw = 0u; pin(iw); } }
                        else if constexpr (u < 5) rot1(ie[i][u - 1], wr[i][4 - u]);
                        else if constexpr (u == 5) shr1b(iw, wr[i].w);
                        else if constexpr (u < 9) mix1(io[i][u - 6], wr[i][9 - u], wr[i][8 - u]);
                        else mix1(io[i][3], wr[i].x, iw);
                    }
                    constexpr int nb = p1_slot(8, s, g, 3) + e_.value;
                    if constexpr (s >= 8 && s < 12 && g < 3) {
                        if constexpr (nb < 10) tc[nb] = *(const v2u*)(smem + (nb < 5 ? tca : tcb) + (nb % 5) * 1088);
                        else if constexpr (nb == 10) { const f32x2* tp = (const f32x2*)(smem + tcl); l8 = tp[2]; l32 = tp[5]; }
                        else if constexpr (nb == 11) l128 = *(const f32x2*)(smem + tcl + 72);
                        static_assert(p1_slot(8, 12, 0, 3) == 12, "");
                    }
                    constexpr int nc = p1_slot(12, s, g, 10) + e_.value;
                    if constexpr (s >= 12 && s < 14 && nc < 10) {
                        if constexpr (nc < 5) { lr[nc] = __uint_as_float(tc[nc].x); xor1(ls[nc], tc[nc].y, sgn); }
                        else { lqr[nc - 5] = __uint_as_float(tc[nc].x); xor1(lqs[nc - 5], tc[nc].y, sgn); }
                        static_assert(p1_slot(12, 14, 0, 10) >= 10, "");
                    }
                    if constexpr (s == 15 && g < 5) SW[g] = __int_as_float(__builtin_amdgcn_ds_bpermute(16 * c + 4 * (g >> 1), (g & 1) ? sw0.z : sw0.x));
                    constexpr bool img = (s == 16 && g >= 5 && g < 9 && e_.value == 0) || (s == 18 && (g == 5 || g == 6) && e_.value == 0);
                    constexpr int nd = p1_slot(15, s, g, 10) + e_.value - 5 - (s > 16 ? 4 : s == 16 && g >= 5 ? (g < 9 ? g - 4 : 4) : 0) - (s > 18 ? 2 : s == 18 && g >= 5 ? (g == 5 ? 1 : 2) : 0);
                    if constexpr (s >= 15 && nd >= 0 && !img) {
                        if constexpr (nd < 4) bc1<nd>(rr_[nd], sg.x);
                        else if constexpr (nd < 44) mulb<8 + (nd - 4) % 8>(tt[(nd - 4) % 8 / 4][(nd - 4) / 8][(nd - 4) % 4], sg.x, SW[(nd - 4) / 8]);
                        else if constexpr (nd < 48) bc1<nd - 40>(rr_[nd - 40], sg.x);
                        static_assert(p1_slot(15, 16, 0, 10) <= 9 && p1_slot(15, 20, 0, 10) >= 59, "");
                    }
                });
                if constexpr (s == 14 && g < 3) *(v4u*)(smem + ime + 1024 * g) = v4u{ie[3 - g][0], ie[3 - g][1], ie[3 - g][2], ie[3 - g][3]};
                if constexpr (s == 16 && g >= 5 && g < 9) *(v4u*)(smem + imo + 1024 * (8 - g)) = v4u{io[g - 5][0], io[g - 5][1], io[g - 5][2], io[g - 5][3]};
                if constexpr (s == 18 && g == 5) *(v4u*)(smem + im3) = v4u{ie[0][0], ie[0][1], ie[0][2], ie[0][3]};
                if constexpr (s == 18 && g == 6) *(unsigned*)(smem + rsa) = sg.x;
                if constexpr (s == 14 && (g == 6 || g == 8)) { zq[g - 6] = 0u; zq[g - 5] = 0u; pin(zq[g - 6]); pin(zq[g - 5]); }
                if constexpr (s == 19 && g == 0) *(v4u*)(smem + zpa) = v4u{zq[0], zq[1], zq[2], zq[3]};
                if constexpr (s == 18 && g == 1) touch_(tch, p.ws);
                if constexpr (s == 19 && g == 2) { if (__builtin_expect(!kearly, 0)) poll2(fko, fwo, p.ws); }
                if constexpr (it >= 0) {
                    if constexpr (d0 && d1) DMA_LD1(it, d1 ? nt_ : 0);
                    else if constexpr (d0) DMA_LD(it);
                    else if constexpr (it == 202) gld2s<0>(sg, sto, p.ws);
                    else if constexpr (it == 203) gld4s<0>(sw0, swo, p.ws);
                    else if constexpr (it == 206) gld2s<0>(fk, fko, p.ws);
                    else if constexpr (it == 207) gldu16(sdq, (unsigned)chn * 2u, p.ssmD);
                    else if constexpr (it == 208) gld2s<0>(fw, fwo, p.ws);
                    else if constexpr (it >= 500) gld_w2<it % 50>(w2o, w2b0 + ((it % 50) / 5) * 5120);
                    else { vm_wait<p1_cnt(s, g) - P1_PRE>(); st64(v2u{tag0, tag1}, rs, fko1); }
                }
                if constexpr (d1 && !d0) DMA_M0(d1 ? nt_ : 0);
                site<2000 + 10 * s + g>();
                SCHED();
            });
        };
        static_for<20>(kstep);
        SCHED();
        rr[0] = f32x4{rr_[0], rr_[1], rr_[2], rr_[3]}; rr[1] = f32x4{rr_[4], rr_[5], rr_[6], rr_[7]};
        ACC_FENCE10(acc[0], acc[1]);
        STAMP(1)
    }

    auto layer_body = LAMBDA(layer_) {
        constexpr int layer = layer_.value;
        const int sga = sgn_;
        const bool cv = layer == 0 && cv0;
        v2u fh;
        unsigned ks[10];
        f32x4 ca[8], a2[4][5];
        float rsr, rsm;
        float kf[8][4];
        float bv[8][5];
        {
            float a0[5], a1[5];
            static_for<2>(LAMBDA(m_) {
                constexpr int mb = m_.value;
#pragma unroll
                for (int nb = 0; nb < 5; ++nb) {
                    const f32x4 v = __builtin_elementwise_fma(acc[mb][nb], rr[mb], f32x4{tt[mb][nb][0], tt[mb][nb][1], tt[mb][nb][2], tt[mb][nb][3]});
#pragma unroll
                    for (int i = 0; i < 4; ++i) bv[4 * mb + i][nb] = v[i];
                }
                SCHED();
                if constexpr (mb == 0) cstep(a0, bv[0], lr, ls, bv[1][0], bv[1][1], bv[1][2], bv[1][3], bv[1][4]);
#pragma unroll
                for (int i8 = mb ? 4 : 2; i8 < 4 + 4 * mb; i8 += 2) {
                    cstep(a1, a0, lr, ls, bv[i8][0], bv[i8][1], bv[i8][2], bv[i8][3], bv[i8][4]);
                    cstep(a0, a1, lr, ls, bv[i8 + 1][0], bv[i8 + 1][1], bv[i8 + 1][2], bv[i8 + 1][3], bv[i8 + 1][4]);
                }
                SCHED();
            });
#pragma unroll
            for (int nb = 0; nb < 5; ++nb) *(float*)(smem + sga + nb * 64) = a0[nb];
        }
        SCHED();
        const int w_ = w0;
        {
        const int w = w_, mt1 = mt1_;
        if (layer == 0 && __builtin_expect(!kearly, 0)) {
            unsigned f0_, f1_, f2_, f3_;
            asm volatile("s_waitcnt vmcnt(0)\n\t" AR("%0", 36, 0) AR("%1", 36, 1) AR("%2", 36, 2) AR("%3", 36, 3) : "=v"(f0_), "=v"(f1_), "=v"(f2_), "=v"(f3_)::"memory");
            kearly = __builtin_amdgcn_ballot_w64(((f0_ ^ tag0) | (f1_ ^ tag1) | (f2_ ^ tag0) | (f3_ ^ tag1)) != 0u) == 0ull;
            ORDER();
            if (kearly) KW_REQ()
        }
        asm volatile("s_waitcnt lgkmcnt(0)" ::: "memory");
        __builtin_amdgcn_s_barrier();
        asm volatile("" ::: "memory");
        STAMP(layer ? 14 : 2)
#pragma unroll
        for (int s = 0; s < 10; ++s) ks[s] = (unsigned)s * 1024u;
        constexpr int NW2A = NW2P + 7 * kCR1 + kXA, NW3 = NW2P + kN3E + kN3;
        auto w2ld = LAMBDA(i_) {
            constexpr int i = i_.value;
            gld_w2<i>(w2o, p.ws + 2048 + ks[i / 5] * 5u);
        };
        auto w2a = LAMBDA(i_) {
            constexpr int k = i_.value;
            if constexpr (k < kXA) { w2ld(std::integral_constant<int, NW2P + 2 * k>{}); w2ld(std::integral_constant<int, NW2P + 2 * k + 1>{}); }
            else w2ld(std::integral_constant<int, NW2P + kXA + k>{});
        };
        auto w2x = LAMBDA(i_) {
            constexpr int i = NW2A + i_.value;
            if constexpr (i < 50) w2ld(std::integral_constant<int, i>{});
            else if constexpr (i < 52) lda4<40 + 4 * (i - 50), (i - 50) * 64>(xso, p.x0);
            else if constexpr (i == 52) lda4<48, 128>(xso - 2u * (unsigned)(bb & 16), p.x0);
        };
        auto w2b = LAMBDA(i_) {
            constexpr int k = i_.value - 7 * kCR1;
            w2x(std::integral_constant<int, k>{});
            if constexpr (k == kFHK) { vm_wait<(NW2A + kFHK + 1 <= 53 ? kFHK + 1 : 53 - NW2A)>(); st64(v2u{tag0, tag1}, rs, fho1); STAMP(39) }
            if constexpr (k == kFHK - 1) { STAMP(38) }
            if constexpr (k == 7 * (8 - kCR1) - kFHP) poll1(fho, p.ws);
            static_assert(NW2A + 7 * (8 - kCR1) >= 53 && NW2A <= 50 && NW3 <= NW2A, "");
        };
        auto partb = [&]() __attribute__((always_inline)) {
            const float* sp = (const float*)(smem + spa);
            rsr = sp[0]; rsm = sp[8];
            if (layer == 1) { pin(rsr); pin(rsm); }
            if (layer == 1) {
                ACC_TIE5(dsk);
#pragma unroll
                for (int nb = 0; nb < 5; ++nb) { a2[3][nb] = xin[nb] + (xin[nb] * rsr + rsm) * unpk4(dsk[nb]); pin(a2[3][nb]); }
            }
        };
        constexpr int LBW = layer ? 2 : 3;
        if (w == 3) {
            site<3 + 10 * layer>(); SCHED();
            f32x2 sv[16];
#pragma unroll
            for (int i = 0; i < 16; ++i) sv[i] = *(const f32x2*)(smem + spl + (l_sg(layer, i >> 2) - SC + (i & 3) * 320));
            f32x2 u[4], v[4], x[4];
            cfma4(u, l8, sv[0], sv[4], sv[8], sv[12], sv[1], sv[5], sv[9], sv[13]);
            cfma4(v, l8, u[0], u[1], u[2], u[3], sv[2], sv[6], sv[10], sv[14]);
            cfma4(x, l8, v[0], v[1], v[2], v[3], sv[3], sv[7], sv[11], sv[15]);
            f32x2 P[4][4];
            P[0][0] = f32x2{0.f, 0.f}; P[0][1] = x[0]; P[0][2] = cfma(l32, x[0], x[1]); P[0][3] = cfma(l32, P[0][2], x[2]);
            const f32x2 E = cfma(l32, P[0][3], x[3]);
            st128(v4u{__float_as_uint(E.x), tag0, __float_as_uint(E.y), tag0}, rs, es);
            SCHED();
            if (layer == 0) { FW_OK() static_for<kN3E>(LAMBDA(i_) { w2ld(std::integral_constant<int, NW2P + i_.value>{}); }); }
#pragma unroll
            for (int kk = 1; kk < 4; ++kk) cfma4(P[kk], l8, P[kk - 1][0], P[kk - 1][1], P[kk - 1][2], P[kk - 1][3], sv[kk - 1], sv[kk + 3], sv[kk + 7], sv[kk + 11]);
#pragma unroll
            for (int i = 0; i < 16; ++i) *(f32x2*)(smem + spl + (l_sg(layer, i >> 2) - SC + (i & 3) * 320)) = P[i & 3][i >> 2];
            partb();
        }
        if (w == LBW) {
            site<5 + 10 * layer>(); SCHED();
            f32x2 wg[17], t4[4];
            unsigned ea[15], tg[15];
            const f32x2 z2 = {0.f, 0.f};
            wg[1] = cmul(l128, l128); wg[2] = cmul(wg[1], wg[1]); wg[3] = cmul(wg[2], wg[1]); wg[4] = cmul(wg[2], wg[2]);
#pragma unroll
            for (int i = 4; i < 16; i += 4) {
                cfma4(t4, wg[i], wg[1], wg[2], wg[3], wg[4], z2, z2, z2, z2);
#pragma unroll
                for (int e = 0; e < 4; ++e) wg[i + 1 + e] = t4[e];
            }
#pragma unroll
            for (int i = 0; i < 15; ++i) {
                const int ch = mt1 - 1 - i;
                if (i) pin(wg[i]);
                ea[i] = ch < 0 ? kNone : (unsigned)ch * 10240u; tg[i] = ch < 0 ? 0u : tag0;
                pins(ea[i]); pins(tg[i]);
            }
            if (layer == 1) partb();
            if constexpr (layer == 0) tour<3>();
            f32x2 H0, H1;
            __builtin_amdgcn_s_sleep(layer ? KLB1 : KLB0);
            unsigned spins = dead ? kSpinCap : 0u;
            bool miss;
#pragma nounroll
            do {
                asm volatile("" ::: "memory");
                v4u eg[15];
#pragma unroll
                for (int i = 0; i < 15; ++i) eg[i] = ld128s(rs, eo, ea[i]);
                SCHED();
                unsigned x = __builtin_amdgcn_bitop3_b32(eg[0].y, eg[0].w, tg[0], 0x7E);
                H0 = f32x2{__uint_as_float(eg[0].x), __uint_as_float(eg[0].z)}; H1 = f32x2{0.f, 0.f};
#pragma unroll
                for (int i = 1; i < 15; i += 2) {
                    x |= __builtin_amdgcn_bitop3_b32(eg[i].y, eg[i].w, tg[i], 0x7E) | __builtin_amdgcn_bitop3_b32(eg[i + 1].y, eg[i + 1].w, tg[i + 1], 0x7E);
                    pin(x);
                    gacc2(H0, H1, wg[i], wg[i + 1], eg[i], eg[i + 1]);
                    SCHED();
                }
                miss = __builtin_amdgcn_ballot_w64(x != 0u) != 0ull;
            } while (miss && ++spins <= kSpinCap);
            dead = dead || miss;
            ORDER();
            *(f32x2*)(smem + spl + (L_CW - SC)) = H0 + H1;
            STAMP(layer ? 36 : 37)
            if (layer == 0) {
                static_for<NW3 - NW2P - kN3E>(LAMBDA(i_) { w2ld(std::integral_constant<int, NW2P + kN3E + i_.value>{}); });
            }
        }
        int w2_ = w;
        pins(w2_);
        if (layer == 0) UNDEF8(ca);
        if (layer == 0 && w2_ != LBW) {
            if (cv) {
                site<2>(); SCHED();
                partb();
                conv_part<0, kCR1>(ca, smem, ab, bb, w2a, LAMBDA(i_) {}, [&]() __attribute__((always_inline)) {
                    FW_OK()
                    st_ur((unsigned)(size_t)(LAS char*)smem + (unsigned)uwa);
                    STAMP(32)
                });
            } else {
                if constexpr (layer == 0) tour<5>();
                FW_OK()
                static_for<NW2A - NW2P>(LAMBDA(i_) { w2ld(std::integral_constant<int, NW2P + i_.value>{}); __builtin_amdgcn_s_sleep(1); });
                partb();
            }
        }
        if (layer == 1 && w < LBW) { site<4 + 10 * layer>(); SCHED(); partb(); if constexpr (layer == 1) { if (w == 0) tour<6>(); } }
        asm volatile("s_waitcnt lgkmcnt(0)" ::: "memory");
        __builtin_amdgcn_s_barrier();
        asm volatile("" ::: "memory");
        STAMP(layer ? 15 : 4)
        {
            float cw[5], ex[5];
#pragma unroll
            for (int nb = 0; nb < 5; ++nb) { cw[nb] = *(const float*)(smem + cwa + nb * 64); ex[nb] = *(const float*)(smem + sga + nb * 64); }
            v2u ta[5], td[5];
            static_for<2>(LAMBDA(h_) {
                constexpr int n0 = 2 * h_.value;
                float hv[9][3];
                cstep3(hv[0], cw + n0, lqr + n0, lqs + n0, ex + n0);
                if constexpr (n0 == 0) {
                    chain24<0>(hv, bv, lr, ls);
                    chain24<0>(hv + 4, bv + 4, lr, ls);
                } else {
                    chain33<2>(hv, bv, lr, ls);
                    SCHED();
#pragma unroll
                    for (int rd = 0; rd < 2; ++rd) st128(v4u{ta[rd].x, ta[rd].y, td[rd].x, td[rd].y}, rs, hso + rd * 256);
                    SCHED();
                    chain33<2>(hv + 3, bv + 3, lr, ls);
                    chain32<2>(hv + 6, bv + 6, lr, ls);
                }
#pragma unroll
                for (int e = 0; e < (n0 ? 3 : 2); ++e) {
                    v4u o;
                    o.x = cvtpk(hv[1][e], hv[2][e]); o.y = cvtpk(hv[3][e], hv[4][e]);
                    o.z = cvtpk(hv[5][e], hv[6][e]); o.w = cvtpk(hv[7][e], hv[8][e]);
                    *(v4u*)(smem + tbw + (n0 + e) * 1024) = o;
                }
#pragma unroll
                for (int rd = n0; rd < (n0 ? 5 : 2); ++rd) { ta[rd] = __builtin_bit_cast(v2u, trread(smem, tbr + rd * 1024)); td[rd] = __builtin_bit_cast(v2u, trread(smem, tbr + rd * 1024 + 512)); }
                SCHED();
            });
#pragma unroll
            for (int rd = 2; rd < 5; ++rd) st128(v4u{ta[rd].x, ta[rd].y, td[rd].x, td[rd].y}, rs, hso + rd * 256);
            SCHED();
        }
#pragma unroll
        for (int s = 0; s < 10; ++s) pins(ks[s]);
        if (layer == 1) { DRAIN(); st64(v2u{tag0, tag1}, rs, fho1); }
        STAMP(layer ? 16 : 5)
        if (cv) {
            if constexpr (layer == 0) site<6>();
            SCHED();
            v2u u[8];
            float uf[2];
            const float sd = lo16f(sdq);
            const int ua = bb + 480 - ((dst[0] >> 5) & 24);
            conv_part<kCR1, 8>(ca, smem, ab, bb, w2b, LAMBDA(g_) {
                constexpr int g = g_.value, n = g - 4;
                if constexpr (g < 4) { u[2 * g] = *(const v2u*)(smem + ua + 1024 * g); u[2 * g + 1] = *(const v2u*)(smem + ua + 1024 * g + 512); }
                else if constexpr ((n & 1) == 0) unp2(uf[0], uf[1], u[n >> 2][(n >> 1) & 1]);
                else mul2(kf[n >> 2][n & 2], kf[n >> 2][(n & 2) + 1], uf[0], uf[1], sd);
            }, []() {});
        } else if (layer == 0) {
            static_for<53 - NW2A>(LAMBDA(i_) { w2x(i_); __builtin_amdgcn_s_sleep(1); if constexpr (i_.value == 52 - NW2A) { vm_wait<53 - NW2A>(); st64(v2u{tag0, tag1}, rs, fho1); } });
            if (w == 3) { static_for<NW2A - NW3>(LAMBDA(i_) { w2ld(std::integral_constant<int, NW3 + i_.value>{}); __builtin_amdgcn_s_sleep(1); }); if constexpr (layer == 0) tour<2>(); }
            poll1(fho, p.ws);
#pragma unroll
            for (int nb = 0; nb < 8; ++nb) asm volatile("" : "=v"(kf[nb][0]), "=v"(kf[nb][1]), "=v"(kf[nb][2]), "=v"(kf[nb][3]));
        }
        if (layer == 0) { unsigned f0_, f1_; asm volatile("s_waitcnt vmcnt(0)\n\t" AR("%0", 36, 0) AR("%1", 36, 1) : "=v"(f0_), "=v"(f1_)::"memory"); fh = v2u{f0_, f1_}; }
        STAMP(layer ? 29 : 6)
        }
        unsigned g[8][2];

            v2u pt[4];
            if (layer == 0) WAVE_RETRY(ISTAG(fh), fh = ld64(rs, fho))
            else { skip<SKIP7>(); pipe_flag(pt, fho, p.ws, (u64)tag0 | ((u64)tag1 << 32), dead); ORDER(); }
            STAMP(layer ? 17 : 31)
            site<7 + 10 * layer>(); SCHED();
            constexpr int RING2 = layer ? KR2 : 20;
            v4u ar[RING2];
#pragma unroll
            for (int i = 0; i < (layer ? RING2 : 12); ++i) { ar[i] = ld128s(rs, ho[i / 10], ks[i % 10]); SCHED(); }
        int lane_o, b_o = b0, w_o = w0;
        asm volatile("v_mbcnt_lo_u32_b32 %0, -1, 0\n\tv_mbcnt_hi_u32_b32 %0, -1, %0" : "=v"(lane_o));
        asm volatile("" : "+s"(b_o), "+s"(w_o));
        const int lane = lane_o, b = b_o, w = w_o;
        const int c = lane & 15, q = lane >> 4;
        TILES()
        const int chn = 80 * nt2 + i32 + 32 * (cv ? w : 0);
        const unsigned fgo = OFF_FG + (unsigned)((nt2 * 96 + (lane & 31)) * 128);
        v4u gl[3];
        v2u f0;
        unsigned sgo, sgs, glo;
        bool gearly = false;
        {
            unsigned fgo1, gso;
            v4u gq[4];
            if (layer == 0) {
                const unsigned dum = (OOB & 1) ? kNone : OFF_DUM + (unsigned)((b * 4 + w) * 128);
                gso = cv ? OFF_GT + (unsigned)(chn * 4096 + (q & 1) * 512 + c * 32 + (q >> 1) * 16) : dum + (lane & 7) * 16;
                fgo1 = w == 0 ? OFF_FG + (unsigned)((nt2 * 96 + i32) * 128) : dum;
                if (!cv) {
                    asm volatile("" : "=v"(g[0][0]), "=v"(g[0][1]), "=v"(g[1][0]), "=v"(g[1][1]), "=v"(g[2][0]), "=v"(g[2][1]), "=v"(g[3][0]), "=v"(g[3][1]),
                                 "=v"(g[4][0]), "=v"(g[4][1]), "=v"(g[5][0]), "=v"(g[5][1]), "=v"(g[6][0]), "=v"(g[6][1]), "=v"(g[7][0]), "=v"(g[7][1]));
                } else {
                    if constexpr (layer == 0) site<8>();
                    SCHED();
                    unsigned one2 = 0x3F803F80u, kk[8][4];
                    pin(one2);
#pragma unroll
                    for (int nb = 0; nb < 8; ++nb)
                        cpx4(kk[nb], __float_as_uint(kf[nb][0]), __float_as_uint(kf[nb][1]), __float_as_uint(kf[nb][2]), __float_as_uint(kf[nb][3]),
                             __float_as_uint(ca[nb][0]), __float_as_uint(ca[nb][1]), __float_as_uint(ca[nb][2]), __float_as_uint(ca[nb][3]));
#pragma unroll
                    for (int nb = 0; nb < 8; ++nb) { unsigned t[4]; d2x4(t, kk[nb][0], kk[nb][1], kk[nb][2], kk[nb][3], one2, one2, one2, one2); kk[nb][0] = t[0]; kk[nb][1] = t[1]; kk[nb][2] = t[2]; kk[nb][3] = t[3]; }
#pragma unroll
                    for (int i = 0; i < 4; ++i) cpx4(&g[2 * i][0], kk[2 * i][0], kk[2 * i][2], kk[2 * i + 1][0], kk[2 * i + 1][2], kk[2 * i][1], kk[2 * i][3], kk[2 * i + 1][1], kk[2 * i + 1][3]);
                }
                SCHED();
#pragma unroll
                for (int i = 0; i < 4; ++i) {
                    const auto r0 = __builtin_amdgcn_permlane16_swap(g[2 * i][0], g[2 * i + 1][0], false, false);
                    const auto r1 = __builtin_amdgcn_permlane16_swap(g[2 * i][1], g[2 * i + 1][1], false, false);
                    gq[i] = v4u{r0[0], r1[0], r0[1], r1[1]};
                    pin(gq[i]);
                }
                pin(gso);
                pin(fgo1);
            }
            SCHED();
            const char* src = smem + SC + w * INBOX + lane * 16;
            unsigned xs_[3][4];
            float zx[4], zd[4], zt[4], zz[5][4];
            if (layer == 0) {
                static_for<12>(LAMBDA(e_) { rda<40 + e_.value>(xs_[e_.value >> 2][e_.value & 3]); });
            }
            if constexpr (STAMPS == 1) { STAMP(layer ? 50 : 40) }
            static_for<40>(LAMBDA(i_) {
                constexpr int i = i_.value, m = i / 10, s = i % 10;
                static_for<5>(LAMBDA(k_) {
                    constexpr int kk = k_.value, n = 5 * (i - 16) + kk - 3, nb = n / 8, u = n % 8, pv = i - 1 + RING2;
                    if constexpr (s == 0 && m < 3) mfa0<kk>(a2[m][kk], ar[i % RING2]); else mfa<5 * s + kk>(a2[m][kk], ar[i % RING2]);
                    SCHED();
                    if constexpr (layer == 0 && i >= 16 && i < 26) {
                        if constexpr (n < 0) swp2(xs_[n + 3][0], xs_[n + 3][2], xs_[n + 3][1], xs_[n + 3][3]);
                        else if constexpr (nb < 5) {
                            if constexpr (u < 2) unp2(zx[2 * u], zx[2 * u + 1], xs_[nb >> 1][2 * (nb & 1) + u]);
                            else if constexpr (u < 4) unp2(zd[2 * u - 4], zd[2 * u - 3], dsk[nb][u - 2]);
                            else if constexpr (u < 6) fma2(zt[2 * u - 8], zt[2 * u - 7], zx[2 * u - 8], zx[2 * u - 7], rsr, rsr, rsm, rsm);
                            else fma2(zz[nb][2 * u - 12], zz[nb][2 * u - 11], zt[2 * u - 12], zt[2 * u - 11], zd[2 * u - 12], zd[2 * u - 11], zx[2 * u - 12], zx[2 * u - 11]);
                        }
                    }
                    if constexpr (i == 0 && kk == 0 && layer == 1) { pin(pt[0]); pin(pt[1]); pin(pt[2]); pin(pt[3]); }
                    if constexpr (layer == 0 && i < 2 && kk > 0) ar[12 + 4 * i + kk - 1] = ld128s(rs, ho[1], ks[2 + 4 * i + kk - 1]);
                    if constexpr (layer == 0 && i == kGSI && kk > 0) st128(gq[kk - 1], rs, gso + (kk - 1) * 1024);
                    if constexpr (layer == 0 && i > kGSI && i <= kGSI + 5 && kk == 2) dsk[i - kGSI - 1] = *(const v2u*)(p.Dskip + 80 * nt2 + 16 * (i - kGSI - 1) + 4 * q);
                    if constexpr (kk == 0 && i > 0 && pv < 40) ar[pv % RING2] = ld128s(rs, ho[pv / 10], ks[pv % 10]);
                    if constexpr (kk == 2 && m >= 1 && s >= 1 && s < 6) *(f32x4*)(smem + dst[m - 1] + (s - 1) * 1024) = a2[m - 1][s - 1];
                    site<4000 + 1000 * layer + 5 * i + kk>();
                    if constexpr (i >= 37) a2[i - 37][kk] = *(const f32x4*)(src + (i - 37) * 5120 + kk * 1024);
                    SCHED();
                });
                if constexpr (i == 10) { asm volatile("s_waitcnt lgkmcnt(0)" ::: "memory"); __builtin_amdgcn_s_barrier(); asm volatile("" ::: "memory"); }
                if constexpr (i == kFGI && layer == 0) {
                    vm_wait<kFGI - kGSI + 5>();
                    __builtin_amdgcn_s_barrier(); asm volatile("" ::: "memory");
                    st64(v2u{tag0, tag1}, rs, fgo1);
                }
                if constexpr (i == 26 && layer == 0) {
#pragma unroll
                    for (int nb = 0; nb < 5; ++nb) { a2[3][nb] = f32x4{zz[nb][0], zz[nb][1], zz[nb][2], zz[nb][3]}; pin(a2[3][nb]); }
                }
                if constexpr (i == 29) {
                    sgo = OFF_SG + (unsigned)(((layer * 32 + mt2) * 4 + w) * 2048 + c * 16);
                    glo = OFF_GT + (unsigned)((80 * nt2 + (lane >> 1)) * 4096 + (64 * mt2 + 16 * w) * 2 + (lane & 1) * 16);
                    sgs = q < 2 ? sgo + (unsigned)(nt2 * 256 + q * 8) : kNone;
                    pin(sgo); pin(sgs);
                    if (layer == 0) pin(glo);
                }
                if constexpr (i == kGPI && layer == 0) gld2s<0>(f0, fgo, p.ws);
                if constexpr (i == kGCI && layer == 0) {
                    wait1(f0);
                    static_for<39 - i>(LAMBDA(r_) { pin(ar[(i + 1 + r_.value) % RING2]); });
                    gearly = __builtin_amdgcn_ballot_w64(!ISTAG(f0)) == 0ull;
                    ORDER();
                    undef3(gl);
                    if (__builtin_expect(gearly, 1)) { gld4t<0>(gl[0], glo, p.ws); gld4t<0>(gl[1], glo, p.ws + 32 * 4096); gld4t<0>(gl[2], glo, p.ws + 48 * 4096); }
                    else if (G2ND) poll2(fgo, fgo, p.ws);
                }
                if constexpr (i == 36) {
                    asm volatile("s_waitcnt lgkmcnt(0)" ::: "memory");
                    __builtin_amdgcn_s_barrier(); asm volatile("" ::: "memory");
                }
                if constexpr (STAMPS == 1 && (i == 0 || i == 9 || i == 10 || i == 15 || i == 25 || i == 35 || i == 36)) { STAMP((layer ? 51 : 41) + (i == 0 ? 0 : i == 9 ? 1 : i == 10 ? 2 : i == 15 ? 3 : i == 25 ? 4 : i == 35 ? 5 : 6)) }
                SCHED();
            });
            STAMP(layer ? 18 : 8)
        }
#pragma unroll
        for (int nb = 0; nb < 5; ++nb) a2[0][nb] = (a2[0][nb] + a2[1][nb]) + a2[2][nb];
        asm volatile("s_nop 1" : "+v"(a2[3][0]), "+v"(a2[3][1]), "+v"(a2[3][2]), "+v"(a2[3][3]), "+v"(a2[3][4]),
                     "+v"(a2[0][0]), "+v"(a2[0][1]), "+v"(a2[0][2]), "+v"(a2[0][3]), "+v"(a2[0][4]));
        float ty;
        {
            f32x4 s1, s2;
#pragma unroll
            for (int nb = 0; nb < 5; ++nb) {
                const f32x4 y = a2[3][nb] + a2[0][nb];
                xin[nb] = y;
                if (nb == 0) { s1 = y; s2 = y * y; } else { s1 += y; s2 += y * y; }
            }
            ty = xsum2h(hsum4(s1), hsum4(s2));
            st64(v2u{__float_as_uint(ty), tag0}, rs, sgs);
        }
        SCHED();
        STAMP(layer ? 19 : 9)
        if (layer == 0 && __builtin_expect(!gearly, 0)) {
            if (G2ND) {
                unsigned f0_, f1_;
                asm volatile("s_waitcnt vmcnt(1)\n\t" AR("%0", 36, 0) AR("%1", 36, 1) : "=v"(f0_), "=v"(f1_)::"memory");
                f0 = v2u{f0_, f1_};
            }
            WAVE_RETRY(ISTAG(f0), f0 = ld64(rs, fgo))
            gld4t<0>(gl[0], glo, p.ws); gld4t<0>(gl[1], glo, p.ws + 32 * 4096); gld4t<0>(gl[2], glo, p.ws + 48 * 4096);
            asm volatile("s_waitcnt vmcnt(0)" : "+v"(gl[0]), "+v"(gl[1]), "+v"(gl[2])::"memory");
        }
        if (layer == 0) {
            asm volatile("s_waitcnt vmcnt(1)" : "+v"(gl[0]), "+v"(gl[1]), "+v"(gl[2])::"memory");
            char* tb = smem + SC + w * INBOX;
            *(v4u*)(tb + lane * 16) = gl[0]; *(v4u*)(tb + 1024 + lane * 16) = gl[1]; *(v4u*)(tb + 1536 + lane * 16) = gl[2];
#pragma unroll
            for (int nb = 0; nb < 5; ++nb) gt[nb] = __builtin_bit_cast(v2u, trread(tb, (16 * nb + 4 * q + (c >> 2)) * 32 + (c & 3) * 8));
        } else ACC_TIE5(gt);
        SCHED();
        f32x4 gf[5], yg[5];
#pragma unroll
        for (int nb = 0; nb < 5; ++nb) { gf[nb] = unpk4(gt[nb]); yg[nb] = xin[nb] * gf[nb]; pin(gf[nb]); pin(yg[nb]); }
        unsigned xs = OFF_X2F + (unsigned)((2 * mt2 + (w >> 1)) * 40960 + ((c >> 2) & 1) * 20480 + (10 * nt2 + 2 * (q & 1) + (q >> 1)) * 256 + (8 * (w & 1) + 4 * (c >> 3) + (c & 3)) * 16);
        unsigned pso = q < 2 ? OFF_PS + (unsigned)(((mt2 * 4 + w) * 8 + nt2) * 128 + c * 8 + q * 4) : kNone;
        unsigned fxo1 = OFF_FX + (unsigned)(((mt1 * 4 + 2 * (mt2 & 1) + (w >> 1)) * 16 + nt2 * 2 + (w & 1)) * 128);
        unsigned oo = (unsigned)(((64 * mt2 + 16 * w + c) * D_ + 80 * nt2 + 4 * q) * 4);
        if (layer == 0) { pin(xs); pin(pso); pin(fxo1); } else pin(oo);
        float c_r, c_m, c_rr, c_rm, c_mm;
        if constexpr (layer == 0 && PSA) {
            f32x4 su = yg[0], sg_ = gf[0], syu = xin[0] * yg[0], suu = yg[0] * yg[0], sug = yg[0] * gf[0], sgg = gf[0] * gf[0];
#pragma unroll
            for (int nb = 1; nb < 5; ++nb) { su += yg[nb]; sg_ += gf[nb]; syu += xin[nb] * yg[nb]; suu += yg[nb] * yg[nb]; sug += yg[nb] * gf[nb]; sgg += gf[nb] * gf[nb]; }
            const float hu = hsum4(su);
            c_r = xsum2h(hu, 2.f * hsum4(syu)); c_m = xsum2h(hsum4(sg_), 2.f * hu);
            c_rr = xsum2h(0.f, hsum4(suu)); c_rm = xsum2h(0.f, 2.f * hsum4(sug)); c_mm = xsum2h(0.f, hsum4(sgg));
            pin(c_r); pin(c_m); pin(c_rr); pin(c_rm); pin(c_mm);
        }
        SCHED();
        {
            float a, d;
            if constexpr (layer == 0) { skip<SKIP4>(); site<9>(); }
            pipe_gran<layer ? PDS : PDS0>(a, d, sgo + q * 512, p.ws, tag0, dead);
            STAMP(layer ? 20 : 10)
            xsum2(a, d);
            const float me = a * (1.0f / D_);
            const float ri = __builtin_amdgcn_rsqf(fmaxf(d * (1.0f / D_) - me * me, 0.f) + 1e-5f), mr = -me * ri;
            if constexpr (layer == 0 && PSA) {
                const float ps_ = ty + ri * (c_r + ri * c_rr + mr * c_rm) + mr * (c_m + mr * c_mm);
                __builtin_amdgcn_raw_buffer_store_b32(__float_as_uint(ps_), rs, pso, 0, SC1);
                SCHED();
            }
#pragma unroll
            for (int nb = 0; nb < 5; ++nb) xin[nb] = yg[nb] * ri + (gf[nb] * mr + xin[nb]);
        }
        if constexpr (layer == 0) {
            v2u pk[5];
#pragma unroll
            for (int nb = 0; nb < 5; ++nb) { const f32x4 x = xin[nb]; pk[nb] = v2u{cvtpk(x.x, x.y), cvtpk(x.z, x.w)}; }
#pragma unroll
            for (int i = 0; i < 3; ++i) {
                const v2u pa = pk[2 * i], pb = pk[i < 2 ? 2 * i + 1 : 4];
                const auto r0 = __builtin_amdgcn_permlane16_swap(pa.x, pb.x, false, false);
                const auto r1 = __builtin_amdgcn_permlane16_swap(pa.y, pb.y, false, false);
                if (i < 2 || (q & 1) == 0) st128(v4u{r0[0], r1[0], r0[1], r1[1]}, rs, xs + i * 1024);
            }
            if constexpr (!PSA) {
                f32x4 s1, s2;
#pragma unroll
                for (int nb = 0; nb < 5; ++nb) { const f32x4 x = xin[nb]; if (nb == 0) { s1 = x; s2 = x * x; } else { s1 += x; s2 += x * x; } }
                __builtin_amdgcn_raw_buffer_store_b32(__float_as_uint(xsum2h(hsum4(s1), hsum4(s2))), rs, pso, 0, SC1);
            }
            SCHED();
            int w1lane0 = (c & 1) * 10240 + (c >> 1) * 128 + ((q ^ ((c & 1) | ((c >> 2) << 1))) * 16);
            int w1lane1 = (c & 1) * 10240 + (c >> 1) * 128 + (((4 + q) ^ ((c & 1) | ((c >> 2) << 1))) * 16);
            unsigned fxo = OFF_FX + (unsigned)((mt1 * 4 + w) * 2048 + c * 128);
            unsigned xo = OFF_X2F + (unsigned)((4 * mt1 + w) * 40960 + lane * 16), xo1 = xo + 20480u;
            const int row = 8 * q + (c & 7);
            unsigned po = OFF_PS + (unsigned)((((2 * mt1 + (w >> 1)) * 4 + 2 * (w & 1) + (row >> 4)) * 8 + 4 * (c >> 3)) * 128 + (row & 15) * 8);
            int w1hi0 = w1lane0 + 61440, w1hi1 = w1lane1 + 61440;
            pin(w1lane0); pin(w1lane1); pin(w1hi0); pin(w1hi1); pin(fxo); pin(xo); pin(xo1); pin(po);
            v4u wf[2][5];
#pragma unroll
            for (int nb = 0; nb < 5; ++nb) wf[0][nb] = *(const v4u*)(smem + w1lane0 + nb * 20480);
            SCHED();
            DRAIN();
            FLAG(fxo1)
            if (b == 255 && w == 0) FLAG(OFF_NONCE)
            STAMP(11)
            if (w == 0) tour<4>();
            addrs(std::integral_constant<int, 1>{}, lane, w, b);
            v2u pt[4];
            skip<SKIP5>();
            pipe_flag(pt, fxo, p.ws, (u64)tag0 | ((u64)tag1 << 32), dead);
            ORDER();
            STAMP(12)
            v4u ar[RING1][2];
#pragma unroll
            for (int s = 0; s < RING1; ++s)
#pragma unroll
                for (int mb = 0; mb < 2; ++mb) { ar[s][mb] = ld128s(rs, mb ? xo1 : xo, s * 1024); SCHED(); }
            v2u ps[4];
#pragma unroll
            for (int i = 0; i < 4; ++i) ps[i] = ld64(rs, po + i * 128);
            SCHED();
            float t1, t2, rinv, mri, rr_[8], rm_[8];
            static_for<20>(LAMBDA(s_) {
                constexpr int s = s_.value, cu = s & 1, nx = cu ^ 1;
                LGKM0() SCHED();
                static_for<10>(LAMBDA(g_) {
                    constexpr int g = g_.value, mb = g / 5, k = g % 5;
                    if constexpr (s == 0) mf0(acc[mb][k], ar[0][mb], wf[0][k]); else mf(acc[mb][k], ar[s % RING1][mb], wf[cu][k]);
                    SCHED();
                    if constexpr (s == 0 && g == 0) { pin(pt[0]); pin(pt[1]); pin(pt[2]); pin(pt[3]); }
                    if constexpr (s < 19 && g < 3) wf[nx][g] = *(const v4u*)(smem + (nx ? w1lane1 : w1lane0) + g * 20480 + ((s + 1) >> 1) * 1024);
                    if constexpr (s < 19 && (g == 3 || g == 4)) wf[nx][g] = *(const v4u*)(smem + (nx ? w1hi1 : w1hi0) + (g - 3) * 20480 + ((s + 1) >> 1) * 1024);
                    if constexpr (g == 5 && s + RING1 < 20) ar[s % RING1][0] = ld128s(rs, xo, (s + RING1) * 1024);
                    if constexpr (g == 2 && s > 0 && s - 1 + RING1 < 20) ar[(s - 1) % RING1][1] = ld128s(rs, xo1, (s - 1 + RING1) * 1024);
                    if constexpr (s == 8 && g == 8) { t1 = __uint_as_float(ps[0].x) + __uint_as_float(ps[1].x); pin(t1); t2 = __uint_as_float(ps[0].y) + __uint_as_float(ps[1].y); pin(t2); }
                    if constexpr (s == 9 && g == 6) { t1 += __uint_as_float(ps[2].x); pin(t1); t2 += __uint_as_float(ps[2].y); pin(t2); }
                    if constexpr (s == 9 && g == 7) { t1 += __uint_as_float(ps[3].x); pin(t1); t2 += __uint_as_float(ps[3].y); pin(t2); }
                    if constexpr (s == 10 && g == 6) { t1 += dpp<0x128>(t1); t2 += dpp<0x128>(t2); pin(t1); pin(t2); }
                    if constexpr (s == 10 && g == 7) { t1 *= (1.0f / D_); pin(t1); t2 *= (1.0f / D_); pin(t2); }
                    if constexpr (s == 10 && g == 8) { t2 = fmaxf(t2 - t1 * t1, 0.f); pin(t2); }
                    if constexpr (s == 11 && g == 6) { rinv = __builtin_amdgcn_rsqf(t2 + 1e-5f); pin(rinv); }
                    if constexpr (s == 11 && g == 8) { mri = -t1 * rinv; pin(mri); }
                    if constexpr (s == 12 && g == 4) *(float*)(smem + l_rs(1, 0) + w * INBOX + lane * 4) = c < 8 ? rinv : mri;
                    if constexpr (s >= 12 && s < 15 && g >= 6 && g < 9 && 3 * (s - 12) + g - 6 < 8) bc2b<3 * (s - 12) + g - 6>(rr_[3 * (s - 12) + g - 6], rm_[3 * (s - 12) + g - 6], rinv, mri);
                    if constexpr (s >= 15 && s < 19 && g >= 5) TT_GAP(5 * (s - 15) + g - 5)
                    site<6000 + 10 * s + g>();
                    SCHED();
                });
            });
            rr[0] = f32x4{rr_[0], rr_[1], rr_[2], rr_[3]}; rr[1] = f32x4{rr_[4], rr_[5], rr_[6], rr_[7]};
            asm volatile("kpf_end2:");
            ACC_FENCE10(acc[0], acc[1]);
            STAMP(13)
        } else {
#pragma unroll
            for (int nb = 0; nb < 5; ++nb) __builtin_amdgcn_raw_buffer_store_b128(__builtin_bit_cast(v4u, xin[nb]), ro, oo + 64 * nb, 0, OUTP);
            STAMP(21)
            STAMP_DUMP()
            asm volatile("kpf_end:");
        }
    };
    layer_body(std::integral_constant<int, 0>{});
    layer_body(std::integral_constant<int, 1>{});
}

extern "C" void kernel_launch(void* const* d_in, const int* in_sizes, int n_in,
                              void* d_out, int out_size, void* d_ws, size_t ws_size,
                              hipStream_t stream) {
    Params p;
    p.x0 = (const u16*)d_in[0];
    p.Lre = (const u16*)d_in[1];
    p.Lim = (const u16*)d_in[2];
    p.Bre = (const u16*)d_in[3];
    p.Bim = (const u16*)d_in[4];
    p.Cre = (const u16*)d_in[5];
    p.Cim = (const u16*)d_in[6];
    p.logdt = (const u16*)d_in[7];
    p.Dskip = (const u16*)d_in[8];
    p.k = (const u16*)d_in[10];
    p.v = (const u16*)d_in[11];
    p.shk = (const u16*)d_in[12];
    p.ssmk = (const u16*)d_in[13];
    p.ssmD = (const u16*)d_in[14];
    p.ws = (char*)d_ws + (size_t)WSOFF * 1048576;
    p.out = (float*)d_out;
    k_main<<<256, 256, 0, stream>>>(p);
}
```

```cpp
#include <hip/hip_runtime.h>
#include <stdint.h>
#include <utility>

#define WSOFF 0
#define STAMPS 0

typedef unsigned short u16;
typedef unsigned long long u64;
typedef unsigned v4u __attribute__((ext_vector_type(4)));
typedef unsigned v2u __attribute__((ext_vector_type(2)));
typedef float f32x4 __attribute__((ext_vector_type(4)));
typedef float f32x2 __attribute__((ext_vector_type(2)));
typedef short s16x4 __attribute__((ext_vector_type(4)));
typedef __bf16 bf16x2v __attribute__((ext_vector_type(2)));
#define LAS __attribute__((address_space(3)))
#define DI __device__ __forceinline__

constexpr int S_ = 2048, D_ = 640;
#define GF 1
#define KG0 8
#define KRT 12
#define KRS 2
#define KSN 0
#define KB1 4
#define KMB 1
#define H3 0
#define KF3 14
#define KR1 5
#define KR2 12
constexpr int RING1 = KR1;
#define KCR1 3
constexpr int kCR1 = KCR1;
#define KXA 7
#define KN3E 14
#define KN3 6
constexpr int kXA = KXA;
constexpr int kN3E = KN3E, kN3 = KN3;
#define KFHK 4
#define KFHP 14
constexpr int kFHK = KFHK, kFHP = KFHP;
#define KGPI 28
#define KGCI 34
#define KFGI 12
#define G2ND 1
#define KGSI 2
constexpr int kGSI = KGSI, kFGI = KFGI, kGPI = KGPI, kGCI = KGCI;

constexpr unsigned OFF_NONCE = 0;
constexpr unsigned OFF_ST = 4096;
constexpr unsigned OFF_FW = OFF_ST + 65536;
constexpr unsigned OFF_FK = OFF_FW + 131072;
constexpr unsigned OFF_FH = OFF_FK + 81920;
constexpr unsigned OFF_FX = OFF_FH + 262144;
constexpr unsigned OFF_FG = OFF_FX + 131072;
constexpr unsigned OFF_EG = OFF_FG + 98304;
constexpr unsigned OFF_SG = OFF_EG + 327680;
constexpr unsigned OFF_PS = OFF_SG + 524288;
constexpr unsigned OFF_KVT = OFF_PS + 131072;
constexpr unsigned OFF_GT = OFF_KVT + 2621440;
constexpr unsigned OFF_W2F = OFF_GT + 2621440;
constexpr unsigned OFF_X2F = OFF_W2F + 1638400;
constexpr unsigned OFF_DUM = OFF_X2F + 2621440;
constexpr unsigned OFF_HF = OFF_DUM + 163840;
constexpr unsigned WS_BYTES = OFF_HF + 2 * 5242880;
constexpr unsigned kNone = 0x40000000u;
constexpr unsigned OFF_DBG = 0x1800000;
#define I32M 1
#define KVS 1
#define KVU 0
#define MAP 0
#define TILES() const int xq = b & 7, j = b >> 3, mt2 = MAP ? 8 * (xq >> 1) + (j >> 2) : 4 * xq + (j >> 3), nt2 = MAP ? 4 * (xq & 1) + (j & 3) : j & 7, mt1 = mt2 >> 1, \
    nt1 = MAP ? 8 * (xq & 1) + (j & 7) : j & 15, i32 = 16 * (mt2 & 1) + mt1;

constexpr int SC = 102400;
constexpr int REGSZ = 13056;
constexpr int L_XR = SC;
constexpr int L_CO = SC + 12288;
constexpr int L_KB = SC + 51712;
constexpr int L_TAB = SC + 55808;
constexpr int L_CW = SC + 39168;
constexpr int L_FL = L_CW + 320;
constexpr int INBOX = 15360;
constexpr int l_rs(int l, int w) { return l ? SC + w * INBOX : L_KB + w * 256; }
constexpr int l_sg(int l, int w) { return l ? SC + w * INBOX + 256 : SC + (w == 0 ? 43264 : w == 1 ? 46080 : w == 2 ? 52736 : 56320); }
constexpr int l_tb(int l, int w) { return l ? SC + w * INBOX + 2048 : SC + 40960 + w * 5120; }
constexpr int LDS_BYTES = 163840;
constexpr int CV_E = 4608;
constexpr int CV_O = CV_E + (1056 + 15) * 4;

struct Params {
    const u16 *x0, *Lre, *Lim, *Bre, *Bim, *Cre, *Cim, *logdt, *Dskip, *k, *v, *shk, *ssmk, *ssmD;
    char* ws;
    float* out;
};

#define MF "v_mfma_f32_16x16x32_bf16 "
DI void mf(f32x4& c, v4u a, v4u b) { asm(MF "%0, %1, %2, %0" : "+v"(c) : "v"(a), "v"(b)); }
DI void mf0(f32x4& c, v4u a, v4u b) { asm(MF "%0, %1, %2, 0" : "=&v"(c) : "v"(a), "v"(b)); }
constexpr int W2R = 56;
#define WA(n) "a[%c" #n ":%c" #n "+3]"
template <int R> DI void mfa(f32x4& c, v4u a) { asm(MF "%0, " WA(2) ", %1, %0" : "+v"(c) : "v"(a), "i"(W2R + 4 * R)); }
template <int R> DI void mfa0(f32x4& c, v4u a) { asm(MF "%0, " WA(2) ", %1, 0" : "=&v"(c) : "v"(a), "i"(W2R + 4 * R)); }
#define ACC_FENCE10(c, d) asm volatile("" : "+v"(c[0]), "+v"(c[1]), "+v"(c[2]), "+v"(c[3]), "+v"(c[4]), "+v"(d[0]), "+v"(d[1]), "+v"(d[2]), "+v"(d[3]), "+v"(d[4]))
#define ACC_TIE5(c) asm volatile("" : "+v"(c[0]), "+v"(c[1]), "+v"(c[2]), "+v"(c[3]), "+v"(c[4]))
#define QP " quad_perm:[1,0,3,2] row_mask:0xf bank_mask:0xf"
DI void cstep(float (&t)[5], const float (&h)[5], const float (&lr)[5], const float (&ls)[5], float b0, float b1, float b2, float b3, float b4) {
    asm("v_fma_f32 %0, %10, %5, %20\n\tv_fma_f32 %1, %11, %6, %21\n\tv_fma_f32 %2, %12, %7, %22\n\tv_fma_f32 %3, %13, %8, %23\n\tv_fma_f32 %4, %14, %9, %24\n\t"
        "v_fmac_f32_dpp %0, %5, %15" QP "\n\tv_fmac_f32_dpp %1, %6, %16" QP "\n\tv_fmac_f32_dpp %2, %7, %17" QP "\n\tv_fmac_f32_dpp %3, %8, %18" QP "\n\tv_fmac_f32_dpp %4, %9, %19" QP
        : "=&v"(t[0]), "=&v"(t[1]), "=&v"(t[2]), "=&v"(t[3]), "=&v"(t[4])
        : "v"(h[0]), "v"(h[1]), "v"(h[2]), "v"(h[3]), "v"(h[4]), "v"(lr[0]), "v"(lr[1]), "v"(lr[2]), "v"(lr[3]), "v"(lr[4]),
          "v"(ls[0]), "v"(ls[1]), "v"(ls[2]), "v"(ls[3]), "v"(ls[4]), "v"(b0), "v"(b1), "v"(b2), "v"(b3), "v"(b4));
}
DI void cstep3(float* t, const float* h, const float* lr, const float* ls, const float* b) {
    asm("v_fma_f32 %0, %6, %3, %12\n\tv_fma_f32 %1, %7, %4, %13\n\tv_fma_f32 %2, %8, %5, %14\n\t"
        "v_fmac_f32_dpp %0, %3, %9" QP "\n\tv_fmac_f32_dpp %1, %4, %10" QP "\n\tv_fmac_f32_dpp %2, %5, %11" QP
        : "=&v"(t[0]), "=&v"(t[1]), "=&v"(t[2])
        : "v"(h[0]), "v"(h[1]), "v"(h[2]), "v"(lr[0]), "v"(lr[1]), "v"(lr[2]), "v"(ls[0]), "v"(ls[1]), "v"(ls[2]), "v"(b[0]), "v"(b[1]), "v"(b[2]));
}
template <int o> DI void chain24(float (*t)[3], const float (*b)[5], const float* lr, const float* ls) {
    asm("v_fma_f32 %0, %18, %16, %8\n\t" "v_fma_f32 %1, %19, %17, %9\n\t" "v_fmac_f32_dpp %0, %16, %20" QP "\n\t" "v_fmac_f32_dpp %1, %17, %21" QP "\n\t"
        "v_fma_f32 %2, %18, %0, %10\n\t" "v_fma_f32 %3, %19, %1, %11\n\t" "v_fmac_f32_dpp %2, %0, %20" QP "\n\t" "v_fmac_f32_dpp %3, %1, %21" QP "\n\t"
        "v_fma_f32 %4, %18, %2, %12\n\t" "v_fma_f32 %5, %19, %3, %13\n\t" "v_fmac_f32_dpp %4, %2, %20" QP "\n\t" "v_fmac_f32_dpp %5, %3, %21" QP "\n\t"
        "v_fma_f32 %6, %18, %4, %14\n\t" "v_fma_f32 %7, %19, %5, %15\n\t" "v_fmac_f32_dpp %6, %4, %20" QP "\n\t" "v_fmac_f32_dpp %7, %5, %21" QP "\n\t"
        : "=&v"(t[1][0]), "=&v"(t[1][1]), "=&v"(t[2][0]), "=&v"(t[2][1]), "=&v"(t[3][0]), "=&v"(t[3][1]), "=&v"(t[4][0]), "=&v"(t[4][1])
        : "v"(b[0][o + 0]), "v"(b[0][o + 1]), "v"(b[1][o + 0]), "v"(b[1][o + 1]), "v"(b[2][o + 0]), "v"(b[2][o + 1]), "v"(b[3][o + 0]), "v"(b[3][o + 1]), "v"(t[0][0]), "v"(t[0][1]), "v"(lr[o + 0]), "v"(lr[o + 1]), "v"(ls[o + 0]), "v"(ls[o + 1]));
}
template <int o> DI void chain33(float (*t)[3], const float (*b)[5], const float* lr, const float* ls) {
    asm("v_fma_f32 %0, %21, %18, %9\n\t" "v_fma_f32 %1, %22, %19, %10\n\t" "v_fma_f32 %2, %23, %20, %11\n\t" "v_fmac_f32_dpp %0, %18, %24" QP "\n\t" "v_fmac_f32_dpp %1, %19, %25" QP "\n\t" "v_fmac_f32_dpp %2, %20, %26" QP "\n\t"
        "v_fma_f32 %3, %21, %0, %12\n\t" "v_fma_f32 %4, %22, %1, %13\n\t" "v_fma_f32 %5, %23, %2, %14\n\t" "v_fmac_f32_dpp %3, %0, %24" QP "\n\t" "v_fmac_f32_dpp %4, %1, %25" QP "\n\t" "v_fmac_f32_dpp %5, %2, %26" QP "\n\t"
        "v_fma_f32 %6, %21, %3, %15\n\t" "v_fma_f32 %7, %22, %4, %16\n\t" "v_fma_f32 %8, %23, %5, %17\n\t" "v_fmac_f32_dpp %6, %3, %24" QP "\n\t" "v_fmac_f32_dpp %7, %4, %25" QP "\n\t" "v_fmac_f32_dpp %8, %5, %26" QP "\n\t"
        : "=&v"(t[1][0]), "=&v"(t[1][1]), "=&v"(t[1][2]), "=&v"(t[2][0]), "=&v"(t[2][1]), "=&v"(t[2][2]), "=&v"(t[3][0]), "=&v"(t[3][1]), "=&v"(t[3][2])
        : "v"(b[0][o + 0]), "v"(b[0][o + 1]), "v"(b[0][o + 2]), "v"(b[1][o + 0]), "v"(b[1][o + 1]), "v"(b[1][o + 2]), "v"(b[2][o + 0]), "v"(b[2][o + 1]), "v"(b[2][o + 2]), "v"(t[0][0]), "v"(t[0][1]), "v"(t[0][2]), "v"(lr[o + 0]), "v"(lr[o + 1]), "v"(lr[o + 2]), "v"(ls[o + 0]), "v"(ls[o + 1]), "v"(ls[o + 2]));
}
template <int o> DI void chain32(float (*t)[3], const float (*b)[5], const float* lr, const float* ls) {
    asm("v_fma_f32 %0, %15, %12, %6\n\t" "v_fma_f32 %1, %16, %13, %7\n\t" "v_fma_f32 %2, %17, %14, %8\n\t" "v_fmac_f32_dpp %0, %12, %18" QP "\n\t" "v_fmac_f32_dpp %1, %13, %19" QP "\n\t" "v_fmac_f32_dpp %2, %14, %20" QP "\n\t"
        "v_fma_f32 %3, %15, %0, %9\n\t" "v_fma_f32 %4, %16, %1, %10\n\t" "v_fma_f32 %5, %17, %2, %11\n\t" "v_fmac_f32_dpp %3, %0, %18" QP "\n\t" "v_fmac_f32_dpp %4, %1, %19" QP "\n\t" "v_fmac_f32_dpp %5, %2, %20" QP "\n\t"
        : "=&v"(t[1][0]), "=&v"(t[1][1]), "=&v"(t[1][2]), "=&v"(t[2][0]), "=&v"(t[2][1]), "=&v"(t[2][2])
        : "v"(b[0][o + 0]), "v"(b[0][o + 1]), "v"(b[0][o + 2]), "v"(b[1][o + 0]), "v"(b[1][o + 1]), "v"(b[1][o + 2]), "v"(t[0][0]), "v"(t[0][1]), "v"(t[0][2]), "v"(lr[o + 0]), "v"(lr[o + 1]), "v"(lr[o + 2]), "v"(ls[o + 0]), "v"(ls[o + 1]), "v"(ls[o + 2]));
}
#define UNDEF8(c) asm volatile("" : "=v"(c[0]), "=v"(c[1]), "=v"(c[2]), "=v"(c[3]), "=v"(c[4]), "=v"(c[5]), "=v"(c[6]), "=v"(c[7]))
#define PIN5(t) asm volatile("s_nop 1" : "+v"(t[0]), "+v"(t[1]), "+v"(t[2]), "+v"(t[3]), "+v"(t[4]))
DI unsigned cvtpk(float lo, float hi) {
    f32x2 v = {lo, hi};
    return __builtin_bit_cast(unsigned, __builtin_convertvector(v, bf16x2v));
}
DI float lo16f(unsigned u) { return __uint_as_float(u << 16); }
DI float hi16f(unsigned u) { return __uint_as_float(u & 0xffff0000u); }
DI float rbf(float v) { return lo16f(cvtpk(v, 0.f)); }
template <int CTRL> DI float dpp(float x) {
    return __int_as_float(__builtin_amdgcn_update_dpp(0, __float_as_int(x), CTRL, 0xF, 0xF, true));
}
DI float rowsum16(float x) {
    x += dpp<0x128>(x); x += dpp<0x124>(x); x += dpp<0x122>(x); x += dpp<0x121>(x);
    return x;
}
DI float xsum32(float x) { auto r = __builtin_amdgcn_permlane32_swap(__float_as_uint(x), __float_as_uint(x), false, false); return __uint_as_float(r[0]) + __uint_as_float(r[1]); }
DI void xsum2(float& a, float& d) {
    auto r = __builtin_amdgcn_permlane16_swap(__float_as_uint(a), __float_as_uint(d), false, false);
    const float s = __uint_as_float(r[0]) + __uint_as_float(r[1]);
    const float t = xsum32(s);
    auto u = __builtin_amdgcn_permlane16_swap(__float_as_uint(t), __float_as_uint(t), false, false);
    a = __uint_as_float(u[0]); d = __uint_as_float(u[1]);
}
DI float xsum2h(float a, float d) {
    auto r = __builtin_amdgcn_permlane16_swap(__float_as_uint(a), __float_as_uint(d), false, false);
    return xsum32(__uint_as_float(r[0]) + __uint_as_float(r[1]));
}
DI float hsum4(f32x4 v) { return (v.x + v.y) + (v.z + v.w); }
typedef __amdgpu_buffer_rsrc_t rsrc_t;
constexpr int SC1 = 16;
DI v4u ld128(rsrc_t rs, unsigned off) { return __builtin_amdgcn_raw_buffer_load_b128(rs, off, 0, SC1); }
DI v4u ld128s(rsrc_t rs, unsigned off, unsigned soff) { return __builtin_amdgcn_raw_buffer_load_b128(rs, off, soff, SC1); }
DI v2u ld64(rsrc_t rs, unsigned off) { return __builtin_amdgcn_raw_buffer_load_b64(rs, off, 0, SC1); }
DI void st128(v4u v, rsrc_t rs, unsigned off) { __builtin_amdgcn_raw_buffer_store_b128(v, rs, off, 0, SC1); }
DI void st64(v2u v, rsrc_t rs, unsigned off) { __builtin_amdgcn_raw_buffer_store_b64(v, rs, off, 0, SC1); }
#define LGKM0() { __builtin_amdgcn_s_waitcnt(0xC07F); asm volatile("" ::: "memory"); }
#define DRAIN() asm volatile("s_waitcnt vmcnt(0)" ::: "memory")
#define ORDER() __builtin_amdgcn_fence(__ATOMIC_ACQUIRE, "wavefront")
#define SCHED() __builtin_amdgcn_sched_barrier(0)
constexpr unsigned kSpinCap = 1u << 17;
#define WAVE_WAIT(...)                                                      \
    {                                                                       \
        unsigned spins_ = dead ? kSpinCap : 0u;                             \
        _Pragma("nounroll") for (;;) {                                      \
            asm volatile("" ::: "memory");                                  \
            bool ok;                                                        \
            __VA_ARGS__;                                                    \
            if (__builtin_amdgcn_ballot_w64(!ok) == 0ull) break;            \
            if (++spins_ > kSpinCap) { dead = true; break; }                \
            __builtin_amdgcn_s_sleep(1);                                    \
        }                                                                   \
        ORDER();                                                            \
    }
#define KPD1 2
#define KPDS 8
#define KPDS0 2
#define KPF0 12
#define KPF1 4
constexpr int PD1 = KPD1, PDS = KPDS, PDS0 = KPDS0;
#define PL(r) "global_load_dwordx2 " r ", %5, %6 sc1\n\t"
#define PS_(r) "s_waitcnt vmcnt(1)\n\tv_cmp_ne_u64 vcc, %7, " r "\n\ts_cbranch_vccz 2f\n\t" PL(r)
DI void pipe_flag(v2u (&t)[4], unsigned off, const void* base, u64 tag, bool& dead) {
    unsigned n = dead ? 0u : kSpinCap;
    asm volatile("s_sleep %8\n\t" PL("%0") "s_sleep %9\n\t" PL("%1")
                 "1:\n\t" PS_("%0") PS_("%1") "s_sub_u32 %4, %4, 1\n\ts_cbranch_scc0 1b\n2:"
                 : "=&v"(t[0]), "=&v"(t[1]), "=&v"(t[2]), "=&v"(t[3]), "+s"(n) : "v"(off), "s"(base), "s"(tag), "i"(KPF0), "i"(KPF1) : "memory", "vcc", "scc");
    dead = __builtin_amdgcn_readfirstlane(n) == ~0u;
}
#define WAVE_RETRY(OK, ...) { if (__builtin_expect(__builtin_amdgcn_ballot_w64(!(OK)) != 0ull, 0)) WAVE_WAIT({ __VA_ARGS__; ok = OK; }) ORDER(); }
#define STAMP(i)
#define STAMP_DUMP()

#define PADS 9000, 9001, 2007, 2181, 2, 9002, 4076, 6173, 5194, 9003, 6, 3119, 9004, 9005, 9006, 9007
#define T1POS 27
#define TW 2
#define KLB0 0
#define KLB1 10
#define SKIP0 26
#define SKIP7 100
#define SKIP5 0
#define SKIP4 0
constexpr int kPads[] = {PADS};
constexpr int pad_next(int id) {
    for (unsigned i = 0; i + 1 < sizeof(kPads) / sizeof(int); ++i) if (kPads[i] == id) return kPads[i + 1];
    return -1;
}
template <int ID> DI void site() {
    if constexpr (pad_next(ID) >= 0) asm volatile("kp%c0:\n\ts_cbranch_execz kp%c1" ::"i"(ID), "i"(pad_next(ID)));
    else asm volatile("");
}
template <int T> DI void tour() {
    if constexpr (pad_next(9000 + T) >= 0 && pad_next(9000 + T) != 9001 + T) asm volatile("s_mov_b64 exec, 0\n\ts_branch kp%c0\nkp%c1:\n\ts_mov_b64 exec, -1" ::"i"(pad_next(9000 + T)), "i"(9001 + T));
}
template <int N> DI void skip() { if constexpr (N > 0) asm volatile("s_branch 1f\n\t.rept %c0\n\ts_nop 0\n\t.endr\n1:" ::"i"(N)); }
template <class F, int... I> DI void static_for_impl(std::integer_sequence<int, I...>, F&& f) { (f(std::integral_constant<int, I>{}), ...); }
template <int N, class F> DI void static_for(F&& f) { static_for_impl(std::make_integer_sequence<int, N>{}, f); }
#define IC(n) std::integral_constant<int, (n)>{}
#define LAMBDA(v) [&](auto v) __attribute__((always_inline))
#define PK2 " op_sel:[1,1,0] op_sel_hi:[1,0,1] neg_lo:[1,0,0]"
DI f32x2 cmul(f32x2 a, f32x2 b) {
    f32x2 t;
    asm("v_pk_mul_f32 %0, %1, %2 op_sel_hi:[0,1]\n\ts_nop 0\n\tv_pk_fma_f32 %0, %1, %2, %0" PK2 "\n\ts_nop 0" : "=&v"(t) : "v"(a), "v"(b));
    return t;
}
DI f32x2 cfma(f32x2 a, f32x2 b, f32x2 c) {
    f32x2 t;
    asm("v_pk_fma_f32 %0, %1, %2, %3 op_sel_hi:[0,1,1]\n\ts_nop 0\n\tv_pk_fma_f32 %0, %1, %2, %0" PK2 "\n\ts_nop 0" : "=&v"(t) : "v"(a), "v"(b), "v"(c));
    return t;
}
DI void cfma4(f32x2 (&t)[4], f32x2 a, f32x2 b0, f32x2 b1, f32x2 b2, f32x2 b3, f32x2 c0, f32x2 c1, f32x2 c2, f32x2 c3) {
    asm("v_pk_fma_f32 %0, %4, %5, %9 op_sel_hi:[0,1,1]\n\tv_pk_fma_f32 %1, %4, %6, %10 op_sel_hi:[0,1,1]\n\tv_pk_fma_f32 %2, %4, %7, %11 op_sel_hi:[0,1,1]\n\tv_pk_fma_f32 %3, %4, %8, %12 op_sel_hi:[0,1,1]\n\t"
        "v_pk_fma_f32 %0, %4, %5, %0" PK2 "\n\tv_pk_fma_f32 %1, %4, %6, %1" PK2 "\n\tv_pk_fma_f32 %2, %4, %7, %2" PK2 "\n\tv_pk_fma_f32 %3, %4, %8, %3" PK2 "\n\ts_nop 0"
        : "=&v"(t[0]), "=&v"(t[1]), "=&v"(t[2]), "=&v"(t[3]) : "v"(a), "v"(b0), "v"(b1), "v"(b2), "v"(b3), "v"(c0), "v"(c1), "v"(c2), "v"(c3));
}
DI void gacc2(f32x2& h0, f32x2& h1, f32x2 w0, f32x2 w1, v4u e0, v4u e1) {
    const v2u a0 = {e0.x, e0.y}, b0 = {e0.z, e0.w}, a1 = {e1.x, e1.y}, b1 = {e1.z, e1.w};
    asm("v_pk_fma_f32 %0, %4, %2, %0 op_sel_hi:[0,1,1]\n\tv_pk_fma_f32 %1, %6, %3, %1 op_sel_hi:[0,1,1]\n\t"
        "v_pk_fma_f32 %0, %5, %2, %0 op_sel:[0,1,0] op_sel_hi:[0,0,1] neg_lo:[1,0,0]\n\tv_pk_fma_f32 %1, %7, %3, %1 op_sel:[0,1,0] op_sel_hi:[0,0,1] neg_lo:[1,0,0]"
        : "+v"(h0), "+v"(h1) : "v"(w0), "v"(w1), "v"(a0), "v"(b0), "v"(a1), "v"(b1));
}
DI s16x4 trread(char* smem, int byteoff) {
    return __builtin_amdgcn_ds_read_tr16_b64_v4i16((LAS s16x4*)(smem + byteoff));
}

#define CV_LOAD_A(A, a, rr_, hf) { const unsigned* ap_ = (const unsigned*)(smem + ab[(a) >> 1] + (((a) & 1) ? 0 : 512) + 64 * (7 - (rr_)) + 8 * (hf)); A[a][2 * (hf)] = ap_[0]; A[a][2 * (hf) + 1] = ap_[1]; }
#define CV_LOAD_B(B, e, rr_) { B[e] = *(const v4u*)(smem + bb + 64 * (7 - (rr_)) + 512 * (e)); }
constexpr int cv_pair(int lo, int hi, int n) {
    for (int a = 0; a < 8; ++a)
        for (int e = 0; e < 8; ++e)
            if (a + e >= lo && a + e < hi && n-- == 0) return 8 * a + e;
    return -1;
}
template <int R, int PF, bool OWN, class F, class G>
DI void conv_round(f32x4 (&ca)[8], v4u (&A)[8], const v4u (&B)[8], v4u (&An)[8], v4u (&Bn)[8], const char* smem, const int (&ab)[4], int bb, F&& fill, G&& last) {
    static_for<(R == 7 ? KG0 : 36)>(LAMBDA(g_) {
        constexpr int g = g_.value, a = cv_pair(0, 8, g) / 8, e = cv_pair(0, 8, g) % 8;
        mf(ca[a + e], A[a], B[e]);
        SCHED();
        if constexpr (OWN && g < 12) { CV_LOAD_A(A, 2 + g / 2, R, g & 1) SCHED(); }
        if constexpr (g < 4 * PF && (g & 3) != 3) { if constexpr ((g & 3) == 2) CV_LOAD_B(Bn, g >> 2, R + 1) else CV_LOAD_A(An, g >> 2, R + 1, g & 1) SCHED(); }
        site<3000 + 36 * R + g>();
        if constexpr (R == 7) { last(g_); SCHED(); }
        if constexpr ((g & 3) == 3 && g < 28) { fill(std::integral_constant<int, 7 * R + g / 4>{}); SCHED(); }
    });
}
template <int R0, int R1, class F, class G, class M>
DI void conv_part(f32x4 (&ca)[8], const char* smem, const int (&ab)[4], int bb, F&& fill, G&& last, M&& mid, v4u (&A)[2][8], v4u (&B)[2][8]) {
    if constexpr (R0 > 0) { CV_LOAD_A(A[0], 0, R0, 0) CV_LOAD_A(A[0], 0, R0, 1) SCHED(); }
    static_for<8>(LAMBDA(a_) { constexpr int a = a_.value; CV_LOAD_B(B[0], a, R0) SCHED(); });
    if constexpr (R0 > 0) { CV_LOAD_A(A[0], 1, R0, 0) CV_LOAD_A(A[0], 1, R0, 1) SCHED(); }
    else {
        CV_LOAD_A(A[0], 0, 0, 0) CV_LOAD_A(A[0], 0, 0, 1) SCHED();
        const v4u ad = mid();
        SCHED();
        static_for<8>(LAMBDA(e_) {
            constexpr int e = e_.value;
            mf0(ca[e], ad, B[0][e]); SCHED();
            if constexpr (e < 7) { CV_LOAD_A(A[0], e + 1, 0, 0) CV_LOAD_A(A[0], e + 1, 0, 1) SCHED(); }
        });
    }
    static_for<R1 - R0>(LAMBDA(r_) {
        constexpr int r = r_.value;
        conv_round<R0 + r, (R0 + r + 1 < R1 ? 8 : R0 == 0 && H3 ? 3 : 0), (r == 0 && R0 > 0)>(ca, A[r & 1], B[r & 1], A[(r & 1) ^ 1], B[(r & 1) ^ 1], smem, ab, bb, fill, last);
    });
    if constexpr (R0 == 0 && H3) static_for<6>(LAMBDA(g_) { constexpr int a = cv_pair(0, 3, g_.value) / 8, e = cv_pair(0, 3, g_.value) % 8; mf(ca[a + e], A[R1 & 1][a], B[R1 & 1][e]); SCHED(); });
}
DI void lds_wait3(int a) {
    unsigned x, y, z;
    asm volatile("1:\n\tds_read_b32 %0, %3\n\tds_read_b32 %1, %3 offset:4\n\tds_read_b32 %2, %3 offset:8\n\ts_waitcnt lgkmcnt(0)\n\t"
                 "v_and_b32 %0, %0, %1\n\tv_and_b32 %0, %0, %2\n\tv_cmp_eq_u32 vcc, 0, %0\n\ts_cbranch_vccnz 1b" : "=&v"(x), "=&v"(y), "=&v"(z) : "v"(a) : "memory", "vcc");
}
DI void add2(float& a, float& b, float x, float y) { asm volatile("v_add_f32 %0, %0, %2\n\tv_add_f32 %1, %1, %3" : "+v"(a), "+v"(b) : "v"(x), "v"(y)); }
template <int IMM> DI void gld(v4u& dst, unsigned voff, const void* sbase) {
    asm volatile("global_load_dwordx4 %0, %1, %2 offset:%3" : "=v"(dst) : "v"(voff), "s"(sbase), "i"(IMM));
}
#define OOB 3
#define OUTP 16
#define NTM 31
template <int IMM, int BIT> DI void gldn(v4u& dst, unsigned voff, const void* sbase) {
    if constexpr ((NTM >> BIT) & 1) asm volatile("global_load_dwordx4 %0, %1, %2 offset:%3 nt" : "=v"(dst) : "v"(voff), "s"(sbase), "i"(IMM));
    else gld<IMM>(dst, voff, sbase);
}
DI void gldu16(unsigned& dst, unsigned voff, const void* sbase) { asm volatile("global_load_ushort %0, %1, %2" : "=v"(dst) : "v"(voff), "s"(sbase)); }
template <int IMM> DI void gld64(v4u& dst, const void* a) {
    if constexpr ((NTM >> 5) & 1) asm volatile("global_load_dwordx4 %0, %1, off offset:%2 nt" : "=v"(dst) : "v"(a), "i"(IMM));
    else asm volatile("global_load_dwordx4 %0, %1, off offset:%2" : "=v"(dst) : "v"(a), "i"(IMM));
}
template <int V> DI void SETM0(unsigned ldsbase) { asm volatile("s_add_u32 m0, %0, %1" ::"s"(ldsbase), "i"(V)); }
template <int GOFF> DI void GLD0(unsigned voff, const char* sbase) { asm volatile("global_load_lds_dwordx4 %0, %1 offset:%2" ::"v"(voff), "s"(sbase), "i"(GOFF) : "memory"); }
template <int GOFF, int V> DI void GLD1(unsigned voff, const char* sbase, unsigned ldsbase) {
    asm volatile("global_load_lds_dwordx4 %0, %1 offset:%3\n\ts_add_u32 m0, %2, %4" ::"v"(voff), "s"(sbase), "s"(ldsbase), "i"(GOFF), "i"(V) : "memory");
}
constexpr int dma_goff(int it) { return it < 40 ? (it >> 2) * 128 : ((it - 100) / 5) * 256; }
constexpr int dma_loff(int it) { return it < 40 ? L_XR + ((it >> 2) % 3) * 4096 + (it & 3) * 1024 : ((it - 100) % 5) * 20480 + ((it - 100) / 5) * 2048; }
#define KVF 1
#define KFD0 10
#define DSE 1
#define KNF 13
#define KFS 6
#define APOS 0
#define PSA 0
#define CWP 1
#define SAF 2
#define KD0 36
#define KDS 3
#define KNW 11
constexpr int kD0 = KD0, kDS = KDS;
static_assert(kD0 + 26 * kDS < 117, "");
#define KW2S 16
#define KW2M 0x4A
#define KFWP 13
constexpr int kW2S = KW2S, kW2M = KW2M;
constexpr int NW2P = (20 - kW2S) * __builtin_popcount(kW2M);
constexpr int P1_PRE = 34;
constexpr int p1_pre(int i) { return i < 5 ? 100 + i : i < 13 ? i - 5 : i < 18 ? 105 + (i - 13) : i < 22 ? 8 + (i - 18) : i < 27 ? 110 + (i - 22) : 400; }
constexpr int p1_item(int s, int g) {
    if (s == KFS && g == 4) return 324;
    if (g == 9) return s < 10 ? 115 + s : s == 11 ? 202 : s == 12 ? 203 : -1;
    if (s == KFWP && g == 1) return 208;
    if (s >= kW2S && ((kW2M >> g) & 1)) return 500 + (s - kW2S) * __builtin_popcount(kW2M) + __builtin_popcount(kW2M & ((1 << g) - 1));
    if ((s & 1) == 0 && s <= 12) return g >= 5 ? 4 * (s / 2 + 3) + (g - 5) : -1;
    if (s == 14) return g == 5 ? 207 : g == 7 ? 206 : -1;
    return -1;
}
constexpr int p1_cnt(int s, int g) {
    int n = P1_PRE;
    for (int t = 0; t <= s; ++t)
        for (int u = 0; u < 10; ++u) {
            if (t == s && u >= g) break;
            if (p1_item(t, u) >= 0 && p1_item(t, u) != 319) ++n;
        }
    return n;
}
constexpr int p1_pos(int code) {
    for (int i = 0; i < P1_PRE; ++i) if (p1_pre(i) == code) return i;
    for (int t = 0; t < 20; ++t)
        for (int u = 0; u < 10; ++u) if (p1_item(t, u) == code) return p1_cnt(t, u);
    return -1000;
}
constexpr int p1_cap(int s, int g) {
    int u = (s < 19 && g < 5) + (g == 9 && s < 19);
    if ((s & 1) && s < 19 && g >= 5 && g < 9) u += 1 + (g == 5);
    if ((s & 1) == 0 && g == 4) u += 1;
    const int it = p1_item(s, g), nt = g < 9 ? p1_item(s, g + 1) : -1;
    u += (it >= 0 && it < 500 && it != 208) + (nt >= 0 && nt < 200) + (it == 324 ? 2 : 0);
    if ((s == 18 && g == 1) || (s == 19 && g == 2) || (g == 9 && ((s & 3) == 2 || s == 17))) u += 2;
    return u < 2 ? 2 - u : 0;
}
constexpr int p1_slot(int s0, int s, int g, int gmax) {
    int n = 0;
    for (int t = s0; t <= s; ++t)
        for (int u = 0; u < gmax && !(t == s && u >= g); ++u) n += p1_cap(t, u);
    return n;
}
constexpr int p1_lim(int n) { return n > 63 ? 63 : n; }
template <int IMM> DI void gld4s(v4u& dst, unsigned voff, const void* sbase) { asm volatile("global_load_dwordx4 %0, %1, %2 offset:%3 sc1" : "=v"(dst) : "v"(voff), "s"(sbase), "i"(IMM)); }
template <int IMM> DI void gld2s(v2u& dst, unsigned voff, const void* sbase) { asm volatile("global_load_dwordx2 %0, %1, %2 offset:%3 sc1" : "=v"(dst) : "v"(voff), "s"(sbase), "i"(IMM)); }
template <class T> DI void pin(T& v) { asm volatile("" : "+v"(v)); }
DI void mul2(float& a, float& b, float x, float y, float s) { asm volatile("v_mul_f32 %0, %2, %4\n\tv_mul_f32 %1, %3, %4" : "=&v"(a), "=&v"(b) : "v"(x), "v"(y), "v"(s)); }
DI void xor2(float& a, float& b, unsigned x, unsigned y, unsigned s) { asm volatile("v_xor_b32 %0, %2, %4\n\tv_xor_b32 %1, %3, %4" : "=&v"(a), "=&v"(b) : "v"(x), "v"(y), "v"(s)); }
DI void rot2(unsigned& a, unsigned& b, unsigned x, unsigned y) { asm volatile("v_alignbit_b32 %0, %2, %2, 16\n\tv_alignbit_b32 %1, %3, %3, 16" : "=&v"(a), "=&v"(b) : "v"(x), "v"(y)); }
DI void mix2(unsigned& a, unsigned& b, unsigned x, unsigned y, unsigned z) { asm volatile("v_bfi_b32 %0, %5, %2, %3\n\tv_bfi_b32 %1, %5, %3, %4" : "=&v"(a), "=&v"(b) : "v"(x), "v"(y), "v"(z), "s"(0xffffu)); }
template <int I> DI void shr1(unsigned& o, unsigned prev, unsigned x) {
    if constexpr (I > 0) asm volatile("v_mov_b32_dpp %0, %1 wave_ror:1 row_mask:0xf bank_mask:0xf\n\tv_mov_b32_dpp %0, %2 wave_shr:1 row_mask:0xf bank_mask:0xf" : "=&v"(o) : "v"(prev), "v"(x));
    else asm volatile("v_mov_b32_dpp %0, %1 wave_shr:1 row_mask:0xf bank_mask:0xf bound_ctrl:0" : "=&v"(o) : "v"(x));
}
template <int G> DI void bc2(float& a, float& b, unsigned x) {
    asm volatile("v_mov_b32_dpp %0, %2 row_newbcast:%3 row_mask:0xf bank_mask:0xf\n\tv_mov_b32_dpp %1, %2 row_newbcast:%4 row_mask:0xf bank_mask:0xf" : "=&v"(a), "=&v"(b) : "v"(x), "i"(G), "i"(G + 8));
}
DI void rot1(unsigned& a, unsigned x) { asm volatile("v_alignbit_b32 %0, %1, %1, 16" : "=v"(a) : "v"(x)); }
DI void mix1(unsigned& a, unsigned x, unsigned y) { asm volatile("v_bfi_b32 %0, %3, %1, %2" : "=v"(a) : "v"(x), "v"(y), "s"(0xffffu)); }
DI void ror1(unsigned& o, unsigned prev) { asm volatile("v_mov_b32_dpp %0, %1 wave_ror:1 row_mask:0xf bank_mask:0xf" : "=v"(o) : "v"(prev)); }
DI void shr1b(unsigned& o, unsigned x) { asm volatile("v_mov_b32_dpp %0, %1 wave_shr:1 row_mask:0xf bank_mask:0xf" : "+v"(o) : "v"(x)); }
DI void xor1(float& a, unsigned x, unsigned s) { asm volatile("v_xor_b32 %0, %1, %2" : "=v"(a) : "v"(x), "v"(s)); }
template <int G> DI void bc1(float& a, unsigned x) { asm volatile("v_mov_b32_dpp %0, %1 row_newbcast:%2 row_mask:0xf bank_mask:0xf" : "=v"(a) : "v"(x), "i"(G)); }
template <int G> DI void mulb(float& o, unsigned x, float y) { asm volatile("v_mul_f32_dpp %0, %1, %2 row_newbcast:%3 row_mask:0xf bank_mask:0xf" : "=v"(o) : "v"(x), "v"(y), "i"(G)); }
template <int N> DI void lgkm() { __builtin_amdgcn_s_waitcnt(0xC07F | (N << 8)); asm volatile("" ::: "memory"); }
DI void unp2(float& a, float& b, unsigned x) { asm volatile("v_lshlrev_b32 %0, 16, %2\n\tv_and_b32 %1, 0xffff0000, %2" : "=&v"(a), "=&v"(b) : "v"(x)); }
#define GL(a) "global_load_dwordx4 a[" #a ":" #a "+3], %7, %8 sc1\n\tglobal_load_dwordx4 a[" #a "+4:" #a "+7], %7, %8 offset:256 sc1\n\t"
#define AR(d, a, o) "v_accvgpr_read_b32 " d ", a[" #a "+" #o "]\n\t"
#define GS_(a) "s_waitcnt vmcnt(4)\n\t" AR("%2", a, 1) AR("%3", a, 3) AR("%4", a, 5) AR("%5", a, 7) AR("%0", a, 0) AR("%1", a, 2)                              \
    "v_xor_b32 %2, %9, %2\n\tv_bitop3_b32 %4, %4, %9, %2 bitop3:0xbe\n\tv_bitop3_b32 %4, %3, %9, %4 bitop3:0xbe\n\tv_bitop3_b32 %4, %5, %9, %4 bitop3:0xbe\n\t" \
    AR("%2", a, 4) AR("%3", a, 6) "v_cmp_ne_u32 vcc, 0, %4\n\ts_cbranch_vccz 2f\n\t" GL(a)
template <int D0> DI void pipe_gran(float& a, float& d, unsigned off, const void* base, unsigned tag, bool& dead) {
    unsigned n = dead ? 0u : kSpinCap, y, z;
    float a1, d1;
    asm volatile("s_sleep %10\n\t" GL(32) "s_sleep %11\n\t" GL(40) "s_sleep %11\n\t" GL(48)
                 "1:\n\t" GS_(32) GS_(40) GS_(48) "s_sub_u32 %6, %6, 1\n\ts_cbranch_scc0 1b\n2:"
                 : "=&v"(a), "=&v"(d), "=&v"(a1), "=&v"(d1), "=&v"(y), "=&v"(z), "+s"(n) : "v"(off), "s"(base), "s"(tag), "i"(D0), "i"(PD1) : "memory", "vcc", "scc");
    dead = __builtin_amdgcn_readfirstlane(n) == ~0u;
    a += a1; d += d1;
}
DI void fma2(float& a, float& b, float x, float y, float s, float t, float u, float v) {
    asm volatile("v_fma_f32 %0, %2, %4, %6\n\tv_fma_f32 %1, %3, %5, %7" : "=&v"(a), "=&v"(b) : "v"(x), "v"(y), "v"(s), "v"(t), "v"(u), "v"(v));
}
DI void cvt2(unsigned& a, unsigned& b, f32x4 v) { asm volatile("v_cvt_pk_bf16_f32 %0, %2, %3\n\tv_cvt_pk_bf16_f32 %1, %4, %5" : "=&v"(a), "=&v"(b) : "v"(v.x), "v"(v.y), "v"(v.z), "v"(v.w)); }
DI void swp2n(unsigned& a, unsigned& b, unsigned& c, unsigned& d) { asm volatile("v_permlane16_swap_b32 %0, %1\n\tv_permlane16_swap_b32 %2, %3" : "+v"(a), "+v"(b), "+v"(c), "+v"(d)); }
DI void swp2(unsigned& a, unsigned& b, unsigned& c, unsigned& d) { asm volatile("s_nop 1\n\tv_permlane16_swap_b32 %0, %1\n\tv_permlane16_swap_b32 %2, %3" : "+v"(a), "+v"(b), "+v"(c), "+v"(d)); }
template <int G> DI void bc2b(float& a, float& b, float x, float y) {
    asm volatile("v_mov_b32_dpp %0, %2 row_newbcast:%4 row_mask:0xf bank_mask:0xf\n\tv_mov_b32_dpp %1, %3 row_newbcast:%4 row_mask:0xf bank_mask:0xf" : "=&v"(a), "=&v"(b) : "v"(x), "v"(y), "i"(G));
}
template <class T> DI void pins(T& v) { asm volatile("" : "+s"(v)); }
template <int I> DI void gld_w2(unsigned voff, const void* sbase) {
    asm volatile("global_load_dwordx4 " WA(2) ", %0, %1 offset:%3 sc1" ::"v"(voff), "s"(sbase), "i"(W2R + 4 * I), "i"((I % 5) * 1024 - 2048));
}
DI void gld2t(v2u& dst, unsigned voff, const void* sbase) { asm volatile("global_load_dwordx2 %0, %1, %2 sc1" : "+v"(dst) : "v"(voff), "s"(sbase)); }
DI void wait2(v2u& v, v4u (&x)[3]) { asm volatile("s_waitcnt vmcnt(0)" : "+v"(v), "+v"(x[0]), "+v"(x[1]), "+v"(x[2])::"memory"); }
template <int IMM> DI void gld4p(v4u& dst, unsigned voff, const void* sbase) { asm volatile("global_load_dwordx4 %0, %1, %2 offset:%3" : "+v"(dst) : "v"(voff), "s"(sbase), "i"(IMM)); }
DI void undef4(v4u (&x)[4]) { asm volatile("" : "=v"(x[0]), "=v"(x[1]), "=v"(x[2]), "=v"(x[3])); }
DI void wait1(v2u& v) { asm volatile("s_waitcnt vmcnt(0)" : "+v"(v)::"memory"); }
template <int N> DI void wait_fkn(v2u& v, v2u& u, unsigned& x) { asm volatile("s_waitcnt vmcnt(%3)" : "+v"(v), "+v"(u), "+v"(x) : "i"(N) : "memory"); }
DI void wait_fk(v2u& v, v2u& u, unsigned& x) { asm volatile("s_waitcnt vmcnt(0)" : "+v"(v), "+v"(u), "+v"(x)::"memory"); }
DI void ld_ur(unsigned o, const void* base) {
    asm volatile("global_load_dwordx4 a[40:40+3], %0, %1 sc1\n\tglobal_load_dwordx4 a[44:44+3], %0, %1 offset:1024 sc1\n\t"
                 "global_load_dwordx4 a[48:48+3], %0, %1 offset:2048 sc1\n\tglobal_load_dwordx4 a[52:52+3], %0, %1 offset:3072 sc1" ::"v"(o), "s"(base));
}
DI void st_ur(unsigned a) {
    asm volatile("s_waitcnt vmcnt(%1)\n\tds_write_b128 %0, a[40:40+3] offset:480\n\tds_write_b128 %0, a[44:44+3] offset:1504\n\t"
                 "ds_write_b128 %0, a[48:48+3] offset:2528\n\tds_write_b128 %0, a[52:52+3] offset:3552" ::"v"(a), "i"(2 * __builtin_popcount(kW2M)) : "memory");
}
DI void wait4(v4u (&x)[4]) { asm volatile("s_waitcnt vmcnt(0)" : "+v"(x[0]), "+v"(x[1]), "+v"(x[2]), "+v"(x[3])::"memory"); }
template <int A, int IMM> DI void lda4(unsigned o, const void* base) { asm volatile("global_load_dwordx4 a[%c2:%c2+3], %0, %1 offset:%3" ::"v"(o), "s"(base), "i"(A), "i"(IMM)); }
template <int A, int IMM> DI void lda2(unsigned o, const void* base) { asm volatile("global_load_dwordx2 a[%c2:%c2+1], %0, %1 offset:%3" ::"v"(o), "s"(base), "i"(A), "i"(IMM)); }
template <int A> DI void rda(unsigned& v) { asm volatile("v_accvgpr_read_b32 %0, a[%c1+0]" : "=v"(v) : "i"(A)); }
DI void poll1(unsigned a, const void* base) { asm volatile("global_load_dwordx2 a[36:36+1], %0, %1 sc1" ::"v"(a), "s"(base)); }
DI void poll2(unsigned a, unsigned b, const void* base) { asm volatile("global_load_dwordx2 a[36:36+1], %0, %2 sc1\n\tglobal_load_dwordx2 a[36+2:36+3], %1, %2 sc1" ::"v"(a), "v"(b), "s"(base)); }
DI void touch_(unsigned a, const void* base) { asm volatile("global_load_dword a[32+1], %0, %1" ::"v"(a), "s"(base)); }
template <int N> DI void wait_st(v2u& a, v4u& b) { asm volatile("s_waitcnt vmcnt(%2)" : "+v"(a), "+v"(b) : "i"(N) : "memory"); }
DI void undef3(v4u (&x)[3]) { asm volatile("" : "=v"(x[0]), "=v"(x[1]), "=v"(x[2])); }
template <int IMM> DI void gld4t(v4u& dst, unsigned voff, const void* sbase) { asm volatile("global_load_dwordx4 %0, %1, %2 offset:%3 sc1" : "+v"(dst) : "v"(voff), "s"(sbase), "i"(IMM)); }
template <int N> DI void vm_wait() { asm volatile("s_waitcnt vmcnt(%0)" ::"i"(N) : "memory"); }
DI float dot2a(unsigned a, unsigned b, float c) {
    return __builtin_amdgcn_fdot2_f32_bf16(__builtin_bit_cast(bf16x2v, a), __builtin_bit_cast(bf16x2v, b), c, false);
}
DI void disc(unsigned l0, unsigned l1, unsigned l2, f32x2& lam, f32x2& co) {
    const float dt = rbf(__builtin_amdgcn_exp2f(lo16f(l2) * 1.44269504088896f));
    const float lre = lo16f(l0), lim = lo16f(l1);
    const float e_ = __builtin_amdgcn_exp2f(dt * lre * 1.44269504088896f);
    const float rv = __builtin_amdgcn_fractf(dt * lim * 0.159154943091895f);
    lam = f32x2{e_ * __builtin_amdgcn_cosf(rv), e_ * __builtin_amdgcn_sinf(rv)};
    const float nr = lam.x - 1.0f, ni = lam.y, id = __builtin_amdgcn_rcpf(lre * lre + lim * lim);
    co = f32x2{(nr * lre + ni * lim) * id, (ni * lre - nr * lim) * id};
}
DI f32x4 unpk4(v2u u) { return f32x4{lo16f(u.x), hi16f(u.x), lo16f(u.y), hi16f(u.y)}; }
#define D2 "v_dot2_f32_bf16 "
#define CP "v_cvt_pk_bf16_f32 "
DI void d2x4(unsigned* o, unsigned a0, unsigned a1, unsigned a2, unsigned a3, unsigned b0, unsigned b1, unsigned b2, unsigned b3) {
    asm volatile(D2 "%0, %4, %8, 0\n\t" D2 "%1, %5, %9, 0\n\t" D2 "%2, %6, %10, 0\n\t" D2 "%3, %7, %11, 0"
                 : "=&v"(o[0]), "=&v"(o[1]), "=&v"(o[2]), "=&v"(o[3]) : "v"(a0), "v"(a1), "v"(a2), "v"(a3), "v"(b0), "v"(b1), "v"(b2), "v"(b3));
}
DI void cpx4(unsigned* o, unsigned a0, unsigned a1, unsigned a2, unsigned a3, unsigned b0, unsigned b1, unsigned b2, unsigned b3) {
    asm volatile(CP "%0, %4, %8\n\t" CP "%1, %5, %9\n\t" CP "%2, %6, %10\n\t" CP "%3, %7, %11"
                 : "=&v"(o[0]), "=&v"(o[1]), "=&v"(o[2]), "=&v"(o[3]) : "v"(a0), "v"(a1), "v"(a2), "v"(a3), "v"(b0), "v"(b1), "v"(b2), "v"(b3));
}
DI void cpx4i(unsigned* t, const unsigned* ka) {
    asm volatile(CP "%0, %0, %4\n\t" CP "%1, %1, %5\n\t" CP "%2, %2, %6\n\t" CP "%3, %3, %7"
                 : "+v"(t[0]), "+v"(t[1]), "+v"(t[2]), "+v"(t[3]) : "v"(ka[0]), "v"(ka[1]), "v"(ka[2]), "v"(ka[3]));
}
template <int IMM> DI void bld4(v4u& dst, unsigned voff, v4u rsrc) {
    if constexpr ((NTM >> 4) & 1) asm volatile("buffer_load_dwordx4 %0, %1, %2, 0 offen offset:%3 nt" : "=v"(dst) : "v"(voff), "s"(rsrc), "i"(IMM));
    else asm volatile("buffer_load_dwordx4 %0, %1, %2, 0 offen offset:%3" : "=v"(dst) : "v"(voff), "s"(rsrc), "i"(IMM));
}

__global__ __launch_bounds__(256, 1) __attribute__((aligned(4096))) void k_main(Params p) {
    __shared__ __attribute__((aligned(16))) char smem[LDS_BYTES];
    const rsrc_t rs = __builtin_amdgcn_make_buffer_rsrc((void*)p.ws, 0, WS_BYTES, 0x00020000);
    const rsrc_t ro = __builtin_amdgcn_make_buffer_rsrc((void*)p.out, 0, S_ * D_ * 4, 0x00020000);
    const int w0 = __builtin_amdgcn_readfirstlane(threadIdx.x >> 6), b0 = blockIdx.x;
    bool dead = false;
    asm volatile("" ::: "a255");
    float SW[5];
    f32x4 xin[5];
    v2u gt[5], dsk[5];
    f32x4 acc[2][5];
    f32x4 rr[2];
    float tt[2][5][4];
    float lr[5], ls[5], lqr[5], lqs[5];
    f32x2 l8, l32, l128;
    bool kearly;
    unsigned sdq;
    int sgn_, ab[4], bb, zpa, uwa, spa, tbw, tbr, cwa, spl, dst[3];
    unsigned sgo, sgs, glo, gso, fgo, fgo1;
    int srca;
    unsigned adm, dso, w2o, wro, fwo, fko, xso, fho1, fho, hso, eo, es, ho[4];
    bool cv0, three;
    int mt1_;
    auto addrs = [&](auto layer_, int lane, int w, int b) __attribute__((always_inline)) {
        constexpr int layer = layer_.value;
        const int c = lane & 15, q = lane >> 4;
        TILES()
        static_assert(l_sg(0, 0) == SC + 0xA900 && l_sg(0, 1) == SC + 0xB400 && l_sg(0, 2) == SC + 0xCE00 && l_sg(0, 3) == SC + 0xDC00, "");
        sgn_ = (layer ? l_sg(1, 0) + w * INBOX : SC + (int)((0xDC00CE00B400A900ull >> (16 * w)) & 0xffff)) + q * 320 + c * 4;
        const int pl = lane < 40 ? lane : 39, tbb = l_tb(layer, 0) + w * (layer ? INBOX : 5120);
        spl = SC + pl * 8;
        eo = OFF_EG + (unsigned)(layer * 163840 + nt1 * 640 + pl * 16);
        es = eo + (unsigned)(mt1 * 10240);
        spa = l_rs(layer, 0) + (2 * (mt2 & 1) + (w >> 1)) * (layer ? INBOX : 256) + (2 * (w & 1) + (c >> 3)) * 64 + (c & 7) * 4;
        tbw = tbb + c * 64 + ((q ^ ((c >> 1) & 3)) * 16);
        tbr = tbb + (2 * (c >> 2) + (q & 1)) * 64 + (((2 * (q >> 1) + ((c >> 1) & 1)) ^ (c >> 2)) * 16) + (c & 1) * 8;
        hso = OFF_HF + (unsigned)(layer * 5242880 + ((8 * mt1 + 2 * w + (q >> 1)) * 160 + 10 * nt1 + 5 * (q & 1)) * 256 + c * 16);
        cwa = L_CW + c * 4;
        fho1 = lane ? kNone : OFF_FH + (unsigned)((((layer * 32 + 2 * mt1 + (w >> 1)) * 4 + (nt1 >> 2)) * 8 + (nt1 & 3) * 2 + (w & 1)) * 128);
        fho = OFF_FH + (unsigned)(((layer * 32 + mt2) * 4 + w) * 1024 + (lane & 7) * 128);
        pin(sgn_); pin(spl); pin(eo); pin(es); pin(spa); pin(tbw); pin(tbr); pin(hso); pin(cwa); pin(fho1); pin(fho);
        const unsigned hb_ = OFF_HF + (unsigned)(layer * 5242880 + (4 * mt2 * 160 + 40 * w) * 256);
#pragma unroll
        for (int m = 0; m < 4; ++m) { ho[m] = hb_ + (unsigned)(((m + 1 + w) & 3) * 40960 + lane * 16); pin(ho[m]); }
#pragma unroll
        for (int m = 0; m < 3; ++m) { dst[m] = SC + ((w + 1 + m) & 3) * INBOX + m * 5120 + lane * 16; pin(dst[m]); }
        mt1_ = mt1;
        sgo = OFF_SG + (unsigned)(((layer * 32 + mt2) * 4 + w) * 2048 + c * 16);
        sgs = q < 2 ? sgo + (unsigned)(nt2 * 256 + q * 8) : kNone;
        sgo += (unsigned)(q * 512);
        pin(sgo); pin(sgs);
        if constexpr (layer == 0) {
            srca = SC + w * INBOX + lane * 16;
            glo = OFF_GT + (unsigned)((80 * nt2 + (lane >> 1)) * 4096 + (64 * mt2 + 16 * w) * 2 + (lane & 1) * 16);
            gso = cv0 ? OFF_GT + (unsigned)((80 * nt2 + i32 + 32 * w) * 4096 + (q & 1) * 512 + c * 32 + (q >> 1) * 16) : kNone;
            fgo = OFF_FG + (unsigned)((nt2 * 96 + (lane & 31)) * 128);
            fgo1 = w == 0 ? OFF_FG + (unsigned)((nt2 * 96 + i32) * 128) : kNone;
            pin(srca); pin(glo); pin(gso); pin(fgo); pin(fgo1);
            const int r0 = SC + (w < 3 ? w : 0) * REGSZ, y0 = 2031 + 8 * q - c, l34 = lane < 33 ? lane : 33;
#pragma unroll
            for (int i = 0; i < 4; ++i) { ab[i] = r0 + ((y0 & 1) ? CV_O : CV_E) + ((y0 + 1) >> 1) * 4 - 448 - 512 - 1024 * i; pin(ab[i]); }
            bb = r0 + 32 * c + 16 * q;
            zpa = w < 3 ? r0 + l34 * 16 + (l34 >= 30 ? CV_E + 4096 - 480 : 0) + (l34 >= 32 ? CV_O + 4100 - 32 - CV_E - 4096 : 0) : SC + 47360 + lane * 16;
            uwa = r0 + lane * 16;
            xso = (unsigned)((64 * mt2 + 16 * w + c) * D_ + 80 * nt2 + 16 * (q & 1) + 8 * (q >> 1)) * 2u;
            dso = (unsigned)(160 * nt2 + 8 * q);
            adm = (unsigned)((c & 1) * 16 + (q == 2 + (c >> 3) ? 256 << ((c & 7) >> 1) : 0));
            if (GF) pin(adm);
            pin(bb); pin(zpa); pin(uwa); pin(xso); if (DSE) pin(dso);
        }
        SCHED();
    };
    unsigned tag0, tag1;
#define FLAG(off) { if (lane == 0) st64(v2u{tag0, tag1}, rs, off); }
#define ISTAG(f) (((f.x ^ tag0) | (f.y ^ tag1)) == 0u)
#define KW_REQ() { if (cv) ld_ur(wro, p.ws + OFF_KVT); static_for<NW2P>(LAMBDA(i_) { gld_w2<i_.value>(w2o, p.ws + 2048 + (i_.value / 5) * 5120); }); }
#define FW_OK() { if (__builtin_expect(!kearly, 0)) { WAVE_WAIT({ const v2u fk_ = ld64(rs, fko); const v2u fw_ = ld64(rs, fwo); ok = ISTAG(fk_) && ISTAG(fw_); }) KW_REQ() } }
#define TT_GAP(n) { constexpr int mb_ = (n) / 10, nb_ = ((n) % 10) / 2, e_ = ((n) & 1) * 2; \
        mul2(tt[mb_][nb_][e_], tt[mb_][nb_][e_ + 1], rm_[4 * mb_ + e_], rm_[4 * mb_ + e_ + 1], SW[nb_]); }
    {
        const int tid = threadIdx.x, lane = tid & 63, w = w0, b = b0;
        const int c = lane & 15, q = lane >> 4;
        TILES()
        STAMP(0)
        const unsigned lds0 = (unsigned)(size_t)(LAS char*)smem;
        v2u nonce;
        unsigned cpf0;
        {
            u64 pc;
            int len, len2;
            asm volatile("s_getpc_b64 %0\nkpf_begin:\n\ts_mov_b32 %1, kpf_end-kpf_begin\n\ts_mov_b32 %2, kpf_end2-kpf_begin" : "=s"(pc), "=s"(len), "=s"(len2));
            len = len > len2 ? len : len2;
            const int o0 = (((j & 15) * 4 + w) + 64 * (lane & 7)) * 128;
            asm volatile("global_load_dword %0, %1, %2" : "=v"(cpf0) : "v"(o0 < len ? o0 : 0), "s"(pc));
            unsigned zo = 0u;
            asm volatile("global_load_dwordx2 %0, %1, %2 sc1" : "=v"(nonce) : "v"(zo), "s"(p.ws + OFF_NONCE));
        }
        const bool cv = w < 2 || (w == 2 && i32 < 16);
        const int chn = 80 * nt2 + i32 + 32 * (cv ? w : 0);
        const bool kw = KVS ? w < 2 || (w == 2 && i32 >= 16) : cv;
        const int kp = w == 2 ? 256 + 8 * nt2 + ((i32 & 15) >> 1) : 32 * nt2 + i32, kh_ = w == 2 ? i32 & 1 : w & 1;
        const int kc = KVU ? kp % 10 : (80 * nt2 + (w == 2 ? 64 + (i32 & 15) : i32 + 32 * (w & 1))) % 10, kr = KVU ? 2 * (kp / 10) + kh_ : (80 * nt2 + (w == 2 ? 64 + (i32 & 15) : i32 + 32 * (w & 1))) / 10;
        const int lk = kw ? lane : 8, lc = cv ? lane : 8;
        v4u ax[15], sa[5], cr[2], wr[4];
        unsigned uL[3];
        const int cu_ = (b < 200 ? b : 0) * 4 + w, cdb = cu_ / 20, ccg = cu_ % 20;
        {
            int ca_ = 32 * ccg, cb_ = 40 * nt1 + 10 * w - 32;
            asm volatile("" : "+s"(ca_), "+s"(cb_));
            const unsigned cpo = (unsigned)((lane < 32 ? ca_ : cb_) + (lane < 41 ? lane : 41)) * 2u;
            gldu16(uL[0], cpo, p.Lre); gldu16(uL[1], cpo, p.Lim); gldu16(uL[2], cpo, p.logdt);
        }
        const char* ssrc;
        {
            u64 pa = (u64)(p.x0 + (128 * mt1 + 8 * nt1 + 2 * w) * D_), pb = (u64)(((mt1 & 1) ? p.Bim : p.Bre) + (40 * nt1 + (mt1 >> 1)) * D_);
            asm volatile("" : "+s"(pa), "+s"(pb));
            const int nbx = 2 * w + q - 2, r = q < 2 ? q : 8 * (nbx < 5 ? nbx : 4);
            ssrc = (const char*)(q < 2 ? pa : pb) + (unsigned)(r * 1280 + c * 16);
            pin(ssrc);
        }
        {
            const unsigned kso = (unsigned)(64 * kc + 8 * (lk & 7)) * 2u, ko = (unsigned)(32 * kr + 4 * (lk >> 3)) * 1280u + kso, kh = ko - 3840u;
            const v4u rk = {(unsigned)(u64)p.k, (unsigned)((u64)p.k >> 32) & 0xffffu, S_ * D_ * 2u, 0x00020000u};
            static_for<4>(LAMBDA(i) { gld<i.value * 16>(ax[11 + i.value], kso * 4u, p.shk); });
            static_for<3>(LAMBDA(i) { bld4<i.value * 1280>(ax[i.value], kh, rk); });
            static_for<4>(LAMBDA(i) { gldn<i.value * 1280, 3>(ax[3 + i.value], ko, p.k); });
            static_for<4>(LAMBDA(i) { gldn<i.value * 1280, 0>(ax[7 + i.value], ko, p.v); });
        }
        if (SAF == 0) static_for<5>(LAMBDA(i) { gld64<256 * i.value>(sa[i.value], ssrc); });
        SCHED();
        if (w == TW && !kw) tour<0>();
        STAMP(22)
        const int row8 = lane >> 3;
        const unsigned wvo = (unsigned)(40 * nt1 + row8) * 1280u + (unsigned)(((lane & 7) ^ ((w & 1) | ((row8 >> 1) << 1))) * 16 + (w >> 1) * 128);
        const char* const wbase = (const char*)((w & 1) ? p.Bim : p.Bre);
        const unsigned wl = lds0 + (unsigned)(((w & 1) * 10 + (w >> 1)) * 1024);
        const unsigned xvo0 = (unsigned)(8 * (row8 >> 1) + 2 * (row8 & 1)) * 1280u + (unsigned)(((lane & 7) ^ ((row8 >> 1) << 1)) * 16);
        const unsigned xvo1 = (xvo0 ^ 16u) + 1280u;
        const char* const xbase = (const char*)p.x0 + (size_t)(128 * mt1 + 32 * w) * 1280;
        const unsigned xl = lds0 + (unsigned)(w * REGSZ);
        unsigned xv[4] = {xvo0, xvo1, xvo0 + 5120u, xvo1 + 5120u}, wv[5];
#pragma unroll
        for (int i = 0; i < 5; ++i) { wv[i] = wvo + i * 10240u; pin(wv[i]); if (i < 4) pin(xv[i]); }
#define DMA_M0(it) SETM0<dma_loff(it) - dma_goff(it)>((it) < 40 ? xl : wl)
#define DMA_LD1(it, nx_) GLD1<dma_goff(it), dma_loff(nx_) - dma_goff(nx_)>((it) < 40 ? xv[(it) & 3] : wv[(it) < 40 ? 0 : ((it) - 100) % 5], (it) < 40 ? xbase : wbase, (nx_) < 40 ? xl : wl)
#define DMA_LD(it) GLD0<dma_goff(it)>((it) < 40 ? xv[(it) & 3] : wv[(it) < 40 ? 0 : ((it) - 100) % 5], (it) < 40 ? xbase : wbase)
        const int swz = (c & 1) | ((c >> 2) << 1);
        int w1lane0 = (c & 1) * 10240 + (c >> 1) * 128 + ((q ^ swz) * 16);
        int w1lane1 = (c & 1) * 10240 + (c >> 1) * 128 + (((4 + q) ^ swz) * 16);
        int xlane0 = L_XR + w * REGSZ + (c & 1) * 1024 + (c >> 1) * 128 + ((q ^ swz) * 16);
        int xlane1 = L_XR + w * REGSZ + (c & 1) * 1024 + (c >> 1) * 128 + (((4 + q) ^ swz) * 16);
        unsigned cvo = (unsigned)((16 * cdb + c) * D_ + 32 * ccg + 8 * q) * 2u, for0, for1;
        {
            const int G = 4 * ccg + q, ch = 10 * (G / 5) + G % 5, ci = ch + 5;
            const unsigned fo = OFF_W2F + (unsigned)(((cdb / 5) * 200 + cdb % 5) * 1024 + c * 16);
            for0 = fo + (unsigned)((ch >> 2) * 5120 + (ch & 3) * 256); for1 = fo + (unsigned)((ci >> 2) * 5120 + (ci & 3) * 256);
        }
        unsigned sto = OFF_ST + (unsigned)((mt1 * 16 + 4 * w + q) * 256 + c * 8), swo = OFF_ST + (unsigned)(((lane >> 2) * 16 + nt1) * 256 + 128 + (lane & 3) * 16);
        unsigned kvo = OFF_KVT + (unsigned)((64 * kc + 8 * (lane >> 3) + ((lane >> 2) & 1)) * 4096 + (32 * kr + 8 * (lane & 3)) * 2);
        int kbw = L_KB + w * 1024 + (lane & 7) * 128 + (((lane >> 3) * 8) ^ ((lane & 7) * 16));
        int kbr = L_KB + w * 1024 + (lane >> 3) * 128 + (((lane & 7) * 16) ^ ((lane >> 3) * 16));
        const unsigned dum = (OOB & 1) ? kNone : OFF_DUM + (unsigned)((b * 4 + w) * 128 + (lane & 7) * 16);
        unsigned fko1 = OFF_FK + (unsigned)((10 * kr + kc) * 128);
        if (!kw) kvo = dum;
        if (b >= 200) { for0 = dum; for1 = dum; cvo = (unsigned)((b * 4 + w) * 128); }
        wro = (unsigned)(chn * 4096 + lc * 16);
        cv0 = cv; three = i32 < 16;
        pin(kvo); pin(kbw); pin(kbr); pin(fko1);
        int w1hi0 = w1lane0 + 61440, w1hi1 = w1lane1 + 61440;
        pin(w1hi0); pin(w1hi1);
        pin(w1lane0); pin(w1lane1); pin(xlane0); pin(xlane1); pin(cvo); pin(for0); pin(for1); pin(sto); pin(swo); pin(wro);
        SCHED();
        auto pre = LAMBDA(i_) {
            constexpr int it = p1_pre(i_.value);
            SCHED();
            DMA_M0(it);
            asm volatile("s_nop 0");
            DMA_LD(it);
            SCHED();
        };
        fko = OFF_FK + (unsigned)(((cv ? lane : 0) * 10 + (chn >> 6)) * 128);
        fwo = OFF_FW + (unsigned)(((5 * nt2 + (lane < 25 ? lane / 5 : 0)) * 20 + 5 * w + (lane < 25 ? lane % 5 : 0)) * 128), w2o = OFF_W2F + (unsigned)((nt2 * 40 + 10 * w) * 5120 + lane * 16);
        int ime = SC + (w < 3 ? w * REGSZ + CV_E : 43264) + 1008 - 16 * lane, imo = SC + (w < 3 ? w * REGSZ + CV_O + 4 : 47360) + 1008 - 16 * lane, im3 = w < 3 ? ime + 3072 : imo;
        int tca = L_TAB + (c >> 1) * 136, tcb = tca + 8 + 32 * w + 8 * q, tcl = L_TAB + (lane < 40 ? lane : 39) * 136, coa = L_CO + w * REGSZ + q * 64, rsa = l_rs(0, 0) + w * 256 + lane * 4;
        unsigned sgn = (c & 1) ? 0u : 0x80000000u;
        fko1 = kw && lane < 32 ? fko1 : OFF_FW + (unsigned)((b * 4 + w) * 128);
        pin(fko); pin(fwo); pin(w2o); pin(fko1); pin(ime); pin(imo); pin(im3); pin(tca); pin(tcb); pin(tcl); pin(coa); pin(rsa); pin(sgn);
        SCHED();
        auto cw_ = [&]() __attribute__((always_inline)) {
            gldn<0, 2>(cr[0], cvo, p.Cre); gldn<0, 2>(cr[1], cvo, p.Cim);
            static_for<4>(LAMBDA(i) { gldn<i.value * 1024, 1>(wr[i.value], wro, p.ssmk); });
        };
        if (SAF == 1) static_for<5>(LAMBDA(i) { gld64<256 * i.value>(sa[i.value], ssrc); });
        if (CWP == 0) cw_();
        STAMP(33)
        asm volatile("s_waitcnt vmcnt(%5)" : "+v"(uL[0]), "+v"(uL[1]), "+v"(uL[2]), "+v"(nonce), "+v"(cpf0) : "i"(15 + (SAF < 2 ? 5 : 0) + (CWP ? 0 : 6)) : "memory");
        {
            f32x2 lam, co, lp[16];
            disc(uL[0], uL[1], uL[2], lam, co);
            if (lane < 32) *(f32x2*)(smem + L_CO + w * REGSZ + lane * 8) = 2.0f * co;
            lp[0] = cmul(lam, lam); lp[0] = cmul(lp[0], lp[0]); lp[1] = cmul(lp[0], lp[0]);
            lp[0] = f32x2{1.f, 0.f};
            lp[2] = cmul(lp[1], lp[1]); lp[3] = cmul(lp[2], lp[1]); lp[4] = cmul(lp[2], lp[2]);
            const f32x2 z2 = {0.f, 0.f};
            f32x2 t4[4];
            cfma4(t4, lp[4], lp[1], lp[2], lp[3], lp[4], z2, z2, z2, z2);
            lp[5] = t4[0]; lp[6] = t4[1]; lp[7] = t4[2]; lp[8] = t4[3];
            cfma4(t4, lp[8], lp[1], lp[2], lp[3], lp[4], z2, z2, z2, z2);
            lp[9] = t4[0]; lp[10] = t4[1]; lp[11] = t4[2]; lp[12] = t4[3];
            cfma4(t4, lp[8], lp[5], lp[6], lp[7], lp[7], z2, z2, z2, z2);
            lp[13] = t4[0]; lp[14] = t4[1]; lp[15] = t4[2];
            if (lane >= 32 && lane < 42) {
                f32x2* tl = (f32x2*)(smem + L_TAB + (10 * w + lane - 32) * 136);
                tl[0] = lam;
#pragma unroll
                for (int i = 0; i < 16; ++i) tl[1 + i] = lp[i];
            }
        }
        SCHED();
        STAMP(35)
        if (SAF == 2) static_for<5>(LAMBDA(i) { gld64<256 * i.value>(sa[i.value], ssrc); });
        if (CWP) cw_();
        if (APOS == 0) addrs(std::integral_constant<int, 0>{}, lane, w, b);
        v4u kvt[4];
        unsigned ka[8], kt[8], se[8], vs[8], ko4[4], one2 = 0x3F803F80u, kq[12];
        float kf_[8];
        pin(one2);
        auto kvf = LAMBDA(n_) {
            constexpr int n = n_.value, h = n / 5, u = n % 5;
            if constexpr (u == 0) {
#pragma unroll
                for (int r = 0; r < 6; ++r) { kq[r] = __builtin_amdgcn_perm(ax[r][h], ax[r + 1][h], 0x05040100u); kq[6 + r] = __builtin_amdgcn_perm(ax[r][h], ax[r + 1][h], 0x07060302u); }
                if constexpr (h > 0) kvt[h - 1] = *(const v4u*)(smem + kbr);
            } else if constexpr (u == 1) {
                unsigned t[8];
                d2x4(t, kq[0], kq[1], kq[2], kq[3], ax[11 + h][1], ax[11 + h][1], ax[11 + h][1], ax[11 + h][1]);
                d2x4(t + 4, kq[6], kq[7], kq[8], kq[9], ax[11 + h][3], ax[11 + h][3], ax[11 + h][3], ax[11 + h][3]);
#pragma unroll
                for (int r = 0; r < 8; ++r) kf_[r] = __uint_as_float(t[r]);
            } else if constexpr (u == 2) {
#pragma unroll
                for (int r = 0; r < 8; ++r) kf_[r] = dot2a(kq[r + 2 * (r >> 2) + 2], ax[11 + h][2 * (r >> 2)], kf_[r]);
            } else if constexpr (u == 3) {
#pragma unroll
                for (int r = 0; r < 4; ++r) { kq[r] = ax[7 + r][h] << 16; kq[4 + r] = ax[7 + r][h] & 0xffff0000u; pin(kq[r]); pin(kq[4 + r]); }
            } else {
#pragma unroll
                for (int r = 0; r < 8; ++r) kf_[r] *= __uint_as_float(kq[r]);
                *(v2u*)(smem + kbw) = v2u{cvtpk(kf_[0], kf_[1]), cvtpk(kf_[2], kf_[3])};
                *(v2u*)(smem + (kbw ^ 64)) = v2u{cvtpk(kf_[4], kf_[5]), cvtpk(kf_[6], kf_[7])};
                if constexpr (h == 3) kvt[3] = *(const v4u*)(smem + kbr);
            }
            SCHED();
        };
        auto kvop = LAMBDA(n_) {
            constexpr int n = n_.value, h = n ? (n - 1) / 29 : -1, u = n ? (n - 1) % 29 : 26;
            if constexpr (u < 2) d2x4(ka + 4 * u, ax[0][h], ax[1][h], ax[2][h], ax[3][h], se[u], se[u], se[u], se[u]);
            else if constexpr (u == 8) { const v4u sk = ax[11 + h]; se[4] = sk[0] >> 16; se[5] = sk[2] & 0xffff0000u; se[6] = sk[0] & 0xffffu; se[7] = sk[2] << 16; pin(se[4]); pin(se[5]); pin(se[6]); pin(se[7]); }
            else if constexpr (u < 21) {
                constexpr int v = u < 8 ? u - 2 : u - 3, tp = 1 + v / 6, st = v % 6, e = st & 1;
                if constexpr (st < 2) d2x4(kt + 4 * e, ax[tp][h], ax[tp + 1][h], ax[tp + 2][h], ax[tp + 3][h], se[2 * tp + e], se[2 * tp + e], se[2 * tp + e], se[2 * tp + e]);
                else if constexpr (st < 4) cpx4i(kt + 4 * e, ka + 4 * e);
                else d2x4(ka + 4 * e, kt[4 * e], kt[4 * e + 1], kt[4 * e + 2], kt[4 * e + 3], one2, one2, one2, one2);
            }
            else if constexpr (u == 21) { vs[0] = ax[7][h] & 0xffffu; vs[1] = ax[8][h] << 16; vs[2] = ax[9][h] & 0xffffu; vs[3] = ax[10][h] << 16; pin(vs[0]); pin(vs[1]); pin(vs[2]); pin(vs[3]); }
            else if constexpr (u == 22) { vs[4] = ax[7][h] >> 16; vs[5] = ax[8][h] & 0xffff0000u; vs[6] = ax[9][h] >> 16; vs[7] = ax[10][h] & 0xffff0000u; pin(vs[4]); pin(vs[5]); pin(vs[6]); pin(vs[7]); }
            else if constexpr (u == 23) cpx4(ko4, ka[0], ka[2], ka[4], ka[6], ka[1], ka[3], ka[5], ka[7]);
            else if constexpr (u < 26) { constexpr int e = u - 24; d2x4(kt + 4 * e, ko4[2 * e], ko4[2 * e], ko4[2 * e + 1], ko4[2 * e + 1], vs[4 * e], vs[4 * e + 1], vs[4 * e + 2], vs[4 * e + 3]); }
            else if constexpr (u == 26) {
                if constexpr (h < 3) { const v4u sk = ax[12 + h]; se[0] = sk[1] >> 16; se[1] = sk[3] & 0xffff0000u; se[2] = sk[1] & 0xffffu; se[3] = sk[3] << 16; pin(se[0]); pin(se[1]); pin(se[2]); pin(se[3]); }
                else asm volatile("s_nop 2");
            }
            else if constexpr (u == 27) {
                cpx4(ko4, kt[0], kt[2], kt[4], kt[6], kt[1], kt[3], kt[5], kt[7]);
                *(v2u*)(smem + kbw) = v2u{ko4[0], ko4[1]};
                *(v2u*)(smem + (kbw ^ 64)) = v2u{ko4[2], ko4[3]};
            }
            else kvt[h] = *(const v4u*)(smem + kbr);
            SCHED();
        };
        asm volatile("s_waitcnt vmcnt(%15)" : "+v"(ax[0]), "+v"(ax[1]), "+v"(ax[2]), "+v"(ax[3]), "+v"(ax[4]), "+v"(ax[5]), "+v"(ax[6]), "+v"(ax[7]), "+v"(ax[8]), "+v"(ax[9]),
                     "+v"(ax[10]), "+v"(ax[11]), "+v"(ax[12]), "+v"(ax[13]), "+v"(ax[14]) : "i"(11) : "memory");
        STAMP(30)
        unsigned tch = 0u;
        constexpr int NF = KVF ? KNF : 27;
        if (kw) {
            if constexpr (KVF) {
                static_for<20>(LAMBDA(n_) {
                    constexpr int n = n_.value, DN = 20 - KFD0;
                    site<1000 + n>(); SCHED();
                    kvf(n_);
                    if constexpr (n >= KFD0) static_for<(n - KFD0 + 1) * NF / (DN ? DN : 1) - (n - KFD0) * NF / (DN ? DN : 1)>(LAMBDA(i_) { pre(std::integral_constant<int, (n - KFD0) * NF / (DN ? DN : 1) + i_.value>{}); });
                });
                if constexpr (KFD0 == 20) static_for<NF>(pre);
            } else
            static_for<117>(LAMBDA(n_) {
                constexpr int n = n_.value;
                site<1000 + n>(); SCHED();
                kvop(n_);
                if constexpr (n >= kD0 && (n - kD0) % kDS == 0 && (n - kD0) / kDS < 27) { pre(std::integral_constant<int, ((n - kD0) / kDS + 270) % 27>{}); SCHED(); }
            });
        } else {
            if (w == 3) {
            const int l = lane, l3 = l & 3;
            unsigned a = OFF_SG + (unsigned)((((((l >> 3) & 1) * 32 + mt2) * 4 + ((l >> 1) & 3)) * 8 + nt2) * 256 + (l & 1) * 128);
            a = l >= 16 ? OFF_EG + (unsigned)((l >= 21 ? 163840 - 640 : 0) + (mt1 * 16 + nt1) * 640 + (l < 26 ? l - 16 : 5) * 128) : a;
            a = l >= 28 ? OFF_PS + (unsigned)(((mt2 * 4 + l3) * 8 + nt2) * 128) : a;
            a = l >= 32 ? OFF_FH + (unsigned)((((((l >> 2) & 1) * 32 + 2 * mt1 + (l3 >> 1)) * 4 + (nt1 >> 2)) * 8 + (nt1 & 3) * 2 + (l3 & 1)) * 128) : a;
            a = l >= 40 ? OFF_FX + (unsigned)(((mt1 * 4 + 2 * (mt2 & 1) + (l3 >> 1)) * 16 + nt2 * 2 + (l3 & 1)) * 128) : a;
            a = l >= 48 ? OFF_FG + (unsigned)((nt2 * 96 + i32) * 128) : a;
            const unsigned big = (l & 7) < 2 ? OFF_HF + (unsigned)((l & 1) * 5242880 + (8 * mt1 * 160 + 10 * nt1) * 256) : (l & 7) == 2 ? OFF_X2F + (unsigned)(2 * mt2 * 40960 + 2560 * nt2)
                               : (l & 7) < 5 ? OFF_KVT + (unsigned)((l & 1) * 2621440 + (80 * nt2 + i32) * 4096) : OFF_W2F + (unsigned)(nt2 * 204800 + ((l & 7) - 5) * 98304);
            a = l >= 56 ? big : a;
            tch = a;
        }
            vm_wait<KNW>();
            static_for<NF>(LAMBDA(i_) { if constexpr (i_.value == T1POS) { if (w == TW) tour<1>(); } pre(i_); });
            if (T1POS == 27 && w == TW) tour<1>();
            skip<SKIP0>();
            undef4(kvt);
        }
        tag0 = __builtin_amdgcn_readfirstlane(nonce.x) + 1u; tag1 = __builtin_amdgcn_readfirstlane(nonce.y) + (tag0 == 0u ? 1u : 0u);
        if constexpr (NF == 27) {
#pragma unroll
            for (int i = 0; i < 4; ++i) st128(kvt[i], rs, kvo + i * 8192);
        }
        SCHED();
        STAMP(34)
        if (APOS == 1) addrs(std::integral_constant<int, 0>{}, lane, w, b);
        v4u orr, oi;
        v2u gr;
        unsigned gro;
        {
            asm volatile("s_waitcnt vmcnt(%7)" : "+v"(cr[0]), "+v"(cr[1]), "+v"(sa[0]), "+v"(sa[1]), "+v"(sa[2]), "+v"(sa[3]), "+v"(sa[4]) : "i"(NF == 27 ? 35 : 4 + NF) : "memory");
#pragma unroll
            for (int e = 0; e < 4; ++e) {
                const f32x4 co = *(const f32x4*)(smem + coa + e * 16);
                const f32x2 o0 = lo16f(cr[0][e]) * f32x2{co.x, -co.y} - lo16f(cr[1][e]) * f32x2{co.y, co.x};
                const f32x2 o1 = hi16f(cr[0][e]) * f32x2{co.z, -co.w} - hi16f(cr[1][e]) * f32x2{co.w, co.z};
                orr[e] = cvtpk(o0.x, o1.x); oi[e] = cvtpk(o0.y, o1.y);
            }
            if constexpr (NF == 27) { st128(orr, rs, for0); st128(oi, rs, for1); } else { pin(orr); pin(oi); }
            SCHED();
        }
        static_for<(27 - NF) / 3>(LAMBDA(i_) { pre(std::integral_constant<int, NF + i_.value>{}); });
        {
            float s1[2] = {0.f, 0.f}, s2[2] = {0.f, 0.f};
#pragma unroll
            for (int i = 0; i < 5; ++i)
#pragma unroll
                for (int e = 0; e < 4; ++e) { s1[e & 1] = dot2a(sa[i][e], one2, s1[e & 1]); s2[e & 1] = dot2a(sa[i][e], sa[i][e], s2[e & 1]); }
            const float a1 = rowsum16(s1[0] + s1[1]), a2 = rowsum16(s2[0] + s2[1]);
            const float mean = a1 * (1.0f / D_);
            const float var = fmaxf(a2 * (1.0f / D_) - mean * mean, 0.f);
            const float ri = __builtin_amdgcn_rcpf(rbf(__builtin_amdgcn_sqrtf(rbf(rbf(var) + 1e-5f))));
            gr = v2u{__float_as_uint(q >= 2 ? a1 : c ? -ri * rbf(mean) : ri), tag0};
            gro = c < 2 ? OFF_ST + (unsigned)((mt1 * 16 + nt1) * 256 + ((q < 2 ? 8 * c : 14) + 2 * w + q) * 8) : kNone;
            pin(gr); pin(gro);
        }
        SCHED();
        STAMP(59)
        if (APOS == 2) addrs(std::integral_constant<int, 0>{}, lane, w, b);
        if constexpr (NF < 27) {
            static_for<27 - NF - (27 - NF) / 3>(LAMBDA(i_) { pre(std::integral_constant<int, NF + (27 - NF) / 3 + i_.value>{}); });
#pragma unroll
            for (int i = 0; i < 4; ++i) st128(kvt[i], rs, kvo + i * 8192);
            st128(orr, rs, for0); st128(oi, rs, for1);
        }
        st64(gr, rs, gro);
        SCHED();
        STAMP(23)
        if (w == 3) tour<7>();
        asm volatile("s_waitcnt vmcnt(%4) lgkmcnt(0)" : "+v"(wr[0]), "+v"(wr[1]), "+v"(wr[2]), "+v"(wr[3]) : "i"(P1_PRE - 9) : "memory");
        __builtin_amdgcn_s_barrier();
        asm volatile("" ::: "memory");
        STAMP(24)
        v4u wf[2][5], af[2][2][2];
#pragma unroll
        for (int nb = 0; nb < 5; ++nb) wf[0][nb] = *(const v4u*)(smem + w1lane0 + nb * 20480 + (0) * 2048);
#pragma unroll
        for (int i = 0; i < 4; ++i) af[0][i >> 1][i & 1] = *(const v4u*)(smem + ((i >> 1) ? xlane1 : xlane0) + (i & 1) * 2048);
        SCHED();
        const char *kvtb = p.ws + OFF_KVT, *w2b0 = p.ws + 2048;
        pins(kvtb); pins(w2b0);
        v2u sg, tc[10], fk, fw;
        v4u sw0;
        unsigned ie[4][4], io[4][4], iw, zq[4];
        float rr_[8];
        auto kstep = LAMBDA(s_) {
            constexpr int s = s_.value, ph = s >> 2, s4 = s & 3, cu = s & 1, nx = cu ^ 1, jj = s >> 1, pp = jj & 1;
            if constexpr (s4 == 3 && ph < 4) {
                constexpr int pw = p1_pos(104 + 5 * (ph + 1)), px = p1_pos(4 * (jj + 1) + 3), pm = pw > px ? pw : px;
                constexpr int vm = p1_lim(p1_cnt(s, 0) - 1 - pm);
                asm volatile("" ::: "memory");
                __builtin_amdgcn_s_waitcnt((vm & 15) | ((vm >> 4) << 14) | 0x70);
                __builtin_amdgcn_s_barrier();
                asm volatile("" ::: "memory");
                STAMP(25 + ph)
                SCHED();
            }
            if constexpr (s == 15) {
                wait_st<p1_cnt(s, 0) - 1 - p1_pos(203)>(sg, sw0);
                WAVE_RETRY((((sg.y ^ tag0) | (sw0.y ^ tag0) | (sw0.w ^ tag0)) == 0u), sg = ld64(rs, sto); sw0 = ld128(rs, swo))
                STAMP(7)
            }
            if constexpr (s == kW2S) { vm_wait<p1_cnt(s, 0) - 1 - p1_pos(208)>(); SCHED(); }
            if constexpr (s == 18) {
                wait_fkn<p1_cnt(s, 0) - 1 - p1_pos(206)>(fk, fw, sdq);
                kearly = __builtin_amdgcn_ballot_w64(!(ISTAG(fk) && ISTAG(fw))) == 0ull;
                ORDER();
                if (kearly && cv) ld_ur(wro, kvtb);
                SCHED();
            }
            if constexpr (s4 != 3 || ph == 4) lgkm<(cu == 0 && s > 0 ? 3 : s == 17 ? 4 : s == 19 ? 2 : 0)>();
            SCHED();
            static_for<10>(LAMBDA(g_) {
                constexpr int g = g_.value, mb = g / 5, k = g % 5, it = p1_item(s, g), nt_ = g < 9 ? p1_item(s, g + 1) : -1;
                constexpr bool d0 = it >= 0 && it < 200, d1 = nt_ >= 0 && nt_ < 200;
                if constexpr (s == 0) mf0(acc[mb][k], af[pp][cu][mb], wf[cu][k]); else mf(acc[mb][k], af[pp][cu][mb], wf[cu][k]);
                SCHED();
                if constexpr (s < 19) {
                    if constexpr (g < 5) wf[nx][g] = *(const v4u*)(smem + (g < 3 ? (nx ? w1lane1 : w1lane0) : (nx ? w1hi1 : w1hi0)) + (g < 3 ? g : g - 3) * 20480 + ((s + 1) >> 1) * 1024);
                    if constexpr (cu == 1 && g == 5) vm_wait<p1_lim(p1_cnt(s, 5) - 1 - p1_pos(4 * (jj + 1) + 3))>();
                    if constexpr (cu == 1 && g >= 5 && g < 9)
                        af[pp ^ 1][(g - 5) >> 1][(g - 5) & 1] = *(const v4u*)(smem + (((g - 5) >> 1) ? xlane1 : xlane0) + ((jj + 1) % 3) * 4096 + ((g - 5) & 1) * 2048);
                }
                if constexpr (cu == 0 && g == 4 && s > 0) lgkm<(s == 8 || s == 10 || s == 14 ? 8 : 5)>();
                static_for<p1_cap(s, g)>(LAMBDA(e_) {
                    constexpr int na = p1_slot(0, s, g, 10) + e_.value, i = (na / 10) & 3, u = na % 10;
                    static_assert(p1_slot(0, 8, 0, 10) >= 40, "");
                    if constexpr (na < 40) {
                        if constexpr (u == 0) { if constexpr (i > 0) ror1(iw, wr[i - 1].w); else { iw = 0u; pin(iw); } }
                        else if constexpr (u < 5) rot1(ie[i][u - 1], wr[i][4 - u]);
                        else if constexpr (u == 5) shr1b(iw, wr[i].w);
                        else if constexpr (u < 9) mix1(io[i][u - 6], wr[i][9 - u], wr[i][8 - u]);
                        else mix1(io[i][3], wr[i].x, iw);
                    }
                    constexpr int nb = p1_slot(8, s, g, 3) + e_.value;
                    if constexpr (s >= 8 && s < 12 && g < 3) {
                        if constexpr (nb < 10) tc[nb] = *(const v2u*)(smem + (nb < 5 ? tca : tcb) + (nb % 5) * 1088);
                        else if constexpr (nb == 10) { const f32x2* tp = (const f32x2*)(smem + tcl); l8 = tp[2]; l32 = tp[5]; }
                        else if constexpr (nb == 11) l128 = *(const f32x2*)(smem + tcl + 72);
                        static_assert(p1_slot(8, 12, 0, 3) == 12, "");
                    }
                    constexpr int nc = p1_slot(12, s, g, 10) + e_.value;
                    if constexpr (s >= 12 && s < 14 && nc < 10) {
                        if constexpr (nc < 5) { lr[nc] = __uint_as_float(tc[nc].x); xor1(ls[nc], tc[nc].y, sgn); }
                        else { lqr[nc - 5] = __uint_as_float(tc[nc].x); xor1(lqs[nc - 5], tc[nc].y, sgn); }
                        static_assert(p1_slot(12, 14, 0, 10) >= 10, "");
                    }
                    if constexpr (s == 15 && g < 5) SW[g] = __int_as_float(__builtin_amdgcn_ds_bpermute(16 * c + 4 * (g >> 1), (g & 1) ? sw0.z : sw0.x));
                    constexpr bool img = (s == 16 && g >= 5 && g < 9 && e_.value == 0) || (s == 18 && (g == 5 || g == 6) && e_.value == 0);
                    constexpr int nd = p1_slot(15, s, g, 10) + e_.value - 5 - (s > 16 ? 4 : s == 16 && g >= 5 ? (g < 9 ? g - 4 : 4) : 0) - (s > 18 ? 2 : s == 18 && g >= 5 ? (g == 5 ? 1 : 2) : 0);
                    if constexpr (s >= 15 && nd >= 0 && !img) {
                        if constexpr (nd < 4) bc1<nd>(rr_[nd], sg.x);
                        else if constexpr (nd < 44) mulb<8 + (nd - 4) % 8>(tt[(nd - 4) % 8 / 4][(nd - 4) / 8][(nd - 4) % 4], sg.x, SW[(nd - 4) / 8]);
                        else if constexpr (nd < 48) bc1<nd - 40>(rr_[nd - 40], sg.x);
                        static_assert(p1_slot(15, 16, 0, 10) <= 9 && p1_slot(15, 20, 0, 10) >= 59, "");
                    }
                });
                if constexpr (s == 14 && g < 3) *(v4u*)(smem + ime + 1024 * g) = v4u{ie[3 - g][0], ie[3 - g][1], ie[3 - g][2], ie[3 - g][3]};
                if constexpr (s == 16 && g >= 5 && g < 9) *(v4u*)(smem + imo + 1024 * (8 - g)) = v4u{io[g - 5][0], io[g - 5][1], io[g - 5][2], io[g - 5][3]};
                if constexpr (s == 18 && g == 5) *(v4u*)(smem + im3) = v4u{ie[0][0], ie[0][1], ie[0][2], ie[0][3]};
                if constexpr (s == 18 && g == 6) *(unsigned*)(smem + rsa) = sg.x;
                if constexpr (s == 14 && (g == 6 || g == 8)) { zq[g - 6] = 0u; zq[g - 5] = 0u; pin(zq[g - 6]); pin(zq[g - 5]); }
                if constexpr (s == 19 && g == 0) *(v4u*)(smem + zpa) = v4u{zq[0], zq[1], zq[2], zq[3]};
                if constexpr (s == 18 && g == 1) touch_(tch, p.ws);
                if constexpr (s == 19 && g == 2) { if (__builtin_expect(!kearly, 0)) poll2(fko, fwo, p.ws); }
                if constexpr (it >= 0) {
                    if constexpr (d0 && d1) DMA_LD1(it, d1 ? nt_ : 0);
                    else if constexpr (d0) DMA_LD(it);
                    else if constexpr (it == 202) gld2s<0>(sg, sto, p.ws);
                    else if constexpr (it == 203) gld4s<0>(sw0, swo, p.ws);
                    else if constexpr (it == 206) gld2s<0>(fk, fko, p.ws);
                    else if constexpr (it == 207) gldu16(sdq, (unsigned)chn * 2u, p.ssmD);
                    else if constexpr (it == 208) gld2s<0>(fw, fwo, p.ws);
                    else if constexpr (it >= 500) gld_w2<it % 50>(w2o, w2b0 + ((it % 50) / 5) * 5120);
                    else { vm_wait<p1_cnt(s, g) - P1_PRE>(); st64(v2u{tag0, tag1}, rs, fko1); }
                }
                if constexpr (d1 && !d0) DMA_M0(d1 ? nt_ : 0);
                site<2000 + 10 * s + g>();
                SCHED();
            });
        };
        static_for<20>(kstep);
        SCHED();
        rr[0] = f32x4{rr_[0], rr_[1], rr_[2], rr_[3]}; rr[1] = f32x4{rr_[4], rr_[5], rr_[6], rr_[7]};
        ACC_FENCE10(acc[0], acc[1]);
        STAMP(1)
    }

    auto layer_body = LAMBDA(layer_) {
        constexpr int layer = layer_.value;
        const int sga = sgn_;
        const bool cv = layer == 0 && cv0;
        v2u fh;
        unsigned ks[10];
        f32x4 ca[8], a2[4][5];
        float rsr, rsm;
        v4u cA[2][8], cB[2][8];
        f32x4 pp[5];
        constexpr int R2 = kCR1 + H3, LP = (7 - R2) & 1;
        static_assert(GF && kFHP >= 7 - KG0 / 4 + 1, "");
        float bv[8][5];
        {
            float a0[5], a1[5];
            static_for<2>(LAMBDA(m_) {
                constexpr int mb = m_.value;
#pragma unroll
                for (int nb = 0; nb < 5; ++nb) {
                    const f32x4 v = __builtin_elementwise_fma(acc[mb][nb], rr[mb], f32x4{tt[mb][nb][0], tt[mb][nb][1], tt[mb][nb][2], tt[mb][nb][3]});
#pragma unroll
                    for (int i = 0; i < 4; ++i) bv[4 * mb + i][nb] = v[i];
                }
                SCHED();
                if constexpr (mb == 0) cstep(a0, bv[0], lr, ls, bv[1][0], bv[1][1], bv[1][2], bv[1][3], bv[1][4]);
#pragma unroll
                for (int i8 = mb ? 4 : 2; i8 < 4 + 4 * mb; i8 += 2) {
                    cstep(a1, a0, lr, ls, bv[i8][0], bv[i8][1], bv[i8][2], bv[i8][3], bv[i8][4]);
                    cstep(a0, a1, lr, ls, bv[i8 + 1][0], bv[i8 + 1][1], bv[i8 + 1][2], bv[i8 + 1][3], bv[i8 + 1][4]);
                }
                SCHED();
            });
#pragma unroll
            for (int nb = 0; nb < 5; ++nb) *(float*)(smem + sga + nb * 64) = a0[nb];
        }
        SCHED();
        const int w_ = w0;
        {
        const int w = w_, mt1 = mt1_;
        if (layer == 0 && __builtin_expect(!kearly, 0)) {
            unsigned f0_, f1_, f2_, f3_;
            asm volatile("s_waitcnt vmcnt(0)\n\t" AR("%0", 36, 0) AR("%1", 36, 1) AR("%2", 36, 2) AR("%3", 36, 3) : "=v"(f0_), "=v"(f1_), "=v"(f2_), "=v"(f3_)::"memory");
            kearly = __builtin_amdgcn_ballot_w64(((f0_ ^ tag0) | (f1_ ^ tag1) | (f2_ ^ tag0) | (f3_ ^ tag1)) != 0u) == 0ull;
            ORDER();
            if (kearly) KW_REQ()
        }
        asm volatile("s_waitcnt lgkmcnt(0)" ::: "memory");
        __builtin_amdgcn_s_barrier();
        asm volatile("" ::: "memory");
        STAMP(layer ? 14 : 2)
#pragma unroll
        for (int s = 0; s < 10; ++s) ks[s] = (unsigned)s * 1024u;
        constexpr int NX = DSE ? 58 : 53;
        constexpr int NW2A = NW2P + 7 * kCR1 + kXA, NW3 = NW2P + kN3E + kN3;
        auto w2ld = LAMBDA(i_) {
            constexpr int i = i_.value;
            gld_w2<i>(w2o, p.ws + 2048 + ks[i / 5] * 5u);
        };
        auto w2a = LAMBDA(i_) {
            constexpr int k = i_.value;
            if constexpr (k < kXA) { w2ld(std::integral_constant<int, NW2P + 2 * k>{}); w2ld(std::integral_constant<int, NW2P + 2 * k + 1>{}); }
            else w2ld(std::integral_constant<int, NW2P + kXA + k>{});
        };
        auto w2x = LAMBDA(i_) {
            constexpr int i = i_.value < NX - 50 ? 50 + i_.value : NW2A + i_.value - (NX - 50) < 50 ? NW2A + i_.value - (NX - 50) : 99;
            if constexpr (i < 50) w2ld(std::integral_constant<int, i>{});
            else if constexpr (i < 52) lda4<40 + 4 * (i - 50), (i - 50) * 64>(xso, p.x0);
            else if constexpr (i == 52) lda4<48, 128>(xso - 2u * (unsigned)(bb & 16), p.x0);
            else if constexpr (i < NX) lda2<20 + 2 * (i - 53), 32 * (i - 53)>(dso, p.Dskip);
        };
        constexpr int NB = NX - NW2A, NB1 = KB1 < NB ? KB1 : NB, KPL = 7 * (8 - R2) - kFHP, NFH = NB - NB1 - (KPL - kFHK) > 0 ? NB - NB1 - (KPL - kFHK) : 0;
        auto w2b = LAMBDA(i_) {
            constexpr int k = i_.value - 7 * R2;
            if constexpr (k < NB1) w2x(std::integral_constant<int, k>{});
            if constexpr (k > kFHK) w2x(std::integral_constant<int, NB1 + k - kFHK - 1>{});
            if constexpr (k == kFHK) { vm_wait<NB1>(); st64(v2u{tag0, tag1}, rs, fho1); STAMP(39) }
            if constexpr (k == kFHK - 1) { STAMP(38) }
            if constexpr (k == KPL) poll1(fho, p.ws);
            static_assert(NB1 <= kFHK + 1 && NB1 + 7 * (7 - R2) + KG0 / 4 - kFHK - 1 >= NB && NW2A <= 50 && NW3 <= NW2A && NFH <= NB - NB1, "");
        };
        auto partb = [&]() __attribute__((always_inline)) {
            const float* sp = (const float*)(smem + spa);
            rsr = sp[0]; rsm = sp[8];
            if (layer == 1) { pin(rsr); pin(rsm); }
            if (layer == 1) {
                ACC_TIE5(dsk);
#pragma unroll
                for (int nb = 0; nb < 5; ++nb) { a2[3][nb] = xin[nb] + (xin[nb] * rsr + rsm) * unpk4(dsk[nb]); pin(a2[3][nb]); }
            }
        };
        constexpr int LBW = layer ? 2 : 3;
        if (w == 3) {
            site<3 + 10 * layer>(); SCHED();
            f32x2 sv[16];
#pragma unroll
            for (int i = 0; i < 16; ++i) sv[i] = *(const f32x2*)(smem + spl + (l_sg(layer, i >> 2) - SC + (i & 3) * 320));
            f32x2 u[4], v[4], x[4];
            cfma4(u, l8, sv[0], sv[4], sv[8], sv[12], sv[1], sv[5], sv[9], sv[13]);
            cfma4(v, l8, u[0], u[1], u[2], u[3], sv[2], sv[6], sv[10], sv[14]);
            cfma4(x, l8, v[0], v[1], v[2], v[3], sv[3], sv[7], sv[11], sv[15]);
            f32x2 P[4][4];
            P[0][0] = f32x2{0.f, 0.f}; P[0][1] = x[0]; P[0][2] = cfma(l32, x[0], x[1]); P[0][3] = cfma(l32, P[0][2], x[2]);
            const f32x2 E = cfma(l32, P[0][3], x[3]);
            st128(v4u{__float_as_uint(E.x), tag0, __float_as_uint(E.y), tag0}, rs, es);
            SCHED();
            if (layer == 0) { FW_OK() static_for<kN3E>(LAMBDA(i_) { w2ld(std::integral_constant<int, NW2P + i_.value>{}); }); }
#pragma unroll
            for (int kk = 1; kk < 4; ++kk) cfma4(P[kk], l8, P[kk - 1][0], P[kk - 1][1], P[kk - 1][2], P[kk - 1][3], sv[kk - 1], sv[kk + 3], sv[kk + 7], sv[kk + 11]);
#pragma unroll
            for (int i = 0; i < 16; ++i) *(f32x2*)(smem + spl + (l_sg(layer, i >> 2) - SC + (i & 3) * 320)) = P[i & 3][i >> 2];
            partb();
            if (layer == 0 && H3) *(unsigned*)(smem + L_FL + (spl & 0x38) / 2) = 0u;
        }
        if (w == LBW) {
            site<5 + 10 * layer>(); SCHED();
            f32x2 wg[17], t4[4];
            unsigned ea[15], tg[15];
            const f32x2 z2 = {0.f, 0.f};
            wg[1] = cmul(l128, l128); wg[2] = cmul(wg[1], wg[1]); wg[3] = cmul(wg[2], wg[1]); wg[4] = cmul(wg[2], wg[2]);
#pragma unroll
            for (int i = 4; i < 16; i += 4) {
                cfma4(t4, wg[i], wg[1], wg[2], wg[3], wg[4], z2, z2, z2, z2);
#pragma unroll
                for (int e = 0; e < 4; ++e) wg[i + 1 + e] = t4[e];
            }
#pragma unroll
            for (int i = 0; i < 15; ++i) {
                const int ch = mt1 - 1 - i;
                if (i) pin(wg[i]);
                ea[i] = ch < 0 ? kNone : (unsigned)ch * 10240u; tg[i] = ch < 0 ? 0u : tag0;
                pins(ea[i]); pins(tg[i]);
            }
            if (layer == 1) partb();
            if constexpr (layer == 0) tour<3>();
            f32x2 H0, H1;
            __builtin_amdgcn_s_sleep(layer ? KLB1 : KLB0);
            unsigned spins = dead ? kSpinCap : 0u;
            bool miss;
#pragma nounroll
            do {
                asm volatile("" ::: "memory");
                v4u eg[15];
#pragma unroll
                for (int i = 0; i < 15; ++i) eg[i] = ld128s(rs, eo, ea[i]);
                SCHED();
                unsigned x = __builtin_amdgcn_bitop3_b32(eg[0].y, eg[0].w, tg[0], 0x7E);
                H0 = f32x2{__uint_as_float(eg[0].x), __uint_as_float(eg[0].z)}; H1 = f32x2{0.f, 0.f};
#pragma unroll
                for (int i = 1; i < 15; i += 2) {
                    x |= __builtin_amdgcn_bitop3_b32(eg[i].y, eg[i].w, tg[i], 0x7E) | __builtin_amdgcn_bitop3_b32(eg[i + 1].y, eg[i + 1].w, tg[i + 1], 0x7E);
                    pin(x);
                    gacc2(H0, H1, wg[i], wg[i + 1], eg[i], eg[i + 1]);
                    SCHED();
                }
                miss = __builtin_amdgcn_ballot_w64(x != 0u) != 0ull;
            } while (miss && ++spins <= kSpinCap);
            dead = dead || miss;
            ORDER();
            *(f32x2*)(smem + spl + (L_CW - SC)) = H0 + H1;
            STAMP(layer ? 36 : 37)
            if (layer == 0) {
                static_for<NW3 - NW2P - kN3E>(LAMBDA(i_) { w2ld(std::integral_constant<int, NW2P + kN3E + i_.value>{}); });
            }
        }
        int w2_ = w;
        pins(w2_);
        if (layer == 0) UNDEF8(ca);
        if (layer == 0 && w2_ != LBW) {
            if (cv) {
                site<2>(); SCHED();
                FW_OK()
                st_ur((unsigned)(size_t)(LAS char*)smem + (unsigned)uwa);
                STAMP(32)
                conv_part<0, kCR1>(ca, smem, ab, bb, w2a, LAMBDA(i_) {}, [&]() __attribute__((always_inline)) {
                    v4u ad;
                    const unsigned dv = sdq << (adm & 16u);
#pragma unroll
                    for (int i = 0; i < 4; ++i) ad[i] = dv & (unsigned)__builtin_amdgcn_sbfe((int)adm, 8 + i, 1);
                    asm volatile("s_nop 1" : "+v"(ad));
                    return ad;
                }, cA, cB);
                partb();
            } else {
                if constexpr (layer == 0) tour<5>();
                FW_OK()
                static_for<NW2A - NW2P>(LAMBDA(i_) { w2ld(std::integral_constant<int, NW2P + i_.value>{}); __builtin_amdgcn_s_sleep(1); });
                partb();
            }
        }
        if (layer == 1 && w < LBW) { site<4 + 10 * layer>(); SCHED(); partb(); if constexpr (layer == 1) { if (w == 0) tour<6>(); } }
        asm volatile("s_waitcnt lgkmcnt(0)" ::: "memory");
        __builtin_amdgcn_s_barrier();
        asm volatile("" ::: "memory");
        STAMP(layer ? 15 : 4)
        {
            float cw[5], ex[5];
#pragma unroll
            for (int nb = 0; nb < 5; ++nb) { cw[nb] = *(const float*)(smem + cwa + nb * 64); ex[nb] = *(const float*)(smem + sga + nb * 64); }
            v2u ta[5], td[5];
            static_for<2>(LAMBDA(h_) {
                constexpr int n0 = 2 * h_.value;
                float hv[9][3];
                cstep3(hv[0], cw + n0, lqr + n0, lqs + n0, ex + n0);
                if constexpr (n0 == 0) {
                    chain24<0>(hv, bv, lr, ls);
                    chain24<0>(hv + 4, bv + 4, lr, ls);
                } else {
                    chain33<2>(hv, bv, lr, ls);
                    SCHED();
#pragma unroll
                    for (int rd = 0; rd < 2; ++rd) st128(v4u{ta[rd].x, ta[rd].y, td[rd].x, td[rd].y}, rs, hso + rd * 256);
                    SCHED();
                    chain33<2>(hv + 3, bv + 3, lr, ls);
                    chain32<2>(hv + 6, bv + 6, lr, ls);
                }
#pragma unroll
                for (int e = 0; e < (n0 ? 3 : 2); ++e) {
                    v4u o;
                    o.x = cvtpk(hv[1][e], hv[2][e]); o.y = cvtpk(hv[3][e], hv[4][e]);
                    o.z = cvtpk(hv[5][e], hv[6][e]); o.w = cvtpk(hv[7][e], hv[8][e]);
                    *(v4u*)(smem + tbw + (n0 + e) * 1024) = o;
                }
#pragma unroll
                for (int rd = n0; rd < (n0 ? 5 : 2); ++rd) { ta[rd] = __builtin_bit_cast(v2u, trread(smem, tbr + rd * 1024)); td[rd] = __builtin_bit_cast(v2u, trread(smem, tbr + rd * 1024 + 512)); }
                SCHED();
            });
#pragma unroll
            for (int rd = 2; rd < 5; ++rd) st128(v4u{ta[rd].x, ta[rd].y, td[rd].x, td[rd].y}, rs, hso + rd * 256);
            SCHED();
        }
#pragma unroll
        for (int s = 0; s < 10; ++s) pins(ks[s]);
        if (layer == 1) { DRAIN(); st64(v2u{tag0, tag1}, rs, fho1); }
        STAMP(layer ? 16 : 5)
        int fa = L_FL + 4 * w, pa = l_tb(0, 0) + (w < 3 ? w : 0) * 5120 + (int)(w2o & 1023u);
        unsigned one = 1u;
        if constexpr (layer == 0 && H3) {
            pin(fa); pin(pa); pin(one);
            *(unsigned*)(smem + fa) = one;
            asm volatile("" ::: "memory");
        }
        if (cv) {
            if constexpr (layer == 0) site<6>();
            SCHED();
            unsigned fl = 1u;
            conv_part<R2, 8>(ca, smem, ab, bb, w2b, LAMBDA(g_) {
                constexpr int g = g_.value - (KG0 - 8);
                if constexpr (H3 && g == 0) { asm volatile("" ::: "memory"); fl = *(const unsigned*)(smem + fa + 16); asm volatile("" ::: "memory"); }
                if constexpr (H3 && g > 0 && g < 6) pp[g - 1] = *(const f32x4*)(smem + pa + (g - 1) * 1024);
                static_assert(!H3 || KG0 >= 8, "");
            }, []() {}, cA, cB);
            while (__builtin_expect(__builtin_amdgcn_readfirstlane(fl) == 0u, 0)) {
                asm volatile("" ::: "memory");
                fl = *(const unsigned*)(smem + fa + 16);
                asm volatile("" ::: "memory");
#pragma unroll
                for (int i = 0; i < 5; ++i) pp[i] = *(const f32x4*)(smem + pa + i * 1024);
            }
        } else if (layer == 0) {
            if (H3 && w == 3) {
                if constexpr (layer == 0) site<16>();
                constexpr int NR = NB - NB1 - NFH, N3 = NR + NW2A - NW3;
                auto fill3 = LAMBDA(n_) {
                    constexpr int n = n_.value, k = n - KF3 - 1;
                    if constexpr (n < NB1) w2x(n_);
                    if constexpr (n == KF3) { vm_wait<NB1>(); st64(v2u{tag0, tag1}, rs, fho1); }
                    if constexpr (k >= 0 && k < NR) w2x(IC(NB1 + k));
                    else if constexpr (k >= NR && k < N3) w2ld(IC(NW3 + k - NR));
                };
                int a3[4] = {ab[0], ab[1], ab[2], ab[3]}, b3 = bb;
                f32x4 cb[2][8];
                auto chan = LAMBDA(x_) {
                    constexpr int x = x_.value;
                    const int (&ab)[4] = a3;
                    const int& bb = b3;
                    if constexpr (x == 0) {
                        CV_LOAD_A(cA[0], 0, kCR1, 0) CV_LOAD_A(cA[0], 0, kCR1, 1)
                        static_for<8>(LAMBDA(e_) { constexpr int e = (e_.value + 3) & 7; CV_LOAD_B(cB[0], e, kCR1) SCHED(); });
                        static_for<7>(LAMBDA(a_) { constexpr int a = a_.value + 1; CV_LOAD_A(cA[0], a, kCR1, 0) CV_LOAD_A(cA[0], a, kCR1, 1) });
                        SCHED();
                        lds_wait3(fa - 12);
                    }
                    static_for<30>(LAMBDA(g_) {
                        constexpr int g = g_.value, a = cv_pair(3, 8, g) / 8, e = cv_pair(3, 8, g) % 8, u = g - 3, f = (u / 3 + 3) & 7;
                        if constexpr (a == 0) mf0(cb[x & 1][e], cA[x & 1][0], cB[x & 1][e]); else mf(cb[x & 1][a + e], cA[x & 1][a], cB[x & 1][e]);
                        SCHED();
                        if constexpr (x < 2 && g < 2) { a3[2 * g] += REGSZ; a3[2 * g + 1] += REGSZ; pin(a3[2 * g]); pin(a3[2 * g + 1]); }
                        if constexpr (x < 2 && g == 2) { b3 += REGSZ; pin(b3); }
                        if constexpr (x < 2 && u >= 0 && u < 24) { if constexpr (u % 3 == 2) CV_LOAD_B(cB[(x & 1) ^ 1], f, kCR1) else CV_LOAD_A(cA[(x & 1) ^ 1], u / 3, kCR1, u % 3) }
                        if constexpr (x > 0 && g % 3 == 2 && g > 2 && g < 18) *(f32x4*)(smem + pa + (x - 1) * 5120 + (g / 3 - 1) * 1024) = cb[(x & 1) ^ 1][2 + g / 3];
                        if constexpr (x > 0 && g == 20) { asm volatile("" ::: "memory"); *(unsigned*)(smem + fa + 4 * x) = one; }
                        SCHED();
                        site<3300 + 30 * x + g>();
                        if constexpr (g % 3 == 1) { fill3(IC(10 * x + g / 3)); SCHED(); }
                    });
                };
                auto out = LAMBDA(x_) {
                    constexpr int x = x_.value;
                    asm volatile("s_nop 7" ::: "memory");
#pragma unroll
                    for (int i = 0; i < 5; ++i) *(f32x4*)(smem + pa + x * 5120 + i * 1024) = cb[x & 1][3 + i];
                    asm volatile("" ::: "memory");
                    *(unsigned*)(smem + fa + 4 + 4 * x) = one;
                    SCHED();
                };
                chan(IC(0));
                chan(IC(1));
                if (three) { chan(IC(2)); out(IC(2)); }
                else { out(IC(1)); static_for<10>(LAMBDA(i_) { fill3(IC(20 + i_.value)); __builtin_amdgcn_s_sleep(1); }); }
                static_for<(KF3 + 1 + N3 > 30 ? KF3 + 1 + N3 - 30 : 0)>(LAMBDA(i_) { fill3(IC(30 + i_.value)); });
                if constexpr (layer == 0) tour<2>();
            } else {
                static_for<NB1>(LAMBDA(i_) { w2x(i_); __builtin_amdgcn_s_sleep(1); });
                vm_wait<NB1>();
                st64(v2u{tag0, tag1}, rs, fho1);
                static_for<NB - NB1 - NFH>(LAMBDA(i_) { w2x(std::integral_constant<int, NB1 + i_.value>{}); __builtin_amdgcn_s_sleep(1); });
                if (!H3 && w == 3) { static_for<NW2A - NW3>(LAMBDA(i_) { w2ld(std::integral_constant<int, NW3 + i_.value>{}); __builtin_amdgcn_s_sleep(1); }); if constexpr (layer == 0 && !H3) tour<2>(); }
                if (KSN) __builtin_amdgcn_s_sleep(KSN);
            }
            poll1(fho, p.ws);
            static_for<NFH>(LAMBDA(i_) { w2x(std::integral_constant<int, NB - NFH + i_.value>{}); __builtin_amdgcn_s_sleep(1); });
            static_assert(NFH <= NB - (NX - 50), "");
            UNDEF8(cA[LP]); UNDEF8(cB[LP]);
            if (H3) asm volatile("" : "=v"(pp[0]), "=v"(pp[1]), "=v"(pp[2]), "=v"(pp[3]), "=v"(pp[4]));
        }
        if (layer == 0) { unsigned f0_, f1_; asm volatile("s_waitcnt vmcnt(%2)\n\t" AR("%0", 36, 0) AR("%1", 36, 1) : "=v"(f0_), "=v"(f1_) : "i"(NFH) : "memory"); fh = v2u{f0_, f1_}; }
        STAMP(layer ? 29 : 6)
        }
        unsigned g[8][2];

            v2u pt[4];
            if (layer == 0) WAVE_RETRY(ISTAG(fh), fh = ld64(rs, fho))
            else { skip<SKIP7>(); pipe_flag(pt, fho, p.ws, (u64)tag0 | ((u64)tag1 << 32), dead); ORDER(); }
            STAMP(layer ? 17 : 31)
            site<7 + 10 * layer>(); SCHED();
            constexpr int RING2 = layer ? KR2 : KRT;
            v4u ar[RING2];
            constexpr int NRT = layer ? RING2 : (36 - KG0) / KRS + 2 < KRT ? (36 - KG0) / KRS + 2 : KRT;
            unsigned xs_[3][4];
            float ys[8][4];
            auto gate = LAMBDA(n_) {
                constexpr int n = n_.value, bl = n / 10, u = n % 10;
                if constexpr (n < 0) {}
                else if constexpr (n >= 100) swp2n(g[2 * (n - 100)][0], g[2 * (n - 100) + 1][0], g[2 * (n - 100)][1], g[2 * (n - 100) + 1][1]);
                else if constexpr (u < 2) { if constexpr (H3) { ys[bl][2 * u] = pp[bl - 3][2 * u]; ys[bl][2 * u + 1] = pp[bl - 3][2 * u + 1]; add2(ys[bl][2 * u], ys[bl][2 * u + 1], ca[bl][2 * u], ca[bl][2 * u + 1]); } }
                else if constexpr (H3 && bl >= 3) cvt2(g[bl][0], g[bl][1], f32x4{ys[bl][0], ys[bl][1], ys[bl][2], ys[bl][3]});
                else cvt2(g[bl][0], g[bl][1], ca[bl]);
            };
            if constexpr (layer == 0) {
#pragma unroll
                for (int i = 0; i < 2; ++i) { ar[i] = ld128s(rs, ho[0], ks[i]); SCHED(); }
                static_for<36 - KG0>(LAMBDA(n_) {
                    constexpr int n = n_.value, g = KG0 + n;
                    constexpr int a = g < 8 ? 0 : g < 15 ? 1 : g < 21 ? 2 : g < 26 ? 3 : g < 30 ? 4 : g < 33 ? 5 : g < 35 ? 6 : 7;
                    constexpr int e = g - (a * 8 - a * (a - 1) / 2), i = 2 + n / KRS;
                    mf(ca[a + e], cA[LP][a], cB[LP][e]);
                    SCHED();
                    if constexpr (n % KRS == 0 && i < NRT) ar[i] = ld128s(rs, ho[i / 10], ks[i % 10]);
                    if constexpr (n < 12) rda<40 + n>(xs_[n >> 2][n & 3]);
                    constexpr int gt_[12] = {2, 12, 22, 30, 100, 31, 32, 40, 41, 101, 42, 50};
                    if constexpr (g >= 24) gate(IC(gt_[g < 24 ? 0 : g - 24]));
                    site<3252 + g>();
                    SCHED();
                });
                static_assert(KG0 <= 15 && NRT >= 12 && DSE, "");
            } else {
#pragma unroll
                for (int i = 0; i < RING2; ++i) { ar[i] = ld128s(rs, ho[i / 10], ks[i % 10]); SCHED(); }
            }
        v4u gl[3];
        v2u f0;
        bool gearly = false;
        {
            const char* src = smem + srca;
            const char *glb1 = p.ws + 32 * 4096, *glb2 = p.ws + 48 * 4096;
            float zx[4], zd[4], zt[4], zz[5][4];
            if constexpr (STAMPS == 1) { STAMP(layer ? 50 : 40) }
            static_for<40>(LAMBDA(i_) {
                constexpr int i = i_.value, m = i / 10, s = i % 10;
                static_for<5>(LAMBDA(k_) {
                    constexpr int kk = k_.value, n = 2 * (i - 3) + kk / 2 - 3, nb = n / 8, u = n % 8, pv = i - 1 + RING2;
                    if constexpr (s == 0 && m < 3) mfa0<kk>(a2[m][kk], ar[i % RING2]); else mfa<5 * s + kk>(a2[m][kk], ar[i % RING2]);
                    SCHED();
                    constexpr int gu_[12] = {51, 52, 60, 61, 102, 62, 70, 71, 72, -1, 103, -1};
                    if constexpr (layer == 0 && 5 * i + kk < 12) gate(IC(gu_[(5 * i + kk) % 12]));
                    if constexpr (layer == 0 && i == 25 && kk == 1) { pins(glb1); pins(glb2); }
                    if constexpr (layer == 0 && i >= 3 && i < 26 && (kk & 1)) {
                        if constexpr (n < 0) swp2n(xs_[n + 3][0], xs_[n + 3][2], xs_[n + 3][1], xs_[n + 3][3]);
                        else if constexpr (nb < 5) {
                            if constexpr (u < 2) unp2(zx[2 * u], zx[2 * u + 1], xs_[nb >> 1][2 * (nb & 1) + u]);
                            else if constexpr (u < 4) unp2(zd[2 * u - 4], zd[2 * u - 3], dsk[nb][u - 2]);
                            else if constexpr (u < 6) fma2(zt[2 * u - 8], zt[2 * u - 7], zx[2 * u - 8], zx[2 * u - 7], rsr, rsr, rsm, rsm);
                            else fma2(zz[nb][2 * u - 12], zz[nb][2 * u - 11], zt[2 * u - 12], zt[2 * u - 11], zd[2 * u - 12], zd[2 * u - 11], zx[2 * u - 12], zx[2 * u - 11]);
                        }
                    }
                    if constexpr (i == 0 && kk == 0 && layer == 1) { pin(pt[0]); pin(pt[1]); pin(pt[2]); pin(pt[3]); }
                    if constexpr (layer == 0 && i < 2 && kk > 0 && 12 + 4 * i + kk - 1 >= NRT && 12 + 4 * i + kk - 1 < RING2) ar[12 + 4 * i + kk - 1] = ld128s(rs, ho[1], ks[2 + 4 * i + kk - 1]);
                    if constexpr (layer == 0 && i == kGSI && kk > 0) st128(v4u{g[2 * kk - 2][0], g[2 * kk - 2][1], g[2 * kk - 1][0], g[2 * kk - 1][1]}, rs, gso + (kk - 1) * 1024);
                    if constexpr (layer == 0 && i > 2 && i <= 7 && kk == (i == kGSI ? 0 : 2)) {
                        if constexpr (DSE) { unsigned d0_, d1_; rda<20 + 2 * (i - 3)>(d0_); rda<21 + 2 * (i - 3)>(d1_); dsk[i - 3] = v2u{d0_, d1_}; }
                    }
                    if constexpr (kk == 0 && i > 0 && pv < 40) ar[pv % RING2] = ld128s(rs, ho[pv / 10], ks[pv % 10]);
                    constexpr int s1 = s - 1 - (KMB && layer == 0 && m == 1 ? kFGI - 10 : 0);
                    if constexpr (kk == 2 && m >= 1 && s1 >= 0 && s1 < 5) *(f32x4*)(smem + dst[m - 1] + s1 * 1024) = a2[m - 1][s1];
                    site<4000 + 1000 * layer + 5 * i + kk>();
                    if constexpr (i >= 37) a2[i - 37][kk] = *(const f32x4*)(src + (i - 37) * 5120 + kk * 1024);
                    SCHED();
                });
                if constexpr (i == 10 && (layer || !KMB)) { asm volatile("s_waitcnt lgkmcnt(0)" ::: "memory"); __builtin_amdgcn_s_barrier(); asm volatile("" ::: "memory"); }
                if constexpr (i == kFGI && layer == 0) {
                    static_assert(kFGI >= 10 && kFGI <= 14, "");
                    asm volatile("s_waitcnt vmcnt(%0) lgkmcnt(0)" ::"i"(kFGI - kGSI) : "memory");
                    __builtin_amdgcn_s_barrier(); asm volatile("" ::: "memory");
                    st64(v2u{tag0, tag1}, rs, fgo1);
                }
                if constexpr (i == 26 && layer == 0) {
#pragma unroll
                    for (int nb = 0; nb < 5; ++nb) { a2[3][nb] = f32x4{zz[nb][0], zz[nb][1], zz[nb][2], zz[nb][3]}; pin(a2[3][nb]); }
                }
                if constexpr (i == kGPI && layer == 0) gld2s<0>(f0, fgo, p.ws);
                if constexpr (i == kGCI && layer == 0) {
                    wait1(f0);
                    static_for<39 - i>(LAMBDA(r_) { pin(ar[(i + 1 + r_.value) % RING2]); });
                    gearly = __builtin_amdgcn_ballot_w64(!ISTAG(f0)) == 0ull;
                    ORDER();
                    undef3(gl);
                    if (__builtin_expect(gearly, 1)) { gld4t<0>(gl[0], glo, p.ws); gld4t<0>(gl[1], glo, glb1); gld4t<0>(gl[2], glo, glb2); }
                    else if (G2ND) poll2(fgo, fgo, p.ws);
                }
                if constexpr (i == 36) {
                    asm volatile("s_waitcnt lgkmcnt(0)" ::: "memory");
                    __builtin_amdgcn_s_barrier(); asm volatile("" ::: "memory");
                }
                if constexpr (STAMPS == 1 && (i == 0 || i == 9 || i == 10 || i == 15 || i == 25 || i == 35 || i == 36)) { STAMP((layer ? 51 : 41) + (i == 0 ? 0 : i == 9 ? 1 : i == 10 ? 2 : i == 15 ? 3 : i == 25 ? 4 : i == 35 ? 5 : 6)) }
                SCHED();
            });
            STAMP(layer ? 18 : 8)
        }
#pragma unroll
        for (int nb = 0; nb < 5; ++nb) a2[0][nb] = (a2[0][nb] + a2[1][nb]) + a2[2][nb];
        asm volatile("s_nop 1" : "+v"(a2[3][0]), "+v"(a2[3][1]), "+v"(a2[3][2]), "+v"(a2[3][3]), "+v"(a2[3][4]),
                     "+v"(a2[0][0]), "+v"(a2[0][1]), "+v"(a2[0][2]), "+v"(a2[0][3]), "+v"(a2[0][4]));
        float ty;
        {
            f32x4 s1, s2;
#pragma unroll
            for (int nb = 0; nb < 5; ++nb) {
                const f32x4 y = a2[3][nb] + a2[0][nb];
                xin[nb] = y;
                if (nb == 0) { s1 = y; s2 = y * y; } else { s1 += y; s2 += y * y; }
            }
            ty = xsum2h(hsum4(s1), hsum4(s2));
            st64(v2u{__float_as_uint(ty), tag0}, rs, sgs);
        }
        SCHED();
        STAMP(layer ? 19 : 9)
        int lane_o, b_o = b0, w_o = w0;
        asm volatile("v_mbcnt_lo_u32_b32 %0, -1, 0\n\tv_mbcnt_hi_u32_b32 %0, -1, %0" : "=v"(lane_o));
        asm volatile("" : "+s"(b_o), "+s"(w_o));
        const int lane = lane_o, b = b_o, w = w_o;
        const int c = lane & 15, q = lane >> 4;
        TILES()
        if (layer == 0 && __builtin_expect(!gearly, 0)) {
            if (G2ND) {
                unsigned f0_, f1_;
                asm volatile("s_waitcnt vmcnt(1)\n\t" AR("%0", 36, 0) AR("%1", 36, 1) : "=v"(f0_), "=v"(f1_)::"memory");
                f0 = v2u{f0_, f1_};
            }
            WAVE_RETRY(ISTAG(f0), f0 = ld64(rs, fgo))
            gld4t<0>(gl[0], glo, p.ws); gld4t<0>(gl[1], glo, p.ws + 32 * 4096); gld4t<0>(gl[2], glo, p.ws + 48 * 4096);
            asm volatile("s_waitcnt vmcnt(0)" : "+v"(gl[0]), "+v"(gl[1]), "+v"(gl[2])::"memory");
        }
        if (layer == 0) {
            asm volatile("s_waitcnt vmcnt(1)" : "+v"(gl[0]), "+v"(gl[1]), "+v"(gl[2])::"memory");
            char* tb = smem + SC + w * INBOX;
            *(v4u*)(tb + lane * 16) = gl[0]; *(v4u*)(tb + 1024 + lane * 16) = gl[1]; *(v4u*)(tb + 1536 + lane * 16) = gl[2];
#pragma unroll
            for (int nb = 0; nb < 5; ++nb) gt[nb] = __builtin_bit_cast(v2u, trread(tb, (16 * nb + 4 * q + (c >> 2)) * 32 + (c & 3) * 8));
        } else ACC_TIE5(gt);
        SCHED();
        f32x4 gf[5], yg[5];
#pragma unroll
        for (int nb = 0; nb < 5; ++nb) { gf[nb] = unpk4(gt[nb]); yg[nb] = xin[nb] * gf[nb]; pin(gf[nb]); pin(yg[nb]); }
        unsigned xs = OFF_X2F + (unsigned)((2 * mt2 + (w >> 1)) * 40960 + ((c >> 2) & 1) * 20480 + (10 * nt2 + 2 * (q & 1) + (q >> 1)) * 256 + (8 * (w & 1) + 4 * (c >> 3) + (c & 3)) * 16);
        unsigned pso = q < 2 ? OFF_PS + (unsigned)(((mt2 * 4 + w) * 8 + nt2) * 128 + c * 8 + q * 4) : kNone;
        unsigned fxo1 = OFF_FX + (unsigned)(((mt1 * 4 + 2 * (mt2 & 1) + (w >> 1)) * 16 + nt2 * 2 + (w & 1)) * 128);
        unsigned oo = (unsigned)(((64 * mt2 + 16 * w + c) * D_ + 80 * nt2 + 4 * q) * 4);
        if (layer == 0) { pin(xs); pin(pso); pin(fxo1); } else pin(oo);
        float c_r, c_m, c_rr, c_rm, c_mm;
        if constexpr (layer == 0 && PSA) {
            f32x4 su = yg[0], sg_ = gf[0], syu = xin[0] * yg[0], suu = yg[0] * yg[0], sug = yg[0] * gf[0], sgg = gf[0] * gf[0];
#pragma unroll
            for (int nb = 1; nb < 5; ++nb) { su += yg[nb]; sg_ += gf[nb]; syu += xin[nb] * yg[nb]; suu += yg[nb] * yg[nb]; sug += yg[nb] * gf[nb]; sgg += gf[nb] * gf[nb]; }
            const float hu = hsum4(su);
            c_r = xsum2h(hu, 2.f * hsum4(syu)); c_m = xsum2h(hsum4(sg_), 2.f * hu);
            c_rr = xsum2h(0.f, hsum4(suu)); c_rm = xsum2h(0.f, 2.f * hsum4(sug)); c_mm = xsum2h(0.f, hsum4(sgg));
            pin(c_r); pin(c_m); pin(c_rr); pin(c_rm); pin(c_mm);
        }
        SCHED();
        {
            float a, d;
            if constexpr (layer == 0) { skip<SKIP4>(); site<9>(); }
            pipe_gran<layer ? PDS : PDS0>(a, d, sgo, p.ws, tag0, dead);
            STAMP(layer ? 20 : 10)
            xsum2(a, d);
            const float me = a * (1.0f / D_);
            const float ri = __builtin_amdgcn_rsqf(fmaxf(d * (1.0f / D_) - me * me, 0.f) + 1e-5f), mr = -me * ri;
            if constexpr (layer == 0 && PSA) {
                const float ps_ = ty + ri * (c_r + ri * c_rr + mr * c_rm) + mr * (c_m + mr * c_mm);
                __builtin_amdgcn_raw_buffer_store_b32(__float_as_uint(ps_), rs, pso, 0, SC1);
                SCHED();
            }
#pragma unroll
            for (int nb = 0; nb < 5; ++nb) xin[nb] = yg[nb] * ri + (gf[nb] * mr + xin[nb]);
        }
        if constexpr (layer == 0) {
            v2u pk[5];
#pragma unroll
            for (int nb = 0; nb < 5; ++nb) { const f32x4 x = xin[nb]; pk[nb] = v2u{cvtpk(x.x, x.y), cvtpk(x.z, x.w)}; }
#pragma unroll
            for (int i = 0; i < 3; ++i) {
                const v2u pa = pk[2 * i], pb = pk[i < 2 ? 2 * i + 1 : 4];
                const auto r0 = __builtin_amdgcn_permlane16_swap(pa.x, pb.x, false, false);
                const auto r1 = __builtin_amdgcn_permlane16_swap(pa.y, pb.y, false, false);
                if (i < 2 || (q & 1) == 0) st128(v4u{r0[0], r1[0], r0[1], r1[1]}, rs, xs + i * 1024);
            }
            if constexpr (!PSA) {
                f32x4 s1, s2;
#pragma unroll
                for (int nb = 0; nb < 5; ++nb) { const f32x4 x = xin[nb]; if (nb == 0) { s1 = x; s2 = x * x; } else { s1 += x; s2 += x * x; } }
                __builtin_amdgcn_raw_buffer_store_b32(__float_as_uint(xsum2h(hsum4(s1), hsum4(s2))), rs, pso, 0, SC1);
            }
            SCHED();
            int w1lane0 = (c & 1) * 10240 + (c >> 1) * 128 + ((q ^ ((c & 1) | ((c >> 2) << 1))) * 16);
            int w1lane1 = (c & 1) * 10240 + (c >> 1) * 128 + (((4 + q) ^ ((c & 1) | ((c >> 2) << 1))) * 16);
            unsigned fxo = OFF_FX + (unsigned)((mt1 * 4 + w) * 2048 + c * 128);
            unsigned xo = OFF_X2F + (unsigned)((4 * mt1 + w) * 40960 + lane * 16), xo1 = xo + 20480u;
            const int row = 8 * q + (c & 7);
            unsigned po = OFF_PS + (unsigned)((((2 * mt1 + (w >> 1)) * 4 + 2 * (w & 1) + (row >> 4)) * 8 + 4 * (c >> 3)) * 128 + (row & 15) * 8);
            int w1hi0 = w1lane0 + 61440, w1hi1 = w1lane1 + 61440;
            pin(w1lane0); pin(w1lane1); pin(w1hi0); pin(w1hi1); pin(fxo); pin(xo); pin(xo1); pin(po);
            v4u wf[2][5];
#pragma unroll
            for (int nb = 0; nb < 5; ++nb) wf[0][nb] = *(const v4u*)(smem + w1lane0 + nb * 20480);
            SCHED();
            DRAIN();
            FLAG(fxo1)
            if (b == 255 && w == 0) FLAG(OFF_NONCE)
            STAMP(11)
            if (w == 0) tour<4>();
            addrs(std::integral_constant<int, 1>{}, lane, w, b);
            v2u pt[4];
            skip<SKIP5>();
            pipe_flag(pt, fxo, p.ws, (u64)tag0 | ((u64)tag1 << 32), dead);
            ORDER();
            STAMP(12)
            v4u ar[RING1][2];
#pragma unroll
            for (int s = 0; s < RING1; ++s)
#pragma unroll
                for (int mb = 0; mb < 2; ++mb) { ar[s][mb] = ld128s(rs, mb ? xo1 : xo, s * 1024); SCHED(); }
            v2u ps[4];
#pragma unroll
            for (int i = 0; i < 4; ++i) ps[i] = ld64(rs, po + i * 128);
            SCHED();
            float t1, t2, rinv, mri, rr_[8], rm_[8];
            static_for<20>(LAMBDA(s_) {
                constexpr int s = s_.value, cu = s & 1, nx = cu ^ 1;
                LGKM0() SCHED();
                static_for<10>(LAMBDA(g_) {
                    constexpr int g = g_.value, mb = g / 5, k = g % 5;
                    if constexpr (s == 0) mf0(acc[mb][k], ar[0][mb], wf[0][k]); else mf(acc[mb][k], ar[s % RING1][mb], wf[cu][k]);
                    SCHED();
                    if constexpr (s == 0 && g == 0) { pin(pt[0]); pin(pt[1]); pin(pt[2]); pin(pt[3]); }
                    if constexpr (s < 19 && g < 3) wf[nx][g] = *(const v4u*)(smem + (nx ? w1lane1 : w1lane0) + g * 20480 + ((s + 1) >> 1) * 1024);
                    if constexpr (s < 19 && (g == 3 || g == 4)) wf[nx][g] = *(const v4u*)(smem + (nx ? w1hi1 : w1hi0) + (g - 3) * 20480 + ((s + 1) >> 1) * 1024);
                    if constexpr (g == 5 && s + RING1 < 20) ar[s % RING1][0] = ld128s(rs, xo, (s + RING1) * 1024);
                    if constexpr (g == 2 && s > 0 && s - 1 + RING1 < 20) ar[(s - 1) % RING1][1] = ld128s(rs, xo1, (s - 1 + RING1) * 1024);
                    if constexpr (s == 8 && g == 8) { t1 = __uint_as_float(ps[0].x) + __uint_as_float(ps[1].x); pin(t1); t2 = __uint_as_float(ps[0].y) + __uint_as_float(ps[1].y); pin(t2); }
                    if constexpr (s == 9 && g == 6) { t1 += __uint_as_float(ps[2].x); pin(t1); t2 += __uint_as_float(ps[2].y); pin(t2); }
                    if constexpr (s == 9 && g == 7) { t1 += __uint_as_float(ps[3].x); pin(t1); t2 += __uint_as_float(ps[3].y); pin(t2); }
                    if constexpr (s == 10 && g == 6) { t1 += dpp<0x128>(t1); t2 += dpp<0x128>(t2); pin(t1); pin(t2); }
                    if constexpr (s == 10 && g == 7) { t1 *= (1.0f / D_); pin(t1); t2 *= (1.0f / D_); pin(t2); }
                    if constexpr (s == 10 && g == 8) { t2 = fmaxf(t2 - t1 * t1, 0.f); pin(t2); }
                    if constexpr (s == 11 && g == 6) { rinv = __builtin_amdgcn_rsqf(t2 + 1e-5f); pin(rinv); }
                    if constexpr (s == 11 && g == 8) { mri = -t1 * rinv; pin(mri); }
                    if constexpr (s == 12 && g == 4) *(float*)(smem + l_rs(1, 0) + w * INBOX + lane * 4) = c < 8 ? rinv : mri;
                    if constexpr (s >= 12 && s < 15 && g >= 6 && g < 9 && 3 * (s - 12) + g - 6 < 8) bc2b<3 * (s - 12) + g - 6>(rr_[3 * (s - 12) + g - 6], rm_[3 * (s - 12) + g - 6], rinv, mri);
                    if constexpr (s >= 15 && s < 19 && g >= 5) TT_GAP(5 * (s - 15) + g - 5)
                    site<6000 + 10 * s + g>();
                    SCHED();
                });
            });
            rr[0] = f32x4{rr_[0], rr_[1], rr_[2], rr_[3]}; rr[1] = f32x4{rr_[4], rr_[5], rr_[6], rr_[7]};
            asm volatile("kpf_end2:");
            ACC_FENCE10(acc[0], acc[1]);
            STAMP(13)
        } else {
#pragma unroll
            for (int nb = 0; nb < 5; ++nb) __builtin_amdgcn_raw_buffer_store_b128(__builtin_bit_cast(v4u, xin[nb]), ro, oo + 64 * nb, 0, OUTP);
            STAMP(21)
            STAMP_DUMP()
            asm volatile("kpf_end:");
        }
    };
    layer_body(std::integral_constant<int, 0>{});
    layer_body(std::integral_constant<int, 1>{});
}

extern "C" void kernel_launch(void* const* d_in, const int* in_sizes, int n_in,
                              void* d_out, int out_size, void* d_ws, size_t ws_size,
                              hipStream_t stream) {
    Params p;
    p.x0 = (const u16*)d_in[0];
    p.Lre = (const u16*)d_in[1];
    p.Lim = (const u16*)d_in[2];
    p.Bre = (const u16*)d_in[3];
    p.Bim = (const u16*)d_in[4];
    p.Cre = (const u16*)d_in[5];
    p.Cim = (const u16*)d_in[6];
    p.logdt = (const u16*)d_in[7];
    p.Dskip = (const u16*)d_in[8];
    p.k = (const u16*)d_in[10];
    p.v = (const u16*)d_in[11];
    p.shk = (const u16*)d_in[12];
    p.ssmk = (const u16*)d_in[13];
    p.ssmD = (const u16*)d_in[14];
    p.ws = (char*)d_ws + (size_t)WSOFF * 1048576;
    p.out = (float*)d_out;
    k_main<<<256, 256, 0, stream>>>(p);
}
```

```cpp
#include <hip/hip_runtime.h>
#include <stdint.h>
#include <utility>

#define WSOFF 0
#define STAMPS 0

typedef unsigned short u16;
typedef unsigned long long u64;
typedef unsigned v4u __attribute__((ext_vector_type(4)));
typedef unsigned v2u __attribute__((ext_vector_type(2)));
typedef float f32x4 __attribute__((ext_vector_type(4)));
typedef float f32x2 __attribute__((ext_vector_type(2)));
typedef short s16x4 __attribute__((ext_vector_type(4)));
typedef __bf16 bf16x2v __attribute__((ext_vector_type(2)));
#define LAS __attribute__((address_space(3)))
#define DI __device__ __forceinline__

constexpr int S_ = 2048, D_ = 640;
#define GF 1
#define KG0 8
#define KRT 12
#define KRS 2
#define KSN 0
#define KB1 4
#define KMB 1
#define H3 0
#define KF3 14
#define SBT 2
#define LAG 1
#define KR1 5
#define KR2 12
constexpr int RING1 = KR1;
#define KCR1 3
constexpr int kCR1 = KCR1;
#define KXA 10
#define KN3E 17
#define KN3 6
constexpr int kXA = KXA;
constexpr int kN3E = KN3E, kN3 = KN3;
#define KFHK 4
#define KFHP 14
constexpr int kFHK = KFHK, kFHP = KFHP;
#define KGPI 28
#define KGCI 34
#define KFGI 12
#define G2ND 1
#define KGSI 2
constexpr int kGSI = KGSI, kFGI = KFGI, kGPI = KGPI, kGCI = KGCI;

constexpr unsigned OFF_NONCE = 0;
constexpr unsigned OFF_ST = 4096;
constexpr unsigned OFF_FW = OFF_ST + 65536;
constexpr unsigned OFF_FK = OFF_FW + 131072;
constexpr unsigned OFF_FH = OFF_FK + 81920;
constexpr unsigned OFF_FX = OFF_FH + 262144;
constexpr unsigned OFF_FG = OFF_FX + 131072;
constexpr unsigned OFF_EG = OFF_FG + 98304;
constexpr unsigned OFF_SG = OFF_EG + 327680;
constexpr unsigned OFF_PS = OFF_SG + 524288;
constexpr unsigned OFF_KVT = OFF_PS + 131072;
constexpr unsigned OFF_GT = OFF_KVT + 2621440;
constexpr unsigned OFF_W2F = OFF_GT + 2621440;
constexpr unsigned OFF_X2F = OFF_W2F + 1638400;
constexpr unsigned OFF_DUM = OFF_X2F + 2621440;
constexpr unsigned OFF_HF = OFF_DUM + 163840;
constexpr unsigned WS_BYTES = OFF_HF + 2 * 5242880;
constexpr unsigned kNone = 0x40000000u;
constexpr unsigned OFF_DBG = 0x1800000;
#define I32M 1
#define KVS 1
#define KVU 0
#define MAP 0
#define TILES() const int xq = b & 7, j = b >> 3, mt2 = MAP ? 8 * (xq >> 1) + (j >> 2) : 4 * xq + (j >> 3), nt2 = MAP ? 4 * (xq & 1) + (j & 3) : j & 7, mt1 = mt2 >> 1, \
    nt1 = MAP ? 8 * (xq & 1) + (j & 7) : j & 15, i32 = 16 * (mt2 & 1) + mt1;

constexpr int SC = 102400;
constexpr int REGSZ = 13056;
constexpr int L_XR = SC;
constexpr int L_CO = SC + 12288;
constexpr int L_KB = SC + 51712;
constexpr int L_TAB = SC + 55808;
constexpr int L_CW = SC + 39168;
constexpr int L_FL = L_CW + 320;
constexpr int INBOX = 15360;
constexpr int l_rs(int l, int w) { return l ? SC + w * INBOX : L_KB + w * 256; }
constexpr int l_sg(int l, int w) { return l ? SC + w * INBOX + 256 : SC + (w == 0 ? 43264 : w == 1 ? 46080 : w == 2 ? 52736 : 56320); }
constexpr int l_tb(int l, int w) { return l ? SC + w * INBOX + 2048 : SC + 40960 + w * 5120; }
constexpr int LDS_BYTES = 163840;
constexpr int CV_E = 4608;
constexpr int CV_O = CV_E + (1056 + 15) * 4;

struct Params {
    const u16 *x0, *Lre, *Lim, *Bre, *Bim, *Cre, *Cim, *logdt, *Dskip, *k, *v, *shk, *ssmk, *ssmD;
    char* ws;
    float* out;
};

#define MF "v_mfma_f32_16x16x32_bf16 "
DI void mf(f32x4& c, v4u a, v4u b) { asm(MF "%0, %1, %2, %0" : "+v"(c) : "v"(a), "v"(b)); }
DI void mf0(f32x4& c, v4u a, v4u b) { asm(MF "%0, %1, %2, 0" : "=&v"(c) : "v"(a), "v"(b)); }
constexpr int W2R = 56;
#define WA(n) "a[%c" #n ":%c" #n "+3]"
template <int R> DI void mfa(f32x4& c, v4u a) { asm(MF "%0, " WA(2) ", %1, %0" : "+v"(c) : "v"(a), "i"(W2R + 4 * R)); }
template <int R> DI void mfa0(f32x4& c, v4u a) { asm(MF "%0, " WA(2) ", %1, 0" : "=&v"(c) : "v"(a), "i"(W2R + 4 * R)); }
#define ACC_FENCE10(c, d) asm volatile("" : "+v"(c[0]), "+v"(c[1]), "+v"(c[2]), "+v"(c[3]), "+v"(c[4]), "+v"(d[0]), "+v"(d[1]), "+v"(d[2]), "+v"(d[3]), "+v"(d[4]))
#define ACC_TIE5(c) asm volatile("" : "+v"(c[0]), "+v"(c[1]), "+v"(c[2]), "+v"(c[3]), "+v"(c[4]))
#define QP " quad_perm:[1,0,3,2] row_mask:0xf bank_mask:0xf"
DI void cstep(float (&t)[5], const float (&h)[5], const float (&lr)[5], const float (&ls)[5], float b0, float b1, float b2, float b3, float b4) {
    asm("v_fma_f32 %0, %10, %5, %20\n\tv_fma_f32 %1, %11, %6, %21\n\tv_fma_f32 %2, %12, %7, %22\n\tv_fma_f32 %3, %13, %8, %23\n\tv_fma_f32 %4, %14, %9, %24\n\t"
        "v_fmac_f32_dpp %0, %5, %15" QP "\n\tv_fmac_f32_dpp %1, %6, %16" QP "\n\tv_fmac_f32_dpp %2, %7, %17" QP "\n\tv_fmac_f32_dpp %3, %8, %18" QP "\n\tv_fmac_f32_dpp %4, %9, %19" QP
        : "=&v"(t[0]), "=&v"(t[1]), "=&v"(t[2]), "=&v"(t[3]), "=&v"(t[4])
        : "v"(h[0]), "v"(h[1]), "v"(h[2]), "v"(h[3]), "v"(h[4]), "v"(lr[0]), "v"(lr[1]), "v"(lr[2]), "v"(lr[3]), "v"(lr[4]),
          "v"(ls[0]), "v"(ls[1]), "v"(ls[2]), "v"(ls[3]), "v"(ls[4]), "v"(b0), "v"(b1), "v"(b2), "v"(b3), "v"(b4));
}
DI void cstep3(float* t, const float* h, const float* lr, const float* ls, const float* b) {
    asm("v_fma_f32 %0, %6, %3, %12\n\tv_fma_f32 %1, %7, %4, %13\n\tv_fma_f32 %2, %8, %5, %14\n\t"
        "v_fmac_f32_dpp %0, %3, %9" QP "\n\tv_fmac_f32_dpp %1, %4, %10" QP "\n\tv_fmac_f32_dpp %2, %5, %11" QP
        : "=&v"(t[0]), "=&v"(t[1]), "=&v"(t[2])
        : "v"(h[0]), "v"(h[1]), "v"(h[2]), "v"(lr[0]), "v"(lr[1]), "v"(lr[2]), "v"(ls[0]), "v"(ls[1]), "v"(ls[2]), "v"(b[0]), "v"(b[1]), "v"(b[2]));
}
template <int o> DI void chain24(float (*t)[3], const float (*b)[5], const float* lr, const float* ls) {
    asm("v_fma_f32 %0, %18, %16, %8\n\t" "v_fma_f32 %1, %19, %17, %9\n\t" "v_fmac_f32_dpp %0, %16, %20" QP "\n\t" "v_fmac_f32_dpp %1, %17, %21" QP "\n\t"
        "v_fma_f32 %2, %18, %0, %10\n\t" "v_fma_f32 %3, %19, %1, %11\n\t" "v_fmac_f32_dpp %2, %0, %20" QP "\n\t" "v_fmac_f32_dpp %3, %1, %21" QP "\n\t"
        "v_fma_f32 %4, %18, %2, %12\n\t" "v_fma_f32 %5, %19, %3, %13\n\t" "v_fmac_f32_dpp %4, %2, %20" QP "\n\t" "v_fmac_f32_dpp %5, %3, %21" QP "\n\t"
        "v_fma_f32 %6, %18, %4, %14\n\t" "v_fma_f32 %7, %19, %5, %15\n\t" "v_fmac_f32_dpp %6, %4, %20" QP "\n\t" "v_fmac_f32_dpp %7, %5, %21" QP "\n\t"
        : "=&v"(t[1][0]), "=&v"(t[1][1]), "=&v"(t[2][0]), "=&v"(t[2][1]), "=&v"(t[3][0]), "=&v"(t[3][1]), "=&v"(t[4][0]), "=&v"(t[4][1])
        : "v"(b[0][o + 0]), "v"(b[0][o + 1]), "v"(b[1][o + 0]), "v"(b[1][o + 1]), "v"(b[2][o + 0]), "v"(b[2][o + 1]), "v"(b[3][o + 0]), "v"(b[3][o + 1]), "v"(t[0][0]), "v"(t[0][1]), "v"(lr[o + 0]), "v"(lr[o + 1]), "v"(ls[o + 0]), "v"(ls[o + 1]));
}
template <int o> DI void chain33(float (*t)[3], const float (*b)[5], const float* lr, const float* ls) {
    asm("v_fma_f32 %0, %21, %18, %9\n\t" "v_fma_f32 %1, %22, %19, %10\n\t" "v_fma_f32 %2, %23, %20, %11\n\t" "v_fmac_f32_dpp %0, %18, %24" QP "\n\t" "v_fmac_f32_dpp %1, %19, %25" QP "\n\t" "v_fmac_f32_dpp %2, %20, %26" QP "\n\t"
        "v_fma_f32 %3, %21, %0, %12\n\t" "v_fma_f32 %4, %22, %1, %13\n\t" "v_fma_f32 %5, %23, %2, %14\n\t" "v_fmac_f32_dpp %3, %0, %24" QP "\n\t" "v_fmac_f32_dpp %4, %1, %25" QP "\n\t" "v_fmac_f32_dpp %5, %2, %26" QP "\n\t"
        "v_fma_f32 %6, %21, %3, %15\n\t" "v_fma_f32 %7, %22, %4, %16\n\t" "v_fma_f32 %8, %23, %5, %17\n\t" "v_fmac_f32_dpp %6, %3, %24" QP "\n\t" "v_fmac_f32_dpp %7, %4, %25" QP "\n\t" "v_fmac_f32_dpp %8, %5, %26" QP "\n\t"
        : "=&v"(t[1][0]), "=&v"(t[1][1]), "=&v"(t[1][2]), "=&v"(t[2][0]), "=&v"(t[2][1]), "=&v"(t[2][2]), "=&v"(t[3][0]), "=&v"(t[3][1]), "=&v"(t[3][2])
        : "v"(b[0][o + 0]), "v"(b[0][o + 1]), "v"(b[0][o + 2]), "v"(b[1][o + 0]), "v"(b[1][o + 1]), "v"(b[1][o + 2]), "v"(b[2][o + 0]), "v"(b[2][o + 1]), "v"(b[2][o + 2]), "v"(t[0][0]), "v"(t[0][1]), "v"(t[0][2]), "v"(lr[o + 0]), "v"(lr[o + 1]), "v"(lr[o + 2]), "v"(ls[o + 0]), "v"(ls[o + 1]), "v"(ls[o + 2]));
}
template <int o> DI void chain32(float (*t)[3], const float (*b)[5], const float* lr, const float* ls) {
    asm("v_fma_f32 %0, %15, %12, %6\n\t" "v_fma_f32 %1, %16, %13, %7\n\t" "v_fma_f32 %2, %17, %14, %8\n\t" "v_fmac_f32_dpp %0, %12, %18" QP "\n\t" "v_fmac_f32_dpp %1, %13, %19" QP "\n\t" "v_fmac_f32_dpp %2, %14, %20" QP "\n\t"
        "v_fma_f32 %3, %15, %0, %9\n\t" "v_fma_f32 %4, %16, %1, %10\n\t" "v_fma_f32 %5, %17, %2, %11\n\t" "v_fmac_f32_dpp %3, %0, %18" QP "\n\t" "v_fmac_f32_dpp %4, %1, %19" QP "\n\t" "v_fmac_f32_dpp %5, %2, %20" QP "\n\t"
        : "=&v"(t[1][0]), "=&v"(t[1][1]), "=&v"(t[1][2]), "=&v"(t[2][0]), "=&v"(t[2][1]), "=&v"(t[2][2])
        : "v"(b[0][o + 0]), "v"(b[0][o + 1]), "v"(b[0][o + 2]), "v"(b[1][o + 0]), "v"(b[1][o + 1]), "v"(b[1][o + 2]), "v"(t[0][0]), "v"(t[0][1]), "v"(t[0][2]), "v"(lr[o + 0]), "v"(lr[o + 1]), "v"(lr[o + 2]), "v"(ls[o + 0]), "v"(ls[o + 1]), "v"(ls[o + 2]));
}
DI void pstep(f32x2& xr, f32x2& xs, f32x2 pr, f32x2 ps, f32x2 m) {
    asm("v_pk_mul_f32 %0, %2, %4 op_sel_hi:[1,0]\n\tv_pk_mul_f32 %1, %3, %4 op_sel_hi:[1,0]\n\t"
        "v_pk_fma_f32 %0, %3, %4, %0 op_sel:[0,1,0] neg_lo:[1,0,0] neg_hi:[1,0,0]\n\tv_pk_fma_f32 %1, %2, %4, %1 op_sel:[0,1,0]"
        : "=&v"(xr), "=&v"(xs) : "v"(pr), "v"(ps), "v"(m));
}
DI void padd(f32x2 (&h)[4], const f32x2 (&pr)[2], const f32x2 (&ps)[2], f32x2 c) {
    f32x2 d;
    asm("v_pk_mul_f32 %4, %6, %9 op_sel:[1,0]\n\tv_pk_fma_f32 %0, %5, %9, %0 op_sel_hi:[1,0,1]\n\tv_pk_fma_f32 %1, %6, %9, %1 op_sel_hi:[1,0,1]\n\t"
        "v_pk_fma_f32 %4, %8, %9, %4 op_sel:[1,1,0] op_sel_hi:[1,0,1] neg_hi:[1,0,0]\n\tv_pk_fma_f32 %0, %7, %9, %0 op_sel:[0,1,0]\n\tv_pk_fma_f32 %1, %8, %9, %1 op_sel:[0,1,0]\n\t"
        "v_pk_fma_f32 %2, %5, %4, %2 op_sel_hi:[1,0,1]\n\tv_pk_fma_f32 %3, %6, %4, %3 op_sel_hi:[1,0,1]\n\tv_pk_fma_f32 %2, %7, %4, %2 op_sel:[0,1,0]\n\tv_pk_fma_f32 %3, %8, %4, %3 op_sel:[0,1,0]"
        : "+v"(h[0]), "+v"(h[1]), "+v"(h[2]), "+v"(h[3]), "=&v"(d) : "v"(pr[0]), "v"(pr[1]), "v"(ps[0]), "v"(ps[1]), "v"(c));
}
DI void cstepi(float& b0, float& b1, float& b2, float& b3, float& b4, float h0, float h1, float h2, float h3, float h4, const float (&lr)[5], const float (&ls)[5]) {
    asm("v_fmac_f32 %0, %10, %5\n\tv_fmac_f32 %1, %11, %6\n\tv_fmac_f32 %2, %12, %7\n\tv_fmac_f32 %3, %13, %8\n\tv_fmac_f32 %4, %14, %9\n\t"
        "v_fmac_f32_dpp %0, %5, %15" QP "\n\tv_fmac_f32_dpp %1, %6, %16" QP "\n\tv_fmac_f32_dpp %2, %7, %17" QP "\n\tv_fmac_f32_dpp %3, %8, %18" QP "\n\tv_fmac_f32_dpp %4, %9, %19" QP
        : "+v"(b0), "+v"(b1), "+v"(b2), "+v"(b3), "+v"(b4)
        : "v"(h0), "v"(h1), "v"(h2), "v"(h3), "v"(h4), "v"(lr[0]), "v"(lr[1]), "v"(lr[2]), "v"(lr[3]), "v"(lr[4]), "v"(ls[0]), "v"(ls[1]), "v"(ls[2]), "v"(ls[3]), "v"(ls[4]));
}
#define UNDEF8(c) asm volatile("" : "=v"(c[0]), "=v"(c[1]), "=v"(c[2]), "=v"(c[3]), "=v"(c[4]), "=v"(c[5]), "=v"(c[6]), "=v"(c[7]))
#define PIN5(t) asm volatile("s_nop 1" : "+v"(t[0]), "+v"(t[1]), "+v"(t[2]), "+v"(t[3]), "+v"(t[4]))
DI unsigned cvtpk(float lo, float hi) {
    f32x2 v = {lo, hi};
    return __builtin_bit_cast(unsigned, __builtin_convertvector(v, bf16x2v));
}
DI float lo16f(unsigned u) { return __uint_as_float(u << 16); }
DI float hi16f(unsigned u) { return __uint_as_float(u & 0xffff0000u); }
DI float rbf(float v) { return lo16f(cvtpk(v, 0.f)); }
template <int CTRL> DI float dpp(float x) {
    return __int_as_float(__builtin_amdgcn_update_dpp(0, __float_as_int(x), CTRL, 0xF, 0xF, true));
}
DI float rowsum16(float x) {
    x += dpp<0x128>(x); x += dpp<0x124>(x); x += dpp<0x122>(x); x += dpp<0x121>(x);
    return x;
}
DI float xsum32(float x) { auto r = __builtin_amdgcn_permlane32_swap(__float_as_uint(x), __float_as_uint(x), false, false); return __uint_as_float(r[0]) + __uint_as_float(r[1]); }
DI void xsum2(float& a, float& d) {
    auto r = __builtin_amdgcn_permlane16_swap(__float_as_uint(a), __float_as_uint(d), false, false);
    const float s = __uint_as_float(r[0]) + __uint_as_float(r[1]);
    const float t = xsum32(s);
    auto u = __builtin_amdgcn_permlane16_swap(__float_as_uint(t), __float_as_uint(t), false, false);
    a = __uint_as_float(u[0]); d = __uint_as_float(u[1]);
}
DI float xsum2h(float a, float d) {
    auto r = __builtin_amdgcn_permlane16_swap(__float_as_uint(a), __float_as_uint(d), false, false);
    return xsum32(__uint_as_float(r[0]) + __uint_as_float(r[1]));
}
DI float hsum4(f32x4 v) { return (v.x + v.y) + (v.z + v.w); }
typedef __amdgpu_buffer_rsrc_t rsrc_t;
constexpr int SC1 = 16;
DI v4u ld128(rsrc_t rs, unsigned off) { return __builtin_amdgcn_raw_buffer_load_b128(rs, off, 0, SC1); }
DI v4u ld128s(rsrc_t rs, unsigned off, unsigned soff) { return __builtin_amdgcn_raw_buffer_load_b128(rs, off, soff, SC1); }
DI v2u ld64(rsrc_t rs, unsigned off) { return __builtin_amdgcn_raw_buffer_load_b64(rs, off, 0, SC1); }
DI void st128(v4u v, rsrc_t rs, unsigned off) { __builtin_amdgcn_raw_buffer_store_b128(v, rs, off, 0, SC1); }
DI void st64(v2u v, rsrc_t rs, unsigned off) { __builtin_amdgcn_raw_buffer_store_b64(v, rs, off, 0, SC1); }
#define LGKM0() { __builtin_amdgcn_s_waitcnt(0xC07F); asm volatile("" ::: "memory"); }
#define DRAIN() asm volatile("s_waitcnt vmcnt(0)" ::: "memory")
#define ORDER() __builtin_amdgcn_fence(__ATOMIC_ACQUIRE, "wavefront")
#define SCHED() __builtin_amdgcn_sched_barrier(0)
constexpr unsigned kSpinCap = 1u << 17;
#define WAVE_WAIT(...)                                                      \
    {                                                                       \
        unsigned spins_ = dead ? kSpinCap : 0u;                             \
        _Pragma("nounroll") for (;;) {                                      \
            asm volatile("" ::: "memory");                                  \
            bool ok;                                                        \
            __VA_ARGS__;                                                    \
            if (__builtin_amdgcn_ballot_w64(!ok) == 0ull) break;            \
            if (++spins_ > kSpinCap) { dead = true; break; }                \
            __builtin_amdgcn_s_sleep(1);                                    \
        }                                                                   \
        ORDER();                                                            \
    }
#define KPD1 2
#define KPDS 8
#define KPDS0 2
#define KPF0 12
#define KPF1 4
constexpr int PD1 = KPD1, PDS = KPDS, PDS0 = KPDS0;
#define PL(r) "global_load_dwordx2 " r ", %5, %6 sc1\n\t"
#define PS_(r) "s_waitcnt vmcnt(1)\n\tv_cmp_ne_u64 vcc, %7, " r "\n\ts_cbranch_vccz 2f\n\t" PL(r)
DI void pipe_flag(v2u (&t)[4], unsigned off, const void* base, u64 tag, bool& dead) {
    unsigned n = dead ? 0u : kSpinCap;
    asm volatile("s_sleep %8\n\t" PL("%0") "s_sleep %9\n\t" PL("%1")
                 "1:\n\t" PS_("%0") PS_("%1") "s_sub_u32 %4, %4, 1\n\ts_cbranch_scc0 1b\n2:"
                 : "=&v"(t[0]), "=&v"(t[1]), "=&v"(t[2]), "=&v"(t[3]), "+s"(n) : "v"(off), "s"(base), "s"(tag), "i"(KPF0), "i"(KPF1) : "memory", "vcc", "scc");
    dead = __builtin_amdgcn_readfirstlane(n) == ~0u;
}
#define WAVE_RETRY(OK, ...) { if (__builtin_expect(__builtin_amdgcn_ballot_w64(!(OK)) != 0ull, 0)) WAVE_WAIT({ __VA_ARGS__; ok = OK; }) ORDER(); }
#define STAMP(i)
#define STAMP_DUMP()

#define PADS 9000, 9001, 9002, 9003, 9004, 9005, 9006, 9007, 9008
#define T1POS 27
#define TW 2
#define KLB0 0
#define KLB1 10
#define SKIP0 0
#define SKIP7 0
#define SKIP5 0
#define SKIP4 0
constexpr int kPads[] = {PADS};
constexpr int pad_next(int id) {
    for (unsigned i = 0; i + 1 < sizeof(kPads) / sizeof(int); ++i) if (kPads[i] == id) return kPads[i + 1];
    return -1;
}
template <int ID> DI void site() {
    if constexpr (pad_next(ID) >= 0) asm volatile("kp%c0:\n\ts_cbranch_execz kp%c1" ::"i"(ID), "i"(pad_next(ID)));
    else asm volatile("");
}
template <int T> DI void tour() {
    if constexpr (pad_next(9000 + T) >= 0 && pad_next(9000 + T) != 9001 + T) asm volatile("s_mov_b64 exec, 0\n\ts_branch kp%c0\nkp%c1:\n\ts_mov_b64 exec, -1" ::"i"(pad_next(9000 + T)), "i"(9001 + T));
}
template <int N> DI void skip() { if constexpr (N > 0) asm volatile("s_branch 1f\n\t.rept %c0\n\ts_nop 0\n\t.endr\n1:" ::"i"(N)); }
template <class F, int... I> DI void static_for_impl(std::integer_sequence<int, I...>, F&& f) { (f(std::integral_constant<int, I>{}), ...); }
template <int N, class F> DI void static_for(F&& f) { static_for_impl(std::make_integer_sequence<int, N>{}, f); }
#define IC(n) std::integral_constant<int, (n)>{}
#define LAMBDA(v) [&](auto v) __attribute__((always_inline))
#define PK2 " op_sel:[1,1,0] op_sel_hi:[1,0,1] neg_lo:[1,0,0]"
DI f32x2 cmul(f32x2 a, f32x2 b) {
    f32x2 t;
    asm("v_pk_mul_f32 %0, %1, %2 op_sel_hi:[0,1]\n\ts_nop 0\n\tv_pk_fma_f32 %0, %1, %2, %0" PK2 "\n\ts_nop 0" : "=&v"(t) : "v"(a), "v"(b));
    return t;
}
DI f32x2 cfma(f32x2 a, f32x2 b, f32x2 c) {
    f32x2 t;
    asm("v_pk_fma_f32 %0, %1, %2, %3 op_sel_hi:[0,1,1]\n\ts_nop 0\n\tv_pk_fma_f32 %0, %1, %2, %0" PK2 "\n\ts_nop 0" : "=&v"(t) : "v"(a), "v"(b), "v"(c));
    return t;
}
DI void cfma4(f32x2 (&t)[4], f32x2 a, f32x2 b0, f32x2 b1, f32x2 b2, f32x2 b3, f32x2 c0, f32x2 c1, f32x2 c2, f32x2 c3) {
    asm("v_pk_fma_f32 %0, %4, %5, %9 op_sel_hi:[0,1,1]\n\tv_pk_fma_f32 %1, %4, %6, %10 op_sel_hi:[0,1,1]\n\tv_pk_fma_f32 %2, %4, %7, %11 op_sel_hi:[0,1,1]\n\tv_pk_fma_f32 %3, %4, %8, %12 op_sel_hi:[0,1,1]\n\t"
        "v_pk_fma_f32 %0, %4, %5, %0" PK2 "\n\tv_pk_fma_f32 %1, %4, %6, %1" PK2 "\n\tv_pk_fma_f32 %2, %4, %7, %2" PK2 "\n\tv_pk_fma_f32 %3, %4, %8, %3" PK2 "\n\ts_nop 0"
        : "=&v"(t[0]), "=&v"(t[1]), "=&v"(t[2]), "=&v"(t[3]) : "v"(a), "v"(b0), "v"(b1), "v"(b2), "v"(b3), "v"(c0), "v"(c1), "v"(c2), "v"(c3));
}
DI void gacc2(f32x2& h0, f32x2& h1, f32x2 w0, f32x2 w1, v4u e0, v4u e1) {
    const v2u a0 = {e0.x, e0.y}, b0 = {e0.z, e0.w}, a1 = {e1.x, e1.y}, b1 = {e1.z, e1.w};
    asm("v_pk_fma_f32 %0, %4, %2, %0 op_sel_hi:[0,1,1]\n\tv_pk_fma_f32 %1, %6, %3, %1 op_sel_hi:[0,1,1]\n\t"
        "v_pk_fma_f32 %0, %5, %2, %0 op_sel:[0,1,0] op_sel_hi:[0,0,1] neg_lo:[1,0,0]\n\tv_pk_fma_f32 %1, %7, %3, %1 op_sel:[0,1,0] op_sel_hi:[0,0,1] neg_lo:[1,0,0]"
        : "+v"(h0), "+v"(h1) : "v"(w0), "v"(w1), "v"(a0), "v"(b0), "v"(a1), "v"(b1));
}
DI s16x4 trread(char* smem, int byteoff) {
    return __builtin_amdgcn_ds_read_tr16_b64_v4i16((LAS s16x4*)(smem + byteoff));
}

#define CV_LOAD_A(A, a, rr_, hf) { const unsigned* ap_ = (const unsigned*)(smem + ab[(a) >> 1] + (((a) & 1) ? 0 : 512) + 64 * (7 - (rr_)) + 8 * (hf)); A[a][2 * (hf)] = ap_[0]; A[a][2 * (hf) + 1] = ap_[1]; }
#define CV_LOAD_B(B, e, rr_) { B[e] = *(const v4u*)(smem + bb + 64 * (7 - (rr_)) + 512 * (e)); }
constexpr int cv_pair(int lo, int hi, int n) {
    for (int a = 0; a < 8; ++a)
        for (int e = 0; e < 8; ++e)
            if (a + e >= lo && a + e < hi && n-- == 0) return 8 * a + e;
    return -1;
}
template <int R, int PF, bool OWN, class F, class G>
DI void conv_round(f32x4 (&ca)[8], v4u (&A)[8], const v4u (&B)[8], v4u (&An)[8], v4u (&Bn)[8], const char* smem, const int (&ab)[4], int bb, F&& fill, G&& last) {
    static_for<(R == 7 ? KG0 : 36)>(LAMBDA(g_) {
        constexpr int g = g_.value, a = cv_pair(0, 8, g) / 8, e = cv_pair(0, 8, g) % 8;
        mf(ca[a + e], A[a], B[e]);
        SCHED();
        if constexpr (OWN && g < 12) { CV_LOAD_A(A, 2 + g / 2, R, g & 1) SCHED(); }
        if constexpr (g < 4 * PF && (g & 3) != 3) { if constexpr ((g & 3) == 2) CV_LOAD_B(Bn, g >> 2, R + 1) else CV_LOAD_A(An, g >> 2, R + 1, g & 1) SCHED(); }
        site<3000 + 36 * R + g>();
        if constexpr (R == 7) { last(g_); SCHED(); }
        if constexpr ((g & 3) == 3 && g < 28) { fill(std::integral_constant<int, 7 * R + g / 4>{}); SCHED(); }
    });
}
template <int R0, int R1, class F, class G, class M>
DI void conv_part(f32x4 (&ca)[8], const char* smem, const int (&ab)[4], int bb, F&& fill, G&& last, M&& mid, v4u (&A)[2][8], v4u (&B)[2][8]) {
    if constexpr (R0 > 0) { CV_LOAD_A(A[0], 0, R0, 0) CV_LOAD_A(A[0], 0, R0, 1) SCHED(); }
    static_for<8>(LAMBDA(a_) { constexpr int a = a_.value; CV_LOAD_B(B[0], a, R0) SCHED(); });
    if constexpr (R0 > 0) { CV_LOAD_A(A[0], 1, R0, 0) CV_LOAD_A(A[0], 1, R0, 1) SCHED(); }
    else {
        CV_LOAD_A(A[0], 0, 0, 0) CV_LOAD_A(A[0], 0, 0, 1) SCHED();
        const v4u ad = mid();
        SCHED();
        static_for<8>(LAMBDA(e_) {
            constexpr int e = e_.value;
            mf0(ca[e], ad, B[0][e]); SCHED();
            if constexpr (e < 7) { CV_LOAD_A(A[0], e + 1, 0, 0) CV_LOAD_A(A[0], e + 1, 0, 1) SCHED(); }
        });
    }
    static_for<R1 - R0>(LAMBDA(r_) {
        constexpr int r = r_.value;
        conv_round<R0 + r, (R0 + r + 1 < R1 ? 8 : R0 == 0 && H3 ? 3 : 0), (r == 0 && R0 > 0)>(ca, A[r & 1], B[r & 1], A[(r & 1) ^ 1], B[(r & 1) ^ 1], smem, ab, bb, fill, last);
    });
    if constexpr (R0 == 0 && H3) static_for<6>(LAMBDA(g_) { constexpr int a = cv_pair(0, 3, g_.value) / 8, e = cv_pair(0, 3, g_.value) % 8; mf(ca[a + e], A[R1 & 1][a], B[R1 & 1][e]); SCHED(); });
}
DI void lds_wait3(int a) {
    unsigned x, y, z;
    asm volatile("1:\n\tds_read_b32 %0, %3\n\tds_read_b32 %1, %3 offset:4\n\tds_read_b32 %2, %3 offset:8\n\ts_waitcnt lgkmcnt(0)\n\t"
                 "v_and_b32 %0, %0, %1\n\tv_and_b32 %0, %0, %2\n\tv_cmp_eq_u32 vcc, 0, %0\n\ts_cbranch_vccnz 1b" : "=&v"(x), "=&v"(y), "=&v"(z) : "v"(a) : "memory", "vcc");
}
DI void add2(float& a, float& b, float x, float y) { asm volatile("v_add_f32 %0, %0, %2\n\tv_add_f32 %1, %1, %3" : "+v"(a), "+v"(b) : "v"(x), "v"(y)); }
template <int IMM> DI void gld(v4u& dst, unsigned voff, const void* sbase) {
    asm volatile("global_load_dwordx4 %0, %1, %2 offset:%3" : "=v"(dst) : "v"(voff), "s"(sbase), "i"(IMM));
}
#define OOB 3
#define OUTP 16
#define NTM 31
template <int IMM, int BIT> DI void gldn(v4u& dst, unsigned voff, const void* sbase) {
    if constexpr ((NTM >> BIT) & 1) asm volatile("global_load_dwordx4 %0, %1, %2 offset:%3 nt" : "=v"(dst) : "v"(voff), "s"(sbase), "i"(IMM));
    else gld<IMM>(dst, voff, sbase);
}
DI void gldu16(unsigned& dst, unsigned voff, const void* sbase) { asm volatile("global_load_ushort %0, %1, %2" : "=v"(dst) : "v"(voff), "s"(sbase)); }
template <int IMM> DI void gld64(v4u& dst, const void* a) {
    if constexpr ((NTM >> 5) & 1) asm volatile("global_load_dwordx4 %0, %1, off offset:%2 nt" : "=v"(dst) : "v"(a), "i"(IMM));
    else asm volatile("global_load_dwordx4 %0, %1, off offset:%2" : "=v"(dst) : "v"(a), "i"(IMM));
}
template <int V> DI void SETM0(unsigned ldsbase) { asm volatile("s_add_u32 m0, %0, %1" ::"s"(ldsbase), "i"(V)); }
template <int GOFF> DI void GLD0(unsigned voff, const char* sbase) { asm volatile("global_load_lds_dwordx4 %0, %1 offset:%2" ::"v"(voff), "s"(sbase), "i"(GOFF) : "memory"); }
template <int GOFF, int V> DI void GLD1(unsigned voff, const char* sbase, unsigned ldsbase) {
    asm volatile("global_load_lds_dwordx4 %0, %1 offset:%3\n\ts_add_u32 m0, %2, %4" ::"v"(voff), "s"(sbase), "s"(ldsbase), "i"(GOFF), "i"(V) : "memory");
}
constexpr int dma_goff(int it) { return it < 40 ? (it >> 2) * 128 : ((it - 100) / 5) * 256; }
constexpr int dma_loff(int it) { return it < 40 ? L_XR + ((it >> 2) % 3) * 4096 + (it & 3) * 1024 : ((it - 100) % 5) * 20480 + ((it - 100) / 5) * 2048; }
#define KVF 1
#define KFD0 10
#define DSE 1
#define KNF 13
#define KFS 6
#define APOS 0
#define PSA 0
#define CWP 1
#define SAF 2
#define KD0 36
#define KDS 3
#define KNW 11
constexpr int kD0 = KD0, kDS = KDS;
static_assert(kD0 + 26 * kDS < 117, "");
#define KW2S 17
#define KW2M 0x4A
#define KFWP 14
constexpr int kW2S = KW2S, kW2M = KW2M;
constexpr int NW2P = (20 - kW2S) * __builtin_popcount(kW2M);
constexpr int P1_PRE = 34;
constexpr int p1_pre(int i) { return i < 5 ? 100 + i : i < 13 ? i - 5 : i < 18 ? 105 + (i - 13) : i < 22 ? 8 + (i - 18) : i < 27 ? 110 + (i - 22) : 400; }
constexpr int p1_item(int s, int g) {
    if (s == 7 && g == 0) return 324;
    if (g == 9) return s < 10 ? 115 + s : s == 11 ? 202 : s == 12 ? 203 : -1;
    if (s == KFWP && g == 1) return 208;
    if (s >= kW2S && ((kW2M >> g) & 1)) return 500 + (s - kW2S) * __builtin_popcount(kW2M) + __builtin_popcount(kW2M & ((1 << g) - 1));
    if ((s & 1) == 0 && s <= 12) return g >= 5 ? 4 * (s / 2 + 3) + (g - 5) : -1;
    if (s == 14 && g == 5) return 207;
    return -1;
}
constexpr int p1_cnt(int s, int g) {
    int n = P1_PRE;
    for (int t = 0; t <= s; ++t)
        for (int u = 0; u < 10; ++u) {
            if (t == s && u >= g) break;
            if (p1_item(t, u) >= 0 && p1_item(t, u) != 319) ++n;
        }
    return n;
}
constexpr int p1_pos(int code) {
    for (int i = 0; i < P1_PRE; ++i) if (p1_pre(i) == code) return i;
    for (int t = 0; t < 20; ++t)
        for (int u = 0; u < 10; ++u) if (p1_item(t, u) == code) return p1_cnt(t, u);
    return -1000;
}
constexpr int p1_cap(int s, int g) {
    int u = (s < 19 && g < 5) + (g == 9 && s < 19);
    if ((s & 1) && s < 19 && g >= 5 && g < 9) u += 1 + (g == 5);
    if ((s & 1) == 0 && g == 4) u += 1;
    const int it = p1_item(s, g), nt = g < 9 ? p1_item(s, g + 1) : -1;
    u += (it >= 0 && it < 500 && it != 208) + (nt >= 0 && nt < 200);
    if ((s == 18 && g == 1) || (s == 19 && g == 2) || (g == 9 && ((s & 3) == 2 || s == 17))) u += 2;
    return u < 2 ? 2 - u : 0;
}
constexpr int p1_slot(int s0, int s, int g, int gmax) {
    int n = 0;
    for (int t = s0; t <= s; ++t)
        for (int u = 0; u < gmax && !(t == s && u >= g); ++u) n += p1_cap(t, u);
    return n;
}
constexpr int p1_lim(int n) { return n > 63 ? 63 : n; }
template <int IMM> DI void gld4s(v4u& dst, unsigned voff, const void* sbase) { asm volatile("global_load_dwordx4 %0, %1, %2 offset:%3 sc1" : "=v"(dst) : "v"(voff), "s"(sbase), "i"(IMM)); }
template <int IMM> DI void gld2s(v2u& dst, unsigned voff, const void* sbase) { asm volatile("global_load_dwordx2 %0, %1, %2 offset:%3 sc1" : "=v"(dst) : "v"(voff), "s"(sbase), "i"(IMM)); }
template <class T> DI void pin(T& v) { asm volatile("" : "+v"(v)); }
DI void mul2(float& a, float& b, float x, float y, float s) { asm volatile("v_mul_f32 %0, %2, %4\n\tv_mul_f32 %1, %3, %4" : "=&v"(a), "=&v"(b) : "v"(x), "v"(y), "v"(s)); }
DI void xor2(float& a, float& b, unsigned x, unsigned y, unsigned s) { asm volatile("v_xor_b32 %0, %2, %4\n\tv_xor_b32 %1, %3, %4" : "=&v"(a), "=&v"(b) : "v"(x), "v"(y), "v"(s)); }
DI void rot2(unsigned& a, unsigned& b, unsigned x, unsigned y) { asm volatile("v_alignbit_b32 %0, %2, %2, 16\n\tv_alignbit_b32 %1, %3, %3, 16" : "=&v"(a), "=&v"(b) : "v"(x), "v"(y)); }
DI void mix2(unsigned& a, unsigned& b, unsigned x, unsigned y, unsigned z) { asm volatile("v_bfi_b32 %0, %5, %2, %3\n\tv_bfi_b32 %1, %5, %3, %4" : "=&v"(a), "=&v"(b) : "v"(x), "v"(y), "v"(z), "s"(0xffffu)); }
template <int I> DI void shr1(unsigned& o, unsigned prev, unsigned x) {
    if constexpr (I > 0) asm volatile("v_mov_b32_dpp %0, %1 wave_ror:1 row_mask:0xf bank_mask:0xf\n\tv_mov_b32_dpp %0, %2 wave_shr:1 row_mask:0xf bank_mask:0xf" : "=&v"(o) : "v"(prev), "v"(x));
    else asm volatile("v_mov_b32_dpp %0, %1 wave_shr:1 row_mask:0xf bank_mask:0xf bound_ctrl:0" : "=&v"(o) : "v"(x));
}
template <int G> DI void bc2(float& a, float& b, unsigned x) {
    asm volatile("v_mov_b32_dpp %0, %2 row_newbcast:%3 row_mask:0xf bank_mask:0xf\n\tv_mov_b32_dpp %1, %2 row_newbcast:%4 row_mask:0xf bank_mask:0xf" : "=&v"(a), "=&v"(b) : "v"(x), "i"(G), "i"(G + 8));
}
DI void rot1(unsigned& a, unsigned x) { asm volatile("v_alignbit_b32 %0, %1, %1, 16" : "=v"(a) : "v"(x)); }
DI void mix1(unsigned& a, unsigned x, unsigned y) { asm volatile("v_bfi_b32 %0, %3, %1, %2" : "=v"(a) : "v"(x), "v"(y), "s"(0xffffu)); }
DI void ror1(unsigned& o, unsigned prev) { asm volatile("v_mov_b32_dpp %0, %1 wave_ror:1 row_mask:0xf bank_mask:0xf" : "=v"(o) : "v"(prev)); }
DI void shr1b(unsigned& o, unsigned x) { asm volatile("v_mov_b32_dpp %0, %1 wave_shr:1 row_mask:0xf bank_mask:0xf" : "+v"(o) : "v"(x)); }
DI void xor1(float& a, unsigned x, unsigned s) { asm volatile("v_xor_b32 %0, %1, %2" : "=v"(a) : "v"(x), "v"(s)); }
template <int G> DI void bc1(float& a, unsigned x) { asm volatile("v_mov_b32_dpp %0, %1 row_newbcast:%2 row_mask:0xf bank_mask:0xf" : "=v"(a) : "v"(x), "i"(G)); }
template <int G> DI void mulb(float& o, unsigned x, float y) { asm volatile("v_mul_f32_dpp %0, %1, %2 row_newbcast:%3 row_mask:0xf bank_mask:0xf" : "=v"(o) : "v"(x), "v"(y), "i"(G)); }
template <int N> DI void lgkm() { __builtin_amdgcn_s_waitcnt(0xC07F | (N << 8)); asm volatile("" ::: "memory"); }
DI void unp2(float& a, float& b, unsigned x) { asm volatile("v_lshlrev_b32 %0, 16, %2\n\tv_and_b32 %1, 0xffff0000, %2" : "=&v"(a), "=&v"(b) : "v"(x)); }
#define GL(a) "global_load_dwordx4 a[" #a ":" #a "+3], %7, %8 sc1\n\tglobal_load_dwordx4 a[" #a "+4:" #a "+7], %7, %8 offset:256 sc1\n\t"
#define AR(d, a, o) "v_accvgpr_read_b32 " d ", a[" #a "+" #o "]\n\t"
#define GS_(a) "s_waitcnt vmcnt(4)\n\t" AR("%2", a, 1) AR("%3", a, 3) AR("%4", a, 5) AR("%5", a, 7) AR("%0", a, 0) AR("%1", a, 2)                              \
    "v_xor_b32 %2, %9, %2\n\tv_bitop3_b32 %4, %4, %9, %2 bitop3:0xbe\n\tv_bitop3_b32 %4, %3, %9, %4 bitop3:0xbe\n\tv_bitop3_b32 %4, %5, %9, %4 bitop3:0xbe\n\t" \
    AR("%2", a, 4) AR("%3", a, 6) "v_cmp_ne_u32 vcc, 0, %4\n\ts_cbranch_vccz 2f\n\t" GL(a)
template <int D0> DI void pipe_gran(float& a, float& d, unsigned off, const void* base, unsigned tag, bool& dead) {
    unsigned n = dead ? 0u : kSpinCap, y, z;
    float a1, d1;
    asm volatile("s_sleep %10\n\t" GL(32) "s_sleep %11\n\t" GL(40) "s_sleep %11\n\t" GL(48)
                 "1:\n\t" GS_(32) GS_(40) GS_(48) "s_sub_u32 %6, %6, 1\n\ts_cbranch_scc0 1b\n2:"
                 : "=&v"(a), "=&v"(d), "=&v"(a1), "=&v"(d1), "=&v"(y), "=&v"(z), "+s"(n) : "v"(off), "s"(base), "s"(tag), "i"(D0), "i"(PD1) : "memory", "vcc", "scc");
    dead = __builtin_amdgcn_readfirstlane(n) == ~0u;
    a += a1; d += d1;
}
DI void fma2(float& a, float& b, float x, float y, float s, float t, float u, float v) {
    asm volatile("v_fma_f32 %0, %2, %4, %6\n\tv_fma_f32 %1, %3, %5, %7" : "=&v"(a), "=&v"(b) : "v"(x), "v"(y), "v"(s), "v"(t), "v"(u), "v"(v));
}
DI void cvt2(unsigned& a, unsigned& b, f32x4 v) { asm volatile("v_cvt_pk_bf16_f32 %0, %2, %3\n\tv_cvt_pk_bf16_f32 %1, %4, %5" : "=&v"(a), "=&v"(b) : "v"(v.x), "v"(v.y), "v"(v.z), "v"(v.w)); }
DI void swp2n(unsigned& a, unsigned& b, unsigned& c, unsigned& d) { asm volatile("v_permlane16_swap_b32 %0, %1\n\tv_permlane16_swap_b32 %2, %3" : "+v"(a), "+v"(b), "+v"(c), "+v"(d)); }
DI void swp2(unsigned& a, unsigned& b, unsigned& c, unsigned& d) { asm volatile("s_nop 1\n\tv_permlane16_swap_b32 %0, %1\n\tv_permlane16_swap_b32 %2, %3" : "+v"(a), "+v"(b), "+v"(c), "+v"(d)); }
template <int G> DI void bc2b(float& a, float& b, float x, float y) {
    asm volatile("v_mov_b32_dpp %0, %2 row_newbcast:%4 row_mask:0xf bank_mask:0xf\n\tv_mov_b32_dpp %1, %3 row_newbcast:%4 row_mask:0xf bank_mask:0xf" : "=&v"(a), "=&v"(b) : "v"(x), "v"(y), "i"(G));
}
template <class T> DI void pins(T& v) { asm volatile("" : "+s"(v)); }
template <int I> DI void gld_w2(unsigned voff, const void* sbase) {
    asm volatile("global_load_dwordx4 " WA(2) ", %0, %1 offset:%3 sc1" ::"v"(voff), "s"(sbase), "i"(W2R + 4 * I), "i"((I % 5) * 1024 - 2048));
}
DI void gld2t(v2u& dst, unsigned voff, const void* sbase) { asm volatile("global_load_dwordx2 %0, %1, %2 sc1" : "+v"(dst) : "v"(voff), "s"(sbase)); }
DI void wait2(v2u& v, v4u (&x)[3]) { asm volatile("s_waitcnt vmcnt(0)" : "+v"(v), "+v"(x[0]), "+v"(x[1]), "+v"(x[2])::"memory"); }
template <int IMM> DI void gld4p(v4u& dst, unsigned voff, const void* sbase) { asm volatile("global_load_dwordx4 %0, %1, %2 offset:%3" : "+v"(dst) : "v"(voff), "s"(sbase), "i"(IMM)); }
DI void undef4(v4u (&x)[4]) { asm volatile("" : "=v"(x[0]), "=v"(x[1]), "=v"(x[2]), "=v"(x[3])); }
DI void wait1(v2u& v) { asm volatile("s_waitcnt vmcnt(0)" : "+v"(v)::"memory"); }
template <int N> DI void wait_fkn(v2u& v, v2u& u, unsigned& x) { asm volatile("s_waitcnt vmcnt(%3)" : "+v"(v), "+v"(u), "+v"(x) : "i"(N) : "memory"); }
DI void wait_fk(v2u& v, v2u& u, unsigned& x) { asm volatile("s_waitcnt vmcnt(0)" : "+v"(v), "+v"(u), "+v"(x)::"memory"); }
DI void ld_ur(unsigned o, const void* base) {
    asm volatile("global_load_dwordx4 a[40:40+3], %0, %1 sc1\n\tglobal_load_dwordx4 a[44:44+3], %0, %1 offset:1024 sc1\n\t"
                 "global_load_dwordx4 a[48:48+3], %0, %1 offset:2048 sc1\n\tglobal_load_dwordx4 a[52:52+3], %0, %1 offset:3072 sc1" ::"v"(o), "s"(base));
}
DI void st_ur(unsigned a) {
    asm volatile("s_waitcnt vmcnt(%1)\n\tds_write_b128 %0, a[40:40+3] offset:480\n\tds_write_b128 %0, a[44:44+3] offset:1504\n\t"
                 "ds_write_b128 %0, a[48:48+3] offset:2528\n\tds_write_b128 %0, a[52:52+3] offset:3552" ::"v"(a), "i"(2 * __builtin_popcount(kW2M)) : "memory");
}
DI void wait4(v4u (&x)[4]) { asm volatile("s_waitcnt vmcnt(0)" : "+v"(x[0]), "+v"(x[1]), "+v"(x[2]), "+v"(x[3])::"memory"); }
template <int A, int IMM> DI void lda4(unsigned o, const void* base) { asm volatile("global_load_dwordx4 a[%c2:%c2+3], %0, %1 offset:%3" ::"v"(o), "s"(base), "i"(A), "i"(IMM)); }
template <int A, int IMM> DI void lda2(unsigned o, const void* base) { asm volatile("global_load_dwordx2 a[%c2:%c2+1], %0, %1 offset:%3" ::"v"(o), "s"(base), "i"(A), "i"(IMM)); }
template <int A> DI void rda(unsigned& v) { asm volatile("v_accvgpr_read_b32 %0, a[%c1+0]" : "=v"(v) : "i"(A)); }
DI void poll1(unsigned a, const void* base) { asm volatile("global_load_dwordx2 a[36:36+1], %0, %1 sc1" ::"v"(a), "s"(base)); }
DI void poll2(unsigned a, unsigned b, const void* base) { asm volatile("global_load_dwordx2 a[36:36+1], %0, %2 sc1\n\tglobal_load_dwordx2 a[36+2:36+3], %1, %2 sc1" ::"v"(a), "v"(b), "s"(base)); }
DI void touch_(unsigned a, const void* base) { asm volatile("global_load_dword a[32+1], %0, %1" ::"v"(a), "s"(base)); }
template <int N> DI void wait_st(v2u& a, v4u& b) { asm volatile("s_waitcnt vmcnt(%2)" : "+v"(a), "+v"(b) : "i"(N) : "memory"); }
DI void undef3(v4u (&x)[3]) { asm volatile("" : "=v"(x[0]), "=v"(x[1]), "=v"(x[2])); }
template <int IMM> DI void gld4t(v4u& dst, unsigned voff, const void* sbase) { asm volatile("global_load_dwordx4 %0, %1, %2 offset:%3 sc1" : "+v"(dst) : "v"(voff), "s"(sbase), "i"(IMM)); }
template <int N> DI void vm_wait() { asm volatile("s_waitcnt vmcnt(%0)" ::"i"(N) : "memory"); }
DI float dot2a(unsigned a, unsigned b, float c) {
    return __builtin_amdgcn_fdot2_f32_bf16(__builtin_bit_cast(bf16x2v, a), __builtin_bit_cast(bf16x2v, b), c, false);
}
DI void disc(unsigned l0, unsigned l1, unsigned l2, f32x2& lam, f32x2& co) {
    const float dt = rbf(__builtin_amdgcn_exp2f(lo16f(l2) * 1.44269504088896f));
    const float lre = lo16f(l0), lim = lo16f(l1);
    const float e_ = __builtin_amdgcn_exp2f(dt * lre * 1.44269504088896f);
    const float rv = __builtin_amdgcn_fractf(dt * lim * 0.159154943091895f);
    lam = f32x2{e_ * __builtin_amdgcn_cosf(rv), e_ * __builtin_amdgcn_sinf(rv)};
    const float nr = lam.x - 1.0f, ni = lam.y, id = __builtin_amdgcn_rcpf(lre * lre + lim * lim);
    co = f32x2{(nr * lre + ni * lim) * id, (ni * lre - nr * lim) * id};
}
DI f32x4 unpk4(v2u u) { return f32x4{lo16f(u.x), hi16f(u.x), lo16f(u.y), hi16f(u.y)}; }
#define D2 "v_dot2_f32_bf16 "
#define CP "v_cvt_pk_bf16_f32 "
DI void d2x4(unsigned* o, unsigned a0, unsigned a1, unsigned a2, unsigned a3, unsigned b0, unsigned b1, unsigned b2, unsigned b3) {
    asm volatile(D2 "%0, %4, %8, 0\n\t" D2 "%1, %5, %9, 0\n\t" D2 "%2, %6, %10, 0\n\t" D2 "%3, %7, %11, 0"
                 : "=&v"(o[0]), "=&v"(o[1]), "=&v"(o[2]), "=&v"(o[3]) : "v"(a0), "v"(a1), "v"(a2), "v"(a3), "v"(b0), "v"(b1), "v"(b2), "v"(b3));
}
DI void cpx4(unsigned* o, unsigned a0, unsigned a1, unsigned a2, unsigned a3, unsigned b0, unsigned b1, unsigned b2, unsigned b3) {
    asm volatile(CP "%0, %4, %8\n\t" CP "%1, %5, %9\n\t" CP "%2, %6, %10\n\t" CP "%3, %7, %11"
                 : "=&v"(o[0]), "=&v"(o[1]), "=&v"(o[2]), "=&v"(o[3]) : "v"(a0), "v"(a1), "v"(a2), "v"(a3), "v"(b0), "v"(b1), "v"(b2), "v"(b3));
}
DI void cpx4i(unsigned* t, const unsigned* ka) {
    asm volatile(CP "%0, %0, %4\n\t" CP "%1, %1, %5\n\t" CP "%2, %2, %6\n\t" CP "%3, %3, %7"
                 : "+v"(t[0]), "+v"(t[1]), "+v"(t[2]), "+v"(t[3]) : "v"(ka[0]), "v"(ka[1]), "v"(ka[2]), "v"(ka[3]));
}
template <int IMM> DI void bld4(v4u& dst, unsigned voff, v4u rsrc) {
    if constexpr ((NTM >> 4) & 1) asm volatile("buffer_load_dwordx4 %0, %1, %2, 0 offen offset:%3 nt" : "=v"(dst) : "v"(voff), "s"(rsrc), "i"(IMM));
    else asm volatile("buffer_load_dwordx4 %0, %1, %2, 0 offen offset:%3" : "=v"(dst) : "v"(voff), "s"(rsrc), "i"(IMM));
}

__global__ __launch_bounds__(256, 1) __attribute__((aligned(4096))) void k_main(Params p) {
    __shared__ __attribute__((aligned(16))) char smem[LDS_BYTES];
    const rsrc_t rs = __builtin_amdgcn_make_buffer_rsrc((void*)p.ws, 0, WS_BYTES, 0x00020000);
    const rsrc_t ro = __builtin_amdgcn_make_buffer_rsrc((void*)p.out, 0, S_ * D_ * 4, 0x00020000);
    const int w0 = __builtin_amdgcn_readfirstlane(threadIdx.x >> 6), b0 = blockIdx.x;
    bool dead = false;
    asm volatile("" ::: "a255");
    float SW[5];
    f32x4 xin[5];
    v2u gt[5], dsk[5];
    f32x4 acc[2][5];
    f32x4 rr[2];
    float tt[2][5][4];
    float lr[5], ls[5], lqr[5], lqs[5];
    f32x2 l8, l32, l128;
    bool kearly;
    unsigned sdq;
    int sgn_, ab[4], bb, zpa, uwa, spa, tbw, tbr, cwa, spl, dst[3];
    unsigned sgo, sgs, glo, gso, fgo, fgo1;
    int srca;
    unsigned adm, dso, w2o, wro, fwo, xso, fho1, fho, hso, eo, es, ho[4];
    bool cv0, three;
    int mt1_;
    auto addrs = [&](auto layer_, int lane, int w, int b) __attribute__((always_inline)) {
        constexpr int layer = layer_.value;
        const int c = lane & 15, q = lane >> 4;
        TILES()
        static_assert(l_sg(0, 0) == SC + 0xA900 && l_sg(0, 1) == SC + 0xB400 && l_sg(0, 2) == SC + 0xCE00 && l_sg(0, 3) == SC + 0xDC00, "");
        sgn_ = (layer ? l_sg(1, 0) + w * INBOX : SC + (int)((0xDC00CE00B400A900ull >> (16 * w)) & 0xffff)) + q * 320 + c * 4;
        const int pl = lane < 40 ? lane : 39, tbb = l_tb(layer, 0) + w * (layer ? INBOX : 5120);
        spl = SC + pl * 8;
        eo = OFF_EG + (unsigned)(layer * 163840 + nt1 * 640 + pl * 16);
        es = eo + (unsigned)(mt1 * 10240);
        spa = l_rs(layer, 0) + (2 * (mt2 & 1) + (w >> 1)) * (layer ? INBOX : 256) + (2 * (w & 1) + (c >> 3)) * 64 + (c & 7) * 4;
        tbw = tbb + c * 64 + ((q ^ ((c >> 1) & 3)) * 16);
        tbr = tbb + (2 * (c >> 2) + (q & 1)) * 64 + (((2 * (q >> 1) + ((c >> 1) & 1)) ^ (c >> 2)) * 16) + (c & 1) * 8;
        hso = OFF_HF + (unsigned)(layer * 5242880 + ((8 * mt1 + 2 * w + (q >> 1)) * 160 + 10 * nt1 + 5 * (q & 1)) * 256 + c * 16);
        cwa = L_CW + c * 4;
        fho1 = lane ? kNone : OFF_FH + (unsigned)((((layer * 32 + 2 * mt1 + (w >> 1)) * 4 + (nt1 >> 2)) * 8 + (nt1 & 3) * 2 + (w & 1)) * 128);
        fho = OFF_FH + (unsigned)(((layer * 32 + mt2) * 4 + w) * 1024 + (lane & 7) * 128);
        pin(sgn_); pin(spl); pin(eo); pin(es); pin(spa); pin(tbw); pin(tbr); pin(hso); pin(cwa); pin(fho1); pin(fho);
        const unsigned hb_ = OFF_HF + (unsigned)(layer * 5242880 + (4 * mt2 * 160 + 40 * w) * 256);
#pragma unroll
        for (int m = 0; m < 4; ++m) { ho[m] = hb_ + (unsigned)(((m + 1 + w) & 3) * 40960 + lane * 16); pin(ho[m]); }
#pragma unroll
        for (int m = 0; m < 3; ++m) { dst[m] = SC + ((w + 1 + m) & 3) * INBOX + m * 5120 + lane * 16; pin(dst[m]); }
        mt1_ = mt1;
        sgo = OFF_SG + (unsigned)(((layer * 32 + mt2) * 4 + w) * 2048 + c * 16);
        sgs = q < 2 ? sgo + (unsigned)(nt2 * 256 + q * 8) : kNone;
        sgo += (unsigned)(q * 512);
        pin(sgo); pin(sgs);
        if constexpr (layer == 0) {
            srca = SC + w * INBOX + lane * 16;
            glo = OFF_GT + (unsigned)((80 * nt2 + (lane >> 1)) * 4096 + (64 * mt2 + 16 * w) * 2 + (lane & 1) * 16);
            gso = cv0 ? OFF_GT + (unsigned)((80 * nt2 + i32 + 32 * w) * 4096 + (q & 1) * 512 + c * 32 + (q >> 1) * 16) : kNone;
            fgo = OFF_FG + (unsigned)((nt2 * 96 + (lane & 31)) * 128);
            fgo1 = w == 0 ? OFF_FG + (unsigned)((nt2 * 96 + i32) * 128) : kNone;
            pin(srca); pin(glo); pin(gso); pin(fgo); pin(fgo1);
            const int r0 = SC + (w < 3 ? w : 0) * REGSZ, y0 = 2031 + 8 * q - c, l34 = lane < 33 ? lane : 33;
#pragma unroll
            for (int i = 0; i < 4; ++i) { ab[i] = r0 + ((y0 & 1) ? CV_O : CV_E) + ((y0 + 1) >> 1) * 4 - 448 - 512 - 1024 * i; pin(ab[i]); }
            bb = r0 + 32 * c + 16 * q;
            zpa = w < 3 ? r0 + l34 * 16 + (l34 >= 30 ? CV_E + 4096 - 480 : 0) + (l34 >= 32 ? CV_O + 4100 - 32 - CV_E - 4096 : 0) : SC + 47360 + lane * 16;
            uwa = r0 + lane * 16;
            xso = (unsigned)((64 * mt2 + 16 * w + c) * D_ + 80 * nt2 + 16 * (q & 1) + 8 * (q >> 1)) * 2u;
            dso = (unsigned)(160 * nt2 + 8 * q);
            adm = (unsigned)((c & 1) * 16 + (q == 2 + (c >> 3) ? 256 << ((c & 7) >> 1) : 0));
            if (GF) pin(adm);
            pin(bb); pin(zpa); pin(uwa); pin(xso); if (DSE) pin(dso);
        }
        SCHED();
    };
    unsigned tag0, tag1;
#define FLAG(off) { if (lane == 0) st64(v2u{tag0, tag1}, rs, off); }
#define ISTAG(f) (((f.x ^ tag0) | (f.y ^ tag1)) == 0u)
#define KW_REQ() { if (cv) ld_ur(wro, p.ws + OFF_KVT); static_for<NW2P>(LAMBDA(i_) { gld_w2<i_.value>(w2o, p.ws + 2048 + (i_.value / 5) * 5120); }); }
#define FW_OK() { if (__builtin_expect(!kearly, 0)) { WAVE_WAIT({ const v2u fw_ = ld64(rs, fwo); ok = ISTAG(fw_); }) KW_REQ() } }
#define TT_GAP(n) { constexpr int mb_ = (n) / 10, nb_ = ((n) % 10) / 2, e_ = ((n) & 1) * 2; \
        mul2(tt[mb_][nb_][e_], tt[mb_][nb_][e_ + 1], rm_[4 * mb_ + e_], rm_[4 * mb_ + e_ + 1], SW[nb_]); }
    {
        const int tid = threadIdx.x, lane = tid & 63, w = w0, b = b0;
        const int c = lane & 15, q = lane >> 4;
        TILES()
        STAMP(0)
        const unsigned lds0 = (unsigned)(size_t)(LAS char*)smem;
        u64 nonce;
        {
            u64 pc;
            int len, len2;
            asm volatile("s_getpc_b64 %0\nkpf_begin:\n\ts_mov_b32 %1, kpf_end-kpf_begin\n\ts_mov_b32 %2, kpf_end2-kpf_begin" : "=s"(pc), "=s"(len), "=s"(len2));
            len = len > len2 ? len : len2;
            const int o0 = (((j & 15) * 4 + w) + 64 * (lane >> 3)) * 128;
            touch_((unsigned)(o0 < len ? o0 : 0), (const void*)pc);
        }
        const bool cv = w < 2 || (w == 2 && i32 < 16);
        const int chn = 80 * nt2 + i32 + 32 * (cv ? w : 0);
        const bool kw = KVS ? w < 2 || (w == 2 && i32 >= 16) : cv;
        const int kp = 16 * nt2 + mt1, ku = 5 * kp + (i32 < 16 ? w & 1 : 2 + (w < 3 ? w : 0)), kc = ku >> 6, kr = ku & 63;
        const int lc = cv ? lane : 8;
        int kwi = 2 + (i32 >> 4) - w;
        v4u ax[15], sa[5], cr[2], wr[4];
        unsigned uL[3];
        const int cu_ = (b < 200 ? b : 0) * 4 + w, cdb = cu_ / 20, ccg = cu_ % 20;
        {
            int ca_ = 32 * ccg, cb_ = 40 * nt1 + 10 * w - 32;
            asm volatile("" : "+s"(ca_), "+s"(cb_), "+s"(kwi));
            unsigned cpo = (unsigned)((lane < 32 ? ca_ : cb_) + (lane < 41 ? lane : 41)) * 2u;
            unsigned kso = (unsigned)(64 * kc + 8 * (lane & 7)) * 2u, ko = (unsigned)(32 * kr + 4 * (lane >> 3)) * 1280u + kso, kh = ko - 3840u;
            kso *= 4u;
            pin(cpo); pin(kso); pin(ko); pin(kh);
            SCHED();
            gldu16(uL[0], cpo, p.Lre); gldu16(uL[1], cpo, p.Lim); gldu16(uL[2], cpo, p.logdt);
            const v4u rk = {(unsigned)(u64)p.k, (unsigned)((u64)p.k >> 32) & 0xffffu, S_ * D_ * 2u, 0x00020000u};
#define L4(d, v, s_, o, f) "global_load_dwordx4 %" #d ", %" #v ", %" #s_ " offset:" #o f "\n\t"
#define B4(d, o) "buffer_load_dwordx4 %" #d ", %17, %21, 0 offen offset:" #o " nt\n\t"
            asm volatile("s_cmp_lt_i32 %22, 1\n\ts_cbranch_scc1 Lrole2\n\t"
                         L4(3, 15, 18, 0, " nt") L4(4, 15, 18, 1280, " nt") L4(5, 15, 18, 2560, " nt") L4(6, 15, 18, 3840, " nt")
                         L4(7, 15, 20, 0, " nt") L4(8, 15, 20, 1280, " nt") L4(9, 15, 20, 2560, " nt") L4(10, 15, 20, 3840, " nt")
                         B4(0, 0) B4(1, 1280) B4(2, 2560) L4(11, 16, 19, 0, "") L4(12, 16, 19, 16, "") L4(13, 16, 19, 32, "") L4(14, 16, 19, 48, "")
                         "\nLrole2:"
                         : "=&v"(ax[0]), "=&v"(ax[1]), "=&v"(ax[2]), "=&v"(ax[3]), "=&v"(ax[4]), "=&v"(ax[5]), "=&v"(ax[6]), "=&v"(ax[7]), "=&v"(ax[8]), "=&v"(ax[9]),
                           "=&v"(ax[10]), "=&v"(ax[11]), "=&v"(ax[12]), "=&v"(ax[13]), "=&v"(ax[14])
                         : "v"(ko), "v"(kso), "v"(kh), "s"(p.k), "s"(p.shk), "s"(p.v), "s"(rk), "s"(kwi) : "scc");
            asm volatile("s_load_dwordx2 %0, %1, 0x0" : "=s"(nonce) : "s"(p.ws + OFF_NONCE));
            SCHED();
        }
        const char* ssrc;
        {
            u64 pa = (u64)(p.x0 + (128 * mt1 + 8 * nt1 + 2 * w) * D_), pb = (u64)(((mt1 & 1) ? p.Bim : p.Bre) + (40 * nt1 + (mt1 >> 1)) * D_);
            asm volatile("" : "+s"(pa), "+s"(pb));
            const int nbx = 2 * w + q - 2, r = q < 2 ? q : 8 * (nbx < 5 ? nbx : 4);
            ssrc = (const char*)(q < 2 ? pa : pb) + (unsigned)(r * 1280 + c * 16);
            pin(ssrc);
        }
        if (SAF == 0) static_for<5>(LAMBDA(i) { gld64<256 * i.value>(sa[i.value], ssrc); });
        SCHED();
        if (w == TW && !kw) tour<0>();
        STAMP(22)
        const int row8 = lane >> 3;
        const unsigned wvo = (unsigned)(40 * nt1 + row8) * 1280u + (unsigned)(((lane & 7) ^ ((w & 1) | ((row8 >> 1) << 1))) * 16 + (w >> 1) * 128);
        const char* const wbase = (const char*)((w & 1) ? p.Bim : p.Bre);
        const unsigned wl = lds0 + (unsigned)(((w & 1) * 10 + (w >> 1)) * 1024);
        const unsigned xvo0 = (unsigned)(8 * (row8 >> 1) + 2 * (row8 & 1)) * 1280u + (unsigned)(((lane & 7) ^ ((row8 >> 1) << 1)) * 16);
        const unsigned xvo1 = (xvo0 ^ 16u) + 1280u;
        const char* const xbase = (const char*)p.x0 + (size_t)(128 * mt1 + 32 * w) * 1280;
        const unsigned xl = lds0 + (unsigned)(w * REGSZ);
        unsigned xv[4] = {xvo0, xvo1, xvo0 + 5120u, xvo1 + 5120u}, wv[5];
#pragma unroll
        for (int i = 0; i < 5; ++i) { wv[i] = wvo + i * 10240u; pin(wv[i]); if (i < 4) pin(xv[i]); }
#define DMA_M0(it) SETM0<dma_loff(it) - dma_goff(it)>((it) < 40 ? xl : wl)
#define DMA_LD1(it, nx_) GLD1<dma_goff(it), dma_loff(nx_) - dma_goff(nx_)>((it) < 40 ? xv[(it) & 3] : wv[(it) < 40 ? 0 : ((it) - 100) % 5], (it) < 40 ? xbase : wbase, (nx_) < 40 ? xl : wl)
#define DMA_LD(it) GLD0<dma_goff(it)>((it) < 40 ? xv[(it) & 3] : wv[(it) < 40 ? 0 : ((it) - 100) % 5], (it) < 40 ? xbase : wbase)
        const int swz = (c & 1) | ((c >> 2) << 1);
        int w1lane0 = (c & 1) * 10240 + (c >> 1) * 128 + ((q ^ swz) * 16);
        int w1lane1 = (c & 1) * 10240 + (c >> 1) * 128 + (((4 + q) ^ swz) * 16);
        int xlane0 = L_XR + w * REGSZ + (c & 1) * 1024 + (c >> 1) * 128 + ((q ^ swz) * 16);
        int xlane1 = L_XR + w * REGSZ + (c & 1) * 1024 + (c >> 1) * 128 + (((4 + q) ^ swz) * 16);
        unsigned cvo = (unsigned)((16 * cdb + c) * D_ + 32 * ccg + 8 * q) * 2u, for0, for1;
        {
            const int G = 4 * ccg + q, ch = 10 * (G / 5) + G % 5, ci = ch + 5;
            const unsigned fo = OFF_W2F + (unsigned)(((cdb / 5) * 200 + cdb % 5) * 1024 + c * 16);
            for0 = fo + (unsigned)((ch >> 2) * 5120 + (ch & 3) * 256); for1 = fo + (unsigned)((ci >> 2) * 5120 + (ci & 3) * 256);
        }
        unsigned sto = OFF_ST + (unsigned)((mt1 * 16 + 4 * w + q) * 256 + c * 8), swo = OFF_ST + (unsigned)(((lane >> 2) * 16 + nt1) * 256 + 128 + (lane & 3) * 16);
        unsigned kvo = OFF_KVT + (unsigned)((64 * kc + 8 * (lane >> 3) + ((lane >> 2) & 1)) * 4096 + (32 * kr + 8 * (lane & 3)) * 2);
        int kbw = L_KB + w * 1024 + (lane & 7) * 128 + (((lane >> 3) * 8) ^ ((lane & 7) * 16));
        int kbr = L_KB + w * 1024 + (lane >> 3) * 128 + (((lane & 7) * 16) ^ ((lane >> 3) * 16));
        const unsigned dum = (OOB & 1) ? kNone : OFF_DUM + (unsigned)((b * 4 + w) * 128 + (lane & 7) * 16);
        if (!kw) kvo = dum;
        if (b >= 200) { for0 = dum; for1 = dum; cvo = (unsigned)((b * 4 + w) * 128); }
        wro = (unsigned)(chn * 4096 + lc * 16);
        cv0 = cv; three = i32 < 16;
        pin(kvo); pin(kbw); pin(kbr);
        int w1hi0 = w1lane0 + 61440, w1hi1 = w1lane1 + 61440;
        pin(w1hi0); pin(w1hi1);
        pin(w1lane0); pin(w1lane1); pin(xlane0); pin(xlane1); pin(cvo); pin(for0); pin(for1); pin(sto); pin(swo); pin(wro);
        SCHED();
        auto pre = LAMBDA(i_) {
            constexpr int it = p1_pre(i_.value);
            SCHED();
            DMA_M0(it);
            asm volatile("s_nop 0");
            DMA_LD(it);
            SCHED();
        };
        {
            int f0 = 2 * ((chn & ~63) / 5), f1 = 2 * ((chn | 63) / 5) + 1, nk = cv ? 28 : 0;
            asm volatile("" : "+s"(f0), "+s"(f1), "+s"(nk));
            const int l10 = lane - 32 < 0 || lane - 32 > 9 ? 0 : lane - 32;
            fwo = lane < nk ? OFF_FK + (unsigned)(f0 + lane < f1 ? f0 + lane : f1) * 128u : OFF_FW + (unsigned)(25 * nt2 + w + 5 * (l10 >> 1) + (l10 & 1)) * 128u;
        }
        w2o = OFF_W2F + (unsigned)((nt2 * 40 + 10 * w) * 5120 + lane * 16);
        int ime = SC + (w < 3 ? w * REGSZ + CV_E : 43264) + 1008 - 16 * lane, imo = SC + (w < 3 ? w * REGSZ + CV_O + 4 : 47360) + 1008 - 16 * lane, im3 = w < 3 ? ime + 3072 : imo;
        int tca = L_TAB + (c >> 1) * 136, tcb = tca + 8 + 32 * w + 8 * q, tcl = L_TAB + (lane < 40 ? lane : 39) * 136, coa = L_CO + w * REGSZ + q * 64, rsa = l_rs(0, 0) + w * 256 + lane * 4;
        unsigned sgn = (c & 1) ? 0u : 0x80000000u;
        unsigned fko1 = w ? kNone : lane < 32 ? OFF_FK + (unsigned)((2 * kp + (i32 >> 4)) * 128) : OFF_FW + (unsigned)(b * 128);
        pin(fwo); pin(w2o); pin(fko1); pin(ime); pin(imo); pin(im3); pin(tca); pin(tcb); pin(tcl); pin(coa); pin(rsa); pin(sgn);
        SCHED();
        auto cw_ = [&]() __attribute__((always_inline)) {
            gldn<0, 2>(cr[0], cvo, p.Cre); gldn<0, 2>(cr[1], cvo, p.Cim);
            static_for<4>(LAMBDA(i) { gldn<i.value * 1024, 1>(wr[i.value], wro, p.ssmk); });
        };
        if (SAF == 1) static_for<5>(LAMBDA(i) { gld64<256 * i.value>(sa[i.value], ssrc); });
        if (CWP == 0) cw_();
        STAMP(33)
        static_assert(SAF == 2 && CWP == 1, "");
        touch_(0u, p.Lre);
        asm volatile("s_waitcnt vmcnt(16) lgkmcnt(0)\n\ts_cmp_gt_i32 %3, 0\n\ts_cbranch_scc1 Lrole3\n\ts_waitcnt vmcnt(1)\nLrole3:" : "+v"(uL[0]), "+v"(uL[1]), "+v"(uL[2]) : "s"(kwi) : "memory", "scc");
        pins(nonce);
        ++nonce;
        tag0 = (unsigned)nonce; tag1 = (unsigned)(nonce >> 32);
        pins(tag0); pins(tag1);
        {
            f32x2 lam, co, lp[16];
            disc(uL[0], uL[1], uL[2], lam, co);
            if (lane < 32) *(f32x2*)(smem + L_CO + w * REGSZ + lane * 8) = 2.0f * co;
            lp[0] = cmul(lam, lam); lp[0] = cmul(lp[0], lp[0]); lp[1] = cmul(lp[0], lp[0]);
            lp[0] = f32x2{1.f, 0.f};
            lp[2] = cmul(lp[1], lp[1]); lp[3] = cmul(lp[2], lp[1]); lp[4] = cmul(lp[2], lp[2]);
            const f32x2 z2 = {0.f, 0.f};
            f32x2 t4[4];
            cfma4(t4, lp[4], lp[1], lp[2], lp[3], lp[4], z2, z2, z2, z2);
            lp[5] = t4[0]; lp[6] = t4[1]; lp[7] = t4[2]; lp[8] = t4[3];
            cfma4(t4, lp[8], lp[1], lp[2], lp[3], lp[4], z2, z2, z2, z2);
            lp[9] = t4[0]; lp[10] = t4[1]; lp[11] = t4[2]; lp[12] = t4[3];
            cfma4(t4, lp[8], lp[5], lp[6], lp[7], lp[7], z2, z2, z2, z2);
            lp[13] = t4[0]; lp[14] = t4[1]; lp[15] = t4[2];
            if (lane >= 32 && lane < 42) {
                f32x2* tl = (f32x2*)(smem + L_TAB + (10 * w + lane - 32) * 136);
                tl[0] = lam;
#pragma unroll
                for (int i = 0; i < 16; ++i) tl[1 + i] = lp[i];
            }
        }
        SCHED();
        STAMP(35)
        if (SAF == 2) static_for<5>(LAMBDA(i) { gld64<256 * i.value>(sa[i.value], ssrc); });
        if (CWP) cw_();
        if (APOS == 0) addrs(std::integral_constant<int, 0>{}, lane, w, b);
        v4u kvt[4];
        unsigned ka[8], kt[8], se[8], vs[8], ko4[4], one2 = 0x3F803F80u, kq[12];
        float kf_[8];
        pin(one2);
        auto kvf = LAMBDA(n_) {
            constexpr int n = n_.value, h = n / 5, u = n % 5;
            if constexpr (u == 0) {
#pragma unroll
                for (int r = 0; r < 6; ++r) { kq[r] = __builtin_amdgcn_perm(ax[r][h], ax[r + 1][h], 0x05040100u); kq[6 + r] = __builtin_amdgcn_perm(ax[r][h], ax[r + 1][h], 0x07060302u); }
                if constexpr (h > 0) kvt[h - 1] = *(const v4u*)(smem + kbr);
            } else if constexpr (u == 1) {
                unsigned t[8];
                d2x4(t, kq[0], kq[1], kq[2], kq[3], ax[11 + h][1], ax[11 + h][1], ax[11 + h][1], ax[11 + h][1]);
                d2x4(t + 4, kq[6], kq[7], kq[8], kq[9], ax[11 + h][3], ax[11 + h][3], ax[11 + h][3], ax[11 + h][3]);
#pragma unroll
                for (int r = 0; r < 8; ++r) kf_[r] = __uint_as_float(t[r]);
            } else if constexpr (u == 2) {
#pragma unroll
                for (int r = 0; r < 8; ++r) kf_[r] = dot2a(kq[r + 2 * (r >> 2) + 2], ax[11 + h][2 * (r >> 2)], kf_[r]);
            } else if constexpr (u == 3) {
#pragma unroll
                for (int r = 0; r < 4; ++r) { kq[r] = ax[7 + r][h] << 16; kq[4 + r] = ax[7 + r][h] & 0xffff0000u; pin(kq[r]); pin(kq[4 + r]); }
            } else {
#pragma unroll
                for (int r = 0; r < 8; ++r) kf_[r] *= __uint_as_float(kq[r]);
                *(v2u*)(smem + kbw) = v2u{cvtpk(kf_[0], kf_[1]), cvtpk(kf_[2], kf_[3])};
                *(v2u*)(smem + (kbw ^ 64)) = v2u{cvtpk(kf_[4], kf_[5]), cvtpk(kf_[6], kf_[7])};
                if constexpr (h == 3) kvt[3] = *(const v4u*)(smem + kbr);
            }
            SCHED();
        };
        auto kvop = LAMBDA(n_) {
            constexpr int n = n_.value, h = n ? (n - 1) / 29 : -1, u = n ? (n - 1) % 29 : 26;
            if constexpr (u < 2) d2x4(ka + 4 * u, ax[0][h], ax[1][h], ax[2][h], ax[3][h], se[u], se[u], se[u], se[u]);
            else if constexpr (u == 8) { const v4u sk = ax[11 + h]; se[4] = sk[0] >> 16; se[5] = sk[2] & 0xffff0000u; se[6] = sk[0] & 0xffffu; se[7] = sk[2] << 16; pin(se[4]); pin(se[5]); pin(se[6]); pin(se[7]); }
            else if constexpr (u < 21) {
                constexpr int v = u < 8 ? u - 2 : u - 3, tp = 1 + v / 6, st = v % 6, e = st & 1;
                if constexpr (st < 2) d2x4(kt + 4 * e, ax[tp][h], ax[tp + 1][h], ax[tp + 2][h], ax[tp + 3][h], se[2 * tp + e], se[2 * tp + e], se[2 * tp + e], se[2 * tp + e]);
                else if constexpr (st < 4) cpx4i(kt + 4 * e, ka + 4 * e);
                else d2x4(ka + 4 * e, kt[4 * e], kt[4 * e + 1], kt[4 * e + 2], kt[4 * e + 3], one2, one2, one2, one2);
            }
            else if constexpr (u == 21) { vs[0] = ax[7][h] & 0xffffu; vs[1] = ax[8][h] << 16; vs[2] = ax[9][h] & 0xffffu; vs[3] = ax[10][h] << 16; pin(vs[0]); pin(vs[1]); pin(vs[2]); pin(vs[3]); }
            else if constexpr (u == 22) { vs[4] = ax[7][h] >> 16; vs[5] = ax[8][h] & 0xffff0000u; vs[6] = ax[9][h] >> 16; vs[7] = ax[10][h] & 0xffff0000u; pin(vs[4]); pin(vs[5]); pin(vs[6]); pin(vs[7]); }
            else if constexpr (u == 23) cpx4(ko4, ka[0], ka[2], ka[4], ka[6], ka[1], ka[3], ka[5], ka[7]);
            else if constexpr (u < 26) { constexpr int e = u - 24; d2x4(kt + 4 * e, ko4[2 * e], ko4[2 * e], ko4[2 * e + 1], ko4[2 * e + 1], vs[4 * e], vs[4 * e + 1], vs[4 * e + 2], vs[4 * e + 3]); }
            else if constexpr (u == 26) {
                if constexpr (h < 3) { const v4u sk = ax[12 + h]; se[0] = sk[1] >> 16; se[1] = sk[3] & 0xffff0000u; se[2] = sk[1] & 0xffffu; se[3] = sk[3] << 16; pin(se[0]); pin(se[1]); pin(se[2]); pin(se[3]); }
                else asm volatile("s_nop 2");
            }
            else if constexpr (u == 27) {
                cpx4(ko4, kt[0], kt[2], kt[4], kt[6], kt[1], kt[3], kt[5], kt[7]);
                *(v2u*)(smem + kbw) = v2u{ko4[0], ko4[1]};
                *(v2u*)(smem + (kbw ^ 64)) = v2u{ko4[2], ko4[3]};
            }
            else kvt[h] = *(const v4u*)(smem + kbr);
            SCHED();
        };
        asm volatile("s_waitcnt vmcnt(%15)" : "+v"(ax[0]), "+v"(ax[1]), "+v"(ax[2]), "+v"(ax[3]), "+v"(ax[4]), "+v"(ax[5]), "+v"(ax[6]), "+v"(ax[7]), "+v"(ax[8]), "+v"(ax[9]),
                     "+v"(ax[10]), "+v"(ax[11]), "+v"(ax[12]), "+v"(ax[13]), "+v"(ax[14]) : "i"(11) : "memory");
        STAMP(30)
        unsigned tch = 0u;
        constexpr int NF = KVF ? KNF : 27;
        if (kw) {
            if constexpr (KVF) {
                static_for<20>(LAMBDA(n_) {
                    constexpr int n = n_.value, DN = 20 - KFD0;
                    site<1000 + n>(); SCHED();
                    kvf(n_);
                    if constexpr (n >= KFD0) static_for<(n - KFD0 + 1) * NF / (DN ? DN : 1) - (n - KFD0) * NF / (DN ? DN : 1)>(LAMBDA(i_) { pre(std::integral_constant<int, (n - KFD0) * NF / (DN ? DN : 1) + i_.value>{}); });
                });
                if constexpr (KFD0 == 20) static_for<NF>(pre);
            } else
            static_for<117>(LAMBDA(n_) {
                constexpr int n = n_.value;
                site<1000 + n>(); SCHED();
                kvop(n_);
                if constexpr (n >= kD0 && (n - kD0) % kDS == 0 && (n - kD0) / kDS < 27) { pre(std::integral_constant<int, ((n - kD0) / kDS + 270) % 27>{}); SCHED(); }
            });
        } else {
            if (w == 3) {
            const int l = lane, l3 = l & 3;
            unsigned a = OFF_SG + (unsigned)((((((l >> 3) & 1) * 32 + mt2) * 4 + ((l >> 1) & 3)) * 8 + nt2) * 256 + (l & 1) * 128);
            a = l >= 16 ? OFF_EG + (unsigned)((l >= 21 ? 163840 - 640 : 0) + (mt1 * 16 + nt1) * 640 + (l < 26 ? l - 16 : 5) * 128) : a;
            a = l >= 28 ? OFF_PS + (unsigned)(((mt2 * 4 + l3) * 8 + nt2) * 128) : a;
            a = l >= 32 ? OFF_FH + (unsigned)((((((l >> 2) & 1) * 32 + 2 * mt1 + (l3 >> 1)) * 4 + (nt1 >> 2)) * 8 + (nt1 & 3) * 2 + (l3 & 1)) * 128) : a;
            a = l >= 40 ? OFF_FX + (unsigned)(((mt1 * 4 + 2 * (mt2 & 1) + (l3 >> 1)) * 16 + nt2 * 2 + (l3 & 1)) * 128) : a;
            a = l >= 48 ? OFF_FG + (unsigned)((nt2 * 96 + i32) * 128) : a;
            const unsigned big = (l & 7) < 2 ? OFF_HF + (unsigned)((l & 1) * 5242880 + (8 * mt1 * 160 + 10 * nt1) * 256) : (l & 7) == 2 ? OFF_X2F + (unsigned)(2 * mt2 * 40960 + 2560 * nt2)
                               : (l & 7) < 5 ? OFF_KVT + (unsigned)((l & 1) * 2621440 + (80 * nt2 + i32) * 4096) : OFF_W2F + (unsigned)(nt2 * 204800 + ((l & 7) - 5) * 98304);
            a = l >= 56 ? big : a;
            tch = a;
        }
            vm_wait<KNW>();
            static_for<NF>(LAMBDA(i_) { if constexpr (i_.value == T1POS) { if (w == TW) tour<1>(); } pre(i_); });
            if (T1POS == 27 && w == TW) tour<1>();
            skip<SKIP0>();
            undef4(kvt);
        }
        if constexpr (NF == 27) {
#pragma unroll
            for (int i = 0; i < 4; ++i) st128(kvt[i], rs, kvo + i * 8192);
        }
        SCHED();
        STAMP(34)
        if (APOS == 1) addrs(std::integral_constant<int, 0>{}, lane, w, b);
        v4u orr, oi;
        v2u gr;
        unsigned gro;
        {
            asm volatile("s_waitcnt vmcnt(%7)" : "+v"(cr[0]), "+v"(cr[1]), "+v"(sa[0]), "+v"(sa[1]), "+v"(sa[2]), "+v"(sa[3]), "+v"(sa[4]) : "i"(NF == 27 ? 35 : 4 + NF) : "memory");
#pragma unroll
            for (int e = 0; e < 4; ++e) {
                const f32x4 co = *(const f32x4*)(smem + coa + e * 16);
                const f32x2 o0 = lo16f(cr[0][e]) * f32x2{co.x, -co.y} - lo16f(cr[1][e]) * f32x2{co.y, co.x};
                const f32x2 o1 = hi16f(cr[0][e]) * f32x2{co.z, -co.w} - hi16f(cr[1][e]) * f32x2{co.w, co.z};
                orr[e] = cvtpk(o0.x, o1.x); oi[e] = cvtpk(o0.y, o1.y);
            }
            if constexpr (NF == 27) { st128(orr, rs, for0); st128(oi, rs, for1); } else { pin(orr); pin(oi); }
            SCHED();
        }
        static_for<(27 - NF) / 3>(LAMBDA(i_) { pre(std::integral_constant<int, NF + i_.value>{}); });
        {
            float s1[2] = {0.f, 0.f}, s2[2] = {0.f, 0.f};
#pragma unroll
            for (int i = 0; i < 5; ++i)
#pragma unroll
                for (int e = 0; e < 4; ++e) { s1[e & 1] = dot2a(sa[i][e], one2, s1[e & 1]); s2[e & 1] = dot2a(sa[i][e], sa[i][e], s2[e & 1]); }
            const float a1 = rowsum16(s1[0] + s1[1]), a2 = rowsum16(s2[0] + s2[1]);
            const float mean = a1 * (1.0f / D_);
            const float var = fmaxf(a2 * (1.0f / D_) - mean * mean, 0.f);
            const float ri = __builtin_amdgcn_rcpf(rbf(__builtin_amdgcn_sqrtf(rbf(rbf(var) + 1e-5f))));
            gr = v2u{__float_as_uint(q >= 2 ? a1 : c ? -ri * rbf(mean) : ri), tag0};
            gro = c < 2 ? OFF_ST + (unsigned)((mt1 * 16 + nt1) * 256 + ((q < 2 ? 8 * c : 14) + 2 * w + q) * 8) : kNone;
            pin(gr); pin(gro);
        }
        SCHED();
        STAMP(59)
        if (APOS == 2) addrs(std::integral_constant<int, 0>{}, lane, w, b);
        if constexpr (NF < 27) {
            static_for<27 - NF - (27 - NF) / 3>(LAMBDA(i_) { pre(std::integral_constant<int, NF + (27 - NF) / 3 + i_.value>{}); });
#pragma unroll
            for (int i = 0; i < 4; ++i) st128(kvt[i], rs, kvo + i * 8192);
            st128(orr, rs, for0); st128(oi, rs, for1);
        }
        st64(gr, rs, gro);
        SCHED();
        STAMP(23)
        if (w == 3) tour<7>();
        asm volatile("s_waitcnt vmcnt(%4) lgkmcnt(0)" : "+v"(wr[0]), "+v"(wr[1]), "+v"(wr[2]), "+v"(wr[3]) : "i"(P1_PRE - 9) : "memory");
        __builtin_amdgcn_s_barrier();
        asm volatile("" ::: "memory");
        STAMP(24)
        v4u wf[2][5], af[2][2][2];
#pragma unroll
        for (int nb = 0; nb < 5; ++nb) wf[0][nb] = *(const v4u*)(smem + w1lane0 + nb * 20480 + (0) * 2048);
#pragma unroll
        for (int i = 0; i < 4; ++i) af[0][i >> 1][i & 1] = *(const v4u*)(smem + ((i >> 1) ? xlane1 : xlane0) + (i & 1) * 2048);
        SCHED();
        const char *kvtb = p.ws + OFF_KVT, *w2b0 = p.ws + 2048;
        pins(kvtb); pins(w2b0);
        v2u sg, tc[10], fw, tg2 = {tag0, tag1};
        pin(tg2);
        v4u sw0;
        unsigned ie[4][4], io[4][4], iw, zq[4];
        float rr_[8];
        auto kstep = LAMBDA(s_) {
            constexpr int s = s_.value, ph = s >> 2, s4 = s & 3, cu = s & 1, nx = cu ^ 1, jj = s >> 1, pp = jj & 1;
            if constexpr (s4 == 3 && ph < 4) {
                constexpr int pw = p1_pos(104 + 5 * (ph + 1)), px = p1_pos(4 * (jj + 1) + 3), pm = pw > px ? pw : px;
                constexpr int vm = p1_lim(p1_cnt(s, 0) - 1 - pm);
                asm volatile("" ::: "memory");
                __builtin_amdgcn_s_waitcnt((vm & 15) | ((vm >> 4) << 14) | 0x70);
                __builtin_amdgcn_s_barrier();
                asm volatile("" ::: "memory");
                STAMP(25 + ph)
                SCHED();
            }
            if constexpr (s == 15) {
                wait_st<p1_cnt(s, 0) - 1 - p1_pos(203)>(sg, sw0);
                WAVE_RETRY((((sg.y ^ tag0) | (sw0.y ^ tag0) | (sw0.w ^ tag0)) == 0u), sg = ld64(rs, sto); sw0 = ld128(rs, swo))
                STAMP(7)
            }
            if constexpr (s == kW2S) { vm_wait<p1_cnt(s, 0) - 1 - p1_pos(208)>(); SCHED(); }
            if constexpr (s == 18) {
                vm_wait<p1_cnt(s, 0) - 1 - (p1_pos(207) > p1_pos(208) ? p1_pos(207) : p1_pos(208))>();
                pin(fw); pin(sdq);
                kearly = __builtin_amdgcn_ballot_w64(!ISTAG(fw)) == 0ull;
                ORDER();
                if (kearly && cv) ld_ur(wro, kvtb);
                SCHED();
            }
            if constexpr (s4 != 3 || ph == 4) lgkm<(cu == 0 && s > 0 ? 3 : s == 17 ? 4 : s == 19 ? 2 : 0)>();
            SCHED();
            static_for<10>(LAMBDA(g_) {
                constexpr int g = g_.value, mb = g / 5, k = g % 5, it = p1_item(s, g), nt_ = g < 9 ? p1_item(s, g + 1) : -1;
                constexpr bool d0 = it >= 0 && it < 200, d1 = nt_ >= 0 && nt_ < 200;
                if constexpr (s == 0) mf0(acc[mb][k], af[pp][cu][mb], wf[cu][k]); else mf(acc[mb][k], af[pp][cu][mb], wf[cu][k]);
                SCHED();
                if constexpr (s < 19) {
                    if constexpr (g < 5) wf[nx][g] = *(const v4u*)(smem + (g < 3 ? (nx ? w1lane1 : w1lane0) : (nx ? w1hi1 : w1hi0)) + (g < 3 ? g : g - 3) * 20480 + ((s + 1) >> 1) * 1024);
                    if constexpr (cu == 1 && g == 5) vm_wait<p1_lim(p1_cnt(s, 5) - 1 - p1_pos(4 * (jj + 1) + 3))>();
                    if constexpr (cu == 1 && g >= 5 && g < 9)
                        af[pp ^ 1][(g - 5) >> 1][(g - 5) & 1] = *(const v4u*)(smem + (((g - 5) >> 1) ? xlane1 : xlane0) + ((jj + 1) % 3) * 4096 + ((g - 5) & 1) * 2048);
                }
                if constexpr (cu == 0 && g == 4 && s > 0) lgkm<(s == 8 || s == 10 || s == 14 ? 8 : 5)>();
                static_for<p1_cap(s, g)>(LAMBDA(e_) {
                    constexpr int na = p1_slot(0, s, g, 10) + e_.value, i = (na / 10) & 3, u = na % 10;
                    static_assert(p1_slot(0, 8, 0, 10) >= 40, "");
                    if constexpr (na < 40) {
                        if constexpr (u == 0) { if constexpr (i > 0) ror1(iw, wr[i - 1].w); else { iw = 0u; pin(iw); } }
                        else if constexpr (u < 5) rot1(ie[i][u - 1], wr[i][4 - u]);
                        else if constexpr (u == 5) shr1b(iw, wr[i].w);
                        else if constexpr (u < 9) mix1(io[i][u - 6], wr[i][9 - u], wr[i][8 - u]);
                        else mix1(io[i][3], wr[i].x, iw);
                    }
                    constexpr int nb = p1_slot(8, s, g, 3) + e_.value;
                    if constexpr (s >= 8 && s < 12 && g < 3) {
                        if constexpr (nb < 10) tc[nb] = *(const v2u*)(smem + (nb < 5 ? tca : tcb) + (nb % 5) * 1088);
                        else if constexpr (nb == 10) { const f32x2* tp = (const f32x2*)(smem + tcl); l8 = tp[2]; l32 = tp[5]; }
                        else if constexpr (nb == 11) l128 = *(const f32x2*)(smem + tcl + 72);
                        static_assert(p1_slot(8, 12, 0, 3) == 12, "");
                    }
                    constexpr int nc = p1_slot(12, s, g, 10) + e_.value;
                    if constexpr (s >= 12 && s < 14 && nc < 10) {
                        if constexpr (nc < 5) { lr[nc] = __uint_as_float(tc[nc].x); xor1(ls[nc], tc[nc].y, sgn); }
                        else { lqr[nc - 5] = __uint_as_float(tc[nc].x); xor1(lqs[nc - 5], tc[nc].y, sgn); }
                        static_assert(p1_slot(12, 14, 0, 10) >= 10, "");
                    }
                    if constexpr (s == 15 && g < 5) SW[g] = __int_as_float(__builtin_amdgcn_ds_bpermute(16 * c + 4 * (g >> 1), (g & 1) ? sw0.z : sw0.x));
                    constexpr bool img = (s == 16 && g >= 5 && g < 9 && e_.value == 0) || (s == 18 && (g == 5 || g == 6) && e_.value == 0);
                    constexpr int nd = p1_slot(15, s, g, 10) + e_.value - 5 - (s > 16 ? 4 : s == 16 && g >= 5 ? (g < 9 ? g - 4 : 4) : 0) - (s > 18 ? 2 : s == 18 && g >= 5 ? (g == 5 ? 1 : 2) : 0);
                    if constexpr (s >= 15 && nd >= 0 && !img) {
                        if constexpr (nd < 4) bc1<nd>(rr_[nd], sg.x);
                        else if constexpr (nd < 44) mulb<8 + (nd - 4) % 8>(tt[(nd - 4) % 8 / 4][(nd - 4) / 8][(nd - 4) % 4], sg.x, SW[(nd - 4) / 8]);
                        else if constexpr (nd < 48) bc1<nd - 40>(rr_[nd - 40], sg.x);
                        static_assert(p1_slot(15, 16, 0, 10) <= 9 && p1_slot(15, 20, 0, 10) >= 59, "");
                    }
                });
                if constexpr (s == 14 && g < 3) *(v4u*)(smem + ime + 1024 * g) = v4u{ie[3 - g][0], ie[3 - g][1], ie[3 - g][2], ie[3 - g][3]};
                if constexpr (s == 16 && g >= 5 && g < 9) *(v4u*)(smem + imo + 1024 * (8 - g)) = v4u{io[g - 5][0], io[g - 5][1], io[g - 5][2], io[g - 5][3]};
                if constexpr (s == 18 && g == 5) *(v4u*)(smem + im3) = v4u{ie[0][0], ie[0][1], ie[0][2], ie[0][3]};
                if constexpr (s == 18 && g == 6) *(unsigned*)(smem + rsa) = sg.x;
                if constexpr (s == 14 && (g == 6 || g == 8)) { zq[g - 6] = 0u; zq[g - 5] = 0u; pin(zq[g - 6]); pin(zq[g - 5]); }
                if constexpr (s == 19 && g == 0) *(v4u*)(smem + zpa) = v4u{zq[0], zq[1], zq[2], zq[3]};
                if constexpr (s == 18 && g == 1) touch_(tch, p.ws);
                if constexpr (s == 19 && g == 2) { if (__builtin_expect(!kearly, 0)) poll1(fwo, p.ws); }
                if constexpr (it >= 0) {
                    if constexpr (d0 && d1) DMA_LD1(it, d1 ? nt_ : 0);
                    else if constexpr (d0) DMA_LD(it);
                    else if constexpr (it == 202) gld2s<0>(sg, sto, p.ws);
                    else if constexpr (it == 203) gld4s<0>(sw0, swo, p.ws);
                    else if constexpr (it == 207) gldu16(sdq, (unsigned)chn * 2u, p.ssmD);
                    else if constexpr (it == 208) gld2s<0>(fw, fwo, p.ws);
                    else if constexpr (it >= 500) gld_w2<it % 50>(w2o, w2b0 + ((it % 50) / 5) * 5120);
                    else { static_assert(p1_pos(19) >= P1_PRE, ""); st64(tg2, rs, fko1); }
                }
                if constexpr (d1 && !d0) DMA_M0(d1 ? nt_ : 0);
                site<2000 + 10 * s + g>();
                SCHED();
            });
        };
        static_for<20>(kstep);
        SCHED();
        rr[0] = f32x4{rr_[0], rr_[1], rr_[2], rr_[3]}; rr[1] = f32x4{rr_[4], rr_[5], rr_[6], rr_[7]};
        ACC_FENCE10(acc[0], acc[1]);
        STAMP(1)
    }

    auto layer_body = LAMBDA(layer_) {
        constexpr int layer = layer_.value;
        const int sga = sgn_;
        const bool cv = layer == 0 && cv0;
        v2u fh;
        unsigned ks[10];
        f32x4 ca[8], a2[4][5];
        float rsr, rsm;
        v4u cA[2][8], cB[2][8];
        f32x4 pp[5];
        constexpr int R2 = kCR1 + H3, LP = (7 - R2) & 1;
        static_assert(GF && kFHP >= 7 - KG0 / 4 + 1, "");
        constexpr bool TB = (SBT >> layer) & 1;
        float bv[8][5];
        f32x2 pr[5][2], ps[5][2];
        auto powers = [&]() __attribute__((always_inline)) {
            if constexpr (TB) {
#pragma unroll
                for (int nb = 0; nb < 5; ++nb) {
                    const f32x2 m = {lr[nb] * lr[nb] - ls[nb] * ls[nb], 2.0f * lr[nb] * ls[nb]};
                    pr[nb][0] = f32x2{lr[nb], m.x}; ps[nb][0] = f32x2{ls[nb], m.y};
#pragma unroll
                    for (int j = 1; j < 2; ++j) pstep(pr[nb][j], ps[nb][j], pr[nb][j - 1], ps[nb][j - 1], m);
#pragma unroll
                    for (int j = 0; j < 2; ++j) { pin(pr[nb][j]); pin(ps[nb][j]); }
                }
                SCHED();
            }
        };
        {
            float a0[5], a1[5];
            static_for<2>(LAMBDA(m_) {
                constexpr int mb = m_.value;
#pragma unroll
                for (int nb = 0; nb < 5; ++nb) {
                    const f32x4 v = __builtin_elementwise_fma(acc[mb][nb], rr[mb], f32x4{tt[mb][nb][0], tt[mb][nb][1], tt[mb][nb][2], tt[mb][nb][3]});
#pragma unroll
                    for (int i = 0; i < 4; ++i) bv[4 * mb + i][nb] = v[i];
                }
                SCHED();
                if constexpr (TB) {
#pragma unroll
                    for (int i = mb ? 4 : 1; i < 4 + 4 * mb; ++i) cstepi(bv[i][0], bv[i][1], bv[i][2], bv[i][3], bv[i][4], bv[i - 1][0], bv[i - 1][1], bv[i - 1][2], bv[i - 1][3], bv[i - 1][4], lr, ls);
#pragma unroll
                    for (int nb = 0; nb < 5; ++nb) a0[nb] = bv[7][nb];
                }
                if constexpr (mb == 0 && !TB) cstep(a0, bv[0], lr, ls, bv[1][0], bv[1][1], bv[1][2], bv[1][3], bv[1][4]);
#pragma unroll
                for (int i8 = mb ? 4 : 2; i8 < (TB ? 0 : 4 + 4 * mb); i8 += 2) {
                    cstep(a1, a0, lr, ls, bv[i8][0], bv[i8][1], bv[i8][2], bv[i8][3], bv[i8][4]);
                    cstep(a0, a1, lr, ls, bv[i8 + 1][0], bv[i8 + 1][1], bv[i8 + 1][2], bv[i8 + 1][3], bv[i8 + 1][4]);
                }
                SCHED();
            });
#pragma unroll
            for (int nb = 0; nb < 5; ++nb) *(float*)(smem + sga + nb * 64) = a0[nb];
        }
        SCHED();
        const int w_ = w0;
        {
        const int w = w_, mt1 = mt1_;
        if (layer == 0 && __builtin_expect(!kearly, 0)) {
            unsigned f0_, f1_;
            asm volatile("s_waitcnt vmcnt(0)\n\t" AR("%0", 36, 0) AR("%1", 36, 1) : "=v"(f0_), "=v"(f1_)::"memory");
            kearly = __builtin_amdgcn_ballot_w64(((f0_ ^ tag0) | (f1_ ^ tag1)) != 0u) == 0ull;
            ORDER();
            if (kearly) KW_REQ()
        }
        asm volatile("s_waitcnt lgkmcnt(0)" ::: "memory");
        __builtin_amdgcn_s_barrier();
        asm volatile("" ::: "memory");
        STAMP(layer ? 14 : 2)
#pragma unroll
        for (int s = 0; s < 10; ++s) ks[s] = (unsigned)s * 1024u;
        constexpr int NX = DSE ? 58 : 53;
        constexpr int NW2A = NW2P + 7 * kCR1 + kXA, NW3 = NW2P + kN3E + kN3;
        auto w2ld = LAMBDA(i_) {
            constexpr int i = i_.value;
            gld_w2<i>(w2o, p.ws + 2048 + ks[i / 5] * 5u);
        };
        auto w2a = LAMBDA(i_) {
            constexpr int k = i_.value;
            if constexpr (k < kXA) { w2ld(std::integral_constant<int, NW2P + 2 * k>{}); w2ld(std::integral_constant<int, NW2P + 2 * k + 1>{}); }
            else w2ld(std::integral_constant<int, NW2P + kXA + k>{});
        };
        auto w2x = LAMBDA(i_) {
            constexpr int i = i_.value < NX - 50 ? 50 + i_.value : NW2A + i_.value - (NX - 50) < 50 ? NW2A + i_.value - (NX - 50) : 99;
            if constexpr (i < 50) w2ld(std::integral_constant<int, i>{});
            else if constexpr (i < 52) lda4<40 + 4 * (i - 50), (i - 50) * 64>(xso, p.x0);
            else if constexpr (i == 52) lda4<48, 128>(xso - 2u * (unsigned)(bb & 16), p.x0);
            else if constexpr (i < NX) lda2<20 + 2 * (i - 53), 32 * (i - 53)>(dso, p.Dskip);
        };
        constexpr int NB = NX - NW2A, NB1 = KB1 < NB ? KB1 : NB, KPL = 7 * (8 - R2) - kFHP, NFH = NB - NB1 - (KPL - kFHK) > 0 ? NB - NB1 - (KPL - kFHK) : 0;
        auto w2b = LAMBDA(i_) {
            constexpr int k = i_.value - 7 * R2;
            if constexpr (k < NB1) w2x(std::integral_constant<int, k>{});
            if constexpr (k > kFHK) w2x(std::integral_constant<int, NB1 + k - kFHK - 1>{});
            if constexpr (k == kFHK) { vm_wait<NB1>(); st64(v2u{tag0, tag1}, rs, fho1); STAMP(39) }
            if constexpr (k == kFHK - 1) { STAMP(38) }
            if constexpr (k == KPL) poll1(fho, p.ws);
            static_assert(NB1 <= kFHK + 1 && NB1 + 7 * (7 - R2) + KG0 / 4 - kFHK - 1 >= NB && NW2A <= 50 && NW3 <= NW2A && NFH <= NB - NB1, "");
        };
        auto partb = [&]() __attribute__((always_inline)) {
            const float* sp = (const float*)(smem + spa);
            rsr = sp[0]; rsm = sp[8];
            if (layer == 1) { pin(rsr); pin(rsm); }
            if (layer == 1) {
                ACC_TIE5(dsk);
#pragma unroll
                for (int nb = 0; nb < 5; ++nb) { a2[3][nb] = xin[nb] + (xin[nb] * rsr + rsm) * unpk4(dsk[nb]); pin(a2[3][nb]); }
            }
        };
        constexpr int LBW = layer ? 2 : 3;
        if (w == 3) {
            site<3 + 10 * layer>(); SCHED();
            f32x2 sv[16];
#pragma unroll
            for (int i = 0; i < 16; ++i) sv[i] = *(const f32x2*)(smem + spl + (l_sg(layer, i >> 2) - SC + (i & 3) * 320));
            f32x2 u[4], v[4], x[4];
            cfma4(u, l8, sv[0], sv[4], sv[8], sv[12], sv[1], sv[5], sv[9], sv[13]);
            cfma4(v, l8, u[0], u[1], u[2], u[3], sv[2], sv[6], sv[10], sv[14]);
            cfma4(x, l8, v[0], v[1], v[2], v[3], sv[3], sv[7], sv[11], sv[15]);
            f32x2 P[4][4];
            P[0][0] = f32x2{0.f, 0.f}; P[0][1] = x[0]; P[0][2] = cfma(l32, x[0], x[1]); P[0][3] = cfma(l32, P[0][2], x[2]);
            const f32x2 E = cfma(l32, P[0][3], x[3]);
            st128(v4u{__float_as_uint(E.x), tag0, __float_as_uint(E.y), tag0}, rs, es);
            SCHED();
            if (layer == 0) { FW_OK() static_for<kN3E>(LAMBDA(i_) { w2ld(std::integral_constant<int, NW2P + i_.value>{}); }); }
#pragma unroll
            for (int kk = 1; kk < 4; ++kk) cfma4(P[kk], l8, P[kk - 1][0], P[kk - 1][1], P[kk - 1][2], P[kk - 1][3], sv[kk - 1], sv[kk + 3], sv[kk + 7], sv[kk + 11]);
#pragma unroll
            for (int i = 0; i < 16; ++i) *(f32x2*)(smem + spl + (l_sg(layer, i >> 2) - SC + (i & 3) * 320)) = P[i & 3][i >> 2];
            if (layer == 0) partb();
            if (layer == 1) powers();
            if (layer == 0 && H3) *(unsigned*)(smem + L_FL + (spl & 0x38) / 2) = 0u;
        }
        if (w == LBW) {
            site<5 + 10 * layer>(); SCHED();
            f32x2 wg[17], t4[4];
            unsigned ea[15], tg[15];
            const f32x2 z2 = {0.f, 0.f};
            wg[1] = cmul(l128, l128); wg[2] = cmul(wg[1], wg[1]); wg[3] = cmul(wg[2], wg[1]); wg[4] = cmul(wg[2], wg[2]);
#pragma unroll
            for (int i = 4; i < 16; i += 4) {
                cfma4(t4, wg[i], wg[1], wg[2], wg[3], wg[4], z2, z2, z2, z2);
#pragma unroll
                for (int e = 0; e < 4; ++e) wg[i + 1 + e] = t4[e];
            }
#pragma unroll
            for (int i = 0; i < 15; ++i) {
                const int ch = mt1 - 1 - i;
                if (i) pin(wg[i]);
                ea[i] = ch < 0 ? kNone : (unsigned)ch * 10240u; tg[i] = ch < 0 ? 0u : tag0;
                pins(ea[i]); pins(tg[i]);
            }
            if constexpr (layer == 0) tour<3>();
            powers();
            f32x2 H0, H1;
            __builtin_amdgcn_s_sleep(layer ? KLB1 : KLB0);
            unsigned spins = dead ? kSpinCap : 0u;
            bool miss;
#pragma nounroll
            do {
                asm volatile("" ::: "memory");
                v4u eg[15];
#pragma unroll
                for (int i = 0; i < 15; ++i) eg[i] = ld128s(rs, eo, ea[i]);
                SCHED();
                unsigned x = __builtin_amdgcn_bitop3_b32(eg[0].y, eg[0].w, tg[0], 0x7E);
                H0 = f32x2{__uint_as_float(eg[0].x), __uint_as_float(eg[0].z)}; H1 = f32x2{0.f, 0.f};
#pragma unroll
                for (int i = 1; i < 15; i += 2) {
                    x |= __builtin_amdgcn_bitop3_b32(eg[i].y, eg[i].w, tg[i], 0x7E) | __builtin_amdgcn_bitop3_b32(eg[i + 1].y, eg[i + 1].w, tg[i + 1], 0x7E);
                    pin(x);
                    gacc2(H0, H1, wg[i], wg[i + 1], eg[i], eg[i + 1]);
                    SCHED();
                }
                miss = __builtin_amdgcn_ballot_w64(x != 0u) != 0ull;
            } while (miss && ++spins <= kSpinCap);
            dead = dead || miss;
            ORDER();
            *(f32x2*)(smem + spl + (L_CW - SC)) = H0 + H1;
            STAMP(layer ? 36 : 37)
            if (layer == 0) {
                static_for<NW3 - NW2P - kN3E>(LAMBDA(i_) { w2ld(std::integral_constant<int, NW2P + kN3E + i_.value>{}); });
            }
        }
        int w2_ = w;
        pins(w2_);
        if (layer == 0) UNDEF8(ca);
        if (layer == 0 && w2_ != LBW) {
            if (cv) {
                site<2>(); SCHED();
                FW_OK()
                st_ur((unsigned)(size_t)(LAS char*)smem + (unsigned)uwa);
                STAMP(32)
                conv_part<0, kCR1>(ca, smem, ab, bb, w2a, LAMBDA(i_) {}, [&]() __attribute__((always_inline)) {
                    v4u ad;
                    const unsigned dv = sdq << (adm & 16u);
#pragma unroll
                    for (int i = 0; i < 4; ++i) ad[i] = dv & (unsigned)__builtin_amdgcn_sbfe((int)adm, 8 + i, 1);
                    asm volatile("s_nop 1" : "+v"(ad));
                    return ad;
                }, cA, cB);
                partb();
                powers();
            } else {
                if constexpr (layer == 0) tour<5>();
                FW_OK()
                static_for<NW2A - NW2P>(LAMBDA(i_) { w2ld(std::integral_constant<int, NW2P + i_.value>{}); __builtin_amdgcn_s_sleep(1); });
                partb();
                powers();
            }
        }
        if (layer == 1 && w < LBW) { site<4 + 10 * layer>(); SCHED(); powers(); if constexpr (layer == 1) { if (w == 0) tour<6>(); } }
        asm volatile("s_waitcnt lgkmcnt(0)" ::: "memory");
        __builtin_amdgcn_s_barrier();
        asm volatile("" ::: "memory");
        STAMP(layer ? 15 : 4)
        {
            float cw[5], ex[5];
#pragma unroll
            for (int nb = 0; nb < 5; ++nb) { cw[nb] = *(const float*)(smem + cwa + nb * 64); ex[nb] = *(const float*)(smem + sga + nb * 64); }
            v2u ta[5], td[5];
            if constexpr (TB) {
                cstepi(ex[0], ex[1], ex[2], ex[3], ex[4], cw[0], cw[1], cw[2], cw[3], cw[4], lqr, lqs);
                f32x2 cs[5];
                PIN5(ex);
#pragma unroll
                for (int nb = 0; nb < 5; ++nb) { cs[nb] = f32x2{ex[nb], dpp<0xB1>(ex[nb])}; pin(cs[nb]); }
                SCHED();
                static_for<5>(LAMBDA(n_) {
                    constexpr int nb = n_.value;
                    f32x2 h[4] = {{bv[0][nb], bv[1][nb]}, {bv[2][nb], bv[3][nb]}, {bv[4][nb], bv[5][nb]}, {bv[6][nb], bv[7][nb]}};
                    padd(h, pr[nb], ps[nb], cs[nb]);
                    *(v4u*)(smem + tbw + nb * 1024) = v4u{cvtpk(h[0].x, h[0].y), cvtpk(h[1].x, h[1].y), cvtpk(h[2].x, h[2].y), cvtpk(h[3].x, h[3].y)};
                    ta[nb] = __builtin_bit_cast(v2u, trread(smem, tbr + nb * 1024)); td[nb] = __builtin_bit_cast(v2u, trread(smem, tbr + nb * 1024 + 512));
                    SCHED();
                    if constexpr (nb >= LAG) { st128(v4u{ta[nb - LAG].x, ta[nb - LAG].y, td[nb - LAG].x, td[nb - LAG].y}, rs, hso + (nb - LAG) * 256); SCHED(); }
                });
            } else
            static_for<2>(LAMBDA(h_) {
                constexpr int n0 = 2 * h_.value;
                float hv[9][3];
                cstep3(hv[0], cw + n0, lqr + n0, lqs + n0, ex + n0);
                if constexpr (n0 == 0) {
                    chain24<0>(hv, bv, lr, ls);
                    chain24<0>(hv + 4, bv + 4, lr, ls);
                } else {
                    chain33<2>(hv, bv, lr, ls);
                    SCHED();
#pragma unroll
                    for (int rd = 0; rd < 2; ++rd) st128(v4u{ta[rd].x, ta[rd].y, td[rd].x, td[rd].y}, rs, hso + rd * 256);
                    SCHED();
                    chain33<2>(hv + 3, bv + 3, lr, ls);
                    chain32<2>(hv + 6, bv + 6, lr, ls);
                }
#pragma unroll
                for (int e = 0; e < (n0 ? 3 : 2); ++e) {
                    v4u o;
                    o.x = cvtpk(hv[1][e], hv[2][e]); o.y = cvtpk(hv[3][e], hv[4][e]);
                    o.z = cvtpk(hv[5][e], hv[6][e]); o.w = cvtpk(hv[7][e], hv[8][e]);
                    *(v4u*)(smem + tbw + (n0 + e) * 1024) = o;
                }
#pragma unroll
                for (int rd = n0; rd < (n0 ? 5 : 2); ++rd) { ta[rd] = __builtin_bit_cast(v2u, trread(smem, tbr + rd * 1024)); td[rd] = __builtin_bit_cast(v2u, trread(smem, tbr + rd * 1024 + 512)); }
                SCHED();
            });
#pragma unroll
            for (int rd = TB ? 5 - LAG : 2; rd < 5; ++rd) st128(v4u{ta[rd].x, ta[rd].y, td[rd].x, td[rd].y}, rs, hso + rd * 256);
            SCHED();
        }
#pragma unroll
        for (int s = 0; s < 10; ++s) pins(ks[s]);
        if (layer == 1) { partb(); SCHED(); DRAIN(); st64(v2u{tag0, tag1}, rs, fho1); }
        STAMP(layer ? 16 : 5)
        int fa = L_FL + 4 * w, pa = l_tb(0, 0) + (w < 3 ? w : 0) * 5120 + (int)(w2o & 1023u);
        unsigned one = 1u;
        if constexpr (layer == 0 && H3) {
            pin(fa); pin(pa); pin(one);
            *(unsigned*)(smem + fa) = one;
            asm volatile("" ::: "memory");
        }
        if (cv) {
            if constexpr (layer == 0) site<6>();
            SCHED();
            unsigned fl = 1u;
            conv_part<R2, 8>(ca, smem, ab, bb, w2b, LAMBDA(g_) {
                constexpr int g = g_.value - (KG0 - 8);
                if constexpr (H3 && g == 0) { asm volatile("" ::: "memory"); fl = *(const unsigned*)(smem + fa + 16); asm volatile("" ::: "memory"); }
                if constexpr (H3 && g > 0 && g < 6) pp[g - 1] = *(const f32x4*)(smem + pa + (g - 1) * 1024);
                static_assert(!H3 || KG0 >= 8, "");
            }, []() {}, cA, cB);
            while (__builtin_expect(__builtin_amdgcn_readfirstlane(fl) == 0u, 0)) {
                asm volatile("" ::: "memory");
                fl = *(const unsigned*)(smem + fa + 16);
                asm volatile("" ::: "memory");
#pragma unroll
                for (int i = 0; i < 5; ++i) pp[i] = *(const f32x4*)(smem + pa + i * 1024);
            }
        } else if (layer == 0) {
            if (H3 && w == 3) {
                if constexpr (layer == 0) site<16>();
                constexpr int NR = NB - NB1 - NFH, N3 = NR + NW2A - NW3;
                auto fill3 = LAMBDA(n_) {
                    constexpr int n = n_.value, k = n - KF3 - 1;
                    if constexpr (n < NB1) w2x(n_);
                    if constexpr (n == KF3) { vm_wait<NB1>(); st64(v2u{tag0, tag1}, rs, fho1); }
                    if constexpr (k >= 0 && k < NR) w2x(IC(NB1 + k));
                    else if constexpr (k >= NR && k < N3) w2ld(IC(NW3 + k - NR));
                };
                int a3[4] = {ab[0], ab[1], ab[2], ab[3]}, b3 = bb;
                f32x4 cb[2][8];
                auto chan = LAMBDA(x_) {
                    constexpr int x = x_.value;
                    const int (&ab)[4] = a3;
                    const int& bb = b3;
                    if constexpr (x == 0) {
                        CV_LOAD_A(cA[0], 0, kCR1, 0) CV_LOAD_A(cA[0], 0, kCR1, 1)
                        static_for<8>(LAMBDA(e_) { constexpr int e = (e_.value + 3) & 7; CV_LOAD_B(cB[0], e, kCR1) SCHED(); });
                        static_for<7>(LAMBDA(a_) { constexpr int a = a_.value + 1; CV_LOAD_A(cA[0], a, kCR1, 0) CV_LOAD_A(cA[0], a, kCR1, 1) });
                        SCHED();
                        lds_wait3(fa - 12);
                    }
                    static_for<30>(LAMBDA(g_) {
                        constexpr int g = g_.value, a = cv_pair(3, 8, g) / 8, e = cv_pair(3, 8, g) % 8, u = g - 3, f = (u / 3 + 3) & 7;
                        if constexpr (a == 0) mf0(cb[x & 1][e], cA[x & 1][0], cB[x & 1][e]); else mf(cb[x & 1][a + e], cA[x & 1][a], cB[x & 1][e]);
                        SCHED();
                        if constexpr (x < 2 && g < 2) { a3[2 * g] += REGSZ; a3[2 * g + 1] += REGSZ; pin(a3[2 * g]); pin(a3[2 * g + 1]); }
                        if constexpr (x < 2 && g == 2) { b3 += REGSZ; pin(b3); }
                        if constexpr (x < 2 && u >= 0 && u < 24) { if constexpr (u % 3 == 2) CV_LOAD_B(cB[(x & 1) ^ 1], f, kCR1) else CV_LOAD_A(cA[(x & 1) ^ 1], u / 3, kCR1, u % 3) }
                        if constexpr (x > 0 && g % 3 == 2 && g > 2 && g < 18) *(f32x4*)(smem + pa + (x - 1) * 5120 + (g / 3 - 1) * 1024) = cb[(x & 1) ^ 1][2 + g / 3];
                        if constexpr (x > 0 && g == 20) { asm volatile("" ::: "memory"); *(unsigned*)(smem + fa + 4 * x) = one; }
                        SCHED();
                        site<3300 + 30 * x + g>();
                        if constexpr (g % 3 == 1) { fill3(IC(10 * x + g / 3)); SCHED(); }
                    });
                };
                auto out = LAMBDA(x_) {
                    constexpr int x = x_.value;
                    asm volatile("s_nop 7" ::: "memory");
#pragma unroll
                    for (int i = 0; i < 5; ++i) *(f32x4*)(smem + pa + x * 5120 + i * 1024) = cb[x & 1][3 + i];
                    asm volatile("" ::: "memory");
                    *(unsigned*)(smem + fa + 4 + 4 * x) = one;
                    SCHED();
                };
                chan(IC(0));
                chan(IC(1));
                if (three) { chan(IC(2)); out(IC(2)); }
                else { out(IC(1)); static_for<10>(LAMBDA(i_) { fill3(IC(20 + i_.value)); __builtin_amdgcn_s_sleep(1); }); }
                static_for<(KF3 + 1 + N3 > 30 ? KF3 + 1 + N3 - 30 : 0)>(LAMBDA(i_) { fill3(IC(30 + i_.value)); });
                if constexpr (layer == 0) tour<2>();
            } else {
                static_for<NB1>(LAMBDA(i_) { w2x(i_); __builtin_amdgcn_s_sleep(1); });
                vm_wait<NB1>();
                st64(v2u{tag0, tag1}, rs, fho1);
                static_for<NB - NB1 - NFH>(LAMBDA(i_) { w2x(std::integral_constant<int, NB1 + i_.value>{}); __builtin_amdgcn_s_sleep(1); });
                if (!H3 && w == 3) { static_for<NW2A - NW3>(LAMBDA(i_) { w2ld(std::integral_constant<int, NW3 + i_.value>{}); __builtin_amdgcn_s_sleep(1); }); if constexpr (layer == 0 && !H3) tour<2>(); }
                if (KSN) __builtin_amdgcn_s_sleep(KSN);
            }
            poll1(fho, p.ws);
            static_for<NFH>(LAMBDA(i_) { w2x(std::integral_constant<int, NB - NFH + i_.value>{}); __builtin_amdgcn_s_sleep(1); });
            static_assert(NFH <= NB - (NX - 50), "");
            UNDEF8(cA[LP]); UNDEF8(cB[LP]);
            if (H3) asm volatile("" : "=v"(pp[0]), "=v"(pp[1]), "=v"(pp[2]), "=v"(pp[3]), "=v"(pp[4]));
        }
        if (layer == 0) { unsigned f0_, f1_; asm volatile("s_waitcnt vmcnt(%2)\n\t" AR("%0", 36, 0) AR("%1", 36, 1) : "=v"(f0_), "=v"(f1_) : "i"(NFH) : "memory"); fh = v2u{f0_, f1_}; }
        STAMP(layer ? 29 : 6)
        }
        unsigned g[8][2];

            v2u pt[4];
            if (layer == 0) WAVE_RETRY(ISTAG(fh), fh = ld64(rs, fho))
            else { skip<SKIP7>(); pipe_flag(pt, fho, p.ws, (u64)tag0 | ((u64)tag1 << 32), dead); ORDER(); }
            STAMP(layer ? 17 : 31)
            site<7 + 10 * layer>(); SCHED();
            constexpr int RING2 = layer ? KR2 : KRT;
            v4u ar[RING2];
            constexpr int NRT = layer ? RING2 : (36 - KG0) / KRS + 2 < KRT ? (36 - KG0) / KRS + 2 : KRT;
            unsigned xs_[3][4];
            float ys[8][4];
            auto gate = LAMBDA(n_) {
                constexpr int n = n_.value, bl = n / 10, u = n % 10;
                if constexpr (n < 0) {}
                else if constexpr (n >= 100) swp2n(g[2 * (n - 100)][0], g[2 * (n - 100) + 1][0], g[2 * (n - 100)][1], g[2 * (n - 100) + 1][1]);
                else if constexpr (u < 2) { if constexpr (H3) { ys[bl][2 * u] = pp[bl - 3][2 * u]; ys[bl][2 * u + 1] = pp[bl - 3][2 * u + 1]; add2(ys[bl][2 * u], ys[bl][2 * u + 1], ca[bl][2 * u], ca[bl][2 * u + 1]); } }
                else if constexpr (H3 && bl >= 3) cvt2(g[bl][0], g[bl][1], f32x4{ys[bl][0], ys[bl][1], ys[bl][2], ys[bl][3]});
                else cvt2(g[bl][0], g[bl][1], ca[bl]);
            };
            if constexpr (layer == 0) {
#pragma unroll
                for (int i = 0; i < 2; ++i) { ar[i] = ld128s(rs, ho[0], ks[i]); SCHED(); }
                static_for<36 - KG0>(LAMBDA(n_) {
                    constexpr int n = n_.value, g = KG0 + n;
                    constexpr int a = g < 8 ? 0 : g < 15 ? 1 : g < 21 ? 2 : g < 26 ? 3 : g < 30 ? 4 : g < 33 ? 5 : g < 35 ? 6 : 7;
                    constexpr int e = g - (a * 8 - a * (a - 1) / 2), i = 2 + n / KRS;
                    mf(ca[a + e], cA[LP][a], cB[LP][e]);
                    SCHED();
                    if constexpr (n % KRS == 0 && i < NRT) ar[i] = ld128s(rs, ho[i / 10], ks[i % 10]);
                    if constexpr (n < 12) rda<40 + n>(xs_[n >> 2][n & 3]);
                    constexpr int gt_[12] = {2, 12, 22, 30, 100, 31, 32, 40, 41, 101, 42, 50};
                    if constexpr (g >= 24) gate(IC(gt_[g < 24 ? 0 : g - 24]));
                    site<3252 + g>();
                    SCHED();
                });
                static_assert(KG0 <= 15 && NRT >= 12 && DSE, "");
            } else {
#pragma unroll
                for (int i = 0; i < RING2; ++i) { ar[i] = ld128s(rs, ho[i / 10], ks[i % 10]); SCHED(); }
            }
        v4u gl[3];
        v2u f0;
        bool gearly = false;
        {
            const char* src = smem + srca;
            const char *glb1 = p.ws + 32 * 4096, *glb2 = p.ws + 48 * 4096;
            float zx[4], zd[4], zt[4], zz[5][4];
            if constexpr (STAMPS == 1) { STAMP(layer ? 50 : 40) }
            static_for<40>(LAMBDA(i_) {
                constexpr int i = i_.value, m = i / 10, s = i % 10;
                static_for<5>(LAMBDA(k_) {
                    constexpr int kk = k_.value, n = 2 * (i - 3) + kk / 2 - 3, nb = n / 8, u = n % 8, pv = i - 1 + RING2;
                    if constexpr (s == 0 && m < 3) mfa0<kk>(a2[m][kk], ar[i % RING2]); else mfa<5 * s + kk>(a2[m][kk], ar[i % RING2]);
                    SCHED();
                    constexpr int gu_[12] = {51, 52, 60, 61, 102, 62, 70, 71, 72, -1, 103, -1};
                    if constexpr (layer == 0 && 5 * i + kk < 12) gate(IC(gu_[(5 * i + kk) % 12]));
                    if constexpr (layer == 0 && i == 25 && kk == 1) { pins(glb1); pins(glb2); }
                    if constexpr (layer == 0 && i >= 3 && i < 26 && (kk & 1)) {
                        if constexpr (n < 0) swp2n(xs_[n + 3][0], xs_[n + 3][2], xs_[n + 3][1], xs_[n + 3][3]);
                        else if constexpr (nb < 5) {
                            if constexpr (u < 2) unp2(zx[2 * u], zx[2 * u + 1], xs_[nb >> 1][2 * (nb & 1) + u]);
                            else if constexpr (u < 4) unp2(zd[2 * u - 4], zd[2 * u - 3], dsk[nb][u - 2]);
                            else if constexpr (u < 6) fma2(zt[2 * u - 8], zt[2 * u - 7], zx[2 * u - 8], zx[2 * u - 7], rsr, rsr, rsm, rsm);
                            else fma2(zz[nb][2 * u - 12], zz[nb][2 * u - 11], zt[2 * u - 12], zt[2 * u - 11], zd[2 * u - 12], zd[2 * u - 11], zx[2 * u - 12], zx[2 * u - 11]);
                        }
                    }
                    if constexpr (i == 0 && kk == 0 && layer == 1) { pin(pt[0]); pin(pt[1]); pin(pt[2]); pin(pt[3]); }
                    if constexpr (layer == 0 && i < 2 && kk > 0 && 12 + 4 * i + kk - 1 >= NRT && 12 + 4 * i + kk - 1 < RING2) ar[12 + 4 * i + kk - 1] = ld128s(rs, ho[1], ks[2 + 4 * i + kk - 1]);
                    if constexpr (layer == 0 && i == kGSI && kk > 0) st128(v4u{g[2 * kk - 2][0], g[2 * kk - 2][1], g[2 * kk - 1][0], g[2 * kk - 1][1]}, rs, gso + (kk - 1) * 1024);
                    if constexpr (layer == 0 && i > 2 && i <= 7 && kk == (i == kGSI ? 0 : 2)) {
                        if constexpr (DSE) { unsigned d0_, d1_; rda<20 + 2 * (i - 3)>(d0_); rda<21 + 2 * (i - 3)>(d1_); dsk[i - 3] = v2u{d0_, d1_}; }
                    }
                    if constexpr (kk == 0 && i > 0 && pv < 40) ar[pv % RING2] = ld128s(rs, ho[pv / 10], ks[pv % 10]);
                    constexpr int s1 = s - 1 - (KMB && layer == 0 && m == 1 ? kFGI - 10 : 0);
                    if constexpr (kk == 2 && m >= 1 && s1 >= 0 && s1 < 5) *(f32x4*)(smem + dst[m - 1] + s1 * 1024) = a2[m - 1][s1];
                    site<4000 + 1000 * layer + 5 * i + kk>();
                    if constexpr (i >= 37) a2[i - 37][kk] = *(const f32x4*)(src + (i - 37) * 5120 + kk * 1024);
                    SCHED();
                });
                if constexpr (i == 10 && (layer || !KMB)) { asm volatile("s_waitcnt lgkmcnt(0)" ::: "memory"); __builtin_amdgcn_s_barrier(); asm volatile("" ::: "memory"); }
                if constexpr (i == kFGI && layer == 0) {
                    static_assert(kFGI >= 10 && kFGI <= 14, "");
                    asm volatile("s_waitcnt vmcnt(%0) lgkmcnt(0)" ::"i"(kFGI - kGSI) : "memory");
                    __builtin_amdgcn_s_barrier(); asm volatile("" ::: "memory");
                    st64(v2u{tag0, tag1}, rs, fgo1);
                }
                if constexpr (i == 26 && layer == 0) {
#pragma unroll
                    for (int nb = 0; nb < 5; ++nb) { a2[3][nb] = f32x4{zz[nb][0], zz[nb][1], zz[nb][2], zz[nb][3]}; pin(a2[3][nb]); }
                }
                if constexpr (i == kGPI && layer == 0) gld2s<0>(f0, fgo, p.ws);
                if constexpr (i == kGCI && layer == 0) {
                    wait1(f0);
                    static_for<39 - i>(LAMBDA(r_) { pin(ar[(i + 1 + r_.value) % RING2]); });
                    gearly = __builtin_amdgcn_ballot_w64(!ISTAG(f0)) == 0ull;
                    ORDER();
                    undef3(gl);
                    if (__builtin_expect(gearly, 1)) { gld4t<0>(gl[0], glo, p.ws); gld4t<0>(gl[1], glo, glb1); gld4t<0>(gl[2], glo, glb2); }
                    else if (G2ND) poll2(fgo, fgo, p.ws);
                }
                if constexpr (i == 36) {
                    asm volatile("s_waitcnt lgkmcnt(0)" ::: "memory");
                    __builtin_amdgcn_s_barrier(); asm volatile("" ::: "memory");
                }
                if constexpr (STAMPS == 1 && (i == 0 || i == 9 || i == 10 || i == 15 || i == 25 || i == 35 || i == 36)) { STAMP((layer ? 51 : 41) + (i == 0 ? 0 : i == 9 ? 1 : i == 10 ? 2 : i == 15 ? 3 : i == 25 ? 4 : i == 35 ? 5 : 6)) }
                SCHED();
            });
            STAMP(layer ? 18 : 8)
        }
#pragma unroll
        for (int nb = 0; nb < 5; ++nb) a2[0][nb] = (a2[0][nb] + a2[1][nb]) + a2[2][nb];
        asm volatile("s_nop 1" : "+v"(a2[3][0]), "+v"(a2[3][1]), "+v"(a2[3][2]), "+v"(a2[3][3]), "+v"(a2[3][4]),
                     "+v"(a2[0][0]), "+v"(a2[0][1]), "+v"(a2[0][2]), "+v"(a2[0][3]), "+v"(a2[0][4]));
        float ty;
        {
            f32x4 s1, s2;
#pragma unroll
            for (int nb = 0; nb < 5; ++nb) {
                const f32x4 y = a2[3][nb] + a2[0][nb];
                xin[nb] = y;
                if (nb == 0) { s1 = y; s2 = y * y; } else { s1 += y; s2 += y * y; }
            }
            ty = xsum2h(hsum4(s1), hsum4(s2));
            st64(v2u{__float_as_uint(ty), tag0}, rs, sgs);
        }
        SCHED();
        STAMP(layer ? 19 : 9)
        int lane_o, b_o = b0, w_o = w0;
        asm volatile("v_mbcnt_lo_u32_b32 %0, -1, 0\n\tv_mbcnt_hi_u32_b32 %0, -1, %0" : "=v"(lane_o));
        asm volatile("" : "+s"(b_o), "+s"(w_o));
        const int lane = lane_o, b = b_o, w = w_o;
        const int c = lane & 15, q = lane >> 4;
        TILES()
        if (layer == 0 && __builtin_expect(!gearly, 0)) {
            if (G2ND) {
                unsigned f0_, f1_;
                asm volatile("s_waitcnt vmcnt(1)\n\t" AR("%0", 36, 0) AR("%1", 36, 1) : "=v"(f0_), "=v"(f1_)::"memory");
                f0 = v2u{f0_, f1_};
            }
            WAVE_RETRY(ISTAG(f0), f0 = ld64(rs, fgo))
            gld4t<0>(gl[0], glo, p.ws); gld4t<0>(gl[1], glo, p.ws + 32 * 4096); gld4t<0>(gl[2], glo, p.ws + 48 * 4096);
            asm volatile("s_waitcnt vmcnt(0)" : "+v"(gl[0]), "+v"(gl[1]), "+v"(gl[2])::"memory");
        }
        if (layer == 0) {
            asm volatile("s_waitcnt vmcnt(1)" : "+v"(gl[0]), "+v"(gl[1]), "+v"(gl[2])::"memory");
            char* tb = smem + SC + w * INBOX;
            *(v4u*)(tb + lane * 16) = gl[0]; *(v4u*)(tb + 1024 + lane * 16) = gl[1]; *(v4u*)(tb + 1536 + lane * 16) = gl[2];
#pragma unroll
            for (int nb = 0; nb < 5; ++nb) gt[nb] = __builtin_bit_cast(v2u, trread(tb, (16 * nb + 4 * q + (c >> 2)) * 32 + (c & 3) * 8));
        } else ACC_TIE5(gt);
        SCHED();
        f32x4 gf[5], yg[5];
#pragma unroll
        for (int nb = 0; nb < 5; ++nb) { gf[nb] = unpk4(gt[nb]); yg[nb] = xin[nb] * gf[nb]; pin(gf[nb]); pin(yg[nb]); }
        unsigned xs = OFF_X2F + (unsigned)((2 * mt2 + (w >> 1)) * 40960 + ((c >> 2) & 1) * 20480 + (10 * nt2 + 2 * (q & 1) + (q >> 1)) * 256 + (8 * (w & 1) + 4 * (c >> 3) + (c & 3)) * 16);
        unsigned pso = q < 2 ? OFF_PS + (unsigned)(((mt2 * 4 + w) * 8 + nt2) * 128 + c * 8 + q * 4) : kNone;
        unsigned fxo1 = OFF_FX + (unsigned)(((mt1 * 4 + 2 * (mt2 & 1) + (w >> 1)) * 16 + nt2 * 2 + (w & 1)) * 128);
        unsigned oo = (unsigned)(((64 * mt2 + 16 * w + c) * D_ + 80 * nt2 + 4 * q) * 4);
        if (layer == 0) { pin(xs); pin(pso); pin(fxo1); } else pin(oo);
        float c_r, c_m, c_rr, c_rm, c_mm;
        if constexpr (layer == 0 && PSA) {
            f32x4 su = yg[0], sg_ = gf[0], syu = xin[0] * yg[0], suu = yg[0] * yg[0], sug = yg[0] * gf[0], sgg = gf[0] * gf[0];
#pragma unroll
            for (int nb = 1; nb < 5; ++nb) { su += yg[nb]; sg_ += gf[nb]; syu += xin[nb] * yg[nb]; suu += yg[nb] * yg[nb]; sug += yg[nb] * gf[nb]; sgg += gf[nb] * gf[nb]; }
            const float hu = hsum4(su);
            c_r = xsum2h(hu, 2.f * hsum4(syu)); c_m = xsum2h(hsum4(sg_), 2.f * hu);
            c_rr = xsum2h(0.f, hsum4(suu)); c_rm = xsum2h(0.f, 2.f * hsum4(sug)); c_mm = xsum2h(0.f, hsum4(sgg));
            pin(c_r); pin(c_m); pin(c_rr); pin(c_rm); pin(c_mm);
        }
        SCHED();
        {
            float a, d;
            if constexpr (layer == 0) { skip<SKIP4>(); site<9>(); }
            pipe_gran<layer ? PDS : PDS0>(a, d, sgo, p.ws, tag0, dead);
            STAMP(layer ? 20 : 10)
            xsum2(a, d);
            const float me = a * (1.0f / D_);
            const float ri = __builtin_amdgcn_rsqf(fmaxf(d * (1.0f / D_) - me * me, 0.f) + 1e-5f), mr = -me * ri;
            if constexpr (layer == 0 && PSA) {
                const float ps_ = ty + ri * (c_r + ri * c_rr + mr * c_rm) + mr * (c_m + mr * c_mm);
                __builtin_amdgcn_raw_buffer_store_b32(__float_as_uint(ps_), rs, pso, 0, SC1);
                SCHED();
            }
#pragma unroll
            for (int nb = 0; nb < 5; ++nb) xin[nb] = yg[nb] * ri + (gf[nb] * mr + xin[nb]);
        }
        if constexpr (layer == 0) {
            v2u pk[5];
#pragma unroll
            for (int nb = 0; nb < 5; ++nb) { const f32x4 x = xin[nb]; pk[nb] = v2u{cvtpk(x.x, x.y), cvtpk(x.z, x.w)}; }
#pragma unroll
            for (int i = 0; i < 3; ++i) {
                const v2u pa = pk[2 * i], pb = pk[i < 2 ? 2 * i + 1 : 4];
                const auto r0 = __builtin_amdgcn_permlane16_swap(pa.x, pb.x, false, false);
                const auto r1 = __builtin_amdgcn_permlane16_swap(pa.y, pb.y, false, false);
                if (i < 2 || (q & 1) == 0) st128(v4u{r0[0], r1[0], r0[1], r1[1]}, rs, xs + i * 1024);
            }
            if constexpr (!PSA) {
                f32x4 s1, s2;
#pragma unroll
                for (int nb = 0; nb < 5; ++nb) { const f32x4 x = xin[nb]; if (nb == 0) { s1 = x; s2 = x * x; } else { s1 += x; s2 += x * x; } }
                __builtin_amdgcn_raw_buffer_store_b32(__float_as_uint(xsum2h(hsum4(s1), hsum4(s2))), rs, pso, 0, SC1);
            }
            SCHED();
            int w1lane0 = (c & 1) * 10240 + (c >> 1) * 128 + ((q ^ ((c & 1) | ((c >> 2) << 1))) * 16);
            int w1lane1 = (c & 1) * 10240 + (c >> 1) * 128 + (((4 + q) ^ ((c & 1) | ((c >> 2) << 1))) * 16);
            unsigned fxo = OFF_FX + (unsigned)((mt1 * 4 + w) * 2048 + c * 128);
            unsigned xo = OFF_X2F + (unsigned)((4 * mt1 + w) * 40960 + lane * 16), xo1 = xo + 20480u;
            const int row = 8 * q + (c & 7);
            unsigned po = OFF_PS + (unsigned)((((2 * mt1 + (w >> 1)) * 4 + 2 * (w & 1) + (row >> 4)) * 8 + 4 * (c >> 3)) * 128 + (row & 15) * 8);
            int w1hi0 = w1lane0 + 61440, w1hi1 = w1lane1 + 61440;
            pin(w1lane0); pin(w1lane1); pin(w1hi0); pin(w1hi1); pin(fxo); pin(xo); pin(xo1); pin(po);
            v4u wf[2][5];
#pragma unroll
            for (int nb = 0; nb < 5; ++nb) wf[0][nb] = *(const v4u*)(smem + w1lane0 + nb * 20480);
            SCHED();
            DRAIN();
            FLAG(fxo1)
            if (b == 255 && w == 0) FLAG(OFF_NONCE)
            STAMP(11)
            if (w == 0) tour<4>();
            addrs(std::integral_constant<int, 1>{}, lane, w, b);
            v2u pt[4];
            skip<SKIP5>();
            pipe_flag(pt, fxo, p.ws, (u64)tag0 | ((u64)tag1 << 32), dead);
            ORDER();
            STAMP(12)
            v4u ar[RING1][2];
#pragma unroll
            for (int s = 0; s < RING1; ++s)
#pragma unroll
                for (int mb = 0; mb < 2; ++mb) { ar[s][mb] = ld128s(rs, mb ? xo1 : xo, s * 1024); SCHED(); }
            v2u ps[4];
#pragma unroll
            for (int i = 0; i < 4; ++i) ps[i] = ld64(rs, po + i * 128);
            SCHED();
            float t1, t2, rinv, mri, rr_[8], rm_[8];
            static_for<20>(LAMBDA(s_) {
                constexpr int s = s_.value, cu = s & 1, nx = cu ^ 1;
                LGKM0() SCHED();
                static_for<10>(LAMBDA(g_) {
                    constexpr int g = g_.value, mb = g / 5, k = g % 5;
                    if constexpr (s == 0) mf0(acc[mb][k], ar[0][mb], wf[0][k]); else mf(acc[mb][k], ar[s % RING1][mb], wf[cu][k]);
                    SCHED();
                    if constexpr (s == 0 && g == 0) { pin(pt[0]); pin(pt[1]); pin(pt[2]); pin(pt[3]); }
                    if constexpr (s < 19 && g < 3) wf[nx][g] = *(const v4u*)(smem + (nx ? w1lane1 : w1lane0) + g * 20480 + ((s + 1) >> 1) * 1024);
                    if constexpr (s < 19 && (g == 3 || g == 4)) wf[nx][g] = *(const v4u*)(smem + (nx ? w1hi1 : w1hi0) + (g - 3) * 20480 + ((s + 1) >> 1) * 1024);
                    if constexpr (g == 5 && s + RING1 < 20) ar[s % RING1][0] = ld128s(rs, xo, (s + RING1) * 1024);
                    if constexpr (g == 2 && s > 0 && s - 1 + RING1 < 20) ar[(s - 1) % RING1][1] = ld128s(rs, xo1, (s - 1 + RING1) * 1024);
                    if constexpr (s == 8 && g == 8) { t1 = __uint_as_float(ps[0].x) + __uint_as_float(ps[1].x); pin(t1); t2 = __uint_as_float(ps[0].y) + __uint_as_float(ps[1].y); pin(t2); }
                    if constexpr (s == 9 && g == 6) { t1 += __uint_as_float(ps[2].x); pin(t1); t2 += __uint_as_float(ps[2].y); pin(t2); }
                    if constexpr (s == 9 && g == 7) { t1 += __uint_as_float(ps[3].x); pin(t1); t2 += __uint_as_float(ps[3].y); pin(t2); }
                    if constexpr (s == 10 && g == 6) { t1 += dpp<0x128>(t1); t2 += dpp<0x128>(t2); pin(t1); pin(t2); }
                    if constexpr (s == 10 && g == 7) { t1 *= (1.0f / D_); pin(t1); t2 *= (1.0f / D_); pin(t2); }
                    if constexpr (s == 10 && g == 8) { t2 = fmaxf(t2 - t1 * t1, 0.f); pin(t2); }
                    if constexpr (s == 11 && g == 6) { rinv = __builtin_amdgcn_rsqf(t2 + 1e-5f); pin(rinv); }
                    if constexpr (s == 11 && g == 8) { mri = -t1 * rinv; pin(mri); }
                    if constexpr (s == 12 && g == 4) *(float*)(smem + l_rs(1, 0) + w * INBOX + lane * 4) = c < 8 ? rinv : mri;
                    if constexpr (s >= 12 && s < 15 && g >= 6 && g < 9 && 3 * (s - 12) + g - 6 < 8) bc2b<3 * (s - 12) + g - 6>(rr_[3 * (s - 12) + g - 6], rm_[3 * (s - 12) + g - 6], rinv, mri);
                    if constexpr (s >= 15 && s < 19 && g >= 5) TT_GAP(5 * (s - 15) + g - 5)
                    site<6000 + 10 * s + g>();
                    SCHED();
                });
            });
            rr[0] = f32x4{rr_[0], rr_[1], rr_[2], rr_[3]}; rr[1] = f32x4{rr_[4], rr_[5], rr_[6], rr_[7]};
            asm volatile("kpf_end2:");
            ACC_FENCE10(acc[0], acc[1]);
            STAMP(13)
        } else {
#pragma unroll
            for (int nb = 0; nb < 5; ++nb) __builtin_amdgcn_raw_buffer_store_b128(__builtin_bit_cast(v4u, xin[nb]), ro, oo + 64 * nb, 0, OUTP);
            STAMP(21)
            STAMP_DUMP()
            asm volatile("kpf_end:");
        }
    };
    layer_body(std::integral_constant<int, 0>{});
    layer_body(std::integral_constant<int, 1>{});
}

extern "C" void kernel_launch(void* const* d_in, const int* in_sizes, int n_in,
                              void* d_out, int out_size, void* d_ws, size_t ws_size,
                              hipStream_t stream) {
    Params p;
    p.x0 = (const u16*)d_in[0];
    p.Lre = (const u16*)d_in[1];
    p.Lim = (const u16*)d_in[2];
    p.Bre = (const u16*)d_in[3];
    p.Bim = (const u16*)d_in[4];
    p.Cre = (const u16*)d_in[5];
    p.Cim = (const u16*)d_in[6];
    p.logdt = (const u16*)d_in[7];
    p.Dskip = (const u16*)d_in[8];
    p.k = (const u16*)d_in[10];
    p.v = (const u16*)d_in[11];
    p.shk = (const u16*)d_in[12];
    p.ssmk = (const u16*)d_in[13];
    p.ssmD = (const u16*)d_in[14];
    p.ws = (char*)d_ws + (size_t)WSOFF * 1048576;
    p.out = (float*)d_out;
    k_main<<<256, 256, 0, stream>>>(p);
}
```

```cpp
#include <hip/hip_runtime.h>
#include <stdint.h>
#include <utility>

#define WSOFF 0
#define STAMPS 0

typedef unsigned short u16;
typedef unsigned long long u64;
typedef unsigned v4u __attribute__((ext_vector_type(4)));
typedef unsigned v2u __attribute__((ext_vector_type(2)));
typedef float f32x4 __attribute__((ext_vector_type(4)));
typedef float f32x2 __attribute__((ext_vector_type(2)));
typedef short s16x4 __attribute__((ext_vector_type(4)));
typedef __bf16 bf16x2v __attribute__((ext_vector_type(2)));
#define LAS __attribute__((address_space(3)))
#define DI __device__ __forceinline__

constexpr int S_ = 2048, D_ = 640;
#define GF 1
#define KG0 8
#define KRT 12
#define KRS 2
#define KSN 0
#define KB1 4
#define KMB 1
#define H3 0
#define KF3 14
#define SBT 2
#define LAG 1
#define KR1 5
#define KR2 12
constexpr int RING1 = KR1;
#define KCR1 3
constexpr int kCR1 = KCR1;
#define KXA 10
#define KN3E 17
#define KN3 6
constexpr int kXA = KXA;
constexpr int kN3E = KN3E, kN3 = KN3;
#define KFHK 4
#define KFHP 14
constexpr int kFHK = KFHK, kFHP = KFHP;
#define KGPI 28
#define KGCI 34
#define KFGI 12
#define G2ND 1
#define KGSI 2
constexpr int kGSI = KGSI, kFGI = KFGI, kGPI = KGPI, kGCI = KGCI;

constexpr unsigned OFF_NONCE = 0;
constexpr unsigned OFF_ST = 4096;
constexpr unsigned OFF_FW = OFF_ST + 65536;
constexpr unsigned OFF_FK = OFF_FW + 131072;
constexpr unsigned OFF_FH = OFF_FK + 81920;
constexpr unsigned OFF_FX = OFF_FH + 262144;
constexpr unsigned OFF_FG = OFF_FX + 131072;
constexpr unsigned OFF_EG = OFF_FG + 98304;
constexpr unsigned OFF_SG = OFF_EG + 327680;
constexpr unsigned OFF_PS = OFF_SG + 524288;
constexpr unsigned OFF_KVT = OFF_PS + 131072;
constexpr unsigned OFF_GT = OFF_KVT + 2621440;
constexpr unsigned OFF_W2F = OFF_GT + 2621440;
constexpr unsigned OFF_X2F = OFF_W2F + 1638400;
constexpr unsigned OFF_DUM = OFF_X2F + 2621440;
constexpr unsigned OFF_HF = OFF_DUM + 163840;
constexpr unsigned WS_BYTES = OFF_HF + 2 * 5242880;
constexpr unsigned kNone = 0x40000000u;
constexpr unsigned OFF_DBG = 0x1800000;
#define I32M 1
#define KVS 1
#define KVU 0
#define MAP 0
#define TILES() const int xq = b & 7, j = b >> 3, mt2 = MAP ? 8 * (xq >> 1) + (j >> 2) : 4 * xq + (j >> 3), nt2 = MAP ? 4 * (xq & 1) + (j & 3) : j & 7, mt1 = mt2 >> 1, \
    nt1 = MAP ? 8 * (xq & 1) + (j & 7) : j & 15, i32 = 16 * (mt2 & 1) + mt1;

constexpr int SC = 102400;
constexpr int REGSZ = 13056;
constexpr int L_XR = SC;
constexpr int L_CO = SC + 12288;
constexpr int L_KB = SC + 51712;
constexpr int L_TAB = SC + 55808;
constexpr int L_CW = SC + 39168;
constexpr int L_FL = L_CW + 320;
constexpr int INBOX = 15360;
constexpr int l_rs(int l, int w) { return l ? SC + w * INBOX : L_KB + w * 256; }
constexpr int l_sg(int l, int w) { return l ? SC + w * INBOX + 256 : SC + (w == 0 ? 43264 : w == 1 ? 46080 : w == 2 ? 52736 : 56320); }
constexpr int l_tb(int l, int w) { return l ? SC + w * INBOX + 2048 : SC + 40960 + w * 5120; }
constexpr int LDS_BYTES = 163840;
constexpr int CV_E = 4608;
constexpr int CV_O = CV_E + (1056 + 15) * 4;

struct Params {
    const u16 *x0, *Lre, *Lim, *Bre, *Bim, *Cre, *Cim, *logdt, *Dskip, *k, *v, *shk, *ssmk, *ssmD;
    char* ws;
    float* out;
};

#define MF "v_mfma_f32_16x16x32_bf16 "
DI void mf(f32x4& c, v4u a, v4u b) { asm(MF "%0, %1, %2, %0" : "+v"(c) : "v"(a), "v"(b)); }
DI void mf0(f32x4& c, v4u a, v4u b) { asm(MF "%0, %1, %2, 0" : "=&v"(c) : "v"(a), "v"(b)); }
constexpr int W2R = 56;
#define WA(n) "a[%c" #n ":%c" #n "+3]"
template <int R> DI void mfa(f32x4& c, v4u a) { asm(MF "%0, " WA(2) ", %1, %0" : "+v"(c) : "v"(a), "i"(W2R + 4 * R)); }
template <int R> DI void mfa0(f32x4& c, v4u a) { asm(MF "%0, " WA(2) ", %1, 0" : "=&v"(c) : "v"(a), "i"(W2R + 4 * R)); }
#define ACC_FENCE10(c, d) asm volatile("" : "+v"(c[0]), "+v"(c[1]), "+v"(c[2]), "+v"(c[3]), "+v"(c[4]), "+v"(d[0]), "+v"(d[1]), "+v"(d[2]), "+v"(d[3]), "+v"(d[4]))
#define ACC_TIE5(c) asm volatile("" : "+v"(c[0]), "+v"(c[1]), "+v"(c[2]), "+v"(c[3]), "+v"(c[4]))
#define QP " quad_perm:[1,0,3,2] row_mask:0xf bank_mask:0xf"
DI void cstep(float (&t)[5], const float (&h)[5], const float (&lr)[5], const float (&ls)[5], float b0, float b1, float b2, float b3, float b4) {
    asm("v_fma_f32 %0, %10, %5, %20\n\tv_fma_f32 %1, %11, %6, %21\n\tv_fma_f32 %2, %12, %7, %22\n\tv_fma_f32 %3, %13, %8, %23\n\tv_fma_f32 %4, %14, %9, %24\n\t"
        "v_fmac_f32_dpp %0, %5, %15" QP "\n\tv_fmac_f32_dpp %1, %6, %16" QP "\n\tv_fmac_f32_dpp %2, %7, %17" QP "\n\tv_fmac_f32_dpp %3, %8, %18" QP "\n\tv_fmac_f32_dpp %4, %9, %19" QP
        : "=&v"(t[0]), "=&v"(t[1]), "=&v"(t[2]), "=&v"(t[3]), "=&v"(t[4])
        : "v"(h[0]), "v"(h[1]), "v"(h[2]), "v"(h[3]), "v"(h[4]), "v"(lr[0]), "v"(lr[1]), "v"(lr[2]), "v"(lr[3]), "v"(lr[4]),
          "v"(ls[0]), "v"(ls[1]), "v"(ls[2]), "v"(ls[3]), "v"(ls[4]), "v"(b0), "v"(b1), "v"(b2), "v"(b3), "v"(b4));
}
DI void cstep3(float* t, const float* h, const float* lr, const float* ls, const float* b) {
    asm("v_fma_f32 %0, %6, %3, %12\n\tv_fma_f32 %1, %7, %4, %13\n\tv_fma_f32 %2, %8, %5, %14\n\t"
        "v_fmac_f32_dpp %0, %3, %9" QP "\n\tv_fmac_f32_dpp %1, %4, %10" QP "\n\tv_fmac_f32_dpp %2, %5, %11" QP
        : "=&v"(t[0]), "=&v"(t[1]), "=&v"(t[2])
        : "v"(h[0]), "v"(h[1]), "v"(h[2]), "v"(lr[0]), "v"(lr[1]), "v"(lr[2]), "v"(ls[0]), "v"(ls[1]), "v"(ls[2]), "v"(b[0]), "v"(b[1]), "v"(b[2]));
}
template <int o> DI void chain24(float (*t)[3], const float (*b)[5], const float* lr, const float* ls) {
    asm("v_fma_f32 %0, %18, %16, %8\n\t" "v_fma_f32 %1, %19, %17, %9\n\t" "v_fmac_f32_dpp %0, %16, %20" QP "\n\t" "v_fmac_f32_dpp %1, %17, %21" QP "\n\t"
        "v_fma_f32 %2, %18, %0, %10\n\t" "v_fma_f32 %3, %19, %1, %11\n\t" "v_fmac_f32_dpp %2, %0, %20" QP "\n\t" "v_fmac_f32_dpp %3, %1, %21" QP "\n\t"
        "v_fma_f32 %4, %18, %2, %12\n\t" "v_fma_f32 %5, %19, %3, %13\n\t" "v_fmac_f32_dpp %4, %2, %20" QP "\n\t" "v_fmac_f32_dpp %5, %3, %21" QP "\n\t"
        "v_fma_f32 %6, %18, %4, %14\n\t" "v_fma_f32 %7, %19, %5, %15\n\t" "v_fmac_f32_dpp %6, %4, %20" QP "\n\t" "v_fmac_f32_dpp %7, %5, %21" QP "\n\t"
        : "=&v"(t[1][0]), "=&v"(t[1][1]), "=&v"(t[2][0]), "=&v"(t[2][1]), "=&v"(t[3][0]), "=&v"(t[3][1]), "=&v"(t[4][0]), "=&v"(t[4][1])
        : "v"(b[0][o + 0]), "v"(b[0][o + 1]), "v"(b[1][o + 0]), "v"(b[1][o + 1]), "v"(b[2][o + 0]), "v"(b[2][o + 1]), "v"(b[3][o + 0]), "v"(b[3][o + 1]), "v"(t[0][0]), "v"(t[0][1]), "v"(lr[o + 0]), "v"(lr[o + 1]), "v"(ls[o + 0]), "v"(ls[o + 1]));
}
template <int o> DI void chain33(float (*t)[3], const float (*b)[5], const float* lr, const float* ls) {
    asm("v_fma_f32 %0, %21, %18, %9\n\t" "v_fma_f32 %1, %22, %19, %10\n\t" "v_fma_f32 %2, %23, %20, %11\n\t" "v_fmac_f32_dpp %0, %18, %24" QP "\n\t" "v_fmac_f32_dpp %1, %19, %25" QP "\n\t" "v_fmac_f32_dpp %2, %20, %26" QP "\n\t"
        "v_fma_f32 %3, %21, %0, %12\n\t" "v_fma_f32 %4, %22, %1, %13\n\t" "v_fma_f32 %5, %23, %2, %14\n\t" "v_fmac_f32_dpp %3, %0, %24" QP "\n\t" "v_fmac_f32_dpp %4, %1, %25" QP "\n\t" "v_fmac_f32_dpp %5, %2, %26" QP "\n\t"
        "v_fma_f32 %6, %21, %3, %15\n\t" "v_fma_f32 %7, %22, %4, %16\n\t" "v_fma_f32 %8, %23, %5, %17\n\t" "v_fmac_f32_dpp %6, %3, %24" QP "\n\t" "v_fmac_f32_dpp %7, %4, %25" QP "\n\t" "v_fmac_f32_dpp %8, %5, %26" QP "\n\t"
        : "=&v"(t[1][0]), "=&v"(t[1][1]), "=&v"(t[1][2]), "=&v"(t[2][0]), "=&v"(t[2][1]), "=&v"(t[2][2]), "=&v"(t[3][0]), "=&v"(t[3][1]), "=&v"(t[3][2])
        : "v"(b[0][o + 0]), "v"(b[0][o + 1]), "v"(b[0][o + 2]), "v"(b[1][o + 0]), "v"(b[1][o + 1]), "v"(b[1][o + 2]), "v"(b[2][o + 0]), "v"(b[2][o + 1]), "v"(b[2][o + 2]), "v"(t[0][0]), "v"(t[0][1]), "v"(t[0][2]), "v"(lr[o + 0]), "v"(lr[o + 1]), "v"(lr[o + 2]), "v"(ls[o + 0]), "v"(ls[o + 1]), "v"(ls[o + 2]));
}
template <int o> DI void chain32(float (*t)[3], const float (*b)[5], const float* lr, const float* ls) {
    asm("v_fma_f32 %0, %15, %12, %6\n\t" "v_fma_f32 %1, %16, %13, %7\n\t" "v_fma_f32 %2, %17, %14, %8\n\t" "v_fmac_f32_dpp %0, %12, %18" QP "\n\t" "v_fmac_f32_dpp %1, %13, %19" QP "\n\t" "v_fmac_f32_dpp %2, %14, %20" QP "\n\t"
        "v_fma_f32 %3, %15, %0, %9\n\t" "v_fma_f32 %4, %16, %1, %10\n\t" "v_fma_f32 %5, %17, %2, %11\n\t" "v_fmac_f32_dpp %3, %0, %18" QP "\n\t" "v_fmac_f32_dpp %4, %1, %19" QP "\n\t" "v_fmac_f32_dpp %5, %2, %20" QP "\n\t"
        : "=&v"(t[1][0]), "=&v"(t[1][1]), "=&v"(t[1][2]), "=&v"(t[2][0]), "=&v"(t[2][1]), "=&v"(t[2][2])
        : "v"(b[0][o + 0]), "v"(b[0][o + 1]), "v"(b[0][o + 2]), "v"(b[1][o + 0]), "v"(b[1][o + 1]), "v"(b[1][o + 2]), "v"(t[0][0]), "v"(t[0][1]), "v"(t[0][2]), "v"(lr[o + 0]), "v"(lr[o + 1]), "v"(lr[o + 2]), "v"(ls[o + 0]), "v"(ls[o + 1]), "v"(ls[o + 2]));
}
DI void pstep(f32x2& xr, f32x2& xs, f32x2 pr, f32x2 ps, f32x2 m) {
    asm("v_pk_mul_f32 %0, %2, %4 op_sel_hi:[1,0]\n\tv_pk_mul_f32 %1, %3, %4 op_sel_hi:[1,0]\n\t"
        "v_pk_fma_f32 %0, %3, %4, %0 op_sel:[0,1,0] neg_lo:[1,0,0] neg_hi:[1,0,0]\n\tv_pk_fma_f32 %1, %2, %4, %1 op_sel:[0,1,0]"
        : "=&v"(xr), "=&v"(xs) : "v"(pr), "v"(ps), "v"(m));
}
DI void padd(f32x2 (&h)[4], const f32x2 (&pr)[2], const f32x2 (&ps)[2], f32x2 c) {
    f32x2 d;
    asm("v_pk_mul_f32 %4, %6, %9 op_sel:[1,0]\n\tv_pk_fma_f32 %0, %5, %9, %0 op_sel_hi:[1,0,1]\n\tv_pk_fma_f32 %1, %6, %9, %1 op_sel_hi:[1,0,1]\n\t"
        "v_pk_fma_f32 %4, %8, %9, %4 op_sel:[1,1,0] op_sel_hi:[1,0,1] neg_hi:[1,0,0]\n\tv_pk_fma_f32 %0, %7, %9, %0 op_sel:[0,1,0]\n\tv_pk_fma_f32 %1, %8, %9, %1 op_sel:[0,1,0]\n\t"
        "v_pk_fma_f32 %2, %5, %4, %2 op_sel_hi:[1,0,1]\n\tv_pk_fma_f32 %3, %6, %4, %3 op_sel_hi:[1,0,1]\n\tv_pk_fma_f32 %2, %7, %4, %2 op_sel:[0,1,0]\n\tv_pk_fma_f32 %3, %8, %4, %3 op_sel:[0,1,0]"
        : "+v"(h[0]), "+v"(h[1]), "+v"(h[2]), "+v"(h[3]), "=&v"(d) : "v"(pr[0]), "v"(pr[1]), "v"(ps[0]), "v"(ps[1]), "v"(c));
}
DI void cstepi(float& b0, float& b1, float& b2, float& b3, float& b4, float h0, float h1, float h2, float h3, float h4, const float (&lr)[5], const float (&ls)[5]) {
    asm("v_fmac_f32 %0, %10, %5\n\tv_fmac_f32 %1, %11, %6\n\tv_fmac_f32 %2, %12, %7\n\tv_fmac_f32 %3, %13, %8\n\tv_fmac_f32 %4, %14, %9\n\t"
        "v_fmac_f32_dpp %0, %5, %15" QP "\n\tv_fmac_f32_dpp %1, %6, %16" QP "\n\tv_fmac_f32_dpp %2, %7, %17" QP "\n\tv_fmac_f32_dpp %3, %8, %18" QP "\n\tv_fmac_f32_dpp %4, %9, %19" QP
        : "+v"(b0), "+v"(b1), "+v"(b2), "+v"(b3), "+v"(b4)
        : "v"(h0), "v"(h1), "v"(h2), "v"(h3), "v"(h4), "v"(lr[0]), "v"(lr[1]), "v"(lr[2]), "v"(lr[3]), "v"(lr[4]), "v"(ls[0]), "v"(ls[1]), "v"(ls[2]), "v"(ls[3]), "v"(ls[4]));
}
#define UNDEF8(c) asm volatile("" : "=v"(c[0]), "=v"(c[1]), "=v"(c[2]), "=v"(c[3]), "=v"(c[4]), "=v"(c[5]), "=v"(c[6]), "=v"(c[7]))
#define PIN5(t) asm volatile("s_nop 1" : "+v"(t[0]), "+v"(t[1]), "+v"(t[2]), "+v"(t[3]), "+v"(t[4]))
DI unsigned cvtpk(float lo, float hi) {
    f32x2 v = {lo, hi};
    return __builtin_bit_cast(unsigned, __builtin_convertvector(v, bf16x2v));
}
DI float lo16f(unsigned u) { return __uint_as_float(u << 16); }
DI float hi16f(unsigned u) { return __uint_as_float(u & 0xffff0000u); }
DI float rbf(float v) { return lo16f(cvtpk(v, 0.f)); }
template <int CTRL> DI float dpp(float x) {
    return __int_as_float(__builtin_amdgcn_update_dpp(0, __float_as_int(x), CTRL, 0xF, 0xF, true));
}
DI float rowsum16(float x) {
    x += dpp<0x128>(x); x += dpp<0x124>(x); x += dpp<0x122>(x); x += dpp<0x121>(x);
    return x;
}
DI float xsum32(float x) { auto r = __builtin_amdgcn_permlane32_swap(__float_as_uint(x), __float_as_uint(x), false, false); return __uint_as_float(r[0]) + __uint_as_float(r[1]); }
DI void xsum2(float& a, float& d) {
    auto r = __builtin_amdgcn_permlane16_swap(__float_as_uint(a), __float_as_uint(d), false, false);
    const float s = __uint_as_float(r[0]) + __uint_as_float(r[1]);
    const float t = xsum32(s);
    auto u = __builtin_amdgcn_permlane16_swap(__float_as_uint(t), __float_as_uint(t), false, false);
    a = __uint_as_float(u[0]); d = __uint_as_float(u[1]);
}
DI float xsum2h(float a, float d) {
    auto r = __builtin_amdgcn_permlane16_swap(__float_as_uint(a), __float_as_uint(d), false, false);
    return xsum32(__uint_as_float(r[0]) + __uint_as_float(r[1]));
}
DI float hsum4(f32x4 v) { return (v.x + v.y) + (v.z + v.w); }
typedef __amdgpu_buffer_rsrc_t rsrc_t;
constexpr int SC1 = 16;
DI v4u ld128(rsrc_t rs, unsigned off) { return __builtin_amdgcn_raw_buffer_load_b128(rs, off, 0, SC1); }
DI v4u ld128s(rsrc_t rs, unsigned off, unsigned soff) { return __builtin_amdgcn_raw_buffer_load_b128(rs, off, soff, SC1); }
DI v2u ld64(rsrc_t rs, unsigned off) { return __builtin_amdgcn_raw_buffer_load_b64(rs, off, 0, SC1); }
DI void st128(v4u v, rsrc_t rs, unsigned off) { __builtin_amdgcn_raw_buffer_store_b128(v, rs, off, 0, SC1); }
DI void st64(v2u v, rsrc_t rs, unsigned off) { __builtin_amdgcn_raw_buffer_store_b64(v, rs, off, 0, SC1); }
#define LGKM0() { __builtin_amdgcn_s_waitcnt(0xC07F); asm volatile("" ::: "memory"); }
#define DRAIN() asm volatile("s_waitcnt vmcnt(0)" ::: "memory")
#define ORDER() __builtin_amdgcn_fence(__ATOMIC_ACQUIRE, "wavefront")
#define SCHED() __builtin_amdgcn_sched_barrier(0)
constexpr unsigned kSpinCap = 1u << 17;
#define WAVE_WAIT(...)                                                      \
    {                                                                       \
        unsigned spins_ = dead ? kSpinCap : 0u;                             \
        _Pragma("nounroll") for (;;) {                                      \
            asm volatile("" ::: "memory");                                  \
            bool ok;                                                        \
            __VA_ARGS__;                                                    \
            if (__builtin_amdgcn_ballot_w64(!ok) == 0ull) break;            \
            if (++spins_ > kSpinCap) { dead = true; break; }                \
            __builtin_amdgcn_s_sleep(1);                                    \
        }                                                                   \
        ORDER();                                                            \
    }
#define KPD1 2
#define KPDS 8
#define KPDS0 2
#define KPF0 12
#define KPF1 4
constexpr int PD1 = KPD1, PDS = KPDS, PDS0 = KPDS0;
#define PL(r) "global_load_dwordx2 " r ", %5, %6 sc1\n\t"
#define PS_(r) "s_waitcnt vmcnt(1)\n\tv_cmp_ne_u64 vcc, %7, " r "\n\ts_cbranch_vccz 2f\n\t" PL(r)
DI void pipe_flag(v2u (&t)[4], unsigned off, const void* base, u64 tag, bool& dead) {
    unsigned n = dead ? 0u : kSpinCap;
    asm volatile("s_sleep %8\n\t" PL("%0") "s_sleep %9\n\t" PL("%1")
                 "1:\n\t" PS_("%0") PS_("%1") "s_sub_u32 %4, %4, 1\n\ts_cbranch_scc0 1b\n2:"
                 : "=&v"(t[0]), "=&v"(t[1]), "=&v"(t[2]), "=&v"(t[3]), "+s"(n) : "v"(off), "s"(base), "s"(tag), "i"(KPF0), "i"(KPF1) : "memory", "vcc", "scc");
    dead = __builtin_amdgcn_readfirstlane(n) == ~0u;
}
#define WAVE_RETRY(OK, ...) { if (__builtin_expect(__builtin_amdgcn_ballot_w64(!(OK)) != 0ull, 0)) WAVE_WAIT({ __VA_ARGS__; ok = OK; }) ORDER(); }
#define STAMP(i) { if ((i) == 12) { asm volatile("s_memrealtime %0\n\ts_waitcnt lgkmcnt(0)" : "=s"(ta_) :: "memory"); } if ((i) == 13) { asm volatile("s_memrealtime %0\n\ts_waitcnt lgkmcnt(0)" : "=s"(tb_) :: "memory"); } }
#define STAMP_DUMP() { if (b0 == 100 && w0 == 0) { const u64 e_ = __builtin_amdgcn_s_memrealtime(); u64 d_ = (tb_ - ta_) * 8ull; d_ = d_ > 40000ull ? 40000ull : d_; while (__builtin_amdgcn_s_memrealtime() - e_ < d_) __builtin_amdgcn_s_sleep(1); } }

#define PADS 9000, 9001, 9002, 9003, 9004, 9005, 9006, 9007, 9008
#define T1POS 27
#define TW 2
#define KLB0 0
#define KLB1 10
#define SKIP0 0
#define SKIP7 0
#define SKIP5 0
#define SKIP4 0
constexpr int kPads[] = {PADS};
constexpr int pad_next(int id) {
    for (unsigned i = 0; i + 1 < sizeof(kPads) / sizeof(int); ++i) if (kPads[i] == id) return kPads[i + 1];
    return -1;
}
template <int ID> DI void site() {
    if constexpr (pad_next(ID) >= 0) asm volatile("kp%c0:\n\ts_cbranch_execz kp%c1" ::"i"(ID), "i"(pad_next(ID)));
    else asm volatile("");
}
template <int T> DI void tour() {
    if constexpr (pad_next(9000 + T) >= 0 && pad_next(9000 + T) != 9001 + T) asm volatile("s_mov_b64 exec, 0\n\ts_branch kp%c0\nkp%c1:\n\ts_mov_b64 exec, -1" ::"i"(pad_next(9000 + T)), "i"(9001 + T));
}
template <int N> DI void skip() { if constexpr (N > 0) asm volatile("s_branch 1f\n\t.rept %c0\n\ts_nop 0\n\t.endr\n1:" ::"i"(N)); }
template <class F, int... I> DI void static_for_impl(std::integer_sequence<int, I...>, F&& f) { (f(std::integral_constant<int, I>{}), ...); }
template <int N, class F> DI void static_for(F&& f) { static_for_impl(std::make_integer_sequence<int, N>{}, f); }
#define IC(n) std::integral_constant<int, (n)>{}
#define LAMBDA(v) [&](auto v) __attribute__((always_inline))
#define PK2 " op_sel:[1,1,0] op_sel_hi:[1,0,1] neg_lo:[1,0,0]"
DI f32x2 cmul(f32x2 a, f32x2 b) {
    f32x2 t;
    asm("v_pk_mul_f32 %0, %1, %2 op_sel_hi:[0,1]\n\ts_nop 0\n\tv_pk_fma_f32 %0, %1, %2, %0" PK2 "\n\ts_nop 0" : "=&v"(t) : "v"(a), "v"(b));
    return t;
}
DI f32x2 cfma(f32x2 a, f32x2 b, f32x2 c) {
    f32x2 t;
    asm("v_pk_fma_f32 %0, %1, %2, %3 op_sel_hi:[0,1,1]\n\ts_nop 0\n\tv_pk_fma_f32 %0, %1, %2, %0" PK2 "\n\ts_nop 0" : "=&v"(t) : "v"(a), "v"(b), "v"(c));
    return t;
}
DI void cfma4(f32x2 (&t)[4], f32x2 a, f32x2 b0, f32x2 b1, f32x2 b2, f32x2 b3, f32x2 c0, f32x2 c1, f32x2 c2, f32x2 c3) {
    asm("v_pk_fma_f32 %0, %4, %5, %9 op_sel_hi:[0,1,1]\n\tv_pk_fma_f32 %1, %4, %6, %10 op_sel_hi:[0,1,1]\n\tv_pk_fma_f32 %2, %4, %7, %11 op_sel_hi:[0,1,1]\n\tv_pk_fma_f32 %3, %4, %8, %12 op_sel_hi:[0,1,1]\n\t"
        "v_pk_fma_f32 %0, %4, %5, %0" PK2 "\n\tv_pk_fma_f32 %1, %4, %6, %1" PK2 "\n\tv_pk_fma_f32 %2, %4, %7, %2" PK2 "\n\tv_pk_fma_f32 %3, %4, %8, %3" PK2 "\n\ts_nop 0"
        : "=&v"(t[0]), "=&v"(t[1]), "=&v"(t[2]), "=&v"(t[3]) : "v"(a), "v"(b0), "v"(b1), "v"(b2), "v"(b3), "v"(c0), "v"(c1), "v"(c2), "v"(c3));
}
DI void gacc2(f32x2& h0, f32x2& h1, f32x2 w0, f32x2 w1, v4u e0, v4u e1) {
    const v2u a0 = {e0.x, e0.y}, b0 = {e0.z, e0.w}, a1 = {e1.x, e1.y}, b1 = {e1.z, e1.w};
    asm("v_pk_fma_f32 %0, %4, %2, %0 op_sel_hi:[0,1,1]\n\tv_pk_fma_f32 %1, %6, %3, %1 op_sel_hi:[0,1,1]\n\t"
        "v_pk_fma_f32 %0, %5, %2, %0 op_sel:[0,1,0] op_sel_hi:[0,0,1] neg_lo:[1,0,0]\n\tv_pk_fma_f32 %1, %7, %3, %1 op_sel:[0,1,0] op_sel_hi:[0,0,1] neg_lo:[1,0,0]"
        : "+v"(h0), "+v"(h1) : "v"(w0), "v"(w1), "v"(a0), "v"(b0), "v"(a1), "v"(b1));
}
DI s16x4 trread(char* smem, int byteoff) {
    return __builtin_amdgcn_ds_read_tr16_b64_v4i16((LAS s16x4*)(smem + byteoff));
}

#define CV_LOAD_A(A, a, rr_, hf) { const unsigned* ap_ = (const unsigned*)(smem + ab[(a) >> 1] + (((a) & 1) ? 0 : 512) + 64 * (7 - (rr_)) + 8 * (hf)); A[a][2 * (hf)] = ap_[0]; A[a][2 * (hf) + 1] = ap_[1]; }
#define CV_LOAD_B(B, e, rr_) { B[e] = *(const v4u*)(smem + bb + 64 * (7 - (rr_)) + 512 * (e)); }
constexpr int cv_pair(int lo, int hi, int n) {
    for (int a = 0; a < 8; ++a)
        for (int e = 0; e < 8; ++e)
            if (a + e >= lo && a + e < hi && n-- == 0) return 8 * a + e;
    return -1;
}
template <int R, int PF, bool OWN, class F, class G>
DI void conv_round(f32x4 (&ca)[8], v4u (&A)[8], const v4u (&B)[8], v4u (&An)[8], v4u (&Bn)[8], const char* smem, const int (&ab)[4], int bb, F&& fill, G&& last) {
    static_for<(R == 7 ? KG0 : 36)>(LAMBDA(g_) {
        constexpr int g = g_.value, a = cv_pair(0, 8, g) / 8, e = cv_pair(0, 8, g) % 8;
        mf(ca[a + e], A[a], B[e]);
        SCHED();
        if constexpr (OWN && g < 12) { CV_LOAD_A(A, 2 + g / 2, R, g & 1) SCHED(); }
        if constexpr (g < 4 * PF && (g & 3) != 3) { if constexpr ((g & 3) == 2) CV_LOAD_B(Bn, g >> 2, R + 1) else CV_LOAD_A(An, g >> 2, R + 1, g & 1) SCHED(); }
        site<3000 + 36 * R + g>();
        if constexpr (R == 7) { last(g_); SCHED(); }
        if constexpr ((g & 3) == 3 && g < 28) { fill(std::integral_constant<int, 7 * R + g / 4>{}); SCHED(); }
    });
}
template <int R0, int R1, class F, class G, class M>
DI void conv_part(f32x4 (&ca)[8], const char* smem, const int (&ab)[4], int bb, F&& fill, G&& last, M&& mid, v4u (&A)[2][8], v4u (&B)[2][8]) {
    if constexpr (R0 > 0) { CV_LOAD_A(A[0], 0, R0, 0) CV_LOAD_A(A[0], 0, R0, 1) SCHED(); }
    static_for<8>(LAMBDA(a_) { constexpr int a = a_.value; CV_LOAD_B(B[0], a, R0) SCHED(); });
    if constexpr (R0 > 0) { CV_LOAD_A(A[0], 1, R0, 0) CV_LOAD_A(A[0], 1, R0, 1) SCHED(); }
    else {
        CV_LOAD_A(A[0], 0, 0, 0) CV_LOAD_A(A[0], 0, 0, 1) SCHED();
        const v4u ad = mid();
        SCHED();
        static_for<8>(LAMBDA(e_) {
            constexpr int e = e_.value;
            mf0(ca[e], ad, B[0][e]); SCHED();
            if constexpr (e < 7) { CV_LOAD_A(A[0], e + 1, 0, 0) CV_LOAD_A(A[0], e + 1, 0, 1) SCHED(); }
        });
    }
    static_for<R1 - R0>(LAMBDA(r_) {
        constexpr int r = r_.value;
        conv_round<R0 + r, (R0 + r + 1 < R1 ? 8 : R0 == 0 && H3 ? 3 : 0), (r == 0 && R0 > 0)>(ca, A[r & 1], B[r & 1], A[(r & 1) ^ 1], B[(r & 1) ^ 1], smem, ab, bb, fill, last);
    });
    if constexpr (R0 == 0 && H3) static_for<6>(LAMBDA(g_) { constexpr int a = cv_pair(0, 3, g_.value) / 8, e = cv_pair(0, 3, g_.value) % 8; mf(ca[a + e], A[R1 & 1][a], B[R1 & 1][e]); SCHED(); });
}
DI void lds_wait3(int a) {
    unsigned x, y, z;
    asm volatile("1:\n\tds_read_b32 %0, %3\n\tds_read_b32 %1, %3 offset:4\n\tds_read_b32 %2, %3 offset:8\n\ts_waitcnt lgkmcnt(0)\n\t"
                 "v_and_b32 %0, %0, %1\n\tv_and_b32 %0, %0, %2\n\tv_cmp_eq_u32 vcc, 0, %0\n\ts_cbranch_vccnz 1b" : "=&v"(x), "=&v"(y), "=&v"(z) : "v"(a) : "memory", "vcc");
}
DI void add2(float& a, float& b, float x, float y) { asm volatile("v_add_f32 %0, %0, %2\n\tv_add_f32 %1, %1, %3" : "+v"(a), "+v"(b) : "v"(x), "v"(y)); }
template <int IMM> DI void gld(v4u& dst, unsigned voff, const void* sbase) {
    asm volatile("global_load_dwordx4 %0, %1, %2 offset:%3" : "=v"(dst) : "v"(voff), "s"(sbase), "i"(IMM));
}
#define OOB 3
#define OUTP 16
#define NTM 31
template <int IMM, int BIT> DI void gldn(v4u& dst, unsigned voff, const void* sbase) {
    if constexpr ((NTM >> BIT) & 1) asm volatile("global_load_dwordx4 %0, %1, %2 offset:%3 nt" : "=v"(dst) : "v"(voff), "s"(sbase), "i"(IMM));
    else gld<IMM>(dst, voff, sbase);
}
DI void gldu16(unsigned& dst, unsigned voff, const void* sbase) { asm volatile("global_load_ushort %0, %1, %2" : "=v"(dst) : "v"(voff), "s"(sbase)); }
template <int IMM> DI void gld64(v4u& dst, const void* a) {
    if constexpr ((NTM >> 5) & 1) asm volatile("global_load_dwordx4 %0, %1, off offset:%2 nt" : "=v"(dst) : "v"(a), "i"(IMM));
    else asm volatile("global_load_dwordx4 %0, %1, off offset:%2" : "=v"(dst) : "v"(a), "i"(IMM));
}
template <int V> DI void SETM0(unsigned ldsbase) { asm volatile("s_add_u32 m0, %0, %1" ::"s"(ldsbase), "i"(V)); }
template <int GOFF> DI void GLD0(unsigned voff, const char* sbase) { asm volatile("global_load_lds_dwordx4 %0, %1 offset:%2" ::"v"(voff), "s"(sbase), "i"(GOFF) : "memory"); }
template <int GOFF, int V> DI void GLD1(unsigned voff, const char* sbase, unsigned ldsbase) {
    asm volatile("global_load_lds_dwordx4 %0, %1 offset:%3\n\ts_add_u32 m0, %2, %4" ::"v"(voff), "s"(sbase), "s"(ldsbase), "i"(GOFF), "i"(V) : "memory");
}
constexpr int dma_goff(int it) { return it < 40 ? (it >> 2) * 128 : ((it - 100) / 5) * 256; }
constexpr int dma_loff(int it) { return it < 40 ? L_XR + ((it >> 2) % 3) * 4096 + (it & 3) * 1024 : ((it - 100) % 5) * 20480 + ((it - 100) / 5) * 2048; }
#define KVF 1
#define KFD0 10
#define DSE 1
#define KNF 13
#define KFS 6
#define APOS 0
#define PSA 0
#define CWP 1
#define SAF 2
#define KD0 36
#define KDS 3
#define KNW 11
constexpr int kD0 = KD0, kDS = KDS;
static_assert(kD0 + 26 * kDS < 117, "");
#define KW2S 17
#define KW2M 0x4A
#define KFWP 14
constexpr int kW2S = KW2S, kW2M = KW2M;
constexpr int NW2P = (20 - kW2S) * __builtin_popcount(kW2M);
constexpr int P1_PRE = 34;
constexpr int p1_pre(int i) { return i < 5 ? 100 + i : i < 13 ? i - 5 : i < 18 ? 105 + (i - 13) : i < 22 ? 8 + (i - 18) : i < 27 ? 110 + (i - 22) : 400; }
constexpr int p1_item(int s, int g) {
    if (s == 7 && g == 0) return 324;
    if (g == 9) return s < 10 ? 115 + s : s == 11 ? 202 : s == 12 ? 203 : -1;
    if (s == KFWP && g == 1) return 208;
    if (s >= kW2S && ((kW2M >> g) & 1)) return 500 + (s - kW2S) * __builtin_popcount(kW2M) + __builtin_popcount(kW2M & ((1 << g) - 1));
    if ((s & 1) == 0 && s <= 12) return g >= 5 ? 4 * (s / 2 + 3) + (g - 5) : -1;
    if (s == 14 && g == 5) return 207;
    return -1;
}
constexpr int p1_cnt(int s, int g) {
    int n = P1_PRE;
    for (int t = 0; t <= s; ++t)
        for (int u = 0; u < 10; ++u) {
            if (t == s && u >= g) break;
            if (p1_item(t, u) >= 0 && p1_item(t, u) != 319) ++n;
        }
    return n;
}
constexpr int p1_pos(int code) {
    for (int i = 0; i < P1_PRE; ++i) if (p1_pre(i) == code) return i;
    for (int t = 0; t < 20; ++t)
        for (int u = 0; u < 10; ++u) if (p1_item(t, u) == code) return p1_cnt(t, u);
    return -1000;
}
constexpr int p1_cap(int s, int g) {
    int u = (s < 19 && g < 5) + (g == 9 && s < 19);
    if ((s & 1) && s < 19 && g >= 5 && g < 9) u += 1 + (g == 5);
    if ((s & 1) == 0 && g == 4) u += 1;
    const int it = p1_item(s, g), nt = g < 9 ? p1_item(s, g + 1) : -1;
    u += (it >= 0 && it < 500 && it != 208) + (nt >= 0 && nt < 200);
    if ((s == 18 && g == 1) || (s == 19 && g == 2) || (g == 9 && ((s & 3) == 2 || s == 17))) u += 2;
    return u < 2 ? 2 - u : 0;
}
constexpr int p1_slot(int s0, int s, int g, int gmax) {
    int n = 0;
    for (int t = s0; t <= s; ++t)
        for (int u = 0; u < gmax && !(t == s && u >= g); ++u) n += p1_cap(t, u);
    return n;
}
constexpr int p1_lim(int n) { return n > 63 ? 63 : n; }
template <int IMM> DI void gld4s(v4u& dst, unsigned voff, const void* sbase) { asm volatile("global_load_dwordx4 %0, %1, %2 offset:%3 sc1" : "=v"(dst) : "v"(voff), "s"(sbase), "i"(IMM)); }
template <int IMM> DI void gld2s(v2u& dst, unsigned voff, const void* sbase) { asm volatile("global_load_dwordx2 %0, %1, %2 offset:%3 sc1" : "=v"(dst) : "v"(voff), "s"(sbase), "i"(IMM)); }
template <class T> DI void pin(T& v) { asm volatile("" : "+v"(v)); }
DI void mul2(float& a, float& b, float x, float y, float s) { asm volatile("v_mul_f32 %0, %2, %4\n\tv_mul_f32 %1, %3, %4" : "=&v"(a), "=&v"(b) : "v"(x), "v"(y), "v"(s)); }
DI void xor2(float& a, float& b, unsigned x, unsigned y, unsigned s) { asm volatile("v_xor_b32 %0, %2, %4\n\tv_xor_b32 %1, %3, %4" : "=&v"(a), "=&v"(b) : "v"(x), "v"(y), "v"(s)); }
DI void rot2(unsigned& a, unsigned& b, unsigned x, unsigned y) { asm volatile("v_alignbit_b32 %0, %2, %2, 16\n\tv_alignbit_b32 %1, %3, %3, 16" : "=&v"(a), "=&v"(b) : "v"(x), "v"(y)); }
DI void mix2(unsigned& a, unsigned& b, unsigned x, unsigned y, unsigned z) { asm volatile("v_bfi_b32 %0, %5, %2, %3\n\tv_bfi_b32 %1, %5, %3, %4" : "=&v"(a), "=&v"(b) : "v"(x), "v"(y), "v"(z), "s"(0xffffu)); }
template <int I> DI void shr1(unsigned& o, unsigned prev, unsigned x) {
    if constexpr (I > 0) asm volatile("v_mov_b32_dpp %0, %1 wave_ror:1 row_mask:0xf bank_mask:0xf\n\tv_mov_b32_dpp %0, %2 wave_shr:1 row_mask:0xf bank_mask:0xf" : "=&v"(o) : "v"(prev), "v"(x));
    else asm volatile("v_mov_b32_dpp %0, %1 wave_shr:1 row_mask:0xf bank_mask:0xf bound_ctrl:0" : "=&v"(o) : "v"(x));
}
template <int G> DI void bc2(float& a, float& b, unsigned x) {
    asm volatile("v_mov_b32_dpp %0, %2 row_newbcast:%3 row_mask:0xf bank_mask:0xf\n\tv_mov_b32_dpp %1, %2 row_newbcast:%4 row_mask:0xf bank_mask:0xf" : "=&v"(a), "=&v"(b) : "v"(x), "i"(G), "i"(G + 8));
}
DI void rot1(unsigned& a, unsigned x) { asm volatile("v_alignbit_b32 %0, %1, %1, 16" : "=v"(a) : "v"(x)); }
DI void mix1(unsigned& a, unsigned x, unsigned y) { asm volatile("v_bfi_b32 %0, %3, %1, %2" : "=v"(a) : "v"(x), "v"(y), "s"(0xffffu)); }
DI void ror1(unsigned& o, unsigned prev) { asm volatile("v_mov_b32_dpp %0, %1 wave_ror:1 row_mask:0xf bank_mask:0xf" : "=v"(o) : "v"(prev)); }
DI void shr1b(unsigned& o, unsigned x) { asm volatile("v_mov_b32_dpp %0, %1 wave_shr:1 row_mask:0xf bank_mask:0xf" : "+v"(o) : "v"(x)); }
DI void xor1(float& a, unsigned x, unsigned s) { asm volatile("v_xor_b32 %0, %1, %2" : "=v"(a) : "v"(x), "v"(s)); }
template <int G> DI void bc1(float& a, unsigned x) { asm volatile("v_mov_b32_dpp %0, %1 row_newbcast:%2 row_mask:0xf bank_mask:0xf" : "=v"(a) : "v"(x), "i"(G)); }
template <int G> DI void mulb(float& o, unsigned x, float y) { asm volatile("v_mul_f32_dpp %0, %1, %2 row_newbcast:%3 row_mask:0xf bank_mask:0xf" : "=v"(o) : "v"(x), "v"(y), "i"(G)); }
template <int N> DI void lgkm() { __builtin_amdgcn_s_waitcnt(0xC07F | (N << 8)); asm volatile("" ::: "memory"); }
DI void unp2(float& a, float& b, unsigned x) { asm volatile("v_lshlrev_b32 %0, 16, %2\n\tv_and_b32 %1, 0xffff0000, %2" : "=&v"(a), "=&v"(b) : "v"(x)); }
#define GL(a) "global_load_dwordx4 a[" #a ":" #a "+3], %7, %8 sc1\n\tglobal_load_dwordx4 a[" #a "+4:" #a "+7], %7, %8 offset:256 sc1\n\t"
#define AR(d, a, o) "v_accvgpr_read_b32 " d ", a[" #a "+" #o "]\n\t"
#define GS_(a) "s_waitcnt vmcnt(4)\n\t" AR("%2", a, 1) AR("%3", a, 3) AR("%4", a, 5) AR("%5", a, 7) AR("%0", a, 0) AR("%1", a, 2)                              \
    "v_xor_b32 %2, %9, %2\n\tv_bitop3_b32 %4, %4, %9, %2 bitop3:0xbe\n\tv_bitop3_b32 %4, %3, %9, %4 bitop3:0xbe\n\tv_bitop3_b32 %4, %5, %9, %4 bitop3:0xbe\n\t" \
    AR("%2", a, 4) AR("%3", a, 6) "v_cmp_ne_u32 vcc, 0, %4\n\ts_cbranch_vccz 2f\n\t" GL(a)
template <int D0> DI void pipe_gran(float& a, float& d, unsigned off, const void* base, unsigned tag, bool& dead) {
    unsigned n = dead ? 0u : kSpinCap, y, z;
    float a1, d1;
    asm volatile("s_sleep %10\n\t" GL(32) "s_sleep %11\n\t" GL(40) "s_sleep %11\n\t" GL(48)
                 "1:\n\t" GS_(32) GS_(40) GS_(48) "s_sub_u32 %6, %6, 1\n\ts_cbranch_scc0 1b\n2:"
                 : "=&v"(a), "=&v"(d), "=&v"(a1), "=&v"(d1), "=&v"(y), "=&v"(z), "+s"(n) : "v"(off), "s"(base), "s"(tag), "i"(D0), "i"(PD1) : "memory", "vcc", "scc");
    dead = __builtin_amdgcn_readfirstlane(n) == ~0u;
    a += a1; d += d1;
}
DI void fma2(float& a, float& b, float x, float y, float s, float t, float u, float v) {
    asm volatile("v_fma_f32 %0, %2, %4, %6\n\tv_fma_f32 %1, %3, %5, %7" : "=&v"(a), "=&v"(b) : "v"(x), "v"(y), "v"(s), "v"(t), "v"(u), "v"(v));
}
DI void cvt2(unsigned& a, unsigned& b, f32x4 v) { asm volatile("v_cvt_pk_bf16_f32 %0, %2, %3\n\tv_cvt_pk_bf16_f32 %1, %4, %5" : "=&v"(a), "=&v"(b) : "v"(v.x), "v"(v.y), "v"(v.z), "v"(v.w)); }
DI void swp2n(unsigned& a, unsigned& b, unsigned& c, unsigned& d) { asm volatile("v_permlane16_swap_b32 %0, %1\n\tv_permlane16_swap_b32 %2, %3" : "+v"(a), "+v"(b), "+v"(c), "+v"(d)); }
DI void swp2(unsigned& a, unsigned& b, unsigned& c, unsigned& d) { asm volatile("s_nop 1\n\tv_permlane16_swap_b32 %0, %1\n\tv_permlane16_swap_b32 %2, %3" : "+v"(a), "+v"(b), "+v"(c), "+v"(d)); }
template <int G> DI void bc2b(float& a, float& b, float x, float y) {
    asm volatile("v_mov_b32_dpp %0, %2 row_newbcast:%4 row_mask:0xf bank_mask:0xf\n\tv_mov_b32_dpp %1, %3 row_newbcast:%4 row_mask:0xf bank_mask:0xf" : "=&v"(a), "=&v"(b) : "v"(x), "v"(y), "i"(G));
}
template <class T> DI void pins(T& v) { asm volatile("" : "+s"(v)); }
template <int I> DI void gld_w2(unsigned voff, const void* sbase) {
    asm volatile("global_load_dwordx4 " WA(2) ", %0, %1 offset:%3 sc1" ::"v"(voff), "s"(sbase), "i"(W2R + 4 * I), "i"((I % 5) * 1024 - 2048));
}
DI void gld2t(v2u& dst, unsigned voff, const void* sbase) { asm volatile("global_load_dwordx2 %0, %1, %2 sc1" : "+v"(dst) : "v"(voff), "s"(sbase)); }
DI void wait2(v2u& v, v4u (&x)[3]) { asm volatile("s_waitcnt vmcnt(0)" : "+v"(v), "+v"(x[0]), "+v"(x[1]), "+v"(x[2])::"memory"); }
template <int IMM> DI void gld4p(v4u& dst, unsigned voff, const void* sbase) { asm volatile("global_load_dwordx4 %0, %1, %2 offset:%3" : "+v"(dst) : "v"(voff), "s"(sbase), "i"(IMM)); }
DI void undef4(v4u (&x)[4]) { asm volatile("" : "=v"(x[0]), "=v"(x[1]), "=v"(x[2]), "=v"(x[3])); }
DI void wait1(v2u& v) { asm volatile("s_waitcnt vmcnt(0)" : "+v"(v)::"memory"); }
template <int N> DI void wait_fkn(v2u& v, v2u& u, unsigned& x) { asm volatile("s_waitcnt vmcnt(%3)" : "+v"(v), "+v"(u), "+v"(x) : "i"(N) : "memory"); }
DI void wait_fk(v2u& v, v2u& u, unsigned& x) { asm volatile("s_waitcnt vmcnt(0)" : "+v"(v), "+v"(u), "+v"(x)::"memory"); }
DI void ld_ur(unsigned o, const void* base) {
    asm volatile("global_load_dwordx4 a[40:40+3], %0, %1 sc1\n\tglobal_load_dwordx4 a[44:44+3], %0, %1 offset:1024 sc1\n\t"
                 "global_load_dwordx4 a[48:48+3], %0, %1 offset:2048 sc1\n\tglobal_load_dwordx4 a[52:52+3], %0, %1 offset:3072 sc1" ::"v"(o), "s"(base));
}
DI void st_ur(unsigned a) {
    asm volatile("s_waitcnt vmcnt(%1)\n\tds_write_b128 %0, a[40:40+3] offset:480\n\tds_write_b128 %0, a[44:44+3] offset:1504\n\t"
                 "ds_write_b128 %0, a[48:48+3] offset:2528\n\tds_write_b128 %0, a[52:52+3] offset:3552" ::"v"(a), "i"(2 * __builtin_popcount(kW2M)) : "memory");
}
DI void wait4(v4u (&x)[4]) { asm volatile("s_waitcnt vmcnt(0)" : "+v"(x[0]), "+v"(x[1]), "+v"(x[2]), "+v"(x[3])::"memory"); }
template <int A, int IMM> DI void lda4(unsigned o, const void* base) { asm volatile("global_load_dwordx4 a[%c2:%c2+3], %0, %1 offset:%3" ::"v"(o), "s"(base), "i"(A), "i"(IMM)); }
template <int A, int IMM> DI void lda2(unsigned o, const void* base) { asm volatile("global_load_dwordx2 a[%c2:%c2+1], %0, %1 offset:%3" ::"v"(o), "s"(base), "i"(A), "i"(IMM)); }
template <int A> DI void rda(unsigned& v) { asm volatile("v_accvgpr_read_b32 %0, a[%c1+0]" : "=v"(v) : "i"(A)); }
DI void poll1(unsigned a, const void* base) { asm volatile("global_load_dwordx2 a[36:36+1], %0, %1 sc1" ::"v"(a), "s"(base)); }
DI void poll2(unsigned a, unsigned b, const void* base) { asm volatile("global_load_dwordx2 a[36:36+1], %0, %2 sc1\n\tglobal_load_dwordx2 a[36+2:36+3], %1, %2 sc1" ::"v"(a), "v"(b), "s"(base)); }
DI void touch_(unsigned a, const void* base) { asm volatile("global_load_dword a[32+1], %0, %1" ::"v"(a), "s"(base)); }
template <int N> DI void wait_st(v2u& a, v4u& b) { asm volatile("s_waitcnt vmcnt(%2)" : "+v"(a), "+v"(b) : "i"(N) : "memory"); }
DI void undef3(v4u (&x)[3]) { asm volatile("" : "=v"(x[0]), "=v"(x[1]), "=v"(x[2])); }
template <int IMM> DI void gld4t(v4u& dst, unsigned voff, const void* sbase) { asm volatile("global_load_dwordx4 %0, %1, %2 offset:%3 sc1" : "+v"(dst) : "v"(voff), "s"(sbase), "i"(IMM)); }
template <int N> DI void vm_wait() { asm volatile("s_waitcnt vmcnt(%0)" ::"i"(N) : "memory"); }
DI float dot2a(unsigned a, unsigned b, float c) {
    return __builtin_amdgcn_fdot2_f32_bf16(__builtin_bit_cast(bf16x2v, a), __builtin_bit_cast(bf16x2v, b), c, false);
}
DI void disc(unsigned l0, unsigned l1, unsigned l2, f32x2& lam, f32x2& co) {
    const float dt = rbf(__builtin_amdgcn_exp2f(lo16f(l2) * 1.44269504088896f));
    const float lre = lo16f(l0), lim = lo16f(l1);
    const float e_ = __builtin_amdgcn_exp2f(dt * lre * 1.44269504088896f);
    const float rv = __builtin_amdgcn_fractf(dt * lim * 0.159154943091895f);
    lam = f32x2{e_ * __builtin_amdgcn_cosf(rv), e_ * __builtin_amdgcn_sinf(rv)};
    const float nr = lam.x - 1.0f, ni = lam.y, id = __builtin_amdgcn_rcpf(lre * lre + lim * lim);
    co = f32x2{(nr * lre + ni * lim) * id, (ni * lre - nr * lim) * id};
}
DI f32x4 unpk4(v2u u) { return f32x4{lo16f(u.x), hi16f(u.x), lo16f(u.y), hi16f(u.y)}; }
#define D2 "v_dot2_f32_bf16 "
#define CP "v_cvt_pk_bf16_f32 "
DI void d2x4(unsigned* o, unsigned a0, unsigned a1, unsigned a2, unsigned a3, unsigned b0, unsigned b1, unsigned b2, unsigned b3) {
    asm volatile(D2 "%0, %4, %8, 0\n\t" D2 "%1, %5, %9, 0\n\t" D2 "%2, %6, %10, 0\n\t" D2 "%3, %7, %11, 0"
                 : "=&v"(o[0]), "=&v"(o[1]), "=&v"(o[2]), "=&v"(o[3]) : "v"(a0), "v"(a1), "v"(a2), "v"(a3), "v"(b0), "v"(b1), "v"(b2), "v"(b3));
}
DI void cpx4(unsigned* o, unsigned a0, unsigned a1, unsigned a2, unsigned a3, unsigned b0, unsigned b1, unsigned b2, unsigned b3) {
    asm volatile(CP "%0, %4, %8\n\t" CP "%1, %5, %9\n\t" CP "%2, %6, %10\n\t" CP "%3, %7, %11"
                 : "=&v"(o[0]), "=&v"(o[1]), "=&v"(o[2]), "=&v"(o[3]) : "v"(a0), "v"(a1), "v"(a2), "v"(a3), "v"(b0), "v"(b1), "v"(b2), "v"(b3));
}
DI void cpx4i(unsigned* t, const unsigned* ka) {
    asm volatile(CP "%0, %0, %4\n\t" CP "%1, %1, %5\n\t" CP "%2, %2, %6\n\t" CP "%3, %3, %7"
                 : "+v"(t[0]), "+v"(t[1]), "+v"(t[2]), "+v"(t[3]) : "v"(ka[0]), "v"(ka[1]), "v"(ka[2]), "v"(ka[3]));
}
template <int IMM> DI void bld4(v4u& dst, unsigned voff, v4u rsrc) {
    if constexpr ((NTM >> 4) & 1) asm volatile("buffer_load_dwordx4 %0, %1, %2, 0 offen offset:%3 nt" : "=v"(dst) : "v"(voff), "s"(rsrc), "i"(IMM));
    else asm volatile("buffer_load_dwordx4 %0, %1, %2, 0 offen offset:%3" : "=v"(dst) : "v"(voff), "s"(rsrc), "i"(IMM));
}

#define ta_ (*pta_)
#define tb_ (*ptb_)
__global__ __launch_bounds__(256, 1) __attribute__((aligned(4096))) void k_main(Params p) {
    __shared__ __attribute__((aligned(16))) char smem[LDS_BYTES];
    const rsrc_t rs = __builtin_amdgcn_make_buffer_rsrc((void*)p.ws, 0, WS_BYTES, 0x00020000);
    const rsrc_t ro = __builtin_amdgcn_make_buffer_rsrc((void*)p.out, 0, S_ * D_ * 4, 0x00020000);
    const int w0 = __builtin_amdgcn_readfirstlane(threadIdx.x >> 6), b0 = blockIdx.x;
    bool dead = false;
    u64 ta0_ = 0, tb0_ = 0; u64* const pta_ = &ta0_; u64* const ptb_ = &tb0_;
    asm volatile("" ::: "a255");
    float SW[5];
    f32x4 xin[5];
    v2u gt[5], dsk[5];
    f32x4 acc[2][5];
    f32x4 rr[2];
    float tt[2][5][4];
    float lr[5], ls[5], lqr[5], lqs[5];
    f32x2 l8, l32, l128;
    bool kearly;
    unsigned sdq;
    int sgn_, ab[4], bb, zpa, uwa, spa, tbw, tbr, cwa, spl, dst[3];
    unsigned sgo, sgs, glo, gso, fgo, fgo1;
    int srca;
    unsigned adm, dso, w2o, wro, fwo, xso, fho1, fho, hso, eo, es, ho[4];
    bool cv0, three;
    int mt1_;
    auto addrs = [&](auto layer_, int lane, int w, int b) __attribute__((always_inline)) {
        constexpr int layer = layer_.value;
        const int c = lane & 15, q = lane >> 4;
        TILES()
        static_assert(l_sg(0, 0) == SC + 0xA900 && l_sg(0, 1) == SC + 0xB400 && l_sg(0, 2) == SC + 0xCE00 && l_sg(0, 3) == SC + 0xDC00, "");
        sgn_ = (layer ? l_sg(1, 0) + w * INBOX : SC + (int)((0xDC00CE00B400A900ull >> (16 * w)) & 0xffff)) + q * 320 + c * 4;
        const int pl = lane < 40 ? lane : 39, tbb = l_tb(layer, 0) + w * (layer ? INBOX : 5120);
        spl = SC + pl * 8;
        eo = OFF_EG + (unsigned)(layer * 163840 + nt1 * 640 + pl * 16);
        es = eo + (unsigned)(mt1 * 10240);
        spa = l_rs(layer, 0) + (2 * (mt2 & 1) + (w >> 1)) * (layer ? INBOX : 256) + (2 * (w & 1) + (c >> 3)) * 64 + (c & 7) * 4;
        tbw = tbb + c * 64 + ((q ^ ((c >> 1) & 3)) * 16);
        tbr = tbb + (2 * (c >> 2) + (q & 1)) * 64 + (((2 * (q >> 1) + ((c >> 1) & 1)) ^ (c >> 2)) * 16) + (c & 1) * 8;
        hso = OFF_HF + (unsigned)(layer * 5242880 + ((8 * mt1 + 2 * w + (q >> 1)) * 160 + 10 * nt1 + 5 * (q & 1)) * 256 + c * 16);
        cwa = L_CW + c * 4;
        fho1 = lane ? kNone : OFF_FH + (unsigned)((((layer * 32 + 2 * mt1 + (w >> 1)) * 4 + (nt1 >> 2)) * 8 + (nt1 & 3) * 2 + (w & 1)) * 128);
        fho = OFF_FH + (unsigned)(((layer * 32 + mt2) * 4 + w) * 1024 + (lane & 7) * 128);
        pin(sgn_); pin(spl); pin(eo); pin(es); pin(spa); pin(tbw); pin(tbr); pin(hso); pin(cwa); pin(fho1); pin(fho);
        const unsigned hb_ = OFF_HF + (unsigned)(layer * 5242880 + (4 * mt2 * 160 + 40 * w) * 256);
#pragma unroll
        for (int m = 0; m < 4; ++m) { ho[m] = hb_ + (unsigned)(((m + 1 + w) & 3) * 40960 + lane * 16); pin(ho[m]); }
#pragma unroll
        for (int m = 0; m < 3; ++m) { dst[m] = SC + ((w + 1 + m) & 3) * INBOX + m * 5120 + lane * 16; pin(dst[m]); }
        mt1_ = mt1;
        sgo = OFF_SG + (unsigned)(((layer * 32 + mt2) * 4 + w) * 2048 + c * 16);
        sgs = q < 2 ? sgo + (unsigned)(nt2 * 256 + q * 8) : kNone;
        sgo += (unsigned)(q * 512);
        pin(sgo); pin(sgs);
        if constexpr (layer == 0) {
            srca = SC + w * INBOX + lane * 16;
            glo = OFF_GT + (unsigned)((80 * nt2 + (lane >> 1)) * 4096 + (64 * mt2 + 16 * w) * 2 + (lane & 1) * 16);
            gso = cv0 ? OFF_GT + (unsigned)((80 * nt2 + i32 + 32 * w) * 4096 + (q & 1) * 512 + c * 32 + (q >> 1) * 16) : kNone;
            fgo = OFF_FG + (unsigned)((nt2 * 96 + (lane & 31)) * 128);
            fgo1 = w == 0 ? OFF_FG + (unsigned)((nt2 * 96 + i32) * 128) : kNone;
            pin(srca); pin(glo); pin(gso); pin(fgo); pin(fgo1);
            const int r0 = SC + (w < 3 ? w : 0) * REGSZ, y0 = 2031 + 8 * q - c, l34 = lane < 33 ? lane : 33;
#pragma unroll
            for (int i = 0; i < 4; ++i) { ab[i] = r0 + ((y0 & 1) ? CV_O : CV_E) + ((y0 + 1) >> 1) * 4 - 448 - 512 - 1024 * i; pin(ab[i]); }
            bb = r0 + 32 * c + 16 * q;
            zpa = w < 3 ? r0 + l34 * 16 + (l34 >= 30 ? CV_E + 4096 - 480 : 0) + (l34 >= 32 ? CV_O + 4100 - 32 - CV_E - 4096 : 0) : SC + 47360 + lane * 16;
            uwa = r0 + lane * 16;
            xso = (unsigned)((64 * mt2 + 16 * w + c) * D_ + 80 * nt2 + 16 * (q & 1) + 8 * (q >> 1)) * 2u;
            dso = (unsigned)(160 * nt2 + 8 * q);
            adm = (unsigned)((c & 1) * 16 + (q == 2 + (c >> 3) ? 256 << ((c & 7) >> 1) : 0));
            if (GF) pin(adm);
            pin(bb); pin(zpa); pin(uwa); pin(xso); if (DSE) pin(dso);
        }
        SCHED();
    };
    unsigned tag0, tag1;
#define FLAG(off) { if (lane == 0) st64(v2u{tag0, tag1}, rs, off); }
#define ISTAG(f) (((f.x ^ tag0) | (f.y ^ tag1)) == 0u)
#define KW_REQ() { if (cv) ld_ur(wro, p.ws + OFF_KVT); static_for<NW2P>(LAMBDA(i_) { gld_w2<i_.value>(w2o, p.ws + 2048 + (i_.value / 5) * 5120); }); }
#define FW_OK() { if (__builtin_expect(!kearly, 0)) { WAVE_WAIT({ const v2u fw_ = ld64(rs, fwo); ok = ISTAG(fw_); }) KW_REQ() } }
#define TT_GAP(n) { constexpr int mb_ = (n) / 10, nb_ = ((n) % 10) / 2, e_ = ((n) & 1) * 2; \
        mul2(tt[mb_][nb_][e_], tt[mb_][nb_][e_ + 1], rm_[4 * mb_ + e_], rm_[4 * mb_ + e_ + 1], SW[nb_]); }
    {
        const int tid = threadIdx.x, lane = tid & 63, w = w0, b = b0;
        const int c = lane & 15, q = lane >> 4;
        TILES()
        STAMP(0)
        const unsigned lds0 = (unsigned)(size_t)(LAS char*)smem;
        u64 nonce;
        {
            u64 pc;
            int len, len2;
            asm volatile("s_getpc_b64 %0\nkpf_begin:\n\ts_mov_b32 %1, kpf_end-kpf_begin\n\ts_mov_b32 %2, kpf_end2-kpf_begin" : "=s"(pc), "=s"(len), "=s"(len2));
            len = len > len2 ? len : len2;
            const int o0 = (((j & 15) * 4 + w) + 64 * (lane >> 3)) * 128;
            touch_((unsigned)(o0 < len ? o0 : 0), (const void*)pc);
        }
        const bool cv = w < 2 || (w == 2 && i32 < 16);
        const int chn = 80 * nt2 + i32 + 32 * (cv ? w : 0);
        const bool kw = KVS ? w < 2 || (w == 2 && i32 >= 16) : cv;
        const int kp = 16 * nt2 + mt1, ku = 5 * kp + (i32 < 16 ? w & 1 : 2 + (w < 3 ? w : 0)), kc = ku >> 6, kr = ku & 63;
        const int lc = cv ? lane : 8;
        int kwi = 2 + (i32 >> 4) - w;
        v4u ax[15], sa[5], cr[2], wr[4];
        unsigned uL[3];
        const int cu_ = (b < 200 ? b : 0) * 4 + w, cdb = cu_ / 20, ccg = cu_ % 20;
        {
            int ca_ = 32 * ccg, cb_ = 40 * nt1 + 10 * w - 32;
            asm volatile("" : "+s"(ca_), "+s"(cb_), "+s"(kwi));
            unsigned cpo = (unsigned)((lane < 32 ? ca_ : cb_) + (lane < 41 ? lane : 41)) * 2u;
            unsigned kso = (unsigned)(64 * kc + 8 * (lane & 7)) * 2u, ko = (unsigned)(32 * kr + 4 * (lane >> 3)) * 1280u + kso, kh = ko - 3840u;
            kso *= 4u;
            pin(cpo); pin(kso); pin(ko); pin(kh);
            SCHED();
            gldu16(uL[0], cpo, p.Lre); gldu16(uL[1], cpo, p.Lim); gldu16(uL[2], cpo, p.logdt);
            const v4u rk = {(unsigned)(u64)p.k, (unsigned)((u64)p.k >> 32) & 0xffffu, S_ * D_ * 2u, 0x00020000u};
#define L4(d, v, s_, o, f) "global_load_dwordx4 %" #d ", %" #v ", %" #s_ " offset:" #o f "\n\t"
#define B4(d, o) "buffer_load_dwordx4 %" #d ", %17, %21, 0 offen offset:" #o " nt\n\t"
            asm volatile("s_cmp_lt_i32 %22, 1\n\ts_cbranch_scc1 Lrole2\n\t"
                         L4(3, 15, 18, 0, " nt") L4(4, 15, 18, 1280, " nt") L4(5, 15, 18, 2560, " nt") L4(6, 15, 18, 3840, " nt")
                         L4(7, 15, 20, 0, " nt") L4(8, 15, 20, 1280, " nt") L4(9, 15, 20, 2560, " nt") L4(10, 15, 20, 3840, " nt")
                         B4(0, 0) B4(1, 1280) B4(2, 2560) L4(11, 16, 19, 0, "") L4(12, 16, 19, 16, "") L4(13, 16, 19, 32, "") L4(14, 16, 19, 48, "")
                         "\nLrole2:"
                         : "=&v"(ax[0]), "=&v"(ax[1]), "=&v"(ax[2]), "=&v"(ax[3]), "=&v"(ax[4]), "=&v"(ax[5]), "=&v"(ax[6]), "=&v"(ax[7]), "=&v"(ax[8]), "=&v"(ax[9]),
                           "=&v"(ax[10]), "=&v"(ax[11]), "=&v"(ax[12]), "=&v"(ax[13]), "=&v"(ax[14])
                         : "v"(ko), "v"(kso), "v"(kh), "s"(p.k), "s"(p.shk), "s"(p.v), "s"(rk), "s"(kwi) : "scc");
            asm volatile("s_load_dwordx2 %0, %1, 0x0" : "=s"(nonce) : "s"(p.ws + OFF_NONCE));
            SCHED();
        }
        const char* ssrc;
        {
            u64 pa = (u64)(p.x0 + (128 * mt1 + 8 * nt1 + 2 * w) * D_), pb = (u64)(((mt1 & 1) ? p.Bim : p.Bre) + (40 * nt1 + (mt1 >> 1)) * D_);
            asm volatile("" : "+s"(pa), "+s"(pb));
            const int nbx = 2 * w + q - 2, r = q < 2 ? q : 8 * (nbx < 5 ? nbx : 4);
            ssrc = (const char*)(q < 2 ? pa : pb) + (unsigned)(r * 1280 + c * 16);
            pin(ssrc);
        }
        if (SAF == 0) static_for<5>(LAMBDA(i) { gld64<256 * i.value>(sa[i.value], ssrc); });
        SCHED();
        if (w == TW && !kw) tour<0>();
        STAMP(22)
        const int row8 = lane >> 3;
        const unsigned wvo = (unsigned)(40 * nt1 + row8) * 1280u + (unsigned)(((lane & 7) ^ ((w & 1) | ((row8 >> 1) << 1))) * 16 + (w >> 1) * 128);
        const char* const wbase = (const char*)((w & 1) ? p.Bim : p.Bre);
        const unsigned wl = lds0 + (unsigned)(((w & 1) * 10 + (w >> 1)) * 1024);
        const unsigned xvo0 = (unsigned)(8 * (row8 >> 1) + 2 * (row8 & 1)) * 1280u + (unsigned)(((lane & 7) ^ ((row8 >> 1) << 1)) * 16);
        const unsigned xvo1 = (xvo0 ^ 16u) + 1280u;
        const char* const xbase = (const char*)p.x0 + (size_t)(128 * mt1 + 32 * w) * 1280;
        const unsigned xl = lds0 + (unsigned)(w * REGSZ);
        unsigned xv[4] = {xvo0, xvo1, xvo0 + 5120u, xvo1 + 5120u}, wv[5];
#pragma unroll
        for (int i = 0; i < 5; ++i) { wv[i] = wvo + i * 10240u; pin(wv[i]); if (i < 4) pin(xv[i]); }
#define DMA_M0(it) SETM0<dma_loff(it) - dma_goff(it)>((it) < 40 ? xl : wl)
#define DMA_LD1(it, nx_) GLD1<dma_goff(it), dma_loff(nx_) - dma_goff(nx_)>((it) < 40 ? xv[(it) & 3] : wv[(it) < 40 ? 0 : ((it) - 100) % 5], (it) < 40 ? xbase : wbase, (nx_) < 40 ? xl : wl)
#define DMA_LD(it) GLD0<dma_goff(it)>((it) < 40 ? xv[(it) & 3] : wv[(it) < 40 ? 0 : ((it) - 100) % 5], (it) < 40 ? xbase : wbase)
        const int swz = (c & 1) | ((c >> 2) << 1);
        int w1lane0 = (c & 1) * 10240 + (c >> 1) * 128 + ((q ^ swz) * 16);
        int w1lane1 = (c & 1) * 10240 + (c >> 1) * 128 + (((4 + q) ^ swz) * 16);
        int xlane0 = L_XR + w * REGSZ + (c & 1) * 1024 + (c >> 1) * 128 + ((q ^ swz) * 16);
        int xlane1 = L_XR + w * REGSZ + (c & 1) * 1024 + (c >> 1) * 128 + (((4 + q) ^ swz) * 16);
        unsigned cvo = (unsigned)((16 * cdb + c) * D_ + 32 * ccg + 8 * q) * 2u, for0, for1;
        {
            const int G = 4 * ccg + q, ch = 10 * (G / 5) + G % 5, ci = ch + 5;
            const unsigned fo = OFF_W2F + (unsigned)(((cdb / 5) * 200 + cdb % 5) * 1024 + c * 16);
            for0 = fo + (unsigned)((ch >> 2) * 5120 + (ch & 3) * 256); for1 = fo + (unsigned)((ci >> 2) * 5120 + (ci & 3) * 256);
        }
        unsigned sto = OFF_ST + (unsigned)((mt1 * 16 + 4 * w + q) * 256 + c * 8), swo = OFF_ST + (unsigned)(((lane >> 2) * 16 + nt1) * 256 + 128 + (lane & 3) * 16);
        unsigned kvo = OFF_KVT + (unsigned)((64 * kc + 8 * (lane >> 3) + ((lane >> 2) & 1)) * 4096 + (32 * kr + 8 * (lane & 3)) * 2);
        int kbw = L_KB + w * 1024 + (lane & 7) * 128 + (((lane >> 3) * 8) ^ ((lane & 7) * 16));
        int kbr = L_KB + w * 1024 + (lane >> 3) * 128 + (((lane & 7) * 16) ^ ((lane >> 3) * 16));
        const unsigned dum = (OOB & 1) ? kNone : OFF_DUM + (unsigned)((b * 4 + w) * 128 + (lane & 7) * 16);
        if (!kw) kvo = dum;
        if (b >= 200) { for0 = dum; for1 = dum; cvo = (unsigned)((b * 4 + w) * 128); }
        wro = (unsigned)(chn * 4096 + lc * 16);
        cv0 = cv; three = i32 < 16;
        pin(kvo); pin(kbw); pin(kbr);
        int w1hi0 = w1lane0 + 61440, w1hi1 = w1lane1 + 61440;
        pin(w1hi0); pin(w1hi1);
        pin(w1lane0); pin(w1lane1); pin(xlane0); pin(xlane1); pin(cvo); pin(for0); pin(for1); pin(sto); pin(swo); pin(wro);
        SCHED();
        auto pre = LAMBDA(i_) {
            constexpr int it = p1_pre(i_.value);
            SCHED();
            DMA_M0(it);
            asm volatile("s_nop 0");
            DMA_LD(it);
            SCHED();
        };
        {
            int f0 = 2 * ((chn & ~63) / 5), f1 = 2 * ((chn | 63) / 5) + 1, nk = cv ? 28 : 0;
            asm volatile("" : "+s"(f0), "+s"(f1), "+s"(nk));
            const int l10 = lane - 32 < 0 || lane - 32 > 9 ? 0 : lane - 32;
            fwo = lane < nk ? OFF_FK + (unsigned)(f0 + lane < f1 ? f0 + lane : f1) * 128u : OFF_FW + (unsigned)(25 * nt2 + w + 5 * (l10 >> 1) + (l10 & 1)) * 128u;
        }
        w2o = OFF_W2F + (unsigned)((nt2 * 40 + 10 * w) * 5120 + lane * 16);
        int ime = SC + (w < 3 ? w * REGSZ + CV_E : 43264) + 1008 - 16 * lane, imo = SC + (w < 3 ? w * REGSZ + CV_O + 4 : 47360) + 1008 - 16 * lane, im3 = w < 3 ? ime + 3072 : imo;
        int tca = L_TAB + (c >> 1) * 136, tcb = tca + 8 + 32 * w + 8 * q, tcl = L_TAB + (lane < 40 ? lane : 39) * 136, coa = L_CO + w * REGSZ + q * 64, rsa = l_rs(0, 0) + w * 256 + lane * 4;
        unsigned sgn = (c & 1) ? 0u : 0x80000000u;
        unsigned fko1 = w ? kNone : lane < 32 ? OFF_FK + (unsigned)((2 * kp + (i32 >> 4)) * 128) : OFF_FW + (unsigned)(b * 128);
        pin(fwo); pin(w2o); pin(fko1); pin(ime); pin(imo); pin(im3); pin(tca); pin(tcb); pin(tcl); pin(coa); pin(rsa); pin(sgn);
        SCHED();
        auto cw_ = [&]() __attribute__((always_inline)) {
            gldn<0, 2>(cr[0], cvo, p.Cre); gldn<0, 2>(cr[1], cvo, p.Cim);
            static_for<4>(LAMBDA(i) { gldn<i.value * 1024, 1>(wr[i.value], wro, p.ssmk); });
        };
        if (SAF == 1) static_for<5>(LAMBDA(i) { gld64<256 * i.value>(sa[i.value], ssrc); });
        if (CWP == 0) cw_();
        STAMP(33)
        static_assert(SAF == 2 && CWP == 1, "");
        touch_(0u, p.Lre);
        asm volatile("s_waitcnt vmcnt(16) lgkmcnt(0)\n\ts_cmp_gt_i32 %3, 0\n\ts_cbranch_scc1 Lrole3\n\ts_waitcnt vmcnt(1)\nLrole3:" : "+v"(uL[0]), "+v"(uL[1]), "+v"(uL[2]) : "s"(kwi) : "memory", "scc");
        pins(nonce);
        ++nonce;
        tag0 = (unsigned)nonce; tag1 = (unsigned)(nonce >> 32);
        pins(tag0); pins(tag1);
        {
            f32x2 lam, co, lp[16];
            disc(uL[0], uL[1], uL[2], lam, co);
            if (lane < 32) *(f32x2*)(smem + L_CO + w * REGSZ + lane * 8) = 2.0f * co;
            lp[0] = cmul(lam, lam); lp[0] = cmul(lp[0], lp[0]); lp[1] = cmul(lp[0], lp[0]);
            lp[0] = f32x2{1.f, 0.f};
            lp[2] = cmul(lp[1], lp[1]); lp[3] = cmul(lp[2], lp[1]); lp[4] = cmul(lp[2], lp[2]);
            const f32x2 z2 = {0.f, 0.f};
            f32x2 t4[4];
            cfma4(t4, lp[4], lp[1], lp[2], lp[3], lp[4], z2, z2, z2, z2);
            lp[5] = t4[0]; lp[6] = t4[1]; lp[7] = t4[2]; lp[8] = t4[3];
            cfma4(t4, lp[8], lp[1], lp[2], lp[3], lp[4], z2, z2, z2, z2);
            lp[9] = t4[0]; lp[10] = t4[1]; lp[11] = t4[2]; lp[12] = t4[3];
            cfma4(t4, lp[8], lp[5], lp[6], lp[7], lp[7], z2, z2, z2, z2);
            lp[13] = t4[0]; lp[14] = t4[1]; lp[15] = t4[2];
            if (lane >= 32 && lane < 42) {
                f32x2* tl = (f32x2*)(smem + L_TAB + (10 * w + lane - 32) * 136);
                tl[0] = lam;
#pragma unroll
                for (int i = 0; i < 16; ++i) tl[1 + i] = lp[i];
            }
        }
        SCHED();
        STAMP(35)
        if (SAF == 2) static_for<5>(LAMBDA(i) { gld64<256 * i.value>(sa[i.value], ssrc); });
        if (CWP) cw_();
        if (APOS == 0) addrs(std::integral_constant<int, 0>{}, lane, w, b);
        v4u kvt[4];
        unsigned ka[8], kt[8], se[8], vs[8], ko4[4], one2 = 0x3F803F80u, kq[12];
        float kf_[8];
        pin(one2);
        auto kvf = LAMBDA(n_) {
            constexpr int n = n_.value, h = n / 5, u = n % 5;
            if constexpr (u == 0) {
#pragma unroll
                for (int r = 0; r < 6; ++r) { kq[r] = __builtin_amdgcn_perm(ax[r][h], ax[r + 1][h], 0x05040100u); kq[6 + r] = __builtin_amdgcn_perm(ax[r][h], ax[r + 1][h], 0x07060302u); }
                if constexpr (h > 0) kvt[h - 1] = *(const v4u*)(smem + kbr);
            } else if constexpr (u == 1) {
                unsigned t[8];
                d2x4(t, kq[0], kq[1], kq[2], kq[3], ax[11 + h][1], ax[11 + h][1], ax[11 + h][1], ax[11 + h][1]);
                d2x4(t + 4, kq[6], kq[7], kq[8], kq[9], ax[11 + h][3], ax[11 + h][3], ax[11 + h][3], ax[11 + h][3]);
#pragma unroll
                for (int r = 0; r < 8; ++r) kf_[r] = __uint_as_float(t[r]);
            } else if constexpr (u == 2) {
#pragma unroll
                for (int r = 0; r < 8; ++r) kf_[r] = dot2a(kq[r + 2 * (r >> 2) + 2], ax[11 + h][2 * (r >> 2)], kf_[r]);
            } else if constexpr (u == 3) {
#pragma unroll
                for (int r = 0; r < 4; ++r) { kq[r] = ax[7 + r][h] << 16; kq[4 + r] = ax[7 + r][h] & 0xffff0000u; pin(kq[r]); pin(kq[4 + r]); }
            } else {
#pragma unroll
                for (int r = 0; r < 8; ++r) kf_[r] *= __uint_as_float(kq[r]);
                *(v2u*)(smem + kbw) = v2u{cvtpk(kf_[0], kf_[1]), cvtpk(kf_[2], kf_[3])};
                *(v2u*)(smem + (kbw ^ 64)) = v2u{cvtpk(kf_[4], kf_[5]), cvtpk(kf_[6], kf_[7])};
                if constexpr (h == 3) kvt[3] = *(const v4u*)(smem + kbr);
            }
            SCHED();
        };
        auto kvop = LAMBDA(n_) {
            constexpr int n = n_.value, h = n ? (n - 1) / 29 : -1, u = n ? (n - 1) % 29 : 26;
            if constexpr (u < 2) d2x4(ka + 4 * u, ax[0][h], ax[1][h], ax[2][h], ax[3][h], se[u], se[u], se[u], se[u]);
            else if constexpr (u == 8) { const v4u sk = ax[11 + h]; se[4] = sk[0] >> 16; se[5] = sk[2] & 0xffff0000u; se[6] = sk[0] & 0xffffu; se[7] = sk[2] << 16; pin(se[4]); pin(se[5]); pin(se[6]); pin(se[7]); }
            else if constexpr (u < 21) {
                constexpr int v = u < 8 ? u - 2 : u - 3, tp = 1 + v / 6, st = v % 6, e = st & 1;
                if constexpr (st < 2) d2x4(kt + 4 * e, ax[tp][h], ax[tp + 1][h], ax[tp + 2][h], ax[tp + 3][h], se[2 * tp + e], se[2 * tp + e], se[2 * tp + e], se[2 * tp + e]);
                else if constexpr (st < 4) cpx4i(kt + 4 * e, ka + 4 * e);
                else d2x4(ka + 4 * e, kt[4 * e], kt[4 * e + 1], kt[4 * e + 2], kt[4 * e + 3], one2, one2, one2, one2);
            }
            else if constexpr (u == 21) { vs[0] = ax[7][h] & 0xffffu; vs[1] = ax[8][h] << 16; vs[2] = ax[9][h] & 0xffffu; vs[3] = ax[10][h] << 16; pin(vs[0]); pin(vs[1]); pin(vs[2]); pin(vs[3]); }
            else if constexpr (u == 22) { vs[4] = ax[7][h] >> 16; vs[5] = ax[8][h] & 0xffff0000u; vs[6] = ax[9][h] >> 16; vs[7] = ax[10][h] & 0xffff0000u; pin(vs[4]); pin(vs[5]); pin(vs[6]); pin(vs[7]); }
            else if constexpr (u == 23) cpx4(ko4, ka[0], ka[2], ka[4], ka[6], ka[1], ka[3], ka[5], ka[7]);
            else if constexpr (u < 26) { constexpr int e = u - 24; d2x4(kt + 4 * e, ko4[2 * e], ko4[2 * e], ko4[2 * e + 1], ko4[2 * e + 1], vs[4 * e], vs[4 * e + 1], vs[4 * e + 2], vs[4 * e + 3]); }
            else if constexpr (u == 26) {
                if constexpr (h < 3) { const v4u sk = ax[12 + h]; se[0] = sk[1] >> 16; se[1] = sk[3] & 0xffff0000u; se[2] = sk[1] & 0xffffu; se[3] = sk[3] << 16; pin(se[0]); pin(se[1]); pin(se[2]); pin(se[3]); }
                else asm volatile("s_nop 2");
            }
            else if constexpr (u == 27) {
                cpx4(ko4, kt[0], kt[2], kt[4], kt[6], kt[1], kt[3], kt[5], kt[7]);
                *(v2u*)(smem + kbw) = v2u{ko4[0], ko4[1]};
                *(v2u*)(smem + (kbw ^ 64)) = v2u{ko4[2], ko4[3]};
            }
            else kvt[h] = *(const v4u*)(smem + kbr);
            SCHED();
        };
        asm volatile("s_waitcnt vmcnt(%15)" : "+v"(ax[0]), "+v"(ax[1]), "+v"(ax[2]), "+v"(ax[3]), "+v"(ax[4]), "+v"(ax[5]), "+v"(ax[6]), "+v"(ax[7]), "+v"(ax[8]), "+v"(ax[9]),
                     "+v"(ax[10]), "+v"(ax[11]), "+v"(ax[12]), "+v"(ax[13]), "+v"(ax[14]) : "i"(11) : "memory");
        STAMP(30)
        unsigned tch = 0u;
        constexpr int NF = KVF ? KNF : 27;
        if (kw) {
            if constexpr (KVF) {
                static_for<20>(LAMBDA(n_) {
                    constexpr int n = n_.value, DN = 20 - KFD0;
                    site<1000 + n>(); SCHED();
                    kvf(n_);
                    if constexpr (n >= KFD0) static_for<(n - KFD0 + 1) * NF / (DN ? DN : 1) - (n - KFD0) * NF / (DN ? DN : 1)>(LAMBDA(i_) { pre(std::integral_constant<int, (n - KFD0) * NF / (DN ? DN : 1) + i_.value>{}); });
                });
                if constexpr (KFD0 == 20) static_for<NF>(pre);
            } else
            static_for<117>(LAMBDA(n_) {
                constexpr int n = n_.value;
                site<1000 + n>(); SCHED();
                kvop(n_);
                if constexpr (n >= kD0 && (n - kD0) % kDS == 0 && (n - kD0) / kDS < 27) { pre(std::integral_constant<int, ((n - kD0) / kDS + 270) % 27>{}); SCHED(); }
            });
        } else {
            if (w == 3) {
            const int l = lane, l3 = l & 3;
            unsigned a = OFF_SG + (unsigned)((((((l >> 3) & 1) * 32 + mt2) * 4 + ((l >> 1) & 3)) * 8 + nt2) * 256 + (l & 1) * 128);
            a = l >= 16 ? OFF_EG + (unsigned)((l >= 21 ? 163840 - 640 : 0) + (mt1 * 16 + nt1) * 640 + (l < 26 ? l - 16 : 5) * 128) : a;
            a = l >= 28 ? OFF_PS + (unsigned)(((mt2 * 4 + l3) * 8 + nt2) * 128) : a;
            a = l >= 32 ? OFF_FH + (unsigned)((((((l >> 2) & 1) * 32 + 2 * mt1 + (l3 >> 1)) * 4 + (nt1 >> 2)) * 8 + (nt1 & 3) * 2 + (l3 & 1)) * 128) : a;
            a = l >= 40 ? OFF_FX + (unsigned)(((mt1 * 4 + 2 * (mt2 & 1) + (l3 >> 1)) * 16 + nt2 * 2 + (l3 & 1)) * 128) : a;
            a = l >= 48 ? OFF_FG + (unsigned)((nt2 * 96 + i32) * 128) : a;
            const unsigned big = (l & 7) < 2 ? OFF_HF + (unsigned)((l & 1) * 5242880 + (8 * mt1 * 160 + 10 * nt1) * 256) : (l & 7) == 2 ? OFF_X2F + (unsigned)(2 * mt2 * 40960 + 2560 * nt2)
                               : (l & 7) < 5 ? OFF_KVT + (unsigned)((l & 1) * 2621440 + (80 * nt2 + i32) * 4096) : OFF_W2F + (unsigned)(nt2 * 204800 + ((l & 7) - 5) * 98304);
            a = l >= 56 ? big : a;
            tch = a;
        }
            vm_wait<KNW>();
            static_for<NF>(LAMBDA(i_) { if constexpr (i_.value == T1POS) { if (w == TW) tour<1>(); } pre(i_); });
            if (T1POS == 27 && w == TW) tour<1>();
            skip<SKIP0>();
            undef4(kvt);
        }
        if constexpr (NF == 27) {
#pragma unroll
            for (int i = 0; i < 4; ++i) st128(kvt[i], rs, kvo + i * 8192);
        }
        SCHED();
        STAMP(34)
        if (APOS == 1) addrs(std::integral_constant<int, 0>{}, lane, w, b);
        v4u orr, oi;
        v2u gr;
        unsigned gro;
        {
            asm volatile("s_waitcnt vmcnt(%7)" : "+v"(cr[0]), "+v"(cr[1]), "+v"(sa[0]), "+v"(sa[1]), "+v"(sa[2]), "+v"(sa[3]), "+v"(sa[4]) : "i"(NF == 27 ? 35 : 4 + NF) : "memory");
#pragma unroll
            for (int e = 0; e < 4; ++e) {
                const f32x4 co = *(const f32x4*)(smem + coa + e * 16);
                const f32x2 o0 = lo16f(cr[0][e]) * f32x2{co.x, -co.y} - lo16f(cr[1][e]) * f32x2{co.y, co.x};
                const f32x2 o1 = hi16f(cr[0][e]) * f32x2{co.z, -co.w} - hi16f(cr[1][e]) * f32x2{co.w, co.z};
                orr[e] = cvtpk(o0.x, o1.x); oi[e] = cvtpk(o0.y, o1.y);
            }
            if constexpr (NF == 27) { st128(orr, rs, for0); st128(oi, rs, for1); } else { pin(orr); pin(oi); }
            SCHED();
        }
        static_for<(27 - NF) / 3>(LAMBDA(i_) { pre(std::integral_constant<int, NF + i_.value>{}); });
        {
            float s1[2] = {0.f, 0.f}, s2[2] = {0.f, 0.f};
#pragma unroll
            for (int i = 0; i < 5; ++i)
#pragma unroll
                for (int e = 0; e < 4; ++e) { s1[e & 1] = dot2a(sa[i][e], one2, s1[e & 1]); s2[e & 1] = dot2a(sa[i][e], sa[i][e], s2[e & 1]); }
            const float a1 = rowsum16(s1[0] + s1[1]), a2 = rowsum16(s2[0] + s2[1]);
            const float mean = a1 * (1.0f / D_);
            const float var = fmaxf(a2 * (1.0f / D_) - mean * mean, 0.f);
            const float ri = __builtin_amdgcn_rcpf(rbf(__builtin_amdgcn_sqrtf(rbf(rbf(var) + 1e-5f))));
            gr = v2u{__float_as_uint(q >= 2 ? a1 : c ? -ri * rbf(mean) : ri), tag0};
            gro = c < 2 ? OFF_ST + (unsigned)((mt1 * 16 + nt1) * 256 + ((q < 2 ? 8 * c : 14) + 2 * w + q) * 8) : kNone;
            pin(gr); pin(gro);
        }
        SCHED();
        STAMP(59)
        if (APOS == 2) addrs(std::integral_constant<int, 0>{}, lane, w, b);
        if constexpr (NF < 27) {
            static_for<27 - NF - (27 - NF) / 3>(LAMBDA(i_) { pre(std::integral_constant<int, NF + (27 - NF) / 3 + i_.value>{}); });
#pragma unroll
            for (int i = 0; i < 4; ++i) st128(kvt[i], rs, kvo + i * 8192);
            st128(orr, rs, for0); st128(oi, rs, for1);
        }
        st64(gr, rs, gro);
        SCHED();
        STAMP(23)
        if (w == 3) tour<7>();
        asm volatile("s_waitcnt vmcnt(%4) lgkmcnt(0)" : "+v"(wr[0]), "+v"(wr[1]), "+v"(wr[2]), "+v"(wr[3]) : "i"(P1_PRE - 9) : "memory");
        __builtin_amdgcn_s_barrier();
        asm volatile("" ::: "memory");
        STAMP(24)
        v4u wf[2][5], af[2][2][2];
#pragma unroll
        for (int nb = 0; nb < 5; ++nb) wf[0][nb] = *(const v4u*)(smem + w1lane0 + nb * 20480 + (0) * 2048);
#pragma unroll
        for (int i = 0; i < 4; ++i) af[0][i >> 1][i & 1] = *(const v4u*)(smem + ((i >> 1) ? xlane1 : xlane0) + (i & 1) * 2048);
        SCHED();
        const char *kvtb = p.ws + OFF_KVT, *w2b0 = p.ws + 2048;
        pins(kvtb); pins(w2b0);
        v2u sg, tc[10], fw, tg2 = {tag0, tag1};
        pin(tg2);
        v4u sw0;
        unsigned ie[4][4], io[4][4], iw, zq[4];
        float rr_[8];
        auto kstep = LAMBDA(s_) {
            constexpr int s = s_.value, ph = s >> 2, s4 = s & 3, cu = s & 1, nx = cu ^ 1, jj = s >> 1, pp = jj & 1;
            if constexpr (s4 == 3 && ph < 4) {
                constexpr int pw = p1_pos(104 + 5 * (ph + 1)), px = p1_pos(4 * (jj + 1) + 3), pm = pw > px ? pw : px;
                constexpr int vm = p1_lim(p1_cnt(s, 0) - 1 - pm);
                asm volatile("" ::: "memory");
                __builtin_amdgcn_s_waitcnt((vm & 15) | ((vm >> 4) << 14) | 0x70);
                __builtin_amdgcn_s_barrier();
                asm volatile("" ::: "memory");
                STAMP(25 + ph)
                SCHED();
            }
            if constexpr (s == 15) {
                wait_st<p1_cnt(s, 0) - 1 - p1_pos(203)>(sg, sw0);
                WAVE_RETRY((((sg.y ^ tag0) | (sw0.y ^ tag0) | (sw0.w ^ tag0)) == 0u), sg = ld64(rs, sto); sw0 = ld128(rs, swo))
                STAMP(7)
            }
            if constexpr (s == kW2S) { vm_wait<p1_cnt(s, 0) - 1 - p1_pos(208)>(); SCHED(); }
            if constexpr (s == 18) {
                vm_wait<p1_cnt(s, 0) - 1 - (p1_pos(207) > p1_pos(208) ? p1_pos(207) : p1_pos(208))>();
                pin(fw); pin(sdq);
                kearly = __builtin_amdgcn_ballot_w64(!ISTAG(fw)) == 0ull;
                ORDER();
                if (kearly && cv) ld_ur(wro, kvtb);
                SCHED();
            }
            if constexpr (s4 != 3 || ph == 4) lgkm<(cu == 0 && s > 0 ? 3 : s == 17 ? 4 : s == 19 ? 2 : 0)>();
            SCHED();
            static_for<10>(LAMBDA(g_) {
                constexpr int g = g_.value, mb = g / 5, k = g % 5, it = p1_item(s, g), nt_ = g < 9 ? p1_item(s, g + 1) : -1;
                constexpr bool d0 = it >= 0 && it < 200, d1 = nt_ >= 0 && nt_ < 200;
                if constexpr (s == 0) mf0(acc[mb][k], af[pp][cu][mb], wf[cu][k]); else mf(acc[mb][k], af[pp][cu][mb], wf[cu][k]);
                SCHED();
                if constexpr (s < 19) {
                    if constexpr (g < 5) wf[nx][g] = *(const v4u*)(smem + (g < 3 ? (nx ? w1lane1 : w1lane0) : (nx ? w1hi1 : w1hi0)) + (g < 3 ? g : g - 3) * 20480 + ((s + 1) >> 1) * 1024);
                    if constexpr (cu == 1 && g == 5) vm_wait<p1_lim(p1_cnt(s, 5) - 1 - p1_pos(4 * (jj + 1) + 3))>();
                    if constexpr (cu == 1 && g >= 5 && g < 9)
                        af[pp ^ 1][(g - 5) >> 1][(g - 5) & 1] = *(const v4u*)(smem + (((g - 5) >> 1) ? xlane1 : xlane0) + ((jj + 1) % 3) * 4096 + ((g - 5) & 1) * 2048);
                }
                if constexpr (cu == 0 && g == 4 && s > 0) lgkm<(s == 8 || s == 10 || s == 14 ? 8 : 5)>();
                static_for<p1_cap(s, g)>(LAMBDA(e_) {
                    constexpr int na = p1_slot(0, s, g, 10) + e_.value, i = (na / 10) & 3, u = na % 10;
                    static_assert(p1_slot(0, 8, 0, 10) >= 40, "");
                    if constexpr (na < 40) {
                        if constexpr (u == 0) { if constexpr (i > 0) ror1(iw, wr[i - 1].w); else { iw = 0u; pin(iw); } }
                        else if constexpr (u < 5) rot1(ie[i][u - 1], wr[i][4 - u]);
                        else if constexpr (u == 5) shr1b(iw, wr[i].w);
                        else if constexpr (u < 9) mix1(io[i][u - 6], wr[i][9 - u], wr[i][8 - u]);
                        else mix1(io[i][3], wr[i].x, iw);
                    }
                    constexpr int nb = p1_slot(8, s, g, 3) + e_.value;
                    if constexpr (s >= 8 && s < 12 && g < 3) {
                        if constexpr (nb < 10) tc[nb] = *(const v2u*)(smem + (nb < 5 ? tca : tcb) + (nb % 5) * 1088);
                        else if constexpr (nb == 10) { const f32x2* tp = (const f32x2*)(smem + tcl); l8 = tp[2]; l32 = tp[5]; }
                        else if constexpr (nb == 11) l128 = *(const f32x2*)(smem + tcl + 72);
                        static_assert(p1_slot(8, 12, 0, 3) == 12, "");
                    }
                    constexpr int nc = p1_slot(12, s, g, 10) + e_.value;
                    if constexpr (s >= 12 && s < 14 && nc < 10) {
                        if constexpr (nc < 5) { lr[nc] = __uint_as_float(tc[nc].x); xor1(ls[nc], tc[nc].y, sgn); }
                        else { lqr[nc - 5] = __uint_as_float(tc[nc].x); xor1(lqs[nc - 5], tc[nc].y, sgn); }
                        static_assert(p1_slot(12, 14, 0, 10) >= 10, "");
                    }
                    if constexpr (s == 15 && g < 5) SW[g] = __int_as_float(__builtin_amdgcn_ds_bpermute(16 * c + 4 * (g >> 1), (g & 1) ? sw0.z : sw0.x));
                    constexpr bool img = (s == 16 && g >= 5 && g < 9 && e_.value == 0) || (s == 18 && (g == 5 || g == 6) && e_.value == 0);
                    constexpr int nd = p1_slot(15, s, g, 10) + e_.value - 5 - (s > 16 ? 4 : s == 16 && g >= 5 ? (g < 9 ? g - 4 : 4) : 0) - (s > 18 ? 2 : s == 18 && g >= 5 ? (g == 5 ? 1 : 2) : 0);
                    if constexpr (s >= 15 && nd >= 0 && !img) {
                        if constexpr (nd < 4) bc1<nd>(rr_[nd], sg.x);
                        else if constexpr (nd < 44) mulb<8 + (nd - 4) % 8>(tt[(nd - 4) % 8 / 4][(nd - 4) / 8][(nd - 4) % 4], sg.x, SW[(nd - 4) / 8]);
                        else if constexpr (nd < 48) bc1<nd - 40>(rr_[nd - 40], sg.x);
                        static_assert(p1_slot(15, 16, 0, 10) <= 9 && p1_slot(15, 20, 0, 10) >= 59, "");
                    }
                });
                if constexpr (s == 14 && g < 3) *(v4u*)(smem + ime + 1024 * g) = v4u{ie[3 - g][0], ie[3 - g][1], ie[3 - g][2], ie[3 - g][3]};
                if constexpr (s == 16 && g >= 5 && g < 9) *(v4u*)(smem + imo + 1024 * (8 - g)) = v4u{io[g - 5][0], io[g - 5][1], io[g - 5][2], io[g - 5][3]};
                if constexpr (s == 18 && g == 5) *(v4u*)(smem + im3) = v4u{ie[0][0], ie[0][1], ie[0][2], ie[0][3]};
                if constexpr (s == 18 && g == 6) *(unsigned*)(smem + rsa) = sg.x;
                if constexpr (s == 14 && (g == 6 || g == 8)) { zq[g - 6] = 0u; zq[g - 5] = 0u; pin(zq[g - 6]); pin(zq[g - 5]); }
                if constexpr (s == 19 && g == 0) *(v4u*)(smem + zpa) = v4u{zq[0], zq[1], zq[2], zq[3]};
                if constexpr (s == 18 && g == 1) touch_(tch, p.ws);
                if constexpr (s == 19 && g == 2) { if (__builtin_expect(!kearly, 0)) poll1(fwo, p.ws); }
                if constexpr (it >= 0) {
                    if constexpr (d0 && d1) DMA_LD1(it, d1 ? nt_ : 0);
                    else if constexpr (d0) DMA_LD(it);
                    else if constexpr (it == 202) gld2s<0>(sg, sto, p.ws);
                    else if constexpr (it == 203) gld4s<0>(sw0, swo, p.ws);
                    else if constexpr (it == 207) gldu16(sdq, (unsigned)chn * 2u, p.ssmD);
                    else if constexpr (it == 208) gld2s<0>(fw, fwo, p.ws);
                    else if constexpr (it >= 500) gld_w2<it % 50>(w2o, w2b0 + ((it % 50) / 5) * 5120);
                    else { static_assert(p1_pos(19) >= P1_PRE, ""); st64(tg2, rs, fko1); }
                }
                if constexpr (d1 && !d0) DMA_M0(d1 ? nt_ : 0);
                site<2000 + 10 * s + g>();
                SCHED();
            });
        };
        static_for<20>(kstep);
        SCHED();
        rr[0] = f32x4{rr_[0], rr_[1], rr_[2], rr_[3]}; rr[1] = f32x4{rr_[4], rr_[5], rr_[6], rr_[7]};
        ACC_FENCE10(acc[0], acc[1]);
        STAMP(1)
    }

    auto layer_body = LAMBDA(layer_) {
        constexpr int layer = layer_.value;
        const int sga = sgn_;
        const bool cv = layer == 0 && cv0;
        v2u fh;
        unsigned ks[10];
        f32x4 ca[8], a2[4][5];
        float rsr, rsm;
        v4u cA[2][8], cB[2][8];
        f32x4 pp[5];
        constexpr int R2 = kCR1 + H3, LP = (7 - R2) & 1;
        static_assert(GF && kFHP >= 7 - KG0 / 4 + 1, "");
        constexpr bool TB = (SBT >> layer) & 1;
        float bv[8][5];
        f32x2 pr[5][2], ps[5][2];
        auto powers = [&]() __attribute__((always_inline)) {
            if constexpr (TB) {
#pragma unroll
                for (int nb = 0; nb < 5; ++nb) {
                    const f32x2 m = {lr[nb] * lr[nb] - ls[nb] * ls[nb], 2.0f * lr[nb] * ls[nb]};
                    pr[nb][0] = f32x2{lr[nb], m.x}; ps[nb][0] = f32x2{ls[nb], m.y};
#pragma unroll
                    for (int j = 1; j < 2; ++j) pstep(pr[nb][j], ps[nb][j], pr[nb][j - 1], ps[nb][j - 1], m);
#pragma unroll
                    for (int j = 0; j < 2; ++j) { pin(pr[nb][j]); pin(ps[nb][j]); }
                }
                SCHED();
            }
        };
        {
            float a0[5], a1[5];
            static_for<2>(LAMBDA(m_) {
                constexpr int mb = m_.value;
#pragma unroll
                for (int nb = 0; nb < 5; ++nb) {
                    const f32x4 v = __builtin_elementwise_fma(acc[mb][nb], rr[mb], f32x4{tt[mb][nb][0], tt[mb][nb][1], tt[mb][nb][2], tt[mb][nb][3]});
#pragma unroll
                    for (int i = 0; i < 4; ++i) bv[4 * mb + i][nb] = v[i];
                }
                SCHED();
                if constexpr (TB) {
#pragma unroll
                    for (int i = mb ? 4 : 1; i < 4 + 4 * mb; ++i) cstepi(bv[i][0], bv[i][1], bv[i][2], bv[i][3], bv[i][4], bv[i - 1][0], bv[i - 1][1], bv[i - 1][2], bv[i - 1][3], bv[i - 1][4], lr, ls);
#pragma unroll
                    for (int nb = 0; nb < 5; ++nb) a0[nb] = bv[7][nb];
                }
                if constexpr (mb == 0 && !TB) cstep(a0, bv[0], lr, ls, bv[1][0], bv[1][1], bv[1][2], bv[1][3], bv[1][4]);
#pragma unroll
                for (int i8 = mb ? 4 : 2; i8 < (TB ? 0 : 4 + 4 * mb); i8 += 2) {
                    cstep(a1, a0, lr, ls, bv[i8][0], bv[i8][1], bv[i8][2], bv[i8][3], bv[i8][4]);
                    cstep(a0, a1, lr, ls, bv[i8 + 1][0], bv[i8 + 1][1], bv[i8 + 1][2], bv[i8 + 1][3], bv[i8 + 1][4]);
                }
                SCHED();
            });
#pragma unroll
            for (int nb = 0; nb < 5; ++nb) *(float*)(smem + sga + nb * 64) = a0[nb];
        }
        SCHED();
        const int w_ = w0;
        {
        const int w = w_, mt1 = mt1_;
        if (layer == 0 && __builtin_expect(!kearly, 0)) {
            unsigned f0_, f1_;
            asm volatile("s_waitcnt vmcnt(0)\n\t" AR("%0", 36, 0) AR("%1", 36, 1) : "=v"(f0_), "=v"(f1_)::"memory");
            kearly = __builtin_amdgcn_ballot_w64(((f0_ ^ tag0) | (f1_ ^ tag1)) != 0u) == 0ull;
            ORDER();
            if (kearly) KW_REQ()
        }
        asm volatile("s_waitcnt lgkmcnt(0)" ::: "memory");
        __builtin_amdgcn_s_barrier();
        asm volatile("" ::: "memory");
        STAMP(layer ? 14 : 2)
#pragma unroll
        for (int s = 0; s < 10; ++s) ks[s] = (unsigned)s * 1024u;
        constexpr int NX = DSE ? 58 : 53;
        constexpr int NW2A = NW2P + 7 * kCR1 + kXA, NW3 = NW2P + kN3E + kN3;
        auto w2ld = LAMBDA(i_) {
            constexpr int i = i_.value;
            gld_w2<i>(w2o, p.ws + 2048 + ks[i / 5] * 5u);
        };
        auto w2a = LAMBDA(i_) {
            constexpr int k = i_.value;
            if constexpr (k < kXA) { w2ld(std::integral_constant<int, NW2P + 2 * k>{}); w2ld(std::integral_constant<int, NW2P + 2 * k + 1>{}); }
            else w2ld(std::integral_constant<int, NW2P + kXA + k>{});
        };
        auto w2x = LAMBDA(i_) {
            constexpr int i = i_.value < NX - 50 ? 50 + i_.value : NW2A + i_.value - (NX - 50) < 50 ? NW2A + i_.value - (NX - 50) : 99;
            if constexpr (i < 50) w2ld(std::integral_constant<int, i>{});
            else if constexpr (i < 52) lda4<40 + 4 * (i - 50), (i - 50) * 64>(xso, p.x0);
            else if constexpr (i == 52) lda4<48, 128>(xso - 2u * (unsigned)(bb & 16), p.x0);
            else if constexpr (i < NX) lda2<20 + 2 * (i - 53), 32 * (i - 53)>(dso, p.Dskip);
        };
        constexpr int NB = NX - NW2A, NB1 = KB1 < NB ? KB1 : NB, KPL = 7 * (8 - R2) - kFHP, NFH = NB - NB1 - (KPL - kFHK) > 0 ? NB - NB1 - (KPL - kFHK) : 0;
        auto w2b = LAMBDA(i_) {
            constexpr int k = i_.value - 7 * R2;
            if constexpr (k < NB1) w2x(std::integral_constant<int, k>{});
            if constexpr (k > kFHK) w2x(std::integral_constant<int, NB1 + k - kFHK - 1>{});
            if constexpr (k == kFHK) { vm_wait<NB1>(); st64(v2u{tag0, tag1}, rs, fho1); STAMP(39) }
            if constexpr (k == kFHK - 1) { STAMP(38) }
            if constexpr (k == KPL) poll1(fho, p.ws);
            static_assert(NB1 <= kFHK + 1 && NB1 + 7 * (7 - R2) + KG0 / 4 - kFHK - 1 >= NB && NW2A <= 50 && NW3 <= NW2A && NFH <= NB - NB1, "");
        };
        auto partb = [&]() __attribute__((always_inline)) {
            const float* sp = (const float*)(smem + spa);
            rsr = sp[0]; rsm = sp[8];
            if (layer == 1) { pin(rsr); pin(rsm); }
            if (layer == 1) {
                ACC_TIE5(dsk);
#pragma unroll
                for (int nb = 0; nb < 5; ++nb) { a2[3][nb] = xin[nb] + (xin[nb] * rsr + rsm) * unpk4(dsk[nb]); pin(a2[3][nb]); }
            }
        };
        constexpr int LBW = layer ? 2 : 3;
        if (w == 3) {
            site<3 + 10 * layer>(); SCHED();
            f32x2 sv[16];
#pragma unroll
            for (int i = 0; i < 16; ++i) sv[i] = *(const f32x2*)(smem + spl + (l_sg(layer, i >> 2) - SC + (i & 3) * 320));
            f32x2 u[4], v[4], x[4];
            cfma4(u, l8, sv[0], sv[4], sv[8], sv[12], sv[1], sv[5], sv[9], sv[13]);
            cfma4(v, l8, u[0], u[1], u[2], u[3], sv[2], sv[6], sv[10], sv[14]);
            cfma4(x, l8, v[0], v[1], v[2], v[3], sv[3], sv[7], sv[11], sv[15]);
            f32x2 P[4][4];
            P[0][0] = f32x2{0.f, 0.f}; P[0][1] = x[0]; P[0][2] = cfma(l32, x[0], x[1]); P[0][3] = cfma(l32, P[0][2], x[2]);
            const f32x2 E = cfma(l32, P[0][3], x[3]);
            st128(v4u{__float_as_uint(E.x), tag0, __float_as_uint(E.y), tag0}, rs, es);
            SCHED();
            if (layer == 0) { FW_OK() static_for<kN3E>(LAMBDA(i_) { w2ld(std::integral_constant<int, NW2P + i_.value>{}); }); }
#pragma unroll
            for (int kk = 1; kk < 4; ++kk) cfma4(P[kk], l8, P[kk - 1][0], P[kk - 1][1], P[kk - 1][2], P[kk - 1][3], sv[kk - 1], sv[kk + 3], sv[kk + 7], sv[kk + 11]);
#pragma unroll
            for (int i = 0; i < 16; ++i) *(f32x2*)(smem + spl + (l_sg(layer, i >> 2) - SC + (i & 3) * 320)) = P[i & 3][i >> 2];
            if (layer == 0) partb();
            if (layer == 1) powers();
            if (layer == 0 && H3) *(unsigned*)(smem + L_FL + (spl & 0x38) / 2) = 0u;
        }
        if (w == LBW) {
            site<5 + 10 * layer>(); SCHED();
            f32x2 wg[17], t4[4];
            unsigned ea[15], tg[15];
            const f32x2 z2 = {0.f, 0.f};
            wg[1] = cmul(l128, l128); wg[2] = cmul(wg[1], wg[1]); wg[3] = cmul(wg[2], wg[1]); wg[4] = cmul(wg[2], wg[2]);
#pragma unroll
            for (int i = 4; i < 16; i += 4) {
                cfma4(t4, wg[i], wg[1], wg[2], wg[3], wg[4], z2, z2, z2, z2);
#pragma unroll
                for (int e = 0; e < 4; ++e) wg[i + 1 + e] = t4[e];
            }
#pragma unroll
            for (int i = 0; i < 15; ++i) {
                const int ch = mt1 - 1 - i;
                if (i) pin(wg[i]);
                ea[i] = ch < 0 ? kNone : (unsigned)ch * 10240u; tg[i] = ch < 0 ? 0u : tag0;
                pins(ea[i]); pins(tg[i]);
            }
            if constexpr (layer == 0) tour<3>();
            powers();
            f32x2 H0, H1;
            __builtin_amdgcn_s_sleep(layer ? KLB1 : KLB0);
            unsigned spins = dead ? kSpinCap : 0u;
            bool miss;
#pragma nounroll
            do {
                asm volatile("" ::: "memory");
                v4u eg[15];
#pragma unroll
                for (int i = 0; i < 15; ++i) eg[i] = ld128s(rs, eo, ea[i]);
                SCHED();
                unsigned x = __builtin_amdgcn_bitop3_b32(eg[0].y, eg[0].w, tg[0], 0x7E);
                H0 = f32x2{__uint_as_float(eg[0].x), __uint_as_float(eg[0].z)}; H1 = f32x2{0.f, 0.f};
#pragma unroll
                for (int i = 1; i < 15; i += 2) {
                    x |= __builtin_amdgcn_bitop3_b32(eg[i].y, eg[i].w, tg[i], 0x7E) | __builtin_amdgcn_bitop3_b32(eg[i + 1].y, eg[i + 1].w, tg[i + 1], 0x7E);
                    pin(x);
                    gacc2(H0, H1, wg[i], wg[i + 1], eg[i], eg[i + 1]);
                    SCHED();
                }
                miss = __builtin_amdgcn_ballot_w64(x != 0u) != 0ull;
            } while (miss && ++spins <= kSpinCap);
            dead = dead || miss;
            ORDER();
            *(f32x2*)(smem + spl + (L_CW - SC)) = H0 + H1;
            STAMP(layer ? 36 : 37)
            if (layer == 0) {
                static_for<NW3 - NW2P - kN3E>(LAMBDA(i_) { w2ld(std::integral_constant<int, NW2P + kN3E + i_.value>{}); });
            }
        }
        int w2_ = w;
        pins(w2_);
        if (layer == 0) UNDEF8(ca);
        if (layer == 0 && w2_ != LBW) {
            if (cv) {
                site<2>(); SCHED();
                FW_OK()
                st_ur((unsigned)(size_t)(LAS char*)smem + (unsigned)uwa);
                STAMP(32)
                conv_part<0, kCR1>(ca, smem, ab, bb, w2a, LAMBDA(i_) {}, [&]() __attribute__((always_inline)) {
                    v4u ad;
                    const unsigned dv = sdq << (adm & 16u);
#pragma unroll
                    for (int i = 0; i < 4; ++i) ad[i] = dv & (unsigned)__builtin_amdgcn_sbfe((int)adm, 8 + i, 1);
                    asm volatile("s_nop 1" : "+v"(ad));
                    return ad;
                }, cA, cB);
                partb();
                powers();
            } else {
                if constexpr (layer == 0) tour<5>();
                FW_OK()
                static_for<NW2A - NW2P>(LAMBDA(i_) { w2ld(std::integral_constant<int, NW2P + i_.value>{}); __builtin_amdgcn_s_sleep(1); });
                partb();
                powers();
            }
        }
        if (layer == 1 && w < LBW) { site<4 + 10 * layer>(); SCHED(); powers(); if constexpr (layer == 1) { if (w == 0) tour<6>(); } }
        asm volatile("s_waitcnt lgkmcnt(0)" ::: "memory");
        __builtin_amdgcn_s_barrier();
        asm volatile("" ::: "memory");
        STAMP(layer ? 15 : 4)
        {
            float cw[5], ex[5];
#pragma unroll
            for (int nb = 0; nb < 5; ++nb) { cw[nb] = *(const float*)(smem + cwa + nb * 64); ex[nb] = *(const float*)(smem + sga + nb * 64); }
            v2u ta[5], td[5];
            if constexpr (TB) {
                cstepi(ex[0], ex[1], ex[2], ex[3], ex[4], cw[0], cw[1], cw[2], cw[3], cw[4], lqr, lqs);
                f32x2 cs[5];
                PIN5(ex);
#pragma unroll
                for (int nb = 0; nb < 5; ++nb) { cs[nb] = f32x2{ex[nb], dpp<0xB1>(ex[nb])}; pin(cs[nb]); }
                SCHED();
                static_for<5>(LAMBDA(n_) {
                    constexpr int nb = n_.value;
                    f32x2 h[4] = {{bv[0][nb], bv[1][nb]}, {bv[2][nb], bv[3][nb]}, {bv[4][nb], bv[5][nb]}, {bv[6][nb], bv[7][nb]}};
                    padd(h, pr[nb], ps[nb], cs[nb]);
                    *(v4u*)(smem + tbw + nb * 1024) = v4u{cvtpk(h[0].x, h[0].y), cvtpk(h[1].x, h[1].y), cvtpk(h[2].x, h[2].y), cvtpk(h[3].x, h[3].y)};
                    ta[nb] = __builtin_bit_cast(v2u, trread(smem, tbr + nb * 1024)); td[nb] = __builtin_bit_cast(v2u, trread(smem, tbr + nb * 1024 + 512));
                    SCHED();
                    if constexpr (nb >= LAG) { st128(v4u{ta[nb - LAG].x, ta[nb - LAG].y, td[nb - LAG].x, td[nb - LAG].y}, rs, hso + (nb - LAG) * 256); SCHED(); }
                });
            } else
            static_for<2>(LAMBDA(h_) {
                constexpr int n0 = 2 * h_.value;
                float hv[9][3];
                cstep3(hv[0], cw + n0, lqr + n0, lqs + n0, ex + n0);
                if constexpr (n0 == 0) {
                    chain24<0>(hv, bv, lr, ls);
                    chain24<0>(hv + 4, bv + 4, lr, ls);
                } else {
                    chain33<2>(hv, bv, lr, ls);
                    SCHED();
#pragma unroll
                    for (int rd = 0; rd < 2; ++rd) st128(v4u{ta[rd].x, ta[rd].y, td[rd].x, td[rd].y}, rs, hso + rd * 256);
                    SCHED();
                    chain33<2>(hv + 3, bv + 3, lr, ls);
                    chain32<2>(hv + 6, bv + 6, lr, ls);
                }
#pragma unroll
                for (int e = 0; e < (n0 ? 3 : 2); ++e) {
                    v4u o;
                    o.x = cvtpk(hv[1][e], hv[2][e]); o.y = cvtpk(hv[3][e], hv[4][e]);
                    o.z = cvtpk(hv[5][e], hv[6][e]); o.w = cvtpk(hv[7][e], hv[8][e]);
                    *(v4u*)(smem + tbw + (n0 + e) * 1024) = o;
                }
#pragma unroll
                for (int rd = n0; rd < (n0 ? 5 : 2); ++rd) { ta[rd] = __builtin_bit_cast(v2u, trread(smem, tbr + rd * 1024)); td[rd] = __builtin_bit_cast(v2u, trread(smem, tbr + rd * 1024 + 512)); }
                SCHED();
            });
#pragma unroll
            for (int rd = TB ? 5 - LAG : 2; rd < 5; ++rd) st128(v4u{ta[rd].x, ta[rd].y, td[rd].x, td[rd].y}, rs, hso + rd * 256);
            SCHED();
        }
#pragma unroll
        for (int s = 0; s < 10; ++s) pins(ks[s]);
        if (layer == 1) { partb(); SCHED(); DRAIN(); st64(v2u{tag0, tag1}, rs, fho1); }
        STAMP(layer ? 16 : 5)
        int fa = L_FL + 4 * w, pa = l_tb(0, 0) + (w < 3 ? w : 0) * 5120 + (int)(w2o & 1023u);
        unsigned one = 1u;
        if constexpr (layer == 0 && H3) {
            pin(fa); pin(pa); pin(one);
            *(unsigned*)(smem + fa) = one;
            asm volatile("" ::: "memory");
        }
        if (cv) {
            if constexpr (layer == 0) site<6>();
            SCHED();
            unsigned fl = 1u;
            conv_part<R2, 8>(ca, smem, ab, bb, w2b, LAMBDA(g_) {
                constexpr int g = g_.value - (KG0 - 8);
                if constexpr (H3 && g == 0) { asm volatile("" ::: "memory"); fl = *(const unsigned*)(smem + fa + 16); asm volatile("" ::: "memory"); }
                if constexpr (H3 && g > 0 && g < 6) pp[g - 1] = *(const f32x4*)(smem + pa + (g - 1) * 1024);
                static_assert(!H3 || KG0 >= 8, "");
            }, []() {}, cA, cB);
            while (__builtin_expect(__builtin_amdgcn_readfirstlane(fl) == 0u, 0)) {
                asm volatile("" ::: "memory");
                fl = *(const unsigned*)(smem + fa + 16);
                asm volatile("" ::: "memory");
#pragma unroll
                for (int i = 0; i < 5; ++i) pp[i] = *(const f32x4*)(smem + pa + i * 1024);
            }
        } else if (layer == 0) {
            if (H3 && w == 3) {
                if constexpr (layer == 0) site<16>();
                constexpr int NR = NB - NB1 - NFH, N3 = NR + NW2A - NW3;
                auto fill3 = LAMBDA(n_) {
                    constexpr int n = n_.value, k = n - KF3 - 1;
                    if constexpr (n < NB1) w2x(n_);
                    if constexpr (n == KF3) { vm_wait<NB1>(); st64(v2u{tag0, tag1}, rs, fho1); }
                    if constexpr (k >= 0 && k < NR) w2x(IC(NB1 + k));
                    else if constexpr (k >= NR && k < N3) w2ld(IC(NW3 + k - NR));
                };
                int a3[4] = {ab[0], ab[1], ab[2], ab[3]}, b3 = bb;
                f32x4 cb[2][8];
                auto chan = LAMBDA(x_) {
                    constexpr int x = x_.value;
                    const int (&ab)[4] = a3;
                    const int& bb = b3;
                    if constexpr (x == 0) {
                        CV_LOAD_A(cA[0], 0, kCR1, 0) CV_LOAD_A(cA[0], 0, kCR1, 1)
                        static_for<8>(LAMBDA(e_) { constexpr int e = (e_.value + 3) & 7; CV_LOAD_B(cB[0], e, kCR1) SCHED(); });
                        static_for<7>(LAMBDA(a_) { constexpr int a = a_.value + 1; CV_LOAD_A(cA[0], a, kCR1, 0) CV_LOAD_A(cA[0], a, kCR1, 1) });
                        SCHED();
                        lds_wait3(fa - 12);
                    }
                    static_for<30>(LAMBDA(g_) {
                        constexpr int g = g_.value, a = cv_pair(3, 8, g) / 8, e = cv_pair(3, 8, g) % 8, u = g - 3, f = (u / 3 + 3) & 7;
                        if constexpr (a == 0) mf0(cb[x & 1][e], cA[x & 1][0], cB[x & 1][e]); else mf(cb[x & 1][a + e], cA[x & 1][a], cB[x & 1][e]);
                        SCHED();
                        if constexpr (x < 2 && g < 2) { a3[2 * g] += REGSZ; a3[2 * g + 1] += REGSZ; pin(a3[2 * g]); pin(a3[2 * g + 1]); }
                        if constexpr (x < 2 && g == 2) { b3 += REGSZ; pin(b3); }
                        if constexpr (x < 2 && u >= 0 && u < 24) { if constexpr (u % 3 == 2) CV_LOAD_B(cB[(x & 1) ^ 1], f, kCR1) else CV_LOAD_A(cA[(x & 1) ^ 1], u / 3, kCR1, u % 3) }
                        if constexpr (x > 0 && g % 3 == 2 && g > 2 && g < 18) *(f32x4*)(smem + pa + (x - 1) * 5120 + (g / 3 - 1) * 1024) = cb[(x & 1) ^ 1][2 + g / 3];
                        if constexpr (x > 0 && g == 20) { asm volatile("" ::: "memory"); *(unsigned*)(smem + fa + 4 * x) = one; }
                        SCHED();
                        site<3300 + 30 * x + g>();
                        if constexpr (g % 3 == 1) { fill3(IC(10 * x + g / 3)); SCHED(); }
                    });
                };
                auto out = LAMBDA(x_) {
                    constexpr int x = x_.value;
                    asm volatile("s_nop 7" ::: "memory");
#pragma unroll
                    for (int i = 0; i < 5; ++i) *(f32x4*)(smem + pa + x * 5120 + i * 1024) = cb[x & 1][3 + i];
                    asm volatile("" ::: "memory");
                    *(unsigned*)(smem + fa + 4 + 4 * x) = one;
                    SCHED();
                };
                chan(IC(0));
                chan(IC(1));
                if (three) { chan(IC(2)); out(IC(2)); }
                else { out(IC(1)); static_for<10>(LAMBDA(i_) { fill3(IC(20 + i_.value)); __builtin_amdgcn_s_sleep(1); }); }
                static_for<(KF3 + 1 + N3 > 30 ? KF3 + 1 + N3 - 30 : 0)>(LAMBDA(i_) { fill3(IC(30 + i_.value)); });
                if constexpr (layer == 0) tour<2>();
            } else {
                static_for<NB1>(LAMBDA(i_) { w2x(i_); __builtin_amdgcn_s_sleep(1); });
                vm_wait<NB1>();
                st64(v2u{tag0, tag1}, rs, fho1);
                static_for<NB - NB1 - NFH>(LAMBDA(i_) { w2x(std::integral_constant<int, NB1 + i_.value>{}); __builtin_amdgcn_s_sleep(1); });
                if (!H3 && w == 3) { static_for<NW2A - NW3>(LAMBDA(i_) { w2ld(std::integral_constant<int, NW3 + i_.value>{}); __builtin_amdgcn_s_sleep(1); }); if constexpr (layer == 0 && !H3) tour<2>(); }
                if (KSN) __builtin_amdgcn_s_sleep(KSN);
            }
            poll1(fho, p.ws);
            static_for<NFH>(LAMBDA(i_) { w2x(std::integral_constant<int, NB - NFH + i_.value>{}); __builtin_amdgcn_s_sleep(1); });
            static_assert(NFH <= NB - (NX - 50), "");
            UNDEF8(cA[LP]); UNDEF8(cB[LP]);
            if (H3) asm volatile("" : "=v"(pp[0]), "=v"(pp[1]), "=v"(pp[2]), "=v"(pp[3]), "=v"(pp[4]));
        }
        if (layer == 0) { unsigned f0_, f1_; asm volatile("s_waitcnt vmcnt(%2)\n\t" AR("%0", 36, 0) AR("%1", 36, 1) : "=v"(f0_), "=v"(f1_) : "i"(NFH) : "memory"); fh = v2u{f0_, f1_}; }
        STAMP(layer ? 29 : 6)
        }
        unsigned g[8][2];

            v2u pt[4];
            if (layer == 0) WAVE_RETRY(ISTAG(fh), fh = ld64(rs, fho))
            else { skip<SKIP7>(); pipe_flag(pt, fho, p.ws, (u64)tag0 | ((u64)tag1 << 32), dead); ORDER(); }
            STAMP(layer ? 17 : 31)
            site<7 + 10 * layer>(); SCHED();
            constexpr int RING2 = layer ? KR2 : KRT;
            v4u ar[RING2];
            constexpr int NRT = layer ? RING2 : (36 - KG0) / KRS + 2 < KRT ? (36 - KG0) / KRS + 2 : KRT;
            unsigned xs_[3][4];
            float ys[8][4];
            auto gate = LAMBDA(n_) {
                constexpr int n = n_.value, bl = n / 10, u = n % 10;
                if constexpr (n < 0) {}
                else if constexpr (n >= 100) swp2n(g[2 * (n - 100)][0], g[2 * (n - 100) + 1][0], g[2 * (n - 100)][1], g[2 * (n - 100) + 1][1]);
                else if constexpr (u < 2) { if constexpr (H3) { ys[bl][2 * u] = pp[bl - 3][2 * u]; ys[bl][2 * u + 1] = pp[bl - 3][2 * u + 1]; add2(ys[bl][2 * u], ys[bl][2 * u + 1], ca[bl][2 * u], ca[bl][2 * u + 1]); } }
                else if constexpr (H3 && bl >= 3) cvt2(g[bl][0], g[bl][1], f32x4{ys[bl][0], ys[bl][1], ys[bl][2], ys[bl][3]});
                else cvt2(g[bl][0], g[bl][1], ca[bl]);
            };
            if constexpr (layer == 0) {
#pragma unroll
                for (int i = 0; i < 2; ++i) { ar[i] = ld128s(rs, ho[0], ks[i]); SCHED(); }
                static_for<36 - KG0>(LAMBDA(n_) {
                    constexpr int n = n_.value, g = KG0 + n;
                    constexpr int a = g < 8 ? 0 : g < 15 ? 1 : g < 21 ? 2 : g < 26 ? 3 : g < 30 ? 4 : g < 33 ? 5 : g < 35 ? 6 : 7;
                    constexpr int e = g - (a * 8 - a * (a - 1) / 2), i = 2 + n / KRS;
                    mf(ca[a + e], cA[LP][a], cB[LP][e]);
                    SCHED();
                    if constexpr (n % KRS == 0 && i < NRT) ar[i] = ld128s(rs, ho[i / 10], ks[i % 10]);
                    if constexpr (n < 12) rda<40 + n>(xs_[n >> 2][n & 3]);
                    constexpr int gt_[12] = {2, 12, 22, 30, 100, 31, 32, 40, 41, 101, 42, 50};
                    if constexpr (g >= 24) gate(IC(gt_[g < 24 ? 0 : g - 24]));
                    site<3252 + g>();
                    SCHED();
                });
                static_assert(KG0 <= 15 && NRT >= 12 && DSE, "");
            } else {
#pragma unroll
                for (int i = 0; i < RING2; ++i) { ar[i] = ld128s(rs, ho[i / 10], ks[i % 10]); SCHED(); }
            }
        v4u gl[3];
        v2u f0;
        bool gearly = false;
        {
            const char* src = smem + srca;
            const char *glb1 = p.ws + 32 * 4096, *glb2 = p.ws + 48 * 4096;
            float zx[4], zd[4], zt[4], zz[5][4];
            if constexpr (STAMPS == 1) { STAMP(layer ? 50 : 40) }
            static_for<40>(LAMBDA(i_) {
                constexpr int i = i_.value, m = i / 10, s = i % 10;
                static_for<5>(LAMBDA(k_) {
                    constexpr int kk = k_.value, n = 2 * (i - 3) + kk / 2 - 3, nb = n / 8, u = n % 8, pv = i - 1 + RING2;
                    if constexpr (s == 0 && m < 3) mfa0<kk>(a2[m][kk], ar[i % RING2]); else mfa<5 * s + kk>(a2[m][kk], ar[i % RING2]);
                    SCHED();
                    constexpr int gu_[12] = {51, 52, 60, 61, 102, 62, 70, 71, 72, -1, 103, -1};
                    if constexpr (layer == 0 && 5 * i + kk < 12) gate(IC(gu_[(5 * i + kk) % 12]));
                    if constexpr (layer == 0 && i == 25 && kk == 1) { pins(glb1); pins(glb2); }
                    if constexpr (layer == 0 && i >= 3 && i < 26 && (kk & 1)) {
                        if constexpr (n < 0) swp2n(xs_[n + 3][0], xs_[n + 3][2], xs_[n + 3][1], xs_[n + 3][3]);
                        else if constexpr (nb < 5) {
                            if constexpr (u < 2) unp2(zx[2 * u], zx[2 * u + 1], xs_[nb >> 1][2 * (nb & 1) + u]);
                            else if constexpr (u < 4) unp2(zd[2 * u - 4], zd[2 * u - 3], dsk[nb][u - 2]);
                            else if constexpr (u < 6) fma2(zt[2 * u - 8], zt[2 * u - 7], zx[2 * u - 8], zx[2 * u - 7], rsr, rsr, rsm, rsm);
                            else fma2(zz[nb][2 * u - 12], zz[nb][2 * u - 11], zt[2 * u - 12], zt[2 * u - 11], zd[2 * u - 12], zd[2 * u - 11], zx[2 * u - 12], zx[2 * u - 11]);
                        }
                    }
                    if constexpr (i == 0 && kk == 0 && layer == 1) { pin(pt[0]); pin(pt[1]); pin(pt[2]); pin(pt[3]); }
                    if constexpr (layer == 0 && i < 2 && kk > 0 && 12 + 4 * i + kk - 1 >= NRT && 12 + 4 * i + kk - 1 < RING2) ar[12 + 4 * i + kk - 1] = ld128s(rs, ho[1], ks[2 + 4 * i + kk - 1]);
                    if constexpr (layer == 0 && i == kGSI && kk > 0) st128(v4u{g[2 * kk - 2][0], g[2 * kk - 2][1], g[2 * kk - 1][0], g[2 * kk - 1][1]}, rs, gso + (kk - 1) * 1024);
                    if constexpr (layer == 0 && i > 2 && i <= 7 && kk == (i == kGSI ? 0 : 2)) {
                        if constexpr (DSE) { unsigned d0_, d1_; rda<20 + 2 * (i - 3)>(d0_); rda<21 + 2 * (i - 3)>(d1_); dsk[i - 3] = v2u{d0_, d1_}; }
                    }
                    if constexpr (kk == 0 && i > 0 && pv < 40) ar[pv % RING2] = ld128s(rs, ho[pv / 10], ks[pv % 10]);
                    constexpr int s1 = s - 1 - (KMB && layer == 0 && m == 1 ? kFGI - 10 : 0);
                    if constexpr (kk == 2 && m >= 1 && s1 >= 0 && s1 < 5) *(f32x4*)(smem + dst[m - 1] + s1 * 1024) = a2[m - 1][s1];
                    site<4000 + 1000 * layer + 5 * i + kk>();
                    if constexpr (i >= 37) a2[i - 37][kk] = *(const f32x4*)(src + (i - 37) * 5120 + kk * 1024);
                    SCHED();
                });
                if constexpr (i == 10 && (layer || !KMB)) { asm volatile("s_waitcnt lgkmcnt(0)" ::: "memory"); __builtin_amdgcn_s_barrier(); asm volatile("" ::: "memory"); }
                if constexpr (i == kFGI && layer == 0) {
                    static_assert(kFGI >= 10 && kFGI <= 14, "");
                    asm volatile("s_waitcnt vmcnt(%0) lgkmcnt(0)" ::"i"(kFGI - kGSI) : "memory");
                    __builtin_amdgcn_s_barrier(); asm volatile("" ::: "memory");
                    st64(v2u{tag0, tag1}, rs, fgo1);
                }
                if constexpr (i == 26 && layer == 0) {
#pragma unroll
                    for (int nb = 0; nb < 5; ++nb) { a2[3][nb] = f32x4{zz[nb][0], zz[nb][1], zz[nb][2], zz[nb][3]}; pin(a2[3][nb]); }
                }
                if constexpr (i == kGPI && layer == 0) gld2s<0>(f0, fgo, p.ws);
                if constexpr (i == kGCI && layer == 0) {
                    wait1(f0);
                    static_for<39 - i>(LAMBDA(r_) { pin(ar[(i + 1 + r_.value) % RING2]); });
                    gearly = __builtin_amdgcn_ballot_w64(!ISTAG(f0)) == 0ull;
                    ORDER();
                    undef3(gl);
                    if (__builtin_expect(gearly, 1)) { gld4t<0>(gl[0], glo, p.ws); gld4t<0>(gl[1], glo, glb1); gld4t<0>(gl[2], glo, glb2); }
                    else if (G2ND) poll2(fgo, fgo, p.ws);
                }
                if constexpr (i == 36) {
                    asm volatile("s_waitcnt lgkmcnt(0)" ::: "memory");
                    __builtin_amdgcn_s_barrier(); asm volatile("" ::: "memory");
                }
                if constexpr (STAMPS == 1 && (i == 0 || i == 9 || i == 10 || i == 15 || i == 25 || i == 35 || i == 36)) { STAMP((layer ? 51 : 41) + (i == 0 ? 0 : i == 9 ? 1 : i == 10 ? 2 : i == 15 ? 3 : i == 25 ? 4 : i == 35 ? 5 : 6)) }
                SCHED();
            });
            STAMP(layer ? 18 : 8)
        }
#pragma unroll
        for (int nb = 0; nb < 5; ++nb) a2[0][nb] = (a2[0][nb] + a2[1][nb]) + a2[2][nb];
        asm volatile("s_nop 1" : "+v"(a2[3][0]), "+v"(a2[3][1]), "+v"(a2[3][2]), "+v"(a2[3][3]), "+v"(a2[3][4]),
                     "+v"(a2[0][0]), "+v"(a2[0][1]), "+v"(a2[0][2]), "+v"(a2[0][3]), "+v"(a2[0][4]));
        float ty;
        {
            f32x4 s1, s2;
#pragma unroll
            for (int nb = 0; nb < 5; ++nb) {
                const f32x4 y = a2[3][nb] + a2[0][nb];
                xin[nb] = y;
                if (nb == 0) { s1 = y; s2 = y * y; } else { s1 += y; s2 += y * y; }
            }
            ty = xsum2h(hsum4(s1), hsum4(s2));
            st64(v2u{__float_as_uint(ty), tag0}, rs, sgs);
        }
        SCHED();
        STAMP(layer ? 19 : 9)
        int lane_o, b_o = b0, w_o = w0;
        asm volatile("v_mbcnt_lo_u32_b32 %0, -1, 0\n\tv_mbcnt_hi_u32_b32 %0, -1, %0" : "=v"(lane_o));
        asm volatile("" : "+s"(b_o), "+s"(w_o));
        const int lane = lane_o, b = b_o, w = w_o;
        const int c = lane & 15, q = lane >> 4;
        TILES()
        if (layer == 0 && __builtin_expect(!gearly, 0)) {
            if (G2ND) {
                unsigned f0_, f1_;
                asm volatile("s_waitcnt vmcnt(1)\n\t" AR("%0", 36, 0) AR("%1", 36, 1) : "=v"(f0_), "=v"(f1_)::"memory");
                f0 = v2u{f0_, f1_};
            }
            WAVE_RETRY(ISTAG(f0), f0 = ld64(rs, fgo))
            gld4t<0>(gl[0], glo, p.ws); gld4t<0>(gl[1], glo, p.ws + 32 * 4096); gld4t<0>(gl[2], glo, p.ws + 48 * 4096);
            asm volatile("s_waitcnt vmcnt(0)" : "+v"(gl[0]), "+v"(gl[1]), "+v"(gl[2])::"memory");
        }
        if (layer == 0) {
            asm volatile("s_waitcnt vmcnt(1)" : "+v"(gl[0]), "+v"(gl[1]), "+v"(gl[2])::"memory");
            char* tb = smem + SC + w * INBOX;
            *(v4u*)(tb + lane * 16) = gl[0]; *(v4u*)(tb + 1024 + lane * 16) = gl[1]; *(v4u*)(tb + 1536 + lane * 16) = gl[2];
#pragma unroll
            for (int nb = 0; nb < 5; ++nb) gt[nb] = __builtin_bit_cast(v2u, trread(tb, (16 * nb + 4 * q + (c >> 2)) * 32 + (c & 3) * 8));
        } else ACC_TIE5(gt);
        SCHED();
        f32x4 gf[5], yg[5];
#pragma unroll
        for (int nb = 0; nb < 5; ++nb) { gf[nb] = unpk4(gt[nb]); yg[nb] = xin[nb] * gf[nb]; pin(gf[nb]); pin(yg[nb]); }
        unsigned xs = OFF_X2F + (unsigned)((2 * mt2 + (w >> 1)) * 40960 + ((c >> 2) & 1) * 20480 + (10 * nt2 + 2 * (q & 1) + (q >> 1)) * 256 + (8 * (w & 1) + 4 * (c >> 3) + (c & 3)) * 16);
        unsigned pso = q < 2 ? OFF_PS + (unsigned)(((mt2 * 4 + w) * 8 + nt2) * 128 + c * 8 + q * 4) : kNone;
        unsigned fxo1 = OFF_FX + (unsigned)(((mt1 * 4 + 2 * (mt2 & 1) + (w >> 1)) * 16 + nt2 * 2 + (w & 1)) * 128);
        unsigned oo = (unsigned)(((64 * mt2 + 16 * w + c) * D_ + 80 * nt2 + 4 * q) * 4);
        if (layer == 0) { pin(xs); pin(pso); pin(fxo1); } else pin(oo);
        float c_r, c_m, c_rr, c_rm, c_mm;
        if constexpr (layer == 0 && PSA) {
            f32x4 su = yg[0], sg_ = gf[0], syu = xin[0] * yg[0], suu = yg[0] * yg[0], sug = yg[0] * gf[0], sgg = gf[0] * gf[0];
#pragma unroll
            for (int nb = 1; nb < 5; ++nb) { su += yg[nb]; sg_ += gf[nb]; syu += xin[nb] * yg[nb]; suu += yg[nb] * yg[nb]; sug += yg[nb] * gf[nb]; sgg += gf[nb] * gf[nb]; }
            const float hu = hsum4(su);
            c_r = xsum2h(hu, 2.f * hsum4(syu)); c_m = xsum2h(hsum4(sg_), 2.f * hu);
            c_rr = xsum2h(0.f, hsum4(suu)); c_rm = xsum2h(0.f, 2.f * hsum4(sug)); c_mm = xsum2h(0.f, hsum4(sgg));
            pin(c_r); pin(c_m); pin(c_rr); pin(c_rm); pin(c_mm);
        }
        SCHED();
        {
            float a, d;
            if constexpr (layer == 0) { skip<SKIP4>(); site<9>(); }
            pipe_gran<layer ? PDS : PDS0>(a, d, sgo, p.ws, tag0, dead);
            STAMP(layer ? 20 : 10)
            xsum2(a, d);
            const float me = a * (1.0f / D_);
            const float ri = __builtin_amdgcn_rsqf(fmaxf(d * (1.0f / D_) - me * me, 0.f) + 1e-5f), mr = -me * ri;
            if constexpr (layer == 0 && PSA) {
                const float ps_ = ty + ri * (c_r + ri * c_rr + mr * c_rm) + mr * (c_m + mr * c_mm);
                __builtin_amdgcn_raw_buffer_store_b32(__float_as_uint(ps_), rs, pso, 0, SC1);
                SCHED();
            }
#pragma unroll
            for (int nb = 0; nb < 5; ++nb) xin[nb] = yg[nb] * ri + (gf[nb] * mr + xin[nb]);
        }
        if constexpr (layer == 0) {
            v2u pk[5];
#pragma unroll
            for (int nb = 0; nb < 5; ++nb) { const f32x4 x = xin[nb]; pk[nb] = v2u{cvtpk(x.x, x.y), cvtpk(x.z, x.w)}; }
#pragma unroll
            for (int i = 0; i < 3; ++i) {
                const v2u pa = pk[2 * i], pb = pk[i < 2 ? 2 * i + 1 : 4];
                const auto r0 = __builtin_amdgcn_permlane16_swap(pa.x, pb.x, false, false);
                const auto r1 = __builtin_amdgcn_permlane16_swap(pa.y, pb.y, false, false);
                if (i < 2 || (q & 1) == 0) st128(v4u{r0[0], r1[0], r0[1], r1[1]}, rs, xs + i * 1024);
            }
            if constexpr (!PSA) {
                f32x4 s1, s2;
#pragma unroll
                for (int nb = 0; nb < 5; ++nb) { const f32x4 x = xin[nb]; if (nb == 0) { s1 = x; s2 = x * x; } else { s1 += x; s2 += x * x; } }
                __builtin_amdgcn_raw_buffer_store_b32(__float_as_uint(xsum2h(hsum4(s1), hsum4(s2))), rs, pso, 0, SC1);
            }
            SCHED();
            int w1lane0 = (c & 1) * 10240 + (c >> 1) * 128 + ((q ^ ((c & 1) | ((c >> 2) << 1))) * 16);
            int w1lane1 = (c & 1) * 10240 + (c >> 1) * 128 + (((4 + q) ^ ((c & 1) | ((c >> 2) << 1))) * 16);
            unsigned fxo = OFF_FX + (unsigned)((mt1 * 4 + w) * 2048 + c * 128);
            unsigned xo = OFF_X2F + (unsigned)((4 * mt1 + w) * 40960 + lane * 16), xo1 = xo + 20480u;
            const int row = 8 * q + (c & 7);
            unsigned po = OFF_PS + (unsigned)((((2 * mt1 + (w >> 1)) * 4 + 2 * (w & 1) + (row >> 4)) * 8 + 4 * (c >> 3)) * 128 + (row & 15) * 8);
            int w1hi0 = w1lane0 + 61440, w1hi1 = w1lane1 + 61440;
            pin(w1lane0); pin(w1lane1); pin(w1hi0); pin(w1hi1); pin(fxo); pin(xo); pin(xo1); pin(po);
            v4u wf[2][5];
#pragma unroll
            for (int nb = 0; nb < 5; ++nb) wf[0][nb] = *(const v4u*)(smem + w1lane0 + nb * 20480);
            SCHED();
            DRAIN();
            FLAG(fxo1)
            if (b == 255 && w == 0) FLAG(OFF_NONCE)
            STAMP(11)
            if (w == 0) tour<4>();
            addrs(std::integral_constant<int, 1>{}, lane, w, b);
            v2u pt[4];
            skip<SKIP5>();
            pipe_flag(pt, fxo, p.ws, (u64)tag0 | ((u64)tag1 << 32), dead);
            ORDER();
            STAMP(12)
            v4u ar[RING1][2];
#pragma unroll
            for (int s = 0; s < RING1; ++s)
#pragma unroll
                for (int mb = 0; mb < 2; ++mb) { ar[s][mb] = ld128s(rs, mb ? xo1 : xo, s * 1024); SCHED(); }
            v2u ps[4];
#pragma unroll
            for (int i = 0; i < 4; ++i) ps[i] = ld64(rs, po + i * 128);
            SCHED();
            float t1, t2, rinv, mri, rr_[8], rm_[8];
            static_for<20>(LAMBDA(s_) {
                constexpr int s = s_.value, cu = s & 1, nx = cu ^ 1;
                LGKM0() SCHED();
                static_for<10>(LAMBDA(g_) {
                    constexpr int g = g_.value, mb = g / 5, k = g % 5;
                    if constexpr (s == 0) mf0(acc[mb][k], ar[0][mb], wf[0][k]); else mf(acc[mb][k], ar[s % RING1][mb], wf[cu][k]);
                    SCHED();
                    if constexpr (s == 0 && g == 0) { pin(pt[0]); pin(pt[1]); pin(pt[2]); pin(pt[3]); }
                    if constexpr (s < 19 && g < 3) wf[nx][g] = *(const v4u*)(smem + (nx ? w1lane1 : w1lane0) + g * 20480 + ((s + 1) >> 1) * 1024);
                    if constexpr (s < 19 && (g == 3 || g == 4)) wf[nx][g] = *(const v4u*)(smem + (nx ? w1hi1 : w1hi0) + (g - 3) * 20480 + ((s + 1) >> 1) * 1024);
                    if constexpr (g == 5 && s + RING1 < 20) ar[s % RING1][0] = ld128s(rs, xo, (s + RING1) * 1024);
                    if constexpr (g == 2 && s > 0 && s - 1 + RING1 < 20) ar[(s - 1) % RING1][1] = ld128s(rs, xo1, (s - 1 + RING1) * 1024);
                    if constexpr (s == 8 && g == 8) { t1 = __uint_as_float(ps[0].x) + __uint_as_float(ps[1].x); pin(t1); t2 = __uint_as_float(ps[0].y) + __uint_as_float(ps[1].y); pin(t2); }
                    if constexpr (s == 9 && g == 6) { t1 += __uint_as_float(ps[2].x); pin(t1); t2 += __uint_as_float(ps[2].y); pin(t2); }
                    if constexpr (s == 9 && g == 7) { t1 += __uint_as_float(ps[3].x); pin(t1); t2 += __uint_as_float(ps[3].y); pin(t2); }
                    if constexpr (s == 10 && g == 6) { t1 += dpp<0x128>(t1); t2 += dpp<0x128>(t2); pin(t1); pin(t2); }
                    if constexpr (s == 10 && g == 7) { t1 *= (1.0f / D_); pin(t1); t2 *= (1.0f / D_); pin(t2); }
                    if constexpr (s == 10 && g == 8) { t2 = fmaxf(t2 - t1 * t1, 0.f); pin(t2); }
                    if constexpr (s == 11 && g == 6) { rinv = __builtin_amdgcn_rsqf(t2 + 1e-5f); pin(rinv); }
                    if constexpr (s == 11 && g == 8) { mri = -t1 * rinv; pin(mri); }
                    if constexpr (s == 12 && g == 4) *(float*)(smem + l_rs(1, 0) + w * INBOX + lane * 4) = c < 8 ? rinv : mri;
                    if constexpr (s >= 12 && s < 15 && g >= 6 && g < 9 && 3 * (s - 12) + g - 6 < 8) bc2b<3 * (s - 12) + g - 6>(rr_[3 * (s - 12) + g - 6], rm_[3 * (s - 12) + g - 6], rinv, mri);
                    if constexpr (s >= 15 && s < 19 && g >= 5) TT_GAP(5 * (s - 15) + g - 5)
                    site<6000 + 10 * s + g>();
                    SCHED();
                });
            });
            rr[0] = f32x4{rr_[0], rr_[1], rr_[2], rr_[3]}; rr[1] = f32x4{rr_[4], rr_[5], rr_[6], rr_[7]};
            asm volatile("kpf_end2:");
            ACC_FENCE10(acc[0], acc[1]);
            STAMP(13)
        } else {
#pragma unroll
            for (int nb = 0; nb < 5; ++nb) __builtin_amdgcn_raw_buffer_store_b128(__builtin_bit_cast(v4u, xin[nb]), ro, oo + 64 * nb, 0, OUTP);
            STAMP(21)
            STAMP_DUMP()
            asm volatile("kpf_end:");
        }
    };
    layer_body(std::integral_constant<int, 0>{});
    layer_body(std::integral_constant<int, 1>{});
}

extern "C" void kernel_launch(void* const* d_in, const int* in_sizes, int n_in,
                              void* d_out, int out_size, void* d_ws, size_t ws_size,
                              hipStream_t stream) {
    Params p;
    p.x0 = (const u16*)d_in[0];
    p.Lre = (const u16*)d_in[1];
    p.Lim = (const u16*)d_in[2];
    p.Bre = (const u16*)d_in[3];
    p.Bim = (const u16*)d_in[4];
    p.Cre = (const u16*)d_in[5];
    p.Cim = (const u16*)d_in[6];
    p.logdt = (const u16*)d_in[7];
    p.Dskip = (const u16*)d_in[8];
    p.k = (const u16*)d_in[10];
    p.v = (const u16*)d_in[11];
    p.shk = (const u16*)d_in[12];
    p.ssmk = (const u16*)d_in[13];
    p.ssmD = (const u16*)d_in[14];
    p.ws = (char*)d_ws + (size_t)WSOFF * 1048576;
    p.out = (float*)d_out;
    k_main<<<256, 256, 0, stream>>>(p);
}
```

```cpp
#include <hip/hip_runtime.h>
#include <stdint.h>
#include <utility>

#define WSOFF 0
#define STAMPS 0

typedef unsigned short u16;
typedef unsigned long long u64;
typedef unsigned v4u __attribute__((ext_vector_type(4)));
typedef unsigned v2u __attribute__((ext_vector_type(2)));
typedef float f32x4 __attribute__((ext_vector_type(4)));
typedef float f32x2 __attribute__((ext_vector_type(2)));
typedef short s16x4 __attribute__((ext_vector_type(4)));
typedef __bf16 bf16x2v __attribute__((ext_vector_type(2)));
#define LAS __attribute__((address_space(3)))
#define DI __device__ __forceinline__

constexpr int S_ = 2048, D_ = 640;
#define GF 1
#define KG0 12
#define KRT 12
#define KRS 2
#define KSN 0
#define KB1 4
#define KMB 1
#define H3 0
#define KF3 14
#define SBT 2
#define LAG 1
#define KR1 5
#define KR2 10
constexpr int RING1 = KR1;
#define KCR1 3
constexpr int kCR1 = KCR1;
#define KXA 10
#define KN3E 17
#define KN3 6
constexpr int kXA = KXA;
constexpr int kN3E = KN3E, kN3 = KN3;
#define KFHK 4
#define KFHP 12
constexpr int kFHK = KFHK, kFHP = KFHP;
#define KGPI 27
#define KGCI 33
#define KFGI 11
#define G2ND 1
#define KGSI 2
constexpr int kGSI = KGSI, kFGI = KFGI, kGPI = KGPI, kGCI = KGCI;

constexpr unsigned OFF_NONCE = 0;
constexpr unsigned OFF_ST = 4096;
constexpr unsigned OFF_FW = OFF_ST + 65536;
constexpr unsigned OFF_FK = OFF_FW + 131072;
constexpr unsigned OFF_FH = OFF_FK + 81920;
constexpr unsigned OFF_FX = OFF_FH + 262144;
constexpr unsigned OFF_FG = OFF_FX + 131072;
constexpr unsigned OFF_EG = OFF_FG + 98304;
constexpr unsigned OFF_SG = OFF_EG + 327680;
constexpr unsigned OFF_PS = OFF_SG + 524288;
constexpr unsigned OFF_KVT = OFF_PS + 131072;
constexpr unsigned OFF_GT = OFF_KVT + 2621440;
constexpr unsigned OFF_W2F = OFF_GT + 2621440;
constexpr unsigned OFF_X2F = OFF_W2F + 1638400;
constexpr unsigned OFF_DUM = OFF_X2F + 2621440;
constexpr unsigned OFF_HF = OFF_DUM + 163840;
constexpr unsigned WS_BYTES = OFF_HF + 2 * 5242880;
constexpr unsigned kNone = 0x40000000u;
constexpr unsigned OFF_DBG = 0x1800000;
#define I32M 1
#define KVS 1
#define KVU 0
#define MAP 0
#define TILES() const int xq = b & 7, j = b >> 3, mt2 = MAP ? 8 * (xq >> 1) + (j >> 2) : 4 * xq + (j >> 3), nt2 = MAP ? 4 * (xq & 1) + (j & 3) : j & 7, mt1 = mt2 >> 1, \
    nt1 = MAP ? 8 * (xq & 1) + (j & 7) : j & 15, i32 = 16 * (mt2 & 1) + mt1;

constexpr int SC = 102400;
constexpr int REGSZ = 13056;
constexpr int L_XR = SC;
constexpr int L_CO = SC + 12288;
constexpr int L_KB = SC + 51712;
constexpr int L_TAB = SC + 55808;
constexpr int L_CW = SC + 39168;
constexpr int L_FL = L_CW + 320;
constexpr int INBOX = 15360;
constexpr int l_rs(int l, int w) { return l ? SC + w * INBOX : L_KB + w * 256; }
constexpr int l_sg(int l, int w) { return l ? SC + w * INBOX + 256 : SC + (w == 0 ? 43264 : w == 1 ? 46080 : w == 2 ? 52736 : 56320); }
constexpr int l_tb(int l, int w) { return l ? SC + w * INBOX + 2048 : SC + 40960 + w * 5120; }
constexpr int LDS_BYTES = 163840;
constexpr int CV_E = 4608;
constexpr int CV_O = CV_E + (1056 + 15) * 4;

struct Params {
    const u16 *x0, *Lre, *Lim, *Bre, *Bim, *Cre, *Cim, *logdt, *Dskip, *k, *v, *shk, *ssmk, *ssmD;
    char* ws;
    float* out;
};

#define MF "v_mfma_f32_16x16x32_bf16 "
DI void mf(f32x4& c, v4u a, v4u b) { asm(MF "%0, %1, %2, %0" : "+v"(c) : "v"(a), "v"(b)); }
DI void mf0(f32x4& c, v4u a, v4u b) { asm(MF "%0, %1, %2, 0" : "=&v"(c) : "v"(a), "v"(b)); }
constexpr int W2R = 56;
#define WA(n) "a[%c" #n ":%c" #n "+3]"
template <int R> DI void mfa(f32x4& c, v4u a) { asm(MF "%0, " WA(2) ", %1, %0" : "+v"(c) : "v"(a), "i"(W2R + 4 * R)); }
template <int R> DI void mfa0(f32x4& c, v4u a) { asm(MF "%0, " WA(2) ", %1, 0" : "=&v"(c) : "v"(a), "i"(W2R + 4 * R)); }
#define ACC_FENCE10(c, d) asm volatile("" : "+v"(c[0]), "+v"(c[1]), "+v"(c[2]), "+v"(c[3]), "+v"(c[4]), "+v"(d[0]), "+v"(d[1]), "+v"(d[2]), "+v"(d[3]), "+v"(d[4]))
#define ACC_TIE5(c) asm volatile("" : "+v"(c[0]), "+v"(c[1]), "+v"(c[2]), "+v"(c[3]), "+v"(c[4]))
#define QP " quad_perm:[1,0,3,2] row_mask:0xf bank_mask:0xf"
DI void cstep(float (&t)[5], const float (&h)[5], const float (&lr)[5], const float (&ls)[5], float b0, float b1, float b2, float b3, float b4) {
    asm("v_fma_f32 %0, %10, %5, %20\n\tv_fma_f32 %1, %11, %6, %21\n\tv_fma_f32 %2, %12, %7, %22\n\tv_fma_f32 %3, %13, %8, %23\n\tv_fma_f32 %4, %14, %9, %24\n\t"
        "v_fmac_f32_dpp %0, %5, %15" QP "\n\tv_fmac_f32_dpp %1, %6, %16" QP "\n\tv_fmac_f32_dpp %2, %7, %17" QP "\n\tv_fmac_f32_dpp %3, %8, %18" QP "\n\tv_fmac_f32_dpp %4, %9, %19" QP
        : "=&v"(t[0]), "=&v"(t[1]), "=&v"(t[2]), "=&v"(t[3]), "=&v"(t[4])
        : "v"(h[0]), "v"(h[1]), "v"(h[2]), "v"(h[3]), "v"(h[4]), "v"(lr[0]), "v"(lr[1]), "v"(lr[2]), "v"(lr[3]), "v"(lr[4]),
          "v"(ls[0]), "v"(ls[1]), "v"(ls[2]), "v"(ls[3]), "v"(ls[4]), "v"(b0), "v"(b1), "v"(b2), "v"(b3), "v"(b4));
}
DI void cstep3(float* t, const float* h, const float* lr, const float* ls, const float* b) {
    asm("v_fma_f32 %0, %6, %3, %12\n\tv_fma_f32 %1, %7, %4, %13\n\tv_fma_f32 %2, %8, %5, %14\n\t"
        "v_fmac_f32_dpp %0, %3, %9" QP "\n\tv_fmac_f32_dpp %1, %4, %10" QP "\n\tv_fmac_f32_dpp %2, %5, %11" QP
        : "=&v"(t[0]), "=&v"(t[1]), "=&v"(t[2])
        : "v"(h[0]), "v"(h[1]), "v"(h[2]), "v"(lr[0]), "v"(lr[1]), "v"(lr[2]), "v"(ls[0]), "v"(ls[1]), "v"(ls[2]), "v"(b[0]), "v"(b[1]), "v"(b[2]));
}
template <int o> DI void chain24(float (*t)[3], const float (*b)[5], const float* lr, const float* ls) {
    asm("v_fma_f32 %0, %18, %16, %8\n\t" "v_fma_f32 %1, %19, %17, %9\n\t" "v_fmac_f32_dpp %0, %16, %20" QP "\n\t" "v_fmac_f32_dpp %1, %17, %21" QP "\n\t"
        "v_fma_f32 %2, %18, %0, %10\n\t" "v_fma_f32 %3, %19, %1, %11\n\t" "v_fmac_f32_dpp %2, %0, %20" QP "\n\t" "v_fmac_f32_dpp %3, %1, %21" QP "\n\t"
        "v_fma_f32 %4, %18, %2, %12\n\t" "v_fma_f32 %5, %19, %3, %13\n\t" "v_fmac_f32_dpp %4, %2, %20" QP "\n\t" "v_fmac_f32_dpp %5, %3, %21" QP "\n\t"
        "v_fma_f32 %6, %18, %4, %14\n\t" "v_fma_f32 %7, %19, %5, %15\n\t" "v_fmac_f32_dpp %6, %4, %20" QP "\n\t" "v_fmac_f32_dpp %7, %5, %21" QP "\n\t"
        : "=&v"(t[1][0]), "=&v"(t[1][1]), "=&v"(t[2][0]), "=&v"(t[2][1]), "=&v"(t[3][0]), "=&v"(t[3][1]), "=&v"(t[4][0]), "=&v"(t[4][1])
        : "v"(b[0][o + 0]), "v"(b[0][o + 1]), "v"(b[1][o + 0]), "v"(b[1][o + 1]), "v"(b[2][o + 0]), "v"(b[2][o + 1]), "v"(b[3][o + 0]), "v"(b[3][o + 1]), "v"(t[0][0]), "v"(t[0][1]), "v"(lr[o + 0]), "v"(lr[o + 1]), "v"(ls[o + 0]), "v"(ls[o + 1]));
}
template <int o> DI void chain33(float (*t)[3], const float (*b)[5], const float* lr, const float* ls) {
    asm("v_fma_f32 %0, %21, %18, %9\n\t" "v_fma_f32 %1, %22, %19, %10\n\t" "v_fma_f32 %2, %23, %20, %11\n\t" "v_fmac_f32_dpp %0, %18, %24" QP "\n\t" "v_fmac_f32_dpp %1, %19, %25" QP "\n\t" "v_fmac_f32_dpp %2, %20, %26" QP "\n\t"
        "v_fma_f32 %3, %21, %0, %12\n\t" "v_fma_f32 %4, %22, %1, %13\n\t" "v_fma_f32 %5, %23, %2, %14\n\t" "v_fmac_f32_dpp %3, %0, %24" QP "\n\t" "v_fmac_f32_dpp %4, %1, %25" QP "\n\t" "v_fmac_f32_dpp %5, %2, %26" QP "\n\t"
        "v_fma_f32 %6, %21, %3, %15\n\t" "v_fma_f32 %7, %22, %4, %16\n\t" "v_fma_f32 %8, %23, %5, %17\n\t" "v_fmac_f32_dpp %6, %3, %24" QP "\n\t" "v_fmac_f32_dpp %7, %4, %25" QP "\n\t" "v_fmac_f32_dpp %8, %5, %26" QP "\n\t"
        : "=&v"(t[1][0]), "=&v"(t[1][1]), "=&v"(t[1][2]), "=&v"(t[2][0]), "=&v"(t[2][1]), "=&v"(t[2][2]), "=&v"(t[3][0]), "=&v"(t[3][1]), "=&v"(t[3][2])
        : "v"(b[0][o + 0]), "v"(b[0][o + 1]), "v"(b[0][o + 2]), "v"(b[1][o + 0]), "v"(b[1][o + 1]), "v"(b[1][o + 2]), "v"(b[2][o + 0]), "v"(b[2][o + 1]), "v"(b[2][o + 2]), "v"(t[0][0]), "v"(t[0][1]), "v"(t[0][2]), "v"(lr[o + 0]), "v"(lr[o + 1]), "v"(lr[o + 2]), "v"(ls[o + 0]), "v"(ls[o + 1]), "v"(ls[o + 2]));
}
template <int o> DI void chain32(float (*t)[3], const float (*b)[5], const float* lr, const float* ls) {
    asm("v_fma_f32 %0, %15, %12, %6\n\t" "v_fma_f32 %1, %16, %13, %7\n\t" "v_fma_f32 %2, %17, %14, %8\n\t" "v_fmac_f32_dpp %0, %12, %18" QP "\n\t" "v_fmac_f32_dpp %1, %13, %19" QP "\n\t" "v_fmac_f32_dpp %2, %14, %20" QP "\n\t"
        "v_fma_f32 %3, %15, %0, %9\n\t" "v_fma_f32 %4, %16, %1, %10\n\t" "v_fma_f32 %5, %17, %2, %11\n\t" "v_fmac_f32_dpp %3, %0, %18" QP "\n\t" "v_fmac_f32_dpp %4, %1, %19" QP "\n\t" "v_fmac_f32_dpp %5, %2, %20" QP "\n\t"
        : "=&v"(t[1][0]), "=&v"(t[1][1]), "=&v"(t[1][2]), "=&v"(t[2][0]), "=&v"(t[2][1]), "=&v"(t[2][2])
        : "v"(b[0][o + 0]), "v"(b[0][o + 1]), "v"(b[0][o + 2]), "v"(b[1][o + 0]), "v"(b[1][o + 1]), "v"(b[1][o + 2]), "v"(t[0][0]), "v"(t[0][1]), "v"(t[0][2]), "v"(lr[o + 0]), "v"(lr[o + 1]), "v"(lr[o + 2]), "v"(ls[o + 0]), "v"(ls[o + 1]), "v"(ls[o + 2]));
}
DI void pstep(f32x2& xr, f32x2& xs, f32x2 pr, f32x2 ps, f32x2 m) {
    asm("v_pk_mul_f32 %0, %2, %4 op_sel_hi:[1,0]\n\tv_pk_mul_f32 %1, %3, %4 op_sel_hi:[1,0]\n\t"
        "v_pk_fma_f32 %0, %3, %4, %0 op_sel:[0,1,0] neg_lo:[1,0,0] neg_hi:[1,0,0]\n\tv_pk_fma_f32 %1, %2, %4, %1 op_sel:[0,1,0]"
        : "=&v"(xr), "=&v"(xs) : "v"(pr), "v"(ps), "v"(m));
}
DI void padd(f32x2 (&h)[4], const f32x2 (&pr)[2], const f32x2 (&ps)[2], f32x2 c) {
    f32x2 d;
    asm("v_pk_mul_f32 %4, %6, %9 op_sel:[1,0]\n\tv_pk_fma_f32 %0, %5, %9, %0 op_sel_hi:[1,0,1]\n\tv_pk_fma_f32 %1, %6, %9, %1 op_sel_hi:[1,0,1]\n\t"
        "v_pk_fma_f32 %4, %8, %9, %4 op_sel:[1,1,0] op_sel_hi:[1,0,1] neg_hi:[1,0,0]\n\tv_pk_fma_f32 %0, %7, %9, %0 op_sel:[0,1,0]\n\tv_pk_fma_f32 %1, %8, %9, %1 op_sel:[0,1,0]\n\t"
        "v_pk_fma_f32 %2, %5, %4, %2 op_sel_hi:[1,0,1]\n\tv_pk_fma_f32 %3, %6, %4, %3 op_sel_hi:[1,0,1]\n\tv_pk_fma_f32 %2, %7, %4, %2 op_sel:[0,1,0]\n\tv_pk_fma_f32 %3, %8, %4, %3 op_sel:[0,1,0]"
        : "+v"(h[0]), "+v"(h[1]), "+v"(h[2]), "+v"(h[3]), "=&v"(d) : "v"(pr[0]), "v"(pr[1]), "v"(ps[0]), "v"(ps[1]), "v"(c));
}
DI void cstepi(float& b0, float& b1, float& b2, float& b3, float& b4, float h0, float h1, float h2, float h3, float h4, const float (&lr)[5], const float (&ls)[5]) {
    asm("v_fmac_f32 %0, %10, %5\n\tv_fmac_f32 %1, %11, %6\n\tv_fmac_f32 %2, %12, %7\n\tv_fmac_f32 %3, %13, %8\n\tv_fmac_f32 %4, %14, %9\n\t"
        "v_fmac_f32_dpp %0, %5, %15" QP "\n\tv_fmac_f32_dpp %1, %6, %16" QP "\n\tv_fmac_f32_dpp %2, %7, %17" QP "\n\tv_fmac_f32_dpp %3, %8, %18" QP "\n\tv_fmac_f32_dpp %4, %9, %19" QP
        : "+v"(b0), "+v"(b1), "+v"(b2), "+v"(b3), "+v"(b4)
        : "v"(h0), "v"(h1), "v"(h2), "v"(h3), "v"(h4), "v"(lr[0]), "v"(lr[1]), "v"(lr[2]), "v"(lr[3]), "v"(lr[4]), "v"(ls[0]), "v"(ls[1]), "v"(ls[2]), "v"(ls[3]), "v"(ls[4]));
}
#define UNDEF8(c) asm volatile("" : "=v"(c[0]), "=v"(c[1]), "=v"(c[2]), "=v"(c[3]), "=v"(c[4]), "=v"(c[5]), "=v"(c[6]), "=v"(c[7]))
#define PIN5(t) asm volatile("s_nop 1" : "+v"(t[0]), "+v"(t[1]), "+v"(t[2]), "+v"(t[3]), "+v"(t[4]))
DI unsigned cvtpk(float lo, float hi) {
    f32x2 v = {lo, hi};
    return __builtin_bit_cast(unsigned, __builtin_convertvector(v, bf16x2v));
}
DI float lo16f(unsigned u) { return __uint_as_float(u << 16); }
DI float hi16f(unsigned u) { return __uint_as_float(u & 0xffff0000u); }
DI float rbf(float v) { return lo16f(cvtpk(v, 0.f)); }
template <int CTRL> DI float dpp(float x) {
    return __int_as_float(__builtin_amdgcn_update_dpp(0, __float_as_int(x), CTRL, 0xF, 0xF, true));
}
DI float rowsum16(float x) {
    x += dpp<0x128>(x); x += dpp<0x124>(x); x += dpp<0x122>(x); x += dpp<0x121>(x);
    return x;
}
DI float xsum32(float x) { auto r = __builtin_amdgcn_permlane32_swap(__float_as_uint(x), __float_as_uint(x), false, false); return __uint_as_float(r[0]) + __uint_as_float(r[1]); }
DI void xsum2(float& a, float& d) {
    auto r = __builtin_amdgcn_permlane16_swap(__float_as_uint(a), __float_as_uint(d), false, false);
    const float s = __uint_as_float(r[0]) + __uint_as_float(r[1]);
    const float t = xsum32(s);
    auto u = __builtin_amdgcn_permlane16_swap(__float_as_uint(t), __float_as_uint(t), false, false);
    a = __uint_as_float(u[0]); d = __uint_as_float(u[1]);
}
DI float xsum2h(float a, float d) {
    auto r = __builtin_amdgcn_permlane16_swap(__float_as_uint(a), __float_as_uint(d), false, false);
    return xsum32(__uint_as_float(r[0]) + __uint_as_float(r[1]));
}
DI float hsum4(f32x4 v) { return (v.x + v.y) + (v.z + v.w); }
typedef __amdgpu_buffer_rsrc_t rsrc_t;
constexpr int SC1 = 16;
DI v4u ld128(rsrc_t rs, unsigned off) { return __builtin_amdgcn_raw_buffer_load_b128(rs, off, 0, SC1); }
DI v4u ld128s(rsrc_t rs, unsigned off, unsigned soff) { return __builtin_amdgcn_raw_buffer_load_b128(rs, off, soff, SC1); }
DI v2u ld64(rsrc_t rs, unsigned off) { return __builtin_amdgcn_raw_buffer_load_b64(rs, off, 0, SC1); }
DI void st128(v4u v, rsrc_t rs, unsigned off) { __builtin_amdgcn_raw_buffer_store_b128(v, rs, off, 0, SC1); }
DI void st64(v2u v, rsrc_t rs, unsigned off) { __builtin_amdgcn_raw_buffer_store_b64(v, rs, off, 0, SC1); }
#define LGKM0() { __builtin_amdgcn_s_waitcnt(0xC07F); asm volatile("" ::: "memory"); }
#define DRAIN() asm volatile("s_waitcnt vmcnt(0)" ::: "memory")
#define ORDER() __builtin_amdgcn_fence(__ATOMIC_ACQUIRE, "wavefront")
#define SCHED() __builtin_amdgcn_sched_barrier(0)
constexpr unsigned kSpinCap = 1u << 17;
#define WAVE_WAIT(...)                                                      \
    {                                                                       \
        unsigned spins_ = dead ? kSpinCap : 0u;                             \
        _Pragma("nounroll") for (;;) {                                      \
            asm volatile("" ::: "memory");                                  \
            bool ok;                                                        \
            __VA_ARGS__;                                                    \
            if (__builtin_amdgcn_ballot_w64(!ok) == 0ull) break;            \
            if (++spins_ > kSpinCap) { dead = true; break; }                \
            __builtin_amdgcn_s_sleep(1);                                    \
        }                                                                   \
        ORDER();                                                            \
    }
#define KPD1 2
#define KPDS 6
#define KPDS0 0
#define KPF0 14
#define KPFX 6
#define KPF1 4
constexpr int PD1 = KPD1, PDS = KPDS, PDS0 = KPDS0;
#define PL(r) "global_load_dwordx2 " r ", %5, %6 sc1\n\t"
#define PS_(r) "s_waitcnt vmcnt(1)\n\tv_cmp_ne_u64 vcc, %7, " r "\n\ts_cbranch_vccz 2f\n\t" PL(r)
template <int D0> DI void pipe_flag(v2u (&t)[4], unsigned off, const void* base, u64 tag, bool& dead) {
    unsigned n = dead ? 0u : kSpinCap;
    asm volatile("s_sleep %8\n\t" PL("%0") "s_sleep %9\n\t" PL("%1")
                 "1:\n\t" PS_("%0") PS_("%1") "s_sub_u32 %4, %4, 1\n\ts_cbranch_scc0 1b\n2:"
                 : "=&v"(t[0]), "=&v"(t[1]), "=&v"(t[2]), "=&v"(t[3]), "+s"(n) : "v"(off), "s"(base), "s"(tag), "i"(D0), "i"(KPF1) : "memory", "vcc", "scc");
    dead = __builtin_amdgcn_readfirstlane(n) == ~0u;
}
#define WAVE_RETRY(OK, ...) { if (__builtin_expect(__builtin_amdgcn_ballot_w64(!(OK)) != 0ull, 0)) WAVE_WAIT({ __VA_ARGS__; ok = OK; }) ORDER(); }
#define STAMP(i)
#define STAMP_DUMP()

#define PADS 9000, 9001, 9002, 9003, 9004, 9005, 9006, 9007, 9008
#define T1POS 27
#define TW 2
#define KLB0 0
#define KLB1 12
#define SKIP0 0
#define SKIP7 0
#define SKIP5 0
#define SKIP4 0
constexpr int kPads[] = {PADS};
constexpr int pad_next(int id) {
    for (unsigned i = 0; i + 1 < sizeof(kPads) / sizeof(int); ++i) if (kPads[i] == id) return kPads[i + 1];
    return -1;
}
template <int ID> DI void site() {
    if constexpr (pad_next(ID) >= 0) asm volatile("kp%c0:\n\ts_cbranch_execz kp%c1" ::"i"(ID), "i"(pad_next(ID)));
    else asm volatile("");
}
template <int T> DI void tour() {
    if constexpr (pad_next(9000 + T) >= 0 && pad_next(9000 + T) != 9001 + T) asm volatile("s_mov_b64 exec, 0\n\ts_branch kp%c0\nkp%c1:\n\ts_mov_b64 exec, -1" ::"i"(pad_next(9000 + T)), "i"(9001 + T));
}
template <int N> DI void skip() { if constexpr (N > 0) asm volatile("s_branch 1f\n\t.rept %c0\n\ts_nop 0\n\t.endr\n1:" ::"i"(N)); }
template <class F, int... I> DI void static_for_impl(std::integer_sequence<int, I...>, F&& f) { (f(std::integral_constant<int, I>{}), ...); }
template <int N, class F> DI void static_for(F&& f) { static_for_impl(std::make_integer_sequence<int, N>{}, f); }
#define IC(n) std::integral_constant<int, (n)>{}
#define LAMBDA(v) [&](auto v) __attribute__((always_inline))
#define PK2 " op_sel:[1,1,0] op_sel_hi:[1,0,1] neg_lo:[1,0,0]"
DI f32x2 cmul(f32x2 a, f32x2 b) {
    f32x2 t;
    asm("v_pk_mul_f32 %0, %1, %2 op_sel_hi:[0,1]\n\ts_nop 0\n\tv_pk_fma_f32 %0, %1, %2, %0" PK2 "\n\ts_nop 0" : "=&v"(t) : "v"(a), "v"(b));
    return t;
}
DI f32x2 cfma(f32x2 a, f32x2 b, f32x2 c) {
    f32x2 t;
    asm("v_pk_fma_f32 %0, %1, %2, %3 op_sel_hi:[0,1,1]\n\ts_nop 0\n\tv_pk_fma_f32 %0, %1, %2, %0" PK2 "\n\ts_nop 0" : "=&v"(t) : "v"(a), "v"(b), "v"(c));
    return t;
}
DI void cfma4(f32x2 (&t)[4], f32x2 a, f32x2 b0, f32x2 b1, f32x2 b2, f32x2 b3, f32x2 c0, f32x2 c1, f32x2 c2, f32x2 c3) {
    asm("v_pk_fma_f32 %0, %4, %5, %9 op_sel_hi:[0,1,1]\n\tv_pk_fma_f32 %1, %4, %6, %10 op_sel_hi:[0,1,1]\n\tv_pk_fma_f32 %2, %4, %7, %11 op_sel_hi:[0,1,1]\n\tv_pk_fma_f32 %3, %4, %8, %12 op_sel_hi:[0,1,1]\n\t"
        "v_pk_fma_f32 %0, %4, %5, %0" PK2 "\n\tv_pk_fma_f32 %1, %4, %6, %1" PK2 "\n\tv_pk_fma_f32 %2, %4, %7, %2" PK2 "\n\tv_pk_fma_f32 %3, %4, %8, %3" PK2 "\n\ts_nop 0"
        : "=&v"(t[0]), "=&v"(t[1]), "=&v"(t[2]), "=&v"(t[3]) : "v"(a), "v"(b0), "v"(b1), "v"(b2), "v"(b3), "v"(c0), "v"(c1), "v"(c2), "v"(c3));
}
DI void gacc2(f32x2& h0, f32x2& h1, f32x2 w0, f32x2 w1, v4u e0, v4u e1) {
    const v2u a0 = {e0.x, e0.y}, b0 = {e0.z, e0.w}, a1 = {e1.x, e1.y}, b1 = {e1.z, e1.w};
    asm("v_pk_fma_f32 %0, %4, %2, %0 op_sel_hi:[0,1,1]\n\tv_pk_fma_f32 %1, %6, %3, %1 op_sel_hi:[0,1,1]\n\t"
        "v_pk_fma_f32 %0, %5, %2, %0 op_sel:[0,1,0] op_sel_hi:[0,0,1] neg_lo:[1,0,0]\n\tv_pk_fma_f32 %1, %7, %3, %1 op_sel:[0,1,0] op_sel_hi:[0,0,1] neg_lo:[1,0,0]"
        : "+v"(h0), "+v"(h1) : "v"(w0), "v"(w1), "v"(a0), "v"(b0), "v"(a1), "v"(b1));
}
DI s16x4 trread(char* smem, int byteoff) {
    return __builtin_amdgcn_ds_read_tr16_b64_v4i16((LAS s16x4*)(smem + byteoff));
}

#define CV_LOAD_A(A, a, rr_, hf) { const unsigned* ap_ = (const unsigned*)(smem + ab[(a) >> 1] + (((a) & 1) ? 0 : 512) + 64 * (7 - (rr_)) + 8 * (hf)); A[a][2 * (hf)] = ap_[0]; A[a][2 * (hf) + 1] = ap_[1]; }
#define CV_LOAD_B(B, e, rr_) { B[e] = *(const v4u*)(smem + bb + 64 * (7 - (rr_)) + 512 * (e)); }
constexpr int cv_pair(int lo, int hi, int n) {
    for (int a = 0; a < 8; ++a)
        for (int e = 0; e < 8; ++e)
            if (a + e >= lo && a + e < hi && n-- == 0) return 8 * a + e;
    return -1;
}
template <int R, int PF, bool OWN, class F, class G>
DI void conv_round(f32x4 (&ca)[8], v4u (&A)[8], const v4u (&B)[8], v4u (&An)[8], v4u (&Bn)[8], const char* smem, const int (&ab)[4], int bb, F&& fill, G&& last) {
    static_for<(R == 7 ? KG0 : 36)>(LAMBDA(g_) {
        constexpr int g = g_.value, a = cv_pair(0, 8, g) / 8, e = cv_pair(0, 8, g) % 8;
        mf(ca[a + e], A[a], B[e]);
        SCHED();
        if constexpr (OWN && g < 12) { CV_LOAD_A(A, 2 + g / 2, R, g & 1) SCHED(); }
        if constexpr (g < 4 * PF && (g & 3) != 3) { if constexpr ((g & 3) == 2) CV_LOAD_B(Bn, g >> 2, R + 1) else CV_LOAD_A(An, g >> 2, R + 1, g & 1) SCHED(); }
        site<3000 + 36 * R + g>();
        if constexpr (R == 7) { last(g_); SCHED(); }
        if constexpr ((g & 3) == 3 && g < 28) { fill(std::integral_constant<int, 7 * R + g / 4>{}); SCHED(); }
    });
}
template <int R0, int R1, class F, class G, class M>
DI void conv_part(f32x4 (&ca)[8], const char* smem, const int (&ab)[4], int bb, F&& fill, G&& last, M&& mid, v4u (&A)[2][8], v4u (&B)[2][8]) {
    if constexpr (R0 > 0) { CV_LOAD_A(A[0], 0, R0, 0) CV_LOAD_A(A[0], 0, R0, 1) SCHED(); }
    static_for<8>(LAMBDA(a_) { constexpr int a = a_.value; CV_LOAD_B(B[0], a, R0) SCHED(); });
    if constexpr (R0 > 0) { CV_LOAD_A(A[0], 1, R0, 0) CV_LOAD_A(A[0], 1, R0, 1) SCHED(); }
    else {
        CV_LOAD_A(A[0], 0, 0, 0) CV_LOAD_A(A[0], 0, 0, 1) SCHED();
        const v4u ad = mid();
        SCHED();
        static_for<8>(LAMBDA(e_) {
            constexpr int e = e_.value;
            mf0(ca[e], ad, B[0][e]); SCHED();
            if constexpr (e < 7) { CV_LOAD_A(A[0], e + 1, 0, 0) CV_LOAD_A(A[0], e + 1, 0, 1) SCHED(); }
        });
    }
    static_for<R1 - R0>(LAMBDA(r_) {
        constexpr int r = r_.value;
        conv_round<R0 + r, (R0 + r + 1 < R1 ? 8 : R0 == 0 && H3 ? 3 : 0), (r == 0 && R0 > 0)>(ca, A[r & 1], B[r & 1], A[(r & 1) ^ 1], B[(r & 1) ^ 1], smem, ab, bb, fill, last);
    });
    if constexpr (R0 == 0 && H3) static_for<6>(LAMBDA(g_) { constexpr int a = cv_pair(0, 3, g_.value) / 8, e = cv_pair(0, 3, g_.value) % 8; mf(ca[a + e], A[R1 & 1][a], B[R1 & 1][e]); SCHED(); });
}
DI void lds_wait3(int a) {
    unsigned x, y, z;
    asm volatile("1:\n\tds_read_b32 %0, %3\n\tds_read_b32 %1, %3 offset:4\n\tds_read_b32 %2, %3 offset:8\n\ts_waitcnt lgkmcnt(0)\n\t"
                 "v_and_b32 %0, %0, %1\n\tv_and_b32 %0, %0, %2\n\tv_cmp_eq_u32 vcc, 0, %0\n\ts_cbranch_vccnz 1b" : "=&v"(x), "=&v"(y), "=&v"(z) : "v"(a) : "memory", "vcc");
}
DI void add2(float& a, float& b, float x, float y) { asm volatile("v_add_f32 %0, %0, %2\n\tv_add_f32 %1, %1, %3" : "+v"(a), "+v"(b) : "v"(x), "v"(y)); }
template <int IMM> DI void gld(v4u& dst, unsigned voff, const void* sbase) {
    asm volatile("global_load_dwordx4 %0, %1, %2 offset:%3" : "=v"(dst) : "v"(voff), "s"(sbase), "i"(IMM));
}
#define OOB 3
#define OUTP 16
#define NTM 31
template <int IMM, int BIT> DI void gldn(v4u& dst, unsigned voff, const void* sbase) {
    if constexpr ((NTM >> BIT) & 1) asm volatile("global_load_dwordx4 %0, %1, %2 offset:%3 nt" : "=v"(dst) : "v"(voff), "s"(sbase), "i"(IMM));
    else gld<IMM>(dst, voff, sbase);
}
DI void gldu16(unsigned& dst, unsigned voff, const void* sbase) { asm volatile("global_load_ushort %0, %1, %2" : "=v"(dst) : "v"(voff), "s"(sbase)); }
template <int IMM> DI void gld64(v4u& dst, const void* a) {
    if constexpr ((NTM >> 5) & 1) asm volatile("global_load_dwordx4 %0, %1, off offset:%2 nt" : "=v"(dst) : "v"(a), "i"(IMM));
    else asm volatile("global_load_dwordx4 %0, %1, off offset:%2" : "=v"(dst) : "v"(a), "i"(IMM));
}
template <int V> DI void SETM0(unsigned ldsbase) { asm volatile("s_add_u32 m0, %0, %1" ::"s"(ldsbase), "i"(V)); }
template <int GOFF> DI void GLD0(unsigned voff, const char* sbase) { asm volatile("global_load_lds_dwordx4 %0, %1 offset:%2" ::"v"(voff), "s"(sbase), "i"(GOFF) : "memory"); }
template <int GOFF, int V> DI void GLD1(unsigned voff, const char* sbase, unsigned ldsbase) {
    asm volatile("global_load_lds_dwordx4 %0, %1 offset:%3\n\ts_add_u32 m0, %2, %4" ::"v"(voff), "s"(sbase), "s"(ldsbase), "i"(GOFF), "i"(V) : "memory");
}
constexpr int dma_goff(int it) { return it < 40 ? (it >> 2) * 128 : ((it - 100) / 5) * 256; }
constexpr int dma_loff(int it) { return it < 40 ? L_XR + ((it >> 2) % 3) * 4096 + (it & 3) * 1024 : ((it - 100) % 5) * 20480 + ((it - 100) / 5) * 2048; }
#define KVF 1
#define KFD0 10
#define DSE 1
#define KNF 13
#define KFS 6
#define APOS 0
#define PSA 0
#define CWP 1
#define SAF 2
#define KD0 36
#define KDS 3
#define KNW 11
constexpr int kD0 = KD0, kDS = KDS;
static_assert(kD0 + 26 * kDS < 117, "");
#define KW2S 17
#define KW2M 0x4A
#define KFWP 14
constexpr int kW2S = KW2S, kW2M = KW2M;
constexpr int NW2P = (20 - kW2S) * __builtin_popcount(kW2M);
constexpr int P1_PRE = 34;
constexpr int p1_pre(int i) { return i < 5 ? 100 + i : i < 13 ? i - 5 : i < 18 ? 105 + (i - 13) : i < 22 ? 8 + (i - 18) : i < 27 ? 110 + (i - 22) : 400; }
constexpr int p1_item(int s, int g) {
    if (s == 7 && g == 0) return 324;
    if (g == 9) return s < 10 ? 115 + s : s == 11 ? 202 : s == 12 ? 203 : -1;
    if (s == KFWP && g == 1) return 208;
    if (s >= kW2S && ((kW2M >> g) & 1)) return 500 + (s - kW2S) * __builtin_popcount(kW2M) + __builtin_popcount(kW2M & ((1 << g) - 1));
    if ((s & 1) == 0 && s <= 12) return g >= 5 ? 4 * (s / 2 + 3) + (g - 5) : -1;
    if (s == 14 && g == 5) return 207;
    return -1;
}
constexpr int p1_cnt(int s, int g) {
    int n = P1_PRE;
    for (int t = 0; t <= s; ++t)
        for (int u = 0; u < 10; ++u) {
            if (t == s && u >= g) break;
            if (p1_item(t, u) >= 0 && p1_item(t, u) != 319) ++n;
        }
    return n;
}
constexpr int p1_pos(int code) {
    for (int i = 0; i < P1_PRE; ++i) if (p1_pre(i) == code) return i;
    for (int t = 0; t < 20; ++t)
        for (int u = 0; u < 10; ++u) if (p1_item(t, u) == code) return p1_cnt(t, u);
    return -1000;
}
constexpr int p1_cap(int s, int g) {
    int u = (s < 19 && g < 5) + (g == 9 && s < 19);
    if ((s & 1) && s < 19 && g >= 5 && g < 9) u += 1 + (g == 5);
    if ((s & 1) == 0 && g == 4) u += 1;
    const int it = p1_item(s, g), nt = g < 9 ? p1_item(s, g + 1) : -1;
    u += (it >= 0 && it < 500 && it != 208) + (nt >= 0 && nt < 200);
    if ((s == 18 && g == 1) || (s == 19 && g == 2) || (g == 9 && ((s & 3) == 2 || s == 17))) u += 2;
    return u < 2 ? 2 - u : 0;
}
constexpr int p1_slot(int s0, int s, int g, int gmax) {
    int n = 0;
    for (int t = s0; t <= s; ++t)
        for (int u = 0; u < gmax && !(t == s && u >= g); ++u) n += p1_cap(t, u);
    return n;
}
constexpr int p1_lim(int n) { return n > 63 ? 63 : n; }
template <int IMM> DI void gld4s(v4u& dst, unsigned voff, const void* sbase) { asm volatile("global_load_dwordx4 %0, %1, %2 offset:%3 sc1" : "=v"(dst) : "v"(voff), "s"(sbase), "i"(IMM)); }
template <int IMM> DI void gld2s(v2u& dst, unsigned voff, const void* sbase) { asm volatile("global_load_dwordx2 %0, %1, %2 offset:%3 sc1" : "=v"(dst) : "v"(voff), "s"(sbase), "i"(IMM)); }
template <class T> DI void pin(T& v) { asm volatile("" : "+v"(v)); }
DI void mul2(float& a, float& b, float x, float y, float s) { asm volatile("v_mul_f32 %0, %2, %4\n\tv_mul_f32 %1, %3, %4" : "=&v"(a), "=&v"(b) : "v"(x), "v"(y), "v"(s)); }
DI void xor2(float& a, float& b, unsigned x, unsigned y, unsigned s) { asm volatile("v_xor_b32 %0, %2, %4\n\tv_xor_b32 %1, %3, %4" : "=&v"(a), "=&v"(b) : "v"(x), "v"(y), "v"(s)); }
DI void rot2(unsigned& a, unsigned& b, unsigned x, unsigned y) { asm volatile("v_alignbit_b32 %0, %2, %2, 16\n\tv_alignbit_b32 %1, %3, %3, 16" : "=&v"(a), "=&v"(b) : "v"(x), "v"(y)); }
DI void mix2(unsigned& a, unsigned& b, unsigned x, unsigned y, unsigned z) { asm volatile("v_bfi_b32 %0, %5, %2, %3\n\tv_bfi_b32 %1, %5, %3, %4" : "=&v"(a), "=&v"(b) : "v"(x), "v"(y), "v"(z), "s"(0xffffu)); }
template <int I> DI void shr1(unsigned& o, unsigned prev, unsigned x) {
    if constexpr (I > 0) asm volatile("v_mov_b32_dpp %0, %1 wave_ror:1 row_mask:0xf bank_mask:0xf\n\tv_mov_b32_dpp %0, %2 wave_shr:1 row_mask:0xf bank_mask:0xf" : "=&v"(o) : "v"(prev), "v"(x));
    else asm volatile("v_mov_b32_dpp %0, %1 wave_shr:1 row_mask:0xf bank_mask:0xf bound_ctrl:0" : "=&v"(o) : "v"(x));
}
template <int G> DI void bc2(float& a, float& b, unsigned x) {
    asm volatile("v_mov_b32_dpp %0, %2 row_newbcast:%3 row_mask:0xf bank_mask:0xf\n\tv_mov_b32_dpp %1, %2 row_newbcast:%4 row_mask:0xf bank_mask:0xf" : "=&v"(a), "=&v"(b) : "v"(x), "i"(G), "i"(G + 8));
}
DI void rot1(unsigned& a, unsigned x) { asm volatile("v_alignbit_b32 %0, %1, %1, 16" : "=v"(a) : "v"(x)); }
DI void mix1(unsigned& a, unsigned x, unsigned y) { asm volatile("v_bfi_b32 %0, %3, %1, %2" : "=v"(a) : "v"(x), "v"(y), "s"(0xffffu)); }
DI void ror1(unsigned& o, unsigned prev) { asm volatile("v_mov_b32_dpp %0, %1 wave_ror:1 row_mask:0xf bank_mask:0xf" : "=v"(o) : "v"(prev)); }
DI void shr1b(unsigned& o, unsigned x) { asm volatile("v_mov_b32_dpp %0, %1 wave_shr:1 row_mask:0xf bank_mask:0xf" : "+v"(o) : "v"(x)); }
DI void xor1(float& a, unsigned x, unsigned s) { asm volatile("v_xor_b32 %0, %1, %2" : "=v"(a) : "v"(x), "v"(s)); }
template <int G> DI void bc1(float& a, unsigned x) { asm volatile("v_mov_b32_dpp %0, %1 row_newbcast:%2 row_mask:0xf bank_mask:0xf" : "=v"(a) : "v"(x), "i"(G)); }
template <int G> DI void mulb(float& o, unsigned x, float y) { asm volatile("v_mul_f32_dpp %0, %1, %2 row_newbcast:%3 row_mask:0xf bank_mask:0xf" : "=v"(o) : "v"(x), "v"(y), "i"(G)); }
template <int N> DI void lgkm() { __builtin_amdgcn_s_waitcnt(0xC07F | (N << 8)); asm volatile("" ::: "memory"); }
DI void unp2(float& a, float& b, unsigned x) { asm volatile("v_lshlrev_b32 %0, 16, %2\n\tv_and_b32 %1, 0xffff0000, %2" : "=&v"(a), "=&v"(b) : "v"(x)); }
#define GL(a) "global_load_dwordx4 a[" #a ":" #a "+3], %7, %8 sc1\n\tglobal_load_dwordx4 a[" #a "+4:" #a "+7], %7, %8 offset:256 sc1\n\t"
#define AR(d, a, o) "v_accvgpr_read_b32 " d ", a[" #a "+" #o "]\n\t"
#define GS_(a) "s_waitcnt vmcnt(4)\n\t" AR("%2", a, 1) AR("%3", a, 3) AR("%4", a, 5) AR("%5", a, 7) AR("%0", a, 0) AR("%1", a, 2)                              \
    "v_xor_b32 %2, %9, %2\n\tv_bitop3_b32 %4, %4, %9, %2 bitop3:0xbe\n\tv_bitop3_b32 %4, %3, %9, %4 bitop3:0xbe\n\tv_bitop3_b32 %4, %5, %9, %4 bitop3:0xbe\n\t" \
    AR("%2", a, 4) AR("%3", a, 6) "v_cmp_ne_u32 vcc, 0, %4\n\ts_cbranch_vccz 2f\n\t" GL(a)
template <int D0> DI void pipe_gran(float& a, float& d, unsigned off, const void* base, unsigned tag, bool& dead) {
    unsigned n = dead ? 0u : kSpinCap, y, z;
    float a1, d1;
    asm volatile("s_sleep %10\n\t" GL(32) "s_sleep %11\n\t" GL(40) "s_sleep %11\n\t" GL(48)
                 "1:\n\t" GS_(32) GS_(40) GS_(48) "s_sub_u32 %6, %6, 1\n\ts_cbranch_scc0 1b\n2:"
                 : "=&v"(a), "=&v"(d), "=&v"(a1), "=&v"(d1), "=&v"(y), "=&v"(z), "+s"(n) : "v"(off), "s"(base), "s"(tag), "i"(D0), "i"(PD1) : "memory", "vcc", "scc");
    dead = __builtin_amdgcn_readfirstlane(n) == ~0u;
    a += a1; d += d1;
}
DI void fma2(float& a, float& b, float x, float y, float s, float t, float u, float v) {
    asm volatile("v_fma_f32 %0, %2, %4, %6\n\tv_fma_f32 %1, %3, %5, %7" : "=&v"(a), "=&v"(b) : "v"(x), "v"(y), "v"(s), "v"(t), "v"(u), "v"(v));
}
DI void cvt2(unsigned& a, unsigned& b, f32x4 v) { asm volatile("v_cvt_pk_bf16_f32 %0, %2, %3\n\tv_cvt_pk_bf16_f32 %1, %4, %5" : "=&v"(a), "=&v"(b) : "v"(v.x), "v"(v.y), "v"(v.z), "v"(v.w)); }
DI void swp2n(unsigned& a, unsigned& b, unsigned& c, unsigned& d) { asm volatile("v_permlane16_swap_b32 %0, %1\n\tv_permlane16_swap_b32 %2, %3" : "+v"(a), "+v"(b), "+v"(c), "+v"(d)); }
DI void swp2(unsigned& a, unsigned& b, unsigned& c, unsigned& d) { asm volatile("s_nop 1\n\tv_permlane16_swap_b32 %0, %1\n\tv_permlane16_swap_b32 %2, %3" : "+v"(a), "+v"(b), "+v"(c), "+v"(d)); }
template <int G> DI void bc2b(float& a, float& b, float x, float y) {
    asm volatile("v_mov_b32_dpp %0, %2 row_newbcast:%4 row_mask:0xf bank_mask:0xf\n\tv_mov_b32_dpp %1, %3 row_newbcast:%4 row_mask:0xf bank_mask:0xf" : "=&v"(a), "=&v"(b) : "v"(x), "v"(y), "i"(G));
}
template <class T> DI void pins(T& v) { asm volatile("" : "+s"(v)); }
template <int I> DI void gld_w2(unsigned voff, const void* sbase) {
    asm volatile("global_load_dwordx4 " WA(2) ", %0, %1 offset:%3 sc1" ::"v"(voff), "s"(sbase), "i"(W2R + 4 * I), "i"((I % 5) * 1024 - 2048));
}
DI void gld2t(v2u& dst, unsigned voff, const void* sbase) { asm volatile("global_load_dwordx2 %0, %1, %2 sc1" : "+v"(dst) : "v"(voff), "s"(sbase)); }
DI void wait2(v2u& v, v4u (&x)[3]) { asm volatile("s_waitcnt vmcnt(0)" : "+v"(v), "+v"(x[0]), "+v"(x[1]), "+v"(x[2])::"memory"); }
template <int IMM> DI void gld4p(v4u& dst, unsigned voff, const void* sbase) { asm volatile("global_load_dwordx4 %0, %1, %2 offset:%3" : "+v"(dst) : "v"(voff), "s"(sbase), "i"(IMM)); }
DI void undef4(v4u (&x)[4]) { asm volatile("" : "=v"(x[0]), "=v"(x[1]), "=v"(x[2]), "=v"(x[3])); }
DI void wait1(v2u& v) { asm volatile("s_waitcnt vmcnt(0)" : "+v"(v)::"memory"); }
template <int N> DI void wait_fkn(v2u& v, v2u& u, unsigned& x) { asm volatile("s_waitcnt vmcnt(%3)" : "+v"(v), "+v"(u), "+v"(x) : "i"(N) : "memory"); }
DI void wait_fk(v2u& v, v2u& u, unsigned& x) { asm volatile("s_waitcnt vmcnt(0)" : "+v"(v), "+v"(u), "+v"(x)::"memory"); }
DI void ld_ur(unsigned o, const void* base) {
    asm volatile("global_load_dwordx4 a[40:40+3], %0, %1 sc1\n\tglobal_load_dwordx4 a[44:44+3], %0, %1 offset:1024 sc1\n\t"
                 "global_load_dwordx4 a[48:48+3], %0, %1 offset:2048 sc1\n\tglobal_load_dwordx4 a[52:52+3], %0, %1 offset:3072 sc1" ::"v"(o), "s"(base));
}
DI void st_ur(unsigned a) {
    asm volatile("s_waitcnt vmcnt(%1)\n\tds_write_b128 %0, a[40:40+3] offset:480\n\tds_write_b128 %0, a[44:44+3] offset:1504\n\t"
                 "ds_write_b128 %0, a[48:48+3] offset:2528\n\tds_write_b128 %0, a[52:52+3] offset:3552" ::"v"(a), "i"(2 * __builtin_popcount(kW2M)) : "memory");
}
DI void wait4(v4u (&x)[4]) { asm volatile("s_waitcnt vmcnt(0)" : "+v"(x[0]), "+v"(x[1]), "+v"(x[2]), "+v"(x[3])::"memory"); }
template <int A, int IMM> DI void lda4(unsigned o, const void* base) { asm volatile("global_load_dwordx4 a[%c2:%c2+3], %0, %1 offset:%3" ::"v"(o), "s"(base), "i"(A), "i"(IMM)); }
template <int A, int IMM> DI void lda2(unsigned o, const void* base) { asm volatile("global_load_dwordx2 a[%c2:%c2+1], %0, %1 offset:%3" ::"v"(o), "s"(base), "i"(A), "i"(IMM)); }
template <int A> DI void rda(unsigned& v) { asm volatile("v_accvgpr_read_b32 %0, a[%c1+0]" : "=v"(v) : "i"(A)); }
DI void poll1(unsigned a, const void* base) { asm volatile("global_load_dwordx2 a[36:36+1], %0, %1 sc1" ::"v"(a), "s"(base)); }
DI void poll2(unsigned a, unsigned b, const void* base) { asm volatile("global_load_dwordx2 a[36:36+1], %0, %2 sc1\n\tglobal_load_dwordx2 a[36+2:36+3], %1, %2 sc1" ::"v"(a), "v"(b), "s"(base)); }
DI void touch_(unsigned a, const void* base) { asm volatile("global_load_dword a[32+1], %0, %1" ::"v"(a), "s"(base)); }
template <int N> DI void wait_st(v2u& a, v4u& b) { asm volatile("s_waitcnt vmcnt(%2)" : "+v"(a), "+v"(b) : "i"(N) : "memory"); }
DI void undef3(v4u (&x)[3]) { asm volatile("" : "=v"(x[0]), "=v"(x[1]), "=v"(x[2])); }
template <int IMM> DI void gld4t(v4u& dst, unsigned voff, const void* sbase) { asm volatile("global_load_dwordx4 %0, %1, %2 offset:%3 sc1" : "+v"(dst) : "v"(voff), "s"(sbase), "i"(IMM)); }
template <int N> DI void vm_wait() { asm volatile("s_waitcnt vmcnt(%0)" ::"i"(N) : "memory"); }
DI float dot2a(unsigned a, unsigned b, float c) {
    return __builtin_amdgcn_fdot2_f32_bf16(__builtin_bit_cast(bf16x2v, a), __builtin_bit_cast(bf16x2v, b), c, false);
}
DI void disc(unsigned l0, unsigned l1, unsigned l2, f32x2& lam, f32x2& co) {
    const float dt = rbf(__builtin_amdgcn_exp2f(lo16f(l2) * 1.44269504088896f));
    const float lre = lo16f(l0), lim = lo16f(l1);
    const float e_ = __builtin_amdgcn_exp2f(dt * lre * 1.44269504088896f);
    const float rv = __builtin_amdgcn_fractf(dt * lim * 0.159154943091895f);
    lam = f32x2{e_ * __builtin_amdgcn_cosf(rv), e_ * __builtin_amdgcn_sinf(rv)};
    const float nr = lam.x - 1.0f, ni = lam.y, id = __builtin_amdgcn_rcpf(lre * lre + lim * lim);
    co = f32x2{(nr * lre + ni * lim) * id, (ni * lre - nr * lim) * id};
}
DI f32x4 unpk4(v2u u) { return f32x4{lo16f(u.x), hi16f(u.x), lo16f(u.y), hi16f(u.y)}; }
#define D2 "v_dot2_f32_bf16 "
#define CP "v_cvt_pk_bf16_f32 "
DI void d2x4(unsigned* o, unsigned a0, unsigned a1, unsigned a2, unsigned a3, unsigned b0, unsigned b1, unsigned b2, unsigned b3) {
    asm volatile(D2 "%0, %4, %8, 0\n\t" D2 "%1, %5, %9, 0\n\t" D2 "%2, %6, %10, 0\n\t" D2 "%3, %7, %11, 0"
                 : "=&v"(o[0]), "=&v"(o[1]), "=&v"(o[2]), "=&v"(o[3]) : "v"(a0), "v"(a1), "v"(a2), "v"(a3), "v"(b0), "v"(b1), "v"(b2), "v"(b3));
}
DI void cpx4(unsigned* o, unsigned a0, unsigned a1, unsigned a2, unsigned a3, unsigned b0, unsigned b1, unsigned b2, unsigned b3) {
    asm volatile(CP "%0, %4, %8\n\t" CP "%1, %5, %9\n\t" CP "%2, %6, %10\n\t" CP "%3, %7, %11"
                 : "=&v"(o[0]), "=&v"(o[1]), "=&v"(o[2]), "=&v"(o[3]) : "v"(a0), "v"(a1), "v"(a2), "v"(a3), "v"(b0), "v"(b1), "v"(b2), "v"(b3));
}
DI void cpx4i(unsigned* t, const unsigned* ka) {
    asm volatile(CP "%0, %0, %4\n\t" CP "%1, %1, %5\n\t" CP "%2, %2, %6\n\t" CP "%3, %3, %7"
                 : "+v"(t[0]), "+v"(t[1]), "+v"(t[2]), "+v"(t[3]) : "v"(ka[0]), "v"(ka[1]), "v"(ka[2]), "v"(ka[3]));
}
template <int IMM> DI void bld4(v4u& dst, unsigned voff, v4u rsrc) {
    if constexpr ((NTM >> 4) & 1) asm volatile("buffer_load_dwordx4 %0, %1, %2, 0 offen offset:%3 nt" : "=v"(dst) : "v"(voff), "s"(rsrc), "i"(IMM));
    else asm volatile("buffer_load_dwordx4 %0, %1, %2, 0 offen offset:%3" : "=v"(dst) : "v"(voff), "s"(rsrc), "i"(IMM));
}

__global__ __launch_bounds__(256, 1) __attribute__((aligned(4096))) void k_main(Params p) {
    __shared__ __attribute__((aligned(16))) char smem[LDS_BYTES];
    const rsrc_t rs = __builtin_amdgcn_make_buffer_rsrc((void*)p.ws, 0, WS_BYTES, 0x00020000);
    const rsrc_t ro = __builtin_amdgcn_make_buffer_rsrc((void*)p.out, 0, S_ * D_ * 4, 0x00020000);
    const int w0 = __builtin_amdgcn_readfirstlane(threadIdx.x >> 6), b0 = blockIdx.x;
    bool dead = false;
    asm volatile("" ::: "a255");
    float SW[5];
    f32x4 xin[5];
    v2u gt[5], dsk[5];
    f32x4 acc[2][5];
    f32x4 rr[2];
    float tt[2][5][4];
    float lr[5], ls[5], lqr[5], lqs[5];
    f32x2 l8, l32, l128;
    bool kearly;
    unsigned sdq;
    int sgn_, ab[4], bb, zpa, uwa, spa, tbw, tbr, cwa, spl, dst[3];
    unsigned sgo, sgs, glo, gso, fgo, fgo1;
    int srca;
    unsigned adm, dso, w2o, wro, fwo, xso, fho1, fho, hso, eo, es, ho[4];
    bool cv0, three;
    int mt1_;
    auto addrs = [&](auto layer_, int lane, int w, int b) __attribute__((always_inline)) {
        constexpr int layer = layer_.value;
        const int c = lane & 15, q = lane >> 4;
        TILES()
        static_assert(l_sg(0, 0) == SC + 0xA900 && l_sg(0, 1) == SC + 0xB400 && l_sg(0, 2) == SC + 0xCE00 && l_sg(0, 3) == SC + 0xDC00, "");
        sgn_ = (layer ? l_sg(1, 0) + w * INBOX : SC + (int)((0xDC00CE00B400A900ull >> (16 * w)) & 0xffff)) + q * 320 + c * 4;
        const int pl = lane < 40 ? lane : 39, tbb = l_tb(layer, 0) + w * (layer ? INBOX : 5120);
        spl = SC + pl * 8;
        eo = OFF_EG + (unsigned)(layer * 163840 + nt1 * 640 + pl * 16);
        es = eo + (unsigned)(mt1 * 10240);
        spa = l_rs(layer, 0) + (2 * (mt2 & 1) + (w >> 1)) * (layer ? INBOX : 256) + (2 * (w & 1) + (c >> 3)) * 64 + (c & 7) * 4;
        tbw = tbb + c * 64 + ((q ^ ((c >> 1) & 3)) * 16);
        tbr = tbb + (2 * (c >> 2) + (q & 1)) * 64 + (((2 * (q >> 1) + ((c >> 1) & 1)) ^ (c >> 2)) * 16) + (c & 1) * 8;
        hso = OFF_HF + (unsigned)(layer * 5242880 + ((8 * mt1 + 2 * w + (q >> 1)) * 160 + 10 * nt1 + 5 * (q & 1)) * 256 + c * 16);
        cwa = L_CW + c * 4;
        fho1 = lane ? kNone : OFF_FH + (unsigned)((((layer * 32 + 2 * mt1 + (w >> 1)) * 4 + (nt1 >> 2)) * 8 + (nt1 & 3) * 2 + (w & 1)) * 128);
        fho = OFF_FH + (unsigned)(((layer * 32 + mt2) * 4 + w) * 1024 + (lane & 7) * 128);
        pin(sgn_); pin(spl); pin(eo); pin(es); pin(spa); pin(tbw); pin(tbr); pin(hso); pin(cwa); pin(fho1); pin(fho);
        const unsigned hb_ = OFF_HF + (unsigned)(layer * 5242880 + (4 * mt2 * 160 + 40 * w) * 256);
#pragma unroll
        for (int m = 0; m < 4; ++m) { ho[m] = hb_ + (unsigned)(((m + 1 + w) & 3) * 40960 + lane * 16); pin(ho[m]); }
#pragma unroll
        for (int m = 0; m < 3; ++m) { dst[m] = SC + ((w + 1 + m) & 3) * INBOX + m * 5120 + lane * 16; pin(dst[m]); }
        mt1_ = mt1;
        sgo = OFF_SG + (unsigned)(((layer * 32 + mt2) * 4 + w) * 2048 + c * 16);
        sgs = q < 2 ? sgo + (unsigned)(nt2 * 256 + q * 8) : kNone;
        sgo += (unsigned)(q * 512);
        pin(sgo); pin(sgs);
        if constexpr (layer == 0) {
            srca = SC + w * INBOX + lane * 16;
            glo = OFF_GT + (unsigned)((80 * nt2 + (lane >> 1)) * 4096 + (64 * mt2 + 16 * w) * 2 + (lane & 1) * 16);
            gso = cv0 ? OFF_GT + (unsigned)((80 * nt2 + i32 + 32 * w) * 4096 + (q & 1) * 512 + c * 32 + (q >> 1) * 16) : kNone;
            fgo = OFF_FG + (unsigned)((nt2 * 96 + (lane & 31)) * 128);
            fgo1 = w == 0 ? OFF_FG + (unsigned)((nt2 * 96 + i32) * 128) : kNone;
            pin(srca); pin(glo); pin(gso); pin(fgo); pin(fgo1);
            const int r0 = SC + (w < 3 ? w : 0) * REGSZ, y0 = 2031 + 8 * q - c, l34 = lane < 33 ? lane : 33;
#pragma unroll
            for (int i = 0; i < 4; ++i) { ab[i] = r0 + ((y0 & 1) ? CV_O : CV_E) + ((y0 + 1) >> 1) * 4 - 448 - 512 - 1024 * i; pin(ab[i]); }
            bb = r0 + 32 * c + 16 * q;
            zpa = w < 3 ? r0 + l34 * 16 + (l34 >= 30 ? CV_E + 4096 - 480 : 0) + (l34 >= 32 ? CV_O + 4100 - 32 - CV_E - 4096 : 0) : SC + 47360 + lane * 16;
            uwa = r0 + lane * 16;
            xso = (unsigned)((64 * mt2 + 16 * w + c) * D_ + 80 * nt2 + 16 * (q & 1) + 8 * (q >> 1)) * 2u;
            dso = (unsigned)(160 * nt2 + 8 * q);
            adm = (unsigned)((c & 1) * 16 + (q == 2 + (c >> 3) ? 256 << ((c & 7) >> 1) : 0));
            if (GF) pin(adm);
            pin(bb); pin(zpa); pin(uwa); pin(xso); if (DSE) pin(dso);
        }
        SCHED();
    };
    unsigned tag0, tag1;
#define FLAG(off) { if (lane == 0) st64(v2u{tag0, tag1}, rs, off); }
#define ISTAG(f) (((f.x ^ tag0) | (f.y ^ tag1)) == 0u)
#define KW_REQ() { if (cv) ld_ur(wro, p.ws + OFF_KVT); static_for<NW2P>(LAMBDA(i_) { gld_w2<i_.value>(w2o, p.ws + 2048 + (i_.value / 5) * 5120); }); }
#define FW_OK() { if (__builtin_expect(!kearly, 0)) { WAVE_WAIT({ const v2u fw_ = ld64(rs, fwo); ok = ISTAG(fw_); }) KW_REQ() } }
#define TT_GAP(n) { constexpr int mb_ = (n) / 10, nb_ = ((n) % 10) / 2, e_ = ((n) & 1) * 2; \
        mul2(tt[mb_][nb_][e_], tt[mb_][nb_][e_ + 1], rm_[4 * mb_ + e_], rm_[4 * mb_ + e_ + 1], SW[nb_]); }
    {
        const int tid = threadIdx.x, lane = tid & 63, w = w0, b = b0;
        const int c = lane & 15, q = lane >> 4;
        TILES()
        STAMP(0)
        const unsigned lds0 = (unsigned)(size_t)(LAS char*)smem;
        v2u nonce;
        {
            u64 pc;
            int len, len2;
            asm volatile("s_getpc_b64 %0\nkpf_begin:\n\ts_mov_b32 %1, kpf_end-kpf_begin\n\ts_mov_b32 %2, kpf_end2-kpf_begin" : "=s"(pc), "=s"(len), "=s"(len2));
            len = len > len2 ? len : len2;
            const int o0 = (((j & 15) * 4 + w) + 64 * (lane >> 3)) * 128;
            touch_((unsigned)(o0 < len ? o0 : 0), (const void*)pc);
        }
        const bool cv = w < 2 || (w == 2 && i32 < 16);
        const int chn = 80 * nt2 + i32 + 32 * (cv ? w : 0);
        const bool kw = KVS ? w < 2 || (w == 2 && i32 >= 16) : cv;
        const int kp = 16 * nt2 + mt1, ku = 5 * kp + (i32 < 16 ? w & 1 : 2 + (w < 3 ? w : 0)), kc = ku >> 6, kr = ku & 63;
        const int lc = cv ? lane : 8;
        int kwi = 2 + (i32 >> 4) - w;
        v4u ax[15], sa[5], cr[2], wr[4];
        unsigned uL[3];
        const int cu_ = (b < 200 ? b : 0) * 4 + w, cdb = cu_ / 20, ccg = cu_ % 20;
        {
            int ca_ = 32 * ccg, cb_ = 40 * nt1 + 10 * w - 32;
            asm volatile("" : "+s"(ca_), "+s"(cb_), "+s"(kwi));
            unsigned cpo = (unsigned)((lane < 32 ? ca_ : cb_) + (lane < 41 ? lane : 41)) * 2u;
            unsigned kso = (unsigned)(64 * kc + 8 * (lane & 7)) * 2u, ko = (unsigned)(32 * kr + 4 * (lane >> 3)) * 1280u + kso, kh = ko - 3840u;
            kso *= 4u;
            pin(cpo); pin(kso); pin(ko); pin(kh);
            SCHED();
            gldu16(uL[0], cpo, p.Lre); gldu16(uL[1], cpo, p.Lim); gldu16(uL[2], cpo, p.logdt);
            const v4u rk = {(unsigned)(u64)p.k, (unsigned)((u64)p.k >> 32) & 0xffffu, S_ * D_ * 2u, 0x00020000u};
#define L4(d, v, s_, o, f) "global_load_dwordx4 %" #d ", %" #v ", %" #s_ " offset:" #o f "\n\t"
#define B4(d, o) "buffer_load_dwordx4 %" #d ", %17, %21, 0 offen offset:" #o " nt\n\t"
            asm volatile("s_cmp_lt_i32 %22, 1\n\ts_cbranch_scc1 Lrole2\n\t"
                         L4(3, 15, 18, 0, " nt") L4(4, 15, 18, 1280, " nt") L4(5, 15, 18, 2560, " nt") L4(6, 15, 18, 3840, " nt")
                         L4(7, 15, 20, 0, " nt") L4(8, 15, 20, 1280, " nt") L4(9, 15, 20, 2560, " nt") L4(10, 15, 20, 3840, " nt")
                         B4(0, 0) B4(1, 1280) B4(2, 2560) L4(11, 16, 19, 0, "") L4(12, 16, 19, 16, "") L4(13, 16, 19, 32, "") L4(14, 16, 19, 48, "")
                         "\nLrole2:"
                         : "=&v"(ax[0]), "=&v"(ax[1]), "=&v"(ax[2]), "=&v"(ax[3]), "=&v"(ax[4]), "=&v"(ax[5]), "=&v"(ax[6]), "=&v"(ax[7]), "=&v"(ax[8]), "=&v"(ax[9]),
                           "=&v"(ax[10]), "=&v"(ax[11]), "=&v"(ax[12]), "=&v"(ax[13]), "=&v"(ax[14])
                         : "v"(ko), "v"(kso), "v"(kh), "s"(p.k), "s"(p.shk), "s"(p.v), "s"(rk), "s"(kwi) : "scc");
            SCHED();
        }
        const char* ssrc;
        {
            u64 pa = (u64)(p.x0 + (128 * mt1 + 8 * nt1 + 2 * w) * D_), pb = (u64)(((mt1 & 1) ? p.Bim : p.Bre) + (40 * nt1 + (mt1 >> 1)) * D_);
            asm volatile("" : "+s"(pa), "+s"(pb));
            const int nbx = 2 * w + q - 2, r = q < 2 ? q : 8 * (nbx < 5 ? nbx : 4);
            ssrc = (const char*)(q < 2 ? pa : pb) + (unsigned)(r * 1280 + c * 16);
            pin(ssrc);
        }
        if (SAF == 0) static_for<5>(LAMBDA(i) { gld64<256 * i.value>(sa[i.value], ssrc); });
        SCHED();
        if (w == TW && !kw) tour<0>();
        STAMP(22)
        const int row8 = lane >> 3;
        const unsigned wvo = (unsigned)(40 * nt1 + row8) * 1280u + (unsigned)(((lane & 7) ^ ((w & 1) | ((row8 >> 1) << 1))) * 16 + (w >> 1) * 128);
        const char* const wbase = (const char*)((w & 1) ? p.Bim : p.Bre);
        const unsigned wl = lds0 + (unsigned)(((w & 1) * 10 + (w >> 1)) * 1024);
        const unsigned xvo0 = (unsigned)(8 * (row8 >> 1) + 2 * (row8 & 1)) * 1280u + (unsigned)(((lane & 7) ^ ((row8 >> 1) << 1)) * 16);
        const unsigned xvo1 = (xvo0 ^ 16u) + 1280u;
        const char* const xbase = (const char*)p.x0 + (size_t)(128 * mt1 + 32 * w) * 1280;
        const unsigned xl = lds0 + (unsigned)(w * REGSZ);
        unsigned xv[4] = {xvo0, xvo1, xvo0 + 5120u, xvo1 + 5120u}, wv[5];
#pragma unroll
        for (int i = 0; i < 5; ++i) { wv[i] = wvo + i * 10240u; pin(wv[i]); if (i < 4) pin(xv[i]); }
#define DMA_M0(it) SETM0<dma_loff(it) - dma_goff(it)>((it) < 40 ? xl : wl)
#define DMA_LD1(it, nx_) GLD1<dma_goff(it), dma_loff(nx_) - dma_goff(nx_)>((it) < 40 ? xv[(it) & 3] : wv[(it) < 40 ? 0 : ((it) - 100) % 5], (it) < 40 ? xbase : wbase, (nx_) < 40 ? xl : wl)
#define DMA_LD(it) GLD0<dma_goff(it)>((it) < 40 ? xv[(it) & 3] : wv[(it) < 40 ? 0 : ((it) - 100) % 5], (it) < 40 ? xbase : wbase)
        const int swz = (c & 1) | ((c >> 2) << 1);
        int w1lane0 = (c & 1) * 10240 + (c >> 1) * 128 + ((q ^ swz) * 16);
        int w1lane1 = (c & 1) * 10240 + (c >> 1) * 128 + (((4 + q) ^ swz) * 16);
        int xlane0 = L_XR + w * REGSZ + (c & 1) * 1024 + (c >> 1) * 128 + ((q ^ swz) * 16);
        int xlane1 = L_XR + w * REGSZ + (c & 1) * 1024 + (c >> 1) * 128 + (((4 + q) ^ swz) * 16);
        unsigned cvo = (unsigned)((16 * cdb + c) * D_ + 32 * ccg + 8 * q) * 2u, for0, for1;
        {
            const int G = 4 * ccg + q, ch = 10 * (G / 5) + G % 5, ci = ch + 5;
            const unsigned fo = OFF_W2F + (unsigned)(((cdb / 5) * 200 + cdb % 5) * 1024 + c * 16);
            for0 = fo + (unsigned)((ch >> 2) * 5120 + (ch & 3) * 256); for1 = fo + (unsigned)((ci >> 2) * 5120 + (ci & 3) * 256);
        }
        unsigned sto = OFF_ST + (unsigned)((mt1 * 16 + 4 * w + q) * 256 + c * 8), swo = OFF_ST + (unsigned)(((lane >> 2) * 16 + nt1) * 256 + 128 + (lane & 3) * 16);
        unsigned kvo = OFF_KVT + (unsigned)((64 * kc + 8 * (lane >> 3) + ((lane >> 2) & 1)) * 4096 + (32 * kr + 8 * (lane & 3)) * 2);
        int kbw = L_KB + w * 1024 + (lane & 7) * 128 + (((lane >> 3) * 8) ^ ((lane & 7) * 16));
        int kbr = L_KB + w * 1024 + (lane >> 3) * 128 + (((lane & 7) * 16) ^ ((lane >> 3) * 16));
        const unsigned dum = (OOB & 1) ? kNone : OFF_DUM + (unsigned)((b * 4 + w) * 128 + (lane & 7) * 16);
        if (!kw) kvo = dum;
        if (b >= 200) { for0 = dum; for1 = dum; cvo = (unsigned)((b * 4 + w) * 128); }
        wro = (unsigned)(chn * 4096 + lc * 16);
        cv0 = cv; three = i32 < 16;
        pin(kvo); pin(kbw); pin(kbr);
        int w1hi0 = w1lane0 + 61440, w1hi1 = w1lane1 + 61440;
        pin(w1hi0); pin(w1hi1);
        pin(w1lane0); pin(w1lane1); pin(xlane0); pin(xlane1); pin(cvo); pin(for0); pin(for1); pin(sto); pin(swo); pin(wro);
        SCHED();
        auto pre = LAMBDA(i_) {
            constexpr int it = p1_pre(i_.value);
            SCHED();
            DMA_M0(it);
            asm volatile("s_nop 0");
            DMA_LD(it);
            SCHED();
        };
        {
            int f0 = 2 * ((chn & ~63) / 5), f1 = 2 * ((chn | 63) / 5) + 1, nk = cv ? 28 : 0;
            asm volatile("" : "+s"(f0), "+s"(f1), "+s"(nk));
            const int l10 = lane - 32 < 0 || lane - 32 > 9 ? 0 : lane - 32;
            fwo = lane < nk ? OFF_FK + (unsigned)(f0 + lane < f1 ? f0 + lane : f1) * 128u : OFF_FW + (unsigned)(25 * nt2 + w + 5 * (l10 >> 1) + (l10 & 1)) * 128u;
        }
        w2o = OFF_W2F + (unsigned)((nt2 * 40 + 10 * w) * 5120 + lane * 16);
        int ime = SC + (w < 3 ? w * REGSZ + CV_E : 43264) + 1008 - 16 * lane, imo = SC + (w < 3 ? w * REGSZ + CV_O + 4 : 47360) + 1008 - 16 * lane, im3 = w < 3 ? ime + 3072 : imo;
        int tca = L_TAB + (c >> 1) * 136, tcb = tca + 8 + 32 * w + 8 * q, tcl = L_TAB + (lane < 40 ? lane : 39) * 136, coa = L_CO + w * REGSZ + q * 64, rsa = l_rs(0, 0) + w * 256 + lane * 4;
        unsigned sgn = (c & 1) ? 0u : 0x80000000u;
        unsigned fko1 = w ? kNone : lane < 32 ? OFF_FK + (unsigned)((2 * kp + (i32 >> 4)) * 128) : OFF_FW + (unsigned)(b * 128);
        pin(fwo); pin(w2o); pin(fko1); pin(ime); pin(imo); pin(im3); pin(tca); pin(tcb); pin(tcl); pin(coa); pin(rsa); pin(sgn);
        SCHED();
        auto cw_ = [&]() __attribute__((always_inline)) {
            gldn<0, 2>(cr[0], cvo, p.Cre); gldn<0, 2>(cr[1], cvo, p.Cim);
            static_for<4>(LAMBDA(i) { gldn<i.value * 1024, 1>(wr[i.value], wro, p.ssmk); });
        };
        if (SAF == 1) static_for<5>(LAMBDA(i) { gld64<256 * i.value>(sa[i.value], ssrc); });
        if (CWP == 0) cw_();
        STAMP(33)
        static_assert(SAF == 2 && CWP == 1, "");
        gld2s<0>(nonce, 0u, p.ws + OFF_NONCE);
        asm volatile("s_waitcnt vmcnt(16)\n\ts_cmp_gt_i32 %3, 0\n\ts_cbranch_scc1 Lrole3\n\ts_waitcnt vmcnt(1)\nLrole3:" : "+v"(uL[0]), "+v"(uL[1]), "+v"(uL[2]) : "s"(kwi) : "memory", "scc");
        {
            f32x2 lam, co, lp[16];
            disc(uL[0], uL[1], uL[2], lam, co);
            if (lane < 32) *(f32x2*)(smem + L_CO + w * REGSZ + lane * 8) = 2.0f * co;
            lp[0] = cmul(lam, lam); lp[0] = cmul(lp[0], lp[0]); lp[1] = cmul(lp[0], lp[0]);
            lp[0] = f32x2{1.f, 0.f};
            lp[2] = cmul(lp[1], lp[1]); lp[3] = cmul(lp[2], lp[1]); lp[4] = cmul(lp[2], lp[2]);
            const f32x2 z2 = {0.f, 0.f};
            f32x2 t4[4];
            cfma4(t4, lp[4], lp[1], lp[2], lp[3], lp[4], z2, z2, z2, z2);
            lp[5] = t4[0]; lp[6] = t4[1]; lp[7] = t4[2]; lp[8] = t4[3];
            cfma4(t4, lp[8], lp[1], lp[2], lp[3], lp[4], z2, z2, z2, z2);
            lp[9] = t4[0]; lp[10] = t4[1]; lp[11] = t4[2]; lp[12] = t4[3];
            cfma4(t4, lp[8], lp[5], lp[6], lp[7], lp[7], z2, z2, z2, z2);
            lp[13] = t4[0]; lp[14] = t4[1]; lp[15] = t4[2];
            if (lane >= 32 && lane < 42) {
                f32x2* tl = (f32x2*)(smem + L_TAB + (10 * w + lane - 32) * 136);
                tl[0] = lam;
#pragma unroll
                for (int i = 0; i < 16; ++i) tl[1 + i] = lp[i];
            }
        }
        SCHED();
        STAMP(35)
        if (SAF == 2) static_for<5>(LAMBDA(i) { gld64<256 * i.value>(sa[i.value], ssrc); });
        if (CWP) cw_();
        if (APOS == 0) addrs(std::integral_constant<int, 0>{}, lane, w, b);
        v4u kvt[4];
        unsigned ka[8], kt[8], se[8], vs[8], ko4[4], one2 = 0x3F803F80u, kq[12];
        float kf_[8];
        pin(one2);
        auto kvf = LAMBDA(n_) {
            constexpr int n = n_.value, h = n / 5, u = n % 5;
            if constexpr (u == 0) {
#pragma unroll
                for (int r = 0; r < 6; ++r) { kq[r] = __builtin_amdgcn_perm(ax[r][h], ax[r + 1][h], 0x05040100u); kq[6 + r] = __builtin_amdgcn_perm(ax[r][h], ax[r + 1][h], 0x07060302u); }
                if constexpr (h > 0) kvt[h - 1] = *(const v4u*)(smem + kbr);
            } else if constexpr (u == 1) {
                unsigned t[8];
                d2x4(t, kq[0], kq[1], kq[2], kq[3], ax[11 + h][1], ax[11 + h][1], ax[11 + h][1], ax[11 + h][1]);
                d2x4(t + 4, kq[6], kq[7], kq[8], kq[9], ax[11 + h][3], ax[11 + h][3], ax[11 + h][3], ax[11 + h][3]);
#pragma unroll
                for (int r = 0; r < 8; ++r) kf_[r] = __uint_as_float(t[r]);
            } else if constexpr (u == 2) {
#pragma unroll
                for (int r = 0; r < 8; ++r) kf_[r] = dot2a(kq[r + 2 * (r >> 2) + 2], ax[11 + h][2 * (r >> 2)], kf_[r]);
            } else if constexpr (u == 3) {
#pragma unroll
                for (int r = 0; r < 4; ++r) { kq[r] = ax[7 + r][h] << 16; kq[4 + r] = ax[7 + r][h] & 0xffff0000u; pin(kq[r]); pin(kq[4 + r]); }
            } else {
#pragma unroll
                for (int r = 0; r < 8; ++r) kf_[r] *= __uint_as_float(kq[r]);
                *(v2u*)(smem + kbw) = v2u{cvtpk(kf_[0], kf_[1]), cvtpk(kf_[2], kf_[3])};
                *(v2u*)(smem + (kbw ^ 64)) = v2u{cvtpk(kf_[4], kf_[5]), cvtpk(kf_[6], kf_[7])};
                if constexpr (h == 3) kvt[3] = *(const v4u*)(smem + kbr);
            }
            SCHED();
        };
        auto kvop = LAMBDA(n_) {
            constexpr int n = n_.value, h = n ? (n - 1) / 29 : -1, u = n ? (n - 1) % 29 : 26;
            if constexpr (u < 2) d2x4(ka + 4 * u, ax[0][h], ax[1][h], ax[2][h], ax[3][h], se[u], se[u], se[u], se[u]);
            else if constexpr (u == 8) { const v4u sk = ax[11 + h]; se[4] = sk[0] >> 16; se[5] = sk[2] & 0xffff0000u; se[6] = sk[0] & 0xffffu; se[7] = sk[2] << 16; pin(se[4]); pin(se[5]); pin(se[6]); pin(se[7]); }
            else if constexpr (u < 21) {
                constexpr int v = u < 8 ? u - 2 : u - 3, tp = 1 + v / 6, st = v % 6, e = st & 1;
                if constexpr (st < 2) d2x4(kt + 4 * e, ax[tp][h], ax[tp + 1][h], ax[tp + 2][h], ax[tp + 3][h], se[2 * tp + e], se[2 * tp + e], se[2 * tp + e], se[2 * tp + e]);
                else if constexpr (st < 4) cpx4i(kt + 4 * e, ka + 4 * e);
                else d2x4(ka + 4 * e, kt[4 * e], kt[4 * e + 1], kt[4 * e + 2], kt[4 * e + 3], one2, one2, one2, one2);
            }
            else if constexpr (u == 21) { vs[0] = ax[7][h] & 0xffffu; vs[1] = ax[8][h] << 16; vs[2] = ax[9][h] & 0xffffu; vs[3] = ax[10][h] << 16; pin(vs[0]); pin(vs[1]); pin(vs[2]); pin(vs[3]); }
            else if constexpr (u == 22) { vs[4] = ax[7][h] >> 16; vs[5] = ax[8][h] & 0xffff0000u; vs[6] = ax[9][h] >> 16; vs[7] = ax[10][h] & 0xffff0000u; pin(vs[4]); pin(vs[5]); pin(vs[6]); pin(vs[7]); }
            else if constexpr (u == 23) cpx4(ko4, ka[0], ka[2], ka[4], ka[6], ka[1], ka[3], ka[5], ka[7]);
            else if constexpr (u < 26) { constexpr int e = u - 24; d2x4(kt + 4 * e, ko4[2 * e], ko4[2 * e], ko4[2 * e + 1], ko4[2 * e + 1], vs[4 * e], vs[4 * e + 1], vs[4 * e + 2], vs[4 * e + 3]); }
            else if constexpr (u == 26) {
                if constexpr (h < 3) { const v4u sk = ax[12 + h]; se[0] = sk[1] >> 16; se[1] = sk[3] & 0xffff0000u; se[2] = sk[1] & 0xffffu; se[3] = sk[3] << 16; pin(se[0]); pin(se[1]); pin(se[2]); pin(se[3]); }
                else asm volatile("s_nop 2");
            }
            else if constexpr (u == 27) {
                cpx4(ko4, kt[0], kt[2], kt[4], kt[6], kt[1], kt[3], kt[5], kt[7]);
                *(v2u*)(smem + kbw) = v2u{ko4[0], ko4[1]};
                *(v2u*)(smem + (kbw ^ 64)) = v2u{ko4[2], ko4[3]};
            }
            else kvt[h] = *(const v4u*)(smem + kbr);
            SCHED();
        };
        asm volatile("s_waitcnt vmcnt(%15)" : "+v"(ax[0]), "+v"(ax[1]), "+v"(ax[2]), "+v"(ax[3]), "+v"(ax[4]), "+v"(ax[5]), "+v"(ax[6]), "+v"(ax[7]), "+v"(ax[8]), "+v"(ax[9]),
                     "+v"(ax[10]), "+v"(ax[11]), "+v"(ax[12]), "+v"(ax[13]), "+v"(ax[14]) : "i"(11) : "memory");
        pin(nonce);
        STAMP(30)
        unsigned tch = 0u;
        constexpr int NF = KVF ? KNF : 27;
        if (kw) {
            if constexpr (KVF) {
                static_for<20>(LAMBDA(n_) {
                    constexpr int n = n_.value, DN = 20 - KFD0;
                    site<1000 + n>(); SCHED();
                    kvf(n_);
                    if constexpr (n >= KFD0) static_for<(n - KFD0 + 1) * NF / (DN ? DN : 1) - (n - KFD0) * NF / (DN ? DN : 1)>(LAMBDA(i_) { pre(std::integral_constant<int, (n - KFD0) * NF / (DN ? DN : 1) + i_.value>{}); });
                });
                if constexpr (KFD0 == 20) static_for<NF>(pre);
            } else
            static_for<117>(LAMBDA(n_) {
                constexpr int n = n_.value;
                site<1000 + n>(); SCHED();
                kvop(n_);
                if constexpr (n >= kD0 && (n - kD0) % kDS == 0 && (n - kD0) / kDS < 27) { pre(std::integral_constant<int, ((n - kD0) / kDS + 270) % 27>{}); SCHED(); }
            });
        } else {
            if (w == 3) {
            const int l = lane, l3 = l & 3;
            unsigned a = OFF_SG + (unsigned)((((((l >> 3) & 1) * 32 + mt2) * 4 + ((l >> 1) & 3)) * 8 + nt2) * 256 + (l & 1) * 128);
            a = l >= 16 ? OFF_EG + (unsigned)((l >= 21 ? 163840 - 640 : 0) + (mt1 * 16 + nt1) * 640 + (l < 26 ? l - 16 : 5) * 128) : a;
            a = l >= 28 ? OFF_PS + (unsigned)(((mt2 * 4 + l3) * 8 + nt2) * 128) : a;
            a = l >= 32 ? OFF_FH + (unsigned)((((((l >> 2) & 1) * 32 + 2 * mt1 + (l3 >> 1)) * 4 + (nt1 >> 2)) * 8 + (nt1 & 3) * 2 + (l3 & 1)) * 128) : a;
            a = l >= 40 ? OFF_FX + (unsigned)(((mt1 * 4 + 2 * (mt2 & 1) + (l3 >> 1)) * 16 + nt2 * 2 + (l3 & 1)) * 128) : a;
            a = l >= 48 ? OFF_FG + (unsigned)((nt2 * 96 + i32) * 128) : a;
            const unsigned big = (l & 7) < 2 ? OFF_HF + (unsigned)((l & 1) * 5242880 + (8 * mt1 * 160 + 10 * nt1) * 256) : (l & 7) == 2 ? OFF_X2F + (unsigned)(2 * mt2 * 40960 + 2560 * nt2)
                               : (l & 7) < 5 ? OFF_KVT + (unsigned)((l & 1) * 2621440 + (80 * nt2 + i32) * 4096) : OFF_W2F + (unsigned)(nt2 * 204800 + ((l & 7) - 5) * 98304);
            a = l >= 56 ? big : a;
            tch = a;
        }
            vm_wait<KNW>();
            static_for<NF>(LAMBDA(i_) { if constexpr (i_.value == T1POS) { if (w == TW) tour<1>(); } pre(i_); });
            if (T1POS == 27 && w == TW) tour<1>();
            skip<SKIP0>();
            undef4(kvt);
        }
        tag0 = __builtin_amdgcn_readfirstlane(nonce.x) + 1u; tag1 = __builtin_amdgcn_readfirstlane(nonce.y) + (tag0 == 0u ? 1u : 0u);
        if constexpr (NF == 27) {
#pragma unroll
            for (int i = 0; i < 4; ++i) st128(kvt[i], rs, kvo + i * 8192);
        }
        SCHED();
        STAMP(34)
        if (APOS == 1) addrs(std::integral_constant<int, 0>{}, lane, w, b);
        v4u orr, oi;
        v2u gr;
        unsigned gro;
        {
            asm volatile("s_waitcnt vmcnt(%7)" : "+v"(cr[0]), "+v"(cr[1]), "+v"(sa[0]), "+v"(sa[1]), "+v"(sa[2]), "+v"(sa[3]), "+v"(sa[4]) : "i"(NF == 27 ? 35 : 4 + NF) : "memory");
#pragma unroll
            for (int e = 0; e < 4; ++e) {
                const f32x4 co = *(const f32x4*)(smem + coa + e * 16);
                const f32x2 o0 = lo16f(cr[0][e]) * f32x2{co.x, -co.y} - lo16f(cr[1][e]) * f32x2{co.y, co.x};
                const f32x2 o1 = hi16f(cr[0][e]) * f32x2{co.z, -co.w} - hi16f(cr[1][e]) * f32x2{co.w, co.z};
                orr[e] = cvtpk(o0.x, o1.x); oi[e] = cvtpk(o0.y, o1.y);
            }
            if constexpr (NF == 27) { st128(orr, rs, for0); st128(oi, rs, for1); } else { pin(orr); pin(oi); }
            SCHED();
        }
        static_for<(27 - NF) / 3>(LAMBDA(i_) { pre(std::integral_constant<int, NF + i_.value>{}); });
        {
            float s1[2] = {0.f, 0.f}, s2[2] = {0.f, 0.f};
#pragma unroll
            for (int i = 0; i < 5; ++i)
#pragma unroll
                for (int e = 0; e < 4; ++e) { s1[e & 1] = dot2a(sa[i][e], one2, s1[e & 1]); s2[e & 1] = dot2a(sa[i][e], sa[i][e], s2[e & 1]); }
            const float a1 = rowsum16(s1[0] + s1[1]), a2 = rowsum16(s2[0] + s2[1]);
            const float mean = a1 * (1.0f / D_);
            const float var = fmaxf(a2 * (1.0f / D_) - mean * mean, 0.f);
            const float ri = __builtin_amdgcn_rcpf(rbf(__builtin_amdgcn_sqrtf(rbf(rbf(var) + 1e-5f))));
            gr = v2u{__float_as_uint(q >= 2 ? a1 : c ? -ri * rbf(mean) : ri), tag0};
            gro = c < 2 ? OFF_ST + (unsigned)((mt1 * 16 + nt1) * 256 + ((q < 2 ? 8 * c : 14) + 2 * w + q) * 8) : kNone;
            pin(gr); pin(gro);
        }
        SCHED();
        STAMP(59)
        if (APOS == 2) addrs(std::integral_constant<int, 0>{}, lane, w, b);
        if constexpr (NF < 27) {
            static_for<27 - NF - (27 - NF) / 3>(LAMBDA(i_) { pre(std::integral_constant<int, NF + (27 - NF) / 3 + i_.value>{}); });
#pragma unroll
            for (int i = 0; i < 4; ++i) st128(kvt[i], rs, kvo + i * 8192);
            st128(orr, rs, for0); st128(oi, rs, for1);
        }
        st64(gr, rs, gro);
        SCHED();
        STAMP(23)
        if (w == 3) tour<7>();
        asm volatile("s_waitcnt vmcnt(%4) lgkmcnt(0)" : "+v"(wr[0]), "+v"(wr[1]), "+v"(wr[2]), "+v"(wr[3]) : "i"(P1_PRE - 9) : "memory");
        __builtin_amdgcn_s_barrier();
        asm volatile("" ::: "memory");
        STAMP(24)
        v4u wf[2][5], af[2][2][2];
#pragma unroll
        for (int nb = 0; nb < 5; ++nb) wf[0][nb] = *(const v4u*)(smem + w1lane0 + nb * 20480 + (0) * 2048);
#pragma unroll
        for (int i = 0; i < 4; ++i) af[0][i >> 1][i & 1] = *(const v4u*)(smem + ((i >> 1) ? xlane1 : xlane0) + (i & 1) * 2048);
        SCHED();
        const char *kvtb = p.ws + OFF_KVT, *w2b0 = p.ws + 2048;
        pins(kvtb); pins(w2b0);
        v2u sg, tc[10], fw, tg2 = {tag0, tag1};
        pin(tg2);
        v4u sw0;
        unsigned ie[4][4], io[4][4], iw, zq[4];
        float rr_[8];
        auto kstep = LAMBDA(s_) {
            constexpr int s = s_.value, ph = s >> 2, s4 = s & 3, cu = s & 1, nx = cu ^ 1, jj = s >> 1, pp = jj & 1;
            if constexpr (s4 == 3 && ph < 4) {
                constexpr int pw = p1_pos(104 + 5 * (ph + 1)), px = p1_pos(4 * (jj + 1) + 3), pm = pw > px ? pw : px;
                constexpr int vm = p1_lim(p1_cnt(s, 0) - 1 - pm);
                asm volatile("" ::: "memory");
                __builtin_amdgcn_s_waitcnt((vm & 15) | ((vm >> 4) << 14) | 0x70);
                __builtin_amdgcn_s_barrier();
                asm volatile("" ::: "memory");
                STAMP(25 + ph)
                SCHED();
            }
            if constexpr (s == 15) {
                wait_st<p1_cnt(s, 0) - 1 - p1_pos(203)>(sg, sw0);
                WAVE_RETRY((((sg.y ^ tag0) | (sw0.y ^ tag0) | (sw0.w ^ tag0)) == 0u), sg = ld64(rs, sto); sw0 = ld128(rs, swo))
                STAMP(7)
            }
            if constexpr (s == kW2S) { vm_wait<p1_cnt(s, 0) - 1 - p1_pos(208)>(); SCHED(); }
            if constexpr (s == 18) {
                vm_wait<p1_cnt(s, 0) - 1 - (p1_pos(207) > p1_pos(208) ? p1_pos(207) : p1_pos(208))>();
                pin(fw); pin(sdq);
                kearly = __builtin_amdgcn_ballot_w64(!ISTAG(fw)) == 0ull;
                ORDER();
                if (kearly && cv) ld_ur(wro, kvtb);
                SCHED();
            }
            if constexpr (s4 != 3 || ph == 4) lgkm<(cu == 0 && s > 0 ? 3 : s == 17 ? 4 : s == 19 ? 2 : 0)>();
            SCHED();
            static_for<10>(LAMBDA(g_) {
                constexpr int g = g_.value, mb = g / 5, k = g % 5, it = p1_item(s, g), nt_ = g < 9 ? p1_item(s, g + 1) : -1;
                constexpr bool d0 = it >= 0 && it < 200, d1 = nt_ >= 0 && nt_ < 200;
                if constexpr (s == 0) mf0(acc[mb][k], af[pp][cu][mb], wf[cu][k]); else mf(acc[mb][k], af[pp][cu][mb], wf[cu][k]);
                SCHED();
                if constexpr (s < 19) {
                    if constexpr (g < 5) wf[nx][g] = *(const v4u*)(smem + (g < 3 ? (nx ? w1lane1 : w1lane0) : (nx ? w1hi1 : w1hi0)) + (g < 3 ? g : g - 3) * 20480 + ((s + 1) >> 1) * 1024);
                    if constexpr (cu == 1 && g == 5) vm_wait<p1_lim(p1_cnt(s, 5) - 1 - p1_pos(4 * (jj + 1) + 3))>();
                    if constexpr (cu == 1 && g >= 5 && g < 9)
                        af[pp ^ 1][(g - 5) >> 1][(g - 5) & 1] = *(const v4u*)(smem + (((g - 5) >> 1) ? xlane1 : xlane0) + ((jj + 1) % 3) * 4096 + ((g - 5) & 1) * 2048);
                }
                if constexpr (cu == 0 && g == 4 && s > 0) lgkm<(s == 8 || s == 10 || s == 14 ? 8 : 5)>();
                static_for<p1_cap(s, g)>(LAMBDA(e_) {
                    constexpr int na = p1_slot(0, s, g, 10) + e_.value, i = (na / 10) & 3, u = na % 10;
                    static_assert(p1_slot(0, 8, 0, 10) >= 40, "");
                    if constexpr (na < 40) {
                        if constexpr (u == 0) { if constexpr (i > 0) ror1(iw, wr[i - 1].w); else { iw = 0u; pin(iw); } }
                        else if constexpr (u < 5) rot1(ie[i][u - 1], wr[i][4 - u]);
                        else if constexpr (u == 5) shr1b(iw, wr[i].w);
                        else if constexpr (u < 9) mix1(io[i][u - 6], wr[i][9 - u], wr[i][8 - u]);
                        else mix1(io[i][3], wr[i].x, iw);
                    }
                    constexpr int nb = p1_slot(8, s, g, 3) + e_.value;
                    if constexpr (s >= 8 && s < 12 && g < 3) {
                        if constexpr (nb < 10) tc[nb] = *(const v2u*)(smem + (nb < 5 ? tca : tcb) + (nb % 5) * 1088);
                        else if constexpr (nb == 10) { const f32x2* tp = (const f32x2*)(smem + tcl); l8 = tp[2]; l32 = tp[5]; }
                        else if constexpr (nb == 11) l128 = *(const f32x2*)(smem + tcl + 72);
                        static_assert(p1_slot(8, 12, 0, 3) == 12, "");
                    }
                    constexpr int nc = p1_slot(12, s, g, 10) + e_.value;
                    if constexpr (s >= 12 && s < 14 && nc < 10) {
                        if constexpr (nc < 5) { lr[nc] = __uint_as_float(tc[nc].x); xor1(ls[nc], tc[nc].y, sgn); }
                        else { lqr[nc - 5] = __uint_as_float(tc[nc].x); xor1(lqs[nc - 5], tc[nc].y, sgn); }
                        static_assert(p1_slot(12, 14, 0, 10) >= 10, "");
                    }
                    if constexpr (s == 15 && g < 5) SW[g] = __int_as_float(__builtin_amdgcn_ds_bpermute(16 * c + 4 * (g >> 1), (g & 1) ? sw0.z : sw0.x));
                    constexpr bool img = (s == 16 && g >= 5 && g < 9 && e_.value == 0) || (s == 18 && (g == 5 || g == 6) && e_.value == 0);
                    constexpr int nd = p1_slot(15, s, g, 10) + e_.value - 5 - (s > 16 ? 4 : s == 16 && g >= 5 ? (g < 9 ? g - 4 : 4) : 0) - (s > 18 ? 2 : s == 18 && g >= 5 ? (g == 5 ? 1 : 2) : 0);
                    if constexpr (s >= 15 && nd >= 0 && !img) {
                        if constexpr (nd < 4) bc1<nd>(rr_[nd], sg.x);
                        else if constexpr (nd < 44) mulb<8 + (nd - 4) % 8>(tt[(nd - 4) % 8 / 4][(nd - 4) / 8][(nd - 4) % 4], sg.x, SW[(nd - 4) / 8]);
                        else if constexpr (nd < 48) bc1<nd - 40>(rr_[nd - 40], sg.x);
                        static_assert(p1_slot(15, 16, 0, 10) <= 9 && p1_slot(15, 20, 0, 10) >= 59, "");
                    }
                });
                if constexpr (s == 14 && g < 3) *(v4u*)(smem + ime + 1024 * g) = v4u{ie[3 - g][0], ie[3 - g][1], ie[3 - g][2], ie[3 - g][3]};
                if constexpr (s == 16 && g >= 5 && g < 9) *(v4u*)(smem + imo + 1024 * (8 - g)) = v4u{io[g - 5][0], io[g - 5][1], io[g - 5][2], io[g - 5][3]};
                if constexpr (s == 18 && g == 5) *(v4u*)(smem + im3) = v4u{ie[0][0], ie[0][1], ie[0][2], ie[0][3]};
                if constexpr (s == 18 && g == 6) *(unsigned*)(smem + rsa) = sg.x;
                if constexpr (s == 14 && (g == 6 || g == 8)) { zq[g - 6] = 0u; zq[g - 5] = 0u; pin(zq[g - 6]); pin(zq[g - 5]); }
                if constexpr (s == 19 && g == 0) *(v4u*)(smem + zpa) = v4u{zq[0], zq[1], zq[2], zq[3]};
                if constexpr (s == 18 && g == 1) touch_(tch, p.ws);
                if constexpr (s == 19 && g == 2) { if (__builtin_expect(!kearly, 0)) poll1(fwo, p.ws); }
                if constexpr (it >= 0) {
                    if constexpr (d0 && d1) DMA_LD1(it, d1 ? nt_ : 0);
                    else if constexpr (d0) DMA_LD(it);
                    else if constexpr (it == 202) gld2s<0>(sg, sto, p.ws);
                    else if constexpr (it == 203) gld4s<0>(sw0, swo, p.ws);
                    else if constexpr (it == 207) gldu16(sdq, (unsigned)chn * 2u, p.ssmD);
                    else if constexpr (it == 208) gld2s<0>(fw, fwo, p.ws);
                    else if constexpr (it >= 500) gld_w2<it % 50>(w2o, w2b0 + ((it % 50) / 5) * 5120);
                    else { static_assert(p1_pos(19) >= P1_PRE, ""); st64(tg2, rs, fko1); }
                }
                if constexpr (d1 && !d0) DMA_M0(d1 ? nt_ : 0);
                site<2000 + 10 * s + g>();
                SCHED();
            });
        };
        static_for<20>(kstep);
        SCHED();
        rr[0] = f32x4{rr_[0], rr_[1], rr_[2], rr_[3]}; rr[1] = f32x4{rr_[4], rr_[5], rr_[6], rr_[7]};
        ACC_FENCE10(acc[0], acc[1]);
        STAMP(1)
    }

    auto layer_body = LAMBDA(layer_) {
        constexpr int layer = layer_.value;
        const int sga = sgn_;
        const bool cv = layer == 0 && cv0;
        v2u fh;
        unsigned ks[10];
        f32x4 ca[8], a2[4][5];
        float rsr, rsm;
        v4u cA[2][8], cB[2][8];
        f32x4 pp[5];
        constexpr int R2 = kCR1 + H3, LP = (7 - R2) & 1;
        static_assert(GF && kFHP >= 7 - KG0 / 4 + 1, "");
        constexpr bool TB = (SBT >> layer) & 1;
        float bv[8][5];
        f32x2 pr[5][2], ps[5][2];
        auto powers = [&]() __attribute__((always_inline)) {
            if constexpr (TB) {
#pragma unroll
                for (int nb = 0; nb < 5; ++nb) {
                    const f32x2 m = {lr[nb] * lr[nb] - ls[nb] * ls[nb], 2.0f * lr[nb] * ls[nb]};
                    pr[nb][0] = f32x2{lr[nb], m.x}; ps[nb][0] = f32x2{ls[nb], m.y};
#pragma unroll
                    for (int j = 1; j < 2; ++j) pstep(pr[nb][j], ps[nb][j], pr[nb][j - 1], ps[nb][j - 1], m);
#pragma unroll
                    for (int j = 0; j < 2; ++j) { pin(pr[nb][j]); pin(ps[nb][j]); }
                }
                SCHED();
            }
        };
        {
            float a0[5], a1[5];
            static_for<2>(LAMBDA(m_) {
                constexpr int mb = m_.value;
#pragma unroll
                for (int nb = 0; nb < 5; ++nb) {
                    const f32x4 v = __builtin_elementwise_fma(acc[mb][nb], rr[mb], f32x4{tt[mb][nb][0], tt[mb][nb][1], tt[mb][nb][2], tt[mb][nb][3]});
#pragma unroll
                    for (int i = 0; i < 4; ++i) bv[4 * mb + i][nb] = v[i];
                }
                SCHED();
                if constexpr (TB) {
#pragma unroll
                    for (int i = mb ? 4 : 1; i < 4 + 4 * mb; ++i) cstepi(bv[i][0], bv[i][1], bv[i][2], bv[i][3], bv[i][4], bv[i - 1][0], bv[i - 1][1], bv[i - 1][2], bv[i - 1][3], bv[i - 1][4], lr, ls);
#pragma unroll
                    for (int nb = 0; nb < 5; ++nb) a0[nb] = bv[7][nb];
                }
                if constexpr (mb == 0 && !TB) cstep(a0, bv[0], lr, ls, bv[1][0], bv[1][1], bv[1][2], bv[1][3], bv[1][4]);
#pragma unroll
                for (int i8 = mb ? 4 : 2; i8 < (TB ? 0 : 4 + 4 * mb); i8 += 2) {
                    cstep(a1, a0, lr, ls, bv[i8][0], bv[i8][1], bv[i8][2], bv[i8][3], bv[i8][4]);
                    cstep(a0, a1, lr, ls, bv[i8 + 1][0], bv[i8 + 1][1], bv[i8 + 1][2], bv[i8 + 1][3], bv[i8 + 1][4]);
                }
                SCHED();
            });
#pragma unroll
            for (int nb = 0; nb < 5; ++nb) *(float*)(smem + sga + nb * 64) = a0[nb];
        }
        SCHED();
        const int w_ = w0;
        {
        const int w = w_, mt1 = mt1_;
        if (layer == 0 && __builtin_expect(!kearly, 0)) {
            unsigned f0_, f1_;
            asm volatile("s_waitcnt vmcnt(0)\n\t" AR("%0", 36, 0) AR("%1", 36, 1) : "=v"(f0_), "=v"(f1_)::"memory");
            kearly = __builtin_amdgcn_ballot_w64(((f0_ ^ tag0) | (f1_ ^ tag1)) != 0u) == 0ull;
            ORDER();
            if (kearly) KW_REQ()
        }
        asm volatile("s_waitcnt lgkmcnt(0)" ::: "memory");
        __builtin_amdgcn_s_barrier();
        asm volatile("" ::: "memory");
        STAMP(layer ? 14 : 2)
#pragma unroll
        for (int s = 0; s < 10; ++s) ks[s] = (unsigned)s * 1024u;
        constexpr int NX = DSE ? 58 : 53;
        constexpr int NW2A = NW2P + 7 * kCR1 + kXA, NW3 = NW2P + kN3E + kN3;
        auto w2ld = LAMBDA(i_) {
            constexpr int i = i_.value;
            gld_w2<i>(w2o, p.ws + 2048 + ks[i / 5] * 5u);
        };
        auto w2a = LAMBDA(i_) {
            constexpr int k = i_.value;
            if constexpr (k < kXA) { w2ld(std::integral_constant<int, NW2P + 2 * k>{}); w2ld(std::integral_constant<int, NW2P + 2 * k + 1>{}); }
            else w2ld(std::integral_constant<int, NW2P + kXA + k>{});
        };
        auto w2x = LAMBDA(i_) {
            constexpr int i = i_.value < NX - 50 ? 50 + i_.value : NW2A + i_.value - (NX - 50) < 50 ? NW2A + i_.value - (NX - 50) : 99;
            if constexpr (i < 50) w2ld(std::integral_constant<int, i>{});
            else if constexpr (i < 52) lda4<40 + 4 * (i - 50), (i - 50) * 64>(xso, p.x0);
            else if constexpr (i == 52) lda4<48, 128>(xso - 2u * (unsigned)(bb & 16), p.x0);
            else if constexpr (i < NX) lda2<20 + 2 * (i - 53), 32 * (i - 53)>(dso, p.Dskip);
        };
        constexpr int NB = NX - NW2A, NB1 = KB1 < NB ? KB1 : NB, KPL = 7 * (8 - R2) - kFHP, NFH = NB - NB1 - (KPL - kFHK) > 0 ? NB - NB1 - (KPL - kFHK) : 0;
        auto w2b = LAMBDA(i_) {
            constexpr int k = i_.value - 7 * R2;
            if constexpr (k < NB1) w2x(std::integral_constant<int, k>{});
            if constexpr (k > kFHK) w2x(std::integral_constant<int, NB1 + k - kFHK - 1>{});
            if constexpr (k == kFHK) { vm_wait<NB1>(); st64(v2u{tag0, tag1}, rs, fho1); STAMP(39) }
            if constexpr (k == kFHK - 1) { STAMP(38) }
            if constexpr (k == KPL) poll1(fho, p.ws);
            static_assert(NB1 <= kFHK + 1 && NB1 + 7 * (7 - R2) + KG0 / 4 - kFHK - 1 >= NB && NW2A <= 50 && NW3 <= NW2A && NFH <= NB - NB1, "");
        };
        auto partb = [&]() __attribute__((always_inline)) {
            const float* sp = (const float*)(smem + spa);
            rsr = sp[0]; rsm = sp[8];
            if (layer == 1) { pin(rsr); pin(rsm); }
            if (layer == 1) {
                ACC_TIE5(dsk);
#pragma unroll
                for (int nb = 0; nb < 5; ++nb) { a2[3][nb] = xin[nb] + (xin[nb] * rsr + rsm) * unpk4(dsk[nb]); pin(a2[3][nb]); }
            }
        };
        constexpr int LBW = layer ? 2 : 3;
        if (w == 3) {
            site<3 + 10 * layer>(); SCHED();
            f32x2 sv[16];
#pragma unroll
            for (int i = 0; i < 16; ++i) sv[i] = *(const f32x2*)(smem + spl + (l_sg(layer, i >> 2) - SC + (i & 3) * 320));
            f32x2 u[4], v[4], x[4];
            cfma4(u, l8, sv[0], sv[4], sv[8], sv[12], sv[1], sv[5], sv[9], sv[13]);
            cfma4(v, l8, u[0], u[1], u[2], u[3], sv[2], sv[6], sv[10], sv[14]);
            cfma4(x, l8, v[0], v[1], v[2], v[3], sv[3], sv[7], sv[11], sv[15]);
            f32x2 P[4][4];
            P[0][0] = f32x2{0.f, 0.f}; P[0][1] = x[0]; P[0][2] = cfma(l32, x[0], x[1]); P[0][3] = cfma(l32, P[0][2], x[2]);
            const f32x2 E = cfma(l32, P[0][3], x[3]);
            st128(v4u{__float_as_uint(E.x), tag0, __float_as_uint(E.y), tag0}, rs, es);
            SCHED();
            if (layer == 0) { FW_OK() static_for<kN3E>(LAMBDA(i_) { w2ld(std::integral_constant<int, NW2P + i_.value>{}); }); }
#pragma unroll
            for (int kk = 1; kk < 4; ++kk) cfma4(P[kk], l8, P[kk - 1][0], P[kk - 1][1], P[kk - 1][2], P[kk - 1][3], sv[kk - 1], sv[kk + 3], sv[kk + 7], sv[kk + 11]);
#pragma unroll
            for (int i = 0; i < 16; ++i) *(f32x2*)(smem + spl + (l_sg(layer, i >> 2) - SC + (i & 3) * 320)) = P[i & 3][i >> 2];
            if (layer == 0) partb();
            if (layer == 1) powers();
            if (layer == 0 && H3) *(unsigned*)(smem + L_FL + (spl & 0x38) / 2) = 0u;
        }
        if (w == LBW) {
            site<5 + 10 * layer>(); SCHED();
            f32x2 wg[17], t4[4];
            unsigned ea[15], tg[15];
            const f32x2 z2 = {0.f, 0.f};
            wg[1] = cmul(l128, l128); wg[2] = cmul(wg[1], wg[1]); wg[3] = cmul(wg[2], wg[1]); wg[4] = cmul(wg[2], wg[2]);
#pragma unroll
            for (int i = 4; i < 16; i += 4) {
                cfma4(t4, wg[i], wg[1], wg[2], wg[3], wg[4], z2, z2, z2, z2);
#pragma unroll
                for (int e = 0; e < 4; ++e) wg[i + 1 + e] = t4[e];
            }
#pragma unroll
            for (int i = 0; i < 15; ++i) {
                const int ch = mt1 - 1 - i;
                if (i) pin(wg[i]);
                ea[i] = ch < 0 ? kNone : (unsigned)ch * 10240u; tg[i] = ch < 0 ? 0u : tag0;
                pins(ea[i]); pins(tg[i]);
            }
            if constexpr (layer == 0) tour<3>();
            powers();
            f32x2 H0, H1;
            __builtin_amdgcn_s_sleep(layer ? KLB1 : KLB0);
            unsigned spins = dead ? kSpinCap : 0u;
            bool miss;
#pragma nounroll
            do {
                asm volatile("" ::: "memory");
                v4u eg[15];
#pragma unroll
                for (int i = 0; i < 15; ++i) eg[i] = ld128s(rs, eo, ea[i]);
                SCHED();
                unsigned x = __builtin_amdgcn_bitop3_b32(eg[0].y, eg[0].w, tg[0], 0x7E);
                H0 = f32x2{__uint_as_float(eg[0].x), __uint_as_float(eg[0].z)}; H1 = f32x2{0.f, 0.f};
#pragma unroll
                for (int i = 1; i < 15; i += 2) {
                    x |= __builtin_amdgcn_bitop3_b32(eg[i].y, eg[i].w, tg[i], 0x7E) | __builtin_amdgcn_bitop3_b32(eg[i + 1].y, eg[i + 1].w, tg[i + 1], 0x7E);
                    pin(x);
                    gacc2(H0, H1, wg[i], wg[i + 1], eg[i], eg[i + 1]);
                    SCHED();
                }
                miss = __builtin_amdgcn_ballot_w64(x != 0u) != 0ull;
            } while (miss && ++spins <= kSpinCap);
            dead = dead || miss;
            ORDER();
            *(f32x2*)(smem + spl + (L_CW - SC)) = H0 + H1;
            STAMP(layer ? 36 : 37)
            if (layer == 0) {
                static_for<NW3 - NW2P - kN3E>(LAMBDA(i_) { w2ld(std::integral_constant<int, NW2P + kN3E + i_.value>{}); });
            }
        }
        int w2_ = w;
        pins(w2_);
        if (layer == 0) UNDEF8(ca);
        if (layer == 0 && w2_ != LBW) {
            if (cv) {
                site<2>(); SCHED();
                FW_OK()
                st_ur((unsigned)(size_t)(LAS char*)smem + (unsigned)uwa);
                STAMP(32)
                conv_part<0, kCR1>(ca, smem, ab, bb, w2a, LAMBDA(i_) {}, [&]() __attribute__((always_inline)) {
                    v4u ad;
                    const unsigned dv = sdq << (adm & 16u);
#pragma unroll
                    for (int i = 0; i < 4; ++i) ad[i] = dv & (unsigned)__builtin_amdgcn_sbfe((int)adm, 8 + i, 1);
                    asm volatile("s_nop 1" : "+v"(ad));
                    return ad;
                }, cA, cB);
                partb();
                powers();
            } else {
                if constexpr (layer == 0) tour<5>();
                FW_OK()
                static_for<NW2A - NW2P>(LAMBDA(i_) { w2ld(std::integral_constant<int, NW2P + i_.value>{}); __builtin_amdgcn_s_sleep(1); });
                partb();
                powers();
            }
        }
        if (layer == 1 && w < LBW) { site<4 + 10 * layer>(); SCHED(); powers(); if constexpr (layer == 1) { if (w == 0) tour<6>(); } }
        asm volatile("s_waitcnt lgkmcnt(0)" ::: "memory");
        __builtin_amdgcn_s_barrier();
        asm volatile("" ::: "memory");
        STAMP(layer ? 15 : 4)
        {
            float cw[5], ex[5];
#pragma unroll
            for (int nb = 0; nb < 5; ++nb) { cw[nb] = *(const float*)(smem + cwa + nb * 64); ex[nb] = *(const float*)(smem + sga + nb * 64); }
            v2u ta[5], td[5];
            if constexpr (TB) {
                cstepi(ex[0], ex[1], ex[2], ex[3], ex[4], cw[0], cw[1], cw[2], cw[3], cw[4], lqr, lqs);
                f32x2 cs[5];
                PIN5(ex);
#pragma unroll
                for (int nb = 0; nb < 5; ++nb) { cs[nb] = f32x2{ex[nb], dpp<0xB1>(ex[nb])}; pin(cs[nb]); }
                SCHED();
                static_for<5>(LAMBDA(n_) {
                    constexpr int nb = n_.value;
                    f32x2 h[4] = {{bv[0][nb], bv[1][nb]}, {bv[2][nb], bv[3][nb]}, {bv[4][nb], bv[5][nb]}, {bv[6][nb], bv[7][nb]}};
                    padd(h, pr[nb], ps[nb], cs[nb]);
                    *(v4u*)(smem + tbw + nb * 1024) = v4u{cvtpk(h[0].x, h[0].y), cvtpk(h[1].x, h[1].y), cvtpk(h[2].x, h[2].y), cvtpk(h[3].x, h[3].y)};
                    ta[nb] = __builtin_bit_cast(v2u, trread(smem, tbr + nb * 1024)); td[nb] = __builtin_bit_cast(v2u, trread(smem, tbr + nb * 1024 + 512));
                    SCHED();
                    if constexpr (nb >= LAG) { st128(v4u{ta[nb - LAG].x, ta[nb - LAG].y, td[nb - LAG].x, td[nb - LAG].y}, rs, hso + (nb - LAG) * 256); SCHED(); }
                });
            } else
            static_for<2>(LAMBDA(h_) {
                constexpr int n0 = 2 * h_.value;
                float hv[9][3];
                cstep3(hv[0], cw + n0, lqr + n0, lqs + n0, ex + n0);
                if constexpr (n0 == 0) {
                    chain24<0>(hv, bv, lr, ls);
                    chain24<0>(hv + 4, bv + 4, lr, ls);
                } else {
                    chain33<2>(hv, bv, lr, ls);
                    SCHED();
#pragma unroll
                    for (int rd = 0; rd < 2; ++rd) st128(v4u{ta[rd].x, ta[rd].y, td[rd].x, td[rd].y}, rs, hso + rd * 256);
                    SCHED();
                    chain33<2>(hv + 3, bv + 3, lr, ls);
                    chain32<2>(hv + 6, bv + 6, lr, ls);
                }
#pragma unroll
                for (int e = 0; e < (n0 ? 3 : 2); ++e) {
                    v4u o;
                    o.x = cvtpk(hv[1][e], hv[2][e]); o.y = cvtpk(hv[3][e], hv[4][e]);
                    o.z = cvtpk(hv[5][e], hv[6][e]); o.w = cvtpk(hv[7][e], hv[8][e]);
                    *(v4u*)(smem + tbw + (n0 + e) * 1024) = o;
                }
#pragma unroll
                for (int rd = n0; rd < (n0 ? 5 : 2); ++rd) { ta[rd] = __builtin_bit_cast(v2u, trread(smem, tbr + rd * 1024)); td[rd] = __builtin_bit_cast(v2u, trread(smem, tbr + rd * 1024 + 512)); }
                SCHED();
            });
#pragma unroll
            for (int rd = TB ? 5 - LAG : 2; rd < 5; ++rd) st128(v4u{ta[rd].x, ta[rd].y, td[rd].x, td[rd].y}, rs, hso + rd * 256);
            SCHED();
        }
#pragma unroll
        for (int s = 0; s < 10; ++s) pins(ks[s]);
        if (layer == 1) { partb(); SCHED(); DRAIN(); st64(v2u{tag0, tag1}, rs, fho1); }
        STAMP(layer ? 16 : 5)
        int fa = L_FL + 4 * w, pa = l_tb(0, 0) + (w < 3 ? w : 0) * 5120 + (int)(w2o & 1023u);
        unsigned one = 1u;
        if constexpr (layer == 0 && H3) {
            pin(fa); pin(pa); pin(one);
            *(unsigned*)(smem + fa) = one;
            asm volatile("" ::: "memory");
        }
        if (cv) {
            if constexpr (layer == 0) site<6>();
            SCHED();
            unsigned fl = 1u;
            conv_part<R2, 8>(ca, smem, ab, bb, w2b, LAMBDA(g_) {
                constexpr int g = g_.value - (KG0 - 8);
                if constexpr (H3 && g == 0) { asm volatile("" ::: "memory"); fl = *(const unsigned*)(smem + fa + 16); asm volatile("" ::: "memory"); }
                if constexpr (H3 && g > 0 && g < 6) pp[g - 1] = *(const f32x4*)(smem + pa + (g - 1) * 1024);
                static_assert(!H3 || KG0 >= 8, "");
            }, []() {}, cA, cB);
            while (__builtin_expect(__builtin_amdgcn_readfirstlane(fl) == 0u, 0)) {
                asm volatile("" ::: "memory");
                fl = *(const unsigned*)(smem + fa + 16);
                asm volatile("" ::: "memory");
#pragma unroll
                for (int i = 0; i < 5; ++i) pp[i] = *(const f32x4*)(smem + pa + i * 1024);
            }
        } else if (layer == 0) {
            if (H3 && w == 3) {
                if constexpr (layer == 0) site<16>();
                constexpr int NR = NB - NB1 - NFH, N3 = NR + NW2A - NW3;
                auto fill3 = LAMBDA(n_) {
                    constexpr int n = n_.value, k = n - KF3 - 1;
                    if constexpr (n < NB1) w2x(n_);
                    if constexpr (n == KF3) { vm_wait<NB1>(); st64(v2u{tag0, tag1}, rs, fho1); }
                    if constexpr (k >= 0 && k < NR) w2x(IC(NB1 + k));
                    else if constexpr (k >= NR && k < N3) w2ld(IC(NW3 + k - NR));
                };
                int a3[4] = {ab[0], ab[1], ab[2], ab[3]}, b3 = bb;
                f32x4 cb[2][8];
                auto chan = LAMBDA(x_) {
                    constexpr int x = x_.value;
                    const int (&ab)[4] = a3;
                    const int& bb = b3;
                    if constexpr (x == 0) {
                        CV_LOAD_A(cA[0], 0, kCR1, 0) CV_LOAD_A(cA[0], 0, kCR1, 1)
                        static_for<8>(LAMBDA(e_) { constexpr int e = (e_.value + 3) & 7; CV_LOAD_B(cB[0], e, kCR1) SCHED(); });
                        static_for<7>(LAMBDA(a_) { constexpr int a = a_.value + 1; CV_LOAD_A(cA[0], a, kCR1, 0) CV_LOAD_A(cA[0], a, kCR1, 1) });
                        SCHED();
                        lds_wait3(fa - 12);
                    }
                    static_for<30>(LAMBDA(g_) {
                        constexpr int g = g_.value, a = cv_pair(3, 8, g) / 8, e = cv_pair(3, 8, g) % 8, u = g - 3, f = (u / 3 + 3) & 7;
                        if constexpr (a == 0) mf0(cb[x & 1][e], cA[x & 1][0], cB[x & 1][e]); else mf(cb[x & 1][a + e], cA[x & 1][a], cB[x & 1][e]);
                        SCHED();
                        if constexpr (x < 2 && g < 2) { a3[2 * g] += REGSZ; a3[2 * g + 1] += REGSZ; pin(a3[2 * g]); pin(a3[2 * g + 1]); }
                        if constexpr (x < 2 && g == 2) { b3 += REGSZ; pin(b3); }
                        if constexpr (x < 2 && u >= 0 && u < 24) { if constexpr (u % 3 == 2) CV_LOAD_B(cB[(x & 1) ^ 1], f, kCR1) else CV_LOAD_A(cA[(x & 1) ^ 1], u / 3, kCR1, u % 3) }
                        if constexpr (x > 0 && g % 3 == 2 && g > 2 && g < 18) *(f32x4*)(smem + pa + (x - 1) * 5120 + (g / 3 - 1) * 1024) = cb[(x & 1) ^ 1][2 + g / 3];
                        if constexpr (x > 0 && g == 20) { asm volatile("" ::: "memory"); *(unsigned*)(smem + fa + 4 * x) = one; }
                        SCHED();
                        site<3300 + 30 * x + g>();
                        if constexpr (g % 3 == 1) { fill3(IC(10 * x + g / 3)); SCHED(); }
                    });
                };
                auto out = LAMBDA(x_) {
                    constexpr int x = x_.value;
                    asm volatile("s_nop 7" ::: "memory");
#pragma unroll
                    for (int i = 0; i < 5; ++i) *(f32x4*)(smem + pa + x * 5120 + i * 1024) = cb[x & 1][3 + i];
                    asm volatile("" ::: "memory");
                    *(unsigned*)(smem + fa + 4 + 4 * x) = one;
                    SCHED();
                };
                chan(IC(0));
                chan(IC(1));
                if (three) { chan(IC(2)); out(IC(2)); }
                else { out(IC(1)); static_for<10>(LAMBDA(i_) { fill3(IC(20 + i_.value)); __builtin_amdgcn_s_sleep(1); }); }
                static_for<(KF3 + 1 + N3 > 30 ? KF3 + 1 + N3 - 30 : 0)>(LAMBDA(i_) { fill3(IC(30 + i_.value)); });
                if constexpr (layer == 0) tour<2>();
            } else {
                static_for<NB1>(LAMBDA(i_) { w2x(i_); __builtin_amdgcn_s_sleep(1); });
                vm_wait<NB1>();
                st64(v2u{tag0, tag1}, rs, fho1);
                static_for<NB - NB1 - NFH>(LAMBDA(i_) { w2x(std::integral_constant<int, NB1 + i_.value>{}); __builtin_amdgcn_s_sleep(1); });
                if (!H3 && w == 3) { static_for<NW2A - NW3>(LAMBDA(i_) { w2ld(std::integral_constant<int, NW3 + i_.value>{}); __builtin_amdgcn_s_sleep(1); }); if constexpr (layer == 0 && !H3) tour<2>(); }
                if (KSN) __builtin_amdgcn_s_sleep(KSN);
            }
            poll1(fho, p.ws);
            static_for<NFH>(LAMBDA(i_) { w2x(std::integral_constant<int, NB - NFH + i_.value>{}); __builtin_amdgcn_s_sleep(1); });
            static_assert(NFH <= NB - (NX - 50), "");
            UNDEF8(cA[LP]); UNDEF8(cB[LP]);
            if (H3) asm volatile("" : "=v"(pp[0]), "=v"(pp[1]), "=v"(pp[2]), "=v"(pp[3]), "=v"(pp[4]));
        }
        if (layer == 0) { unsigned f0_, f1_; asm volatile("s_waitcnt vmcnt(%2)\n\t" AR("%0", 36, 0) AR("%1", 36, 1) : "=v"(f0_), "=v"(f1_) : "i"(NFH) : "memory"); fh = v2u{f0_, f1_}; }
        STAMP(layer ? 29 : 6)
        }
        unsigned g[8][2];

            v2u pt[4];
            if (layer == 0) WAVE_RETRY(ISTAG(fh), fh = ld64(rs, fho))
            else { skip<SKIP7>(); pipe_flag<KPF0>(pt, fho, p.ws, (u64)tag0 | ((u64)tag1 << 32), dead); ORDER(); }
            STAMP(layer ? 17 : 31)
            site<7 + 10 * layer>(); SCHED();
            constexpr int RING2 = layer ? KR2 : KRT;
            v4u ar[RING2];
            constexpr int NRT = layer ? RING2 : (36 - KG0) / KRS + 2 < KRT ? (36 - KG0) / KRS + 2 : KRT;
            unsigned xs_[3][4];
            float ys[8][4];
            auto gate = LAMBDA(n_) {
                constexpr int n = n_.value, bl = n / 10, u = n % 10;
                if constexpr (n < 0) {}
                else if constexpr (n >= 100) swp2n(g[2 * (n - 100)][0], g[2 * (n - 100) + 1][0], g[2 * (n - 100)][1], g[2 * (n - 100) + 1][1]);
                else if constexpr (u < 2) { if constexpr (H3) { ys[bl][2 * u] = pp[bl - 3][2 * u]; ys[bl][2 * u + 1] = pp[bl - 3][2 * u + 1]; add2(ys[bl][2 * u], ys[bl][2 * u + 1], ca[bl][2 * u], ca[bl][2 * u + 1]); } }
                else if constexpr (H3 && bl >= 3) cvt2(g[bl][0], g[bl][1], f32x4{ys[bl][0], ys[bl][1], ys[bl][2], ys[bl][3]});
                else cvt2(g[bl][0], g[bl][1], ca[bl]);
            };
            if constexpr (layer == 0) {
#pragma unroll
                for (int i = 0; i < 2; ++i) { ar[i] = ld128s(rs, ho[0], ks[i]); SCHED(); }
                static_for<36 - KG0>(LAMBDA(n_) {
                    constexpr int n = n_.value, g = KG0 + n;
                    constexpr int a = g < 8 ? 0 : g < 15 ? 1 : g < 21 ? 2 : g < 26 ? 3 : g < 30 ? 4 : g < 33 ? 5 : g < 35 ? 6 : 7;
                    constexpr int e = g - (a * 8 - a * (a - 1) / 2), i = 2 + n / KRS;
                    mf(ca[a + e], cA[LP][a], cB[LP][e]);
                    SCHED();
                    if constexpr (n % KRS == 0 && i < NRT) ar[i] = ld128s(rs, ho[i / 10], ks[i % 10]);
                    if constexpr (n < 12) rda<40 + n>(xs_[n >> 2][n & 3]);
                    constexpr int gt_[12] = {2, 12, 22, 30, 100, 31, 32, 40, 41, 101, 42, 50};
                    if constexpr (g >= 24) gate(IC(gt_[g < 24 ? 0 : g - 24]));
                    site<3252 + g>();
                    SCHED();
                });
                static_assert(KG0 <= 15 && NRT >= 12 && DSE, "");
            } else {
#pragma unroll
                for (int i = 0; i < RING2; ++i) { ar[i] = ld128s(rs, ho[i / 10], ks[i % 10]); SCHED(); }
            }
        v4u gl[3];
        v2u f0;
        bool gearly = false;
        {
            const char* src = smem + srca;
            const char *glb1 = p.ws + 32 * 4096, *glb2 = p.ws + 48 * 4096;
            float zx[4], zd[4], zt[4], zz[5][4];
            if constexpr (STAMPS == 1) { STAMP(layer ? 50 : 40) }
            static_for<40>(LAMBDA(i_) {
                constexpr int i = i_.value, m = i / 10, s = i % 10;
                static_for<5>(LAMBDA(k_) {
                    constexpr int kk = k_.value, n = 2 * (i - 3) + kk / 2 - 3, nb = n / 8, u = n % 8, pv = i - 1 + RING2;
                    if constexpr (s == 0 && m < 3) mfa0<kk>(a2[m][kk], ar[i % RING2]); else mfa<5 * s + kk>(a2[m][kk], ar[i % RING2]);
                    SCHED();
                    constexpr int gu_[12] = {51, 52, 60, 61, 102, 62, 70, 71, 72, -1, 103, -1};
                    if constexpr (layer == 0 && 5 * i + kk < 12) gate(IC(gu_[(5 * i + kk) % 12]));
                    if constexpr (layer == 0 && i == 25 && kk == 1) { pins(glb1); pins(glb2); }
                    if constexpr (layer == 0 && i >= 3 && i < 26 && (kk & 1)) {
                        if constexpr (n < 0) swp2n(xs_[n + 3][0], xs_[n + 3][2], xs_[n + 3][1], xs_[n + 3][3]);
                        else if constexpr (nb < 5) {
                            if constexpr (u < 2) unp2(zx[2 * u], zx[2 * u + 1], xs_[nb >> 1][2 * (nb & 1) + u]);
                            else if constexpr (u < 4) unp2(zd[2 * u - 4], zd[2 * u - 3], dsk[nb][u - 2]);
                            else if constexpr (u < 6) fma2(zt[2 * u - 8], zt[2 * u - 7], zx[2 * u - 8], zx[2 * u - 7], rsr, rsr, rsm, rsm);
                            else fma2(zz[nb][2 * u - 12], zz[nb][2 * u - 11], zt[2 * u - 12], zt[2 * u - 11], zd[2 * u - 12], zd[2 * u - 11], zx[2 * u - 12], zx[2 * u - 11]);
                        }
                    }
                    if constexpr (i == 0 && kk == 0 && layer == 1) { pin(pt[0]); pin(pt[1]); pin(pt[2]); pin(pt[3]); }
                    if constexpr (layer == 0 && i < 2 && kk > 0 && 12 + 4 * i + kk - 1 >= NRT && 12 + 4 * i + kk - 1 < RING2) ar[12 + 4 * i + kk - 1] = ld128s(rs, ho[1], ks[2 + 4 * i + kk - 1]);
                    if constexpr (layer == 0 && i == kGSI && kk > 0) st128(v4u{g[2 * kk - 2][0], g[2 * kk - 2][1], g[2 * kk - 1][0], g[2 * kk - 1][1]}, rs, gso + (kk - 1) * 1024);
                    if constexpr (layer == 0 && i > 2 && i <= 7 && kk == (i == kGSI ? 0 : 2)) {
                        if constexpr (DSE) { unsigned d0_, d1_; rda<20 + 2 * (i - 3)>(d0_); rda<21 + 2 * (i - 3)>(d1_); dsk[i - 3] = v2u{d0_, d1_}; }
                    }
                    if constexpr (kk == 0 && i > 0 && pv < 40) ar[pv % RING2] = ld128s(rs, ho[pv / 10], ks[pv % 10]);
                    constexpr int s1 = s - 1 - (KMB && layer == 0 && m == 1 ? kFGI - 10 : 0);
                    if constexpr (kk == 2 && m >= 1 && s1 >= 0 && s1 < 5) *(f32x4*)(smem + dst[m - 1] + s1 * 1024) = a2[m - 1][s1];
                    site<4000 + 1000 * layer + 5 * i + kk>();
                    if constexpr (i >= 37) a2[i - 37][kk] = *(const f32x4*)(src + (i - 37) * 5120 + kk * 1024);
                    SCHED();
                });
                if constexpr (i == 10 && (layer || !KMB)) { asm volatile("s_waitcnt lgkmcnt(0)" ::: "memory"); __builtin_amdgcn_s_barrier(); asm volatile("" ::: "memory"); }
                if constexpr (i == kFGI && layer == 0) {
                    static_assert(kFGI >= 10 && kFGI <= 14, "");
                    asm volatile("s_waitcnt vmcnt(%0) lgkmcnt(0)" ::"i"(kFGI - kGSI) : "memory");
                    __builtin_amdgcn_s_barrier(); asm volatile("" ::: "memory");
                    st64(v2u{tag0, tag1}, rs, fgo1);
                }
                if constexpr (i == 26 && layer == 0) {
#pragma unroll
                    for (int nb = 0; nb < 5; ++nb) { a2[3][nb] = f32x4{zz[nb][0], zz[nb][1], zz[nb][2], zz[nb][3]}; pin(a2[3][nb]); }
                }
                if constexpr (i == kGPI && layer == 0) gld2s<0>(f0, fgo, p.ws);
                if constexpr (i == kGCI && layer == 0) {
                    wait1(f0);
                    static_for<39 - i>(LAMBDA(r_) { pin(ar[(i + 1 + r_.value) % RING2]); });
                    gearly = __builtin_amdgcn_ballot_w64(!ISTAG(f0)) == 0ull;
                    ORDER();
                    undef3(gl);
                    if (__builtin_expect(gearly, 1)) { gld4t<0>(gl[0], glo, p.ws); gld4t<0>(gl[1], glo, glb1); gld4t<0>(gl[2], glo, glb2); }
                    else if (G2ND) poll2(fgo, fgo, p.ws);
                }
                if constexpr (i == 36) {
                    asm volatile("s_waitcnt lgkmcnt(0)" ::: "memory");
                    __builtin_amdgcn_s_barrier(); asm volatile("" ::: "memory");
                }
                if constexpr (STAMPS == 1 && (i == 0 || i == 9 || i == 10 || i == 15 || i == 25 || i == 35 || i == 36)) { STAMP((layer ? 51 : 41) + (i == 0 ? 0 : i == 9 ? 1 : i == 10 ? 2 : i == 15 ? 3 : i == 25 ? 4 : i == 35 ? 5 : 6)) }
                SCHED();
            });
            STAMP(layer ? 18 : 8)
        }
#pragma unroll
        for (int nb = 0; nb < 5; ++nb) a2[0][nb] = (a2[0][nb] + a2[1][nb]) + a2[2][nb];
        asm volatile("s_nop 1" : "+v"(a2[3][0]), "+v"(a2[3][1]), "+v"(a2[3][2]), "+v"(a2[3][3]), "+v"(a2[3][4]),
                     "+v"(a2[0][0]), "+v"(a2[0][1]), "+v"(a2[0][2]), "+v"(a2[0][3]), "+v"(a2[0][4]));
        float ty;
        {
            f32x4 s1, s2;
#pragma unroll
            for (int nb = 0; nb < 5; ++nb) {
                const f32x4 y = a2[3][nb] + a2[0][nb];
                xin[nb] = y;
                if (nb == 0) { s1 = y; s2 = y * y; } else { s1 += y; s2 += y * y; }
            }
            ty = xsum2h(hsum4(s1), hsum4(s2));
            st64(v2u{__float_as_uint(ty), tag0}, rs, sgs);
        }
        SCHED();
        STAMP(layer ? 19 : 9)
        int lane_o, b_o = b0, w_o = w0;
        asm volatile("v_mbcnt_lo_u32_b32 %0, -1, 0\n\tv_mbcnt_hi_u32_b32 %0, -1, %0" : "=v"(lane_o));
        asm volatile("" : "+s"(b_o), "+s"(w_o));
        const int lane = lane_o, b = b_o, w = w_o;
        const int c = lane & 15, q = lane >> 4;
        TILES()
        if (layer == 0 && __builtin_expect(!gearly, 0)) {
            if (G2ND) {
                unsigned f0_, f1_;
                asm volatile("s_waitcnt vmcnt(1)\n\t" AR("%0", 36, 0) AR("%1", 36, 1) : "=v"(f0_), "=v"(f1_)::"memory");
                f0 = v2u{f0_, f1_};
            }
            WAVE_RETRY(ISTAG(f0), f0 = ld64(rs, fgo))
            gld4t<0>(gl[0], glo, p.ws); gld4t<0>(gl[1], glo, p.ws + 32 * 4096); gld4t<0>(gl[2], glo, p.ws + 48 * 4096);
            asm volatile("s_waitcnt vmcnt(0)" : "+v"(gl[0]), "+v"(gl[1]), "+v"(gl[2])::"memory");
        }
        if (layer == 0) {
            asm volatile("s_waitcnt vmcnt(1)" : "+v"(gl[0]), "+v"(gl[1]), "+v"(gl[2])::"memory");
            char* tb = smem + SC + w * INBOX;
            *(v4u*)(tb + lane * 16) = gl[0]; *(v4u*)(tb + 1024 + lane * 16) = gl[1]; *(v4u*)(tb + 1536 + lane * 16) = gl[2];
#pragma unroll
            for (int nb = 0; nb < 5; ++nb) gt[nb] = __builtin_bit_cast(v2u, trread(tb, (16 * nb + 4 * q + (c >> 2)) * 32 + (c & 3) * 8));
        } else ACC_TIE5(gt);
        SCHED();
        f32x4 gf[5], yg[5];
#pragma unroll
        for (int nb = 0; nb < 5; ++nb) { gf[nb] = unpk4(gt[nb]); yg[nb] = xin[nb] * gf[nb]; pin(gf[nb]); pin(yg[nb]); }
        unsigned xs = OFF_X2F + (unsigned)((2 * mt2 + (w >> 1)) * 40960 + ((c >> 2) & 1) * 20480 + (10 * nt2 + 2 * (q & 1) + (q >> 1)) * 256 + (8 * (w & 1) + 4 * (c >> 3) + (c & 3)) * 16);
        unsigned pso = q < 2 ? OFF_PS + (unsigned)(((mt2 * 4 + w) * 8 + nt2) * 128 + c * 8 + q * 4) : kNone;
        unsigned fxo1 = lane ? kNone : OFF_FX + (unsigned)(((mt1 * 4 + 2 * (mt2 & 1) + (w >> 1)) * 16 + nt2 * 2 + (w & 1)) * 128);
        unsigned oo = (unsigned)(((64 * mt2 + 16 * w + c) * D_ + 80 * nt2 + 4 * q) * 4);
        if (layer == 0) { pin(xs); pin(pso); pin(fxo1); } else pin(oo);
        float c_r, c_m, c_rr, c_rm, c_mm;
        if constexpr (layer == 0 && PSA) {
            f32x4 su = yg[0], sg_ = gf[0], syu = xin[0] * yg[0], suu = yg[0] * yg[0], sug = yg[0] * gf[0], sgg = gf[0] * gf[0];
#pragma unroll
            for (int nb = 1; nb < 5; ++nb) { su += yg[nb]; sg_ += gf[nb]; syu += xin[nb] * yg[nb]; suu += yg[nb] * yg[nb]; sug += yg[nb] * gf[nb]; sgg += gf[nb] * gf[nb]; }
            const float hu = hsum4(su);
            c_r = xsum2h(hu, 2.f * hsum4(syu)); c_m = xsum2h(hsum4(sg_), 2.f * hu);
            c_rr = xsum2h(0.f, hsum4(suu)); c_rm = xsum2h(0.f, 2.f * hsum4(sug)); c_mm = xsum2h(0.f, hsum4(sgg));
            pin(c_r); pin(c_m); pin(c_rr); pin(c_rm); pin(c_mm);
        }
        SCHED();
        {
            float a, d;
            if constexpr (layer == 0) { skip<SKIP4>(); site<9>(); }
            pipe_gran<layer ? PDS : PDS0>(a, d, sgo, p.ws, tag0, dead);
            STAMP(layer ? 20 : 10)
            xsum2(a, d);
            const float me = a * (1.0f / D_);
            const float ri = __builtin_amdgcn_rsqf(fmaxf(d * (1.0f / D_) - me * me, 0.f) + 1e-5f), mr = -me * ri;
            if constexpr (layer == 0 && PSA) {
                const float ps_ = ty + ri * (c_r + ri * c_rr + mr * c_rm) + mr * (c_m + mr * c_mm);
                __builtin_amdgcn_raw_buffer_store_b32(__float_as_uint(ps_), rs, pso, 0, SC1);
                SCHED();
            }
#pragma unroll
            for (int nb = 0; nb < 5; ++nb) xin[nb] = yg[nb] * ri + (gf[nb] * mr + xin[nb]);
        }
        if constexpr (layer == 0) {
            v2u pk[5];
#pragma unroll
            for (int nb = 0; nb < 5; ++nb) { const f32x4 x = xin[nb]; pk[nb] = v2u{cvtpk(x.x, x.y), cvtpk(x.z, x.w)}; }
#pragma unroll
            for (int i = 0; i < 3; ++i) {
                const v2u pa = pk[2 * i], pb = pk[i < 2 ? 2 * i + 1 : 4];
                const auto r0 = __builtin_amdgcn_permlane16_swap(pa.x, pb.x, false, false);
                const auto r1 = __builtin_amdgcn_permlane16_swap(pa.y, pb.y, false, false);
                if (i < 2 || (q & 1) == 0) st128(v4u{r0[0], r1[0], r0[1], r1[1]}, rs, xs + i * 1024);
            }
            if constexpr (!PSA) {
                f32x4 s1, s2;
#pragma unroll
                for (int nb = 0; nb < 5; ++nb) { const f32x4 x = xin[nb]; if (nb == 0) { s1 = x; s2 = x * x; } else { s1 += x; s2 += x * x; } }
                __builtin_amdgcn_raw_buffer_store_b32(__float_as_uint(xsum2h(hsum4(s1), hsum4(s2))), rs, pso, 0, SC1);
            }
            SCHED();
            int w1lane0 = (c & 1) * 10240 + (c >> 1) * 128 + ((q ^ ((c & 1) | ((c >> 2) << 1))) * 16);
            int w1lane1 = (c & 1) * 10240 + (c >> 1) * 128 + (((4 + q) ^ ((c & 1) | ((c >> 2) << 1))) * 16);
            unsigned fxo = OFF_FX + (unsigned)((mt1 * 4 + w) * 2048 + c * 128);
            unsigned xo = OFF_X2F + (unsigned)((4 * mt1 + w) * 40960 + lane * 16), xo1 = xo + 20480u;
            const int row = 8 * q + (c & 7);
            unsigned po = OFF_PS + (unsigned)((((2 * mt1 + (w >> 1)) * 4 + 2 * (w & 1) + (row >> 4)) * 8 + 4 * (c >> 3)) * 128 + (row & 15) * 8);
            int w1hi0 = w1lane0 + 61440, w1hi1 = w1lane1 + 61440;
            pin(w1lane0); pin(w1lane1); pin(w1hi0); pin(w1hi1); pin(fxo); pin(xo); pin(xo1); pin(po);
            v4u wf[2][5];
#pragma unroll
            for (int nb = 0; nb < 5; ++nb) wf[0][nb] = *(const v4u*)(smem + w1lane0 + nb * 20480);
            v2u tg2 = {tag0, tag1};
            pin(tg2);
            SCHED();
            DRAIN();
            st64(tg2, rs, fxo1);
            if (b == 255 && w == 0) FLAG(OFF_NONCE)
            STAMP(11)
            if (w == 0) tour<4>();
            addrs(std::integral_constant<int, 1>{}, lane, w, b);
            v2u pt[4];
            skip<SKIP5>();
            pipe_flag<KPFX>(pt, fxo, p.ws, (u64)tag0 | ((u64)tag1 << 32), dead);
            ORDER();
            STAMP(12)
            v4u ar[RING1][2];
#pragma unroll
            for (int s = 0; s < RING1; ++s)
#pragma unroll
                for (int mb = 0; mb < 2; ++mb) { ar[s][mb] = ld128s(rs, mb ? xo1 : xo, s * 1024); SCHED(); }
            v2u ps[4];
#pragma unroll
            for (int i = 0; i < 4; ++i) ps[i] = ld64(rs, po + i * 128);
            SCHED();
            float t1, t2, rinv, mri, rr_[8], rm_[8];
            static_for<20>(LAMBDA(s_) {
                constexpr int s = s_.value, cu = s & 1, nx = cu ^ 1;
                LGKM0() SCHED();
                static_for<10>(LAMBDA(g_) {
                    constexpr int g = g_.value, mb = g / 5, k = g % 5;
                    if constexpr (s == 0) mf0(acc[mb][k], ar[0][mb], wf[0][k]); else mf(acc[mb][k], ar[s % RING1][mb], wf[cu][k]);
                    SCHED();
                    if constexpr (s == 0 && g == 0) { pin(pt[0]); pin(pt[1]); pin(pt[2]); pin(pt[3]); }
                    if constexpr (s < 19 && g < 3) wf[nx][g] = *(const v4u*)(smem + (nx ? w1lane1 : w1lane0) + g * 20480 + ((s + 1) >> 1) * 1024);
                    if constexpr (s < 19 && (g == 3 || g == 4)) wf[nx][g] = *(const v4u*)(smem + (nx ? w1hi1 : w1hi0) + (g - 3) * 20480 + ((s + 1) >> 1) * 1024);
                    if constexpr (g == 5 && s + RING1 < 20) ar[s % RING1][0] = ld128s(rs, xo, (s + RING1) * 1024);
                    if constexpr (g == 2 && s > 0 && s - 1 + RING1 < 20) ar[(s - 1) % RING1][1] = ld128s(rs, xo1, (s - 1 + RING1) * 1024);
                    if constexpr (s == 8 && g == 8) { t1 = __uint_as_float(ps[0].x) + __uint_as_float(ps[1].x); pin(t1); t2 = __uint_as_float(ps[0].y) + __uint_as_float(ps[1].y); pin(t2); }
                    if constexpr (s == 9 && g == 6) { t1 += __uint_as_float(ps[2].x); pin(t1); t2 += __uint_as_float(ps[2].y); pin(t2); }
                    if constexpr (s == 9 && g == 7) { t1 += __uint_as_float(ps[3].x); pin(t1); t2 += __uint_as_float(ps[3].y); pin(t2); }
                    if constexpr (s == 10 && g == 6) { t1 += dpp<0x128>(t1); t2 += dpp<0x128>(t2); pin(t1); pin(t2); }
                    if constexpr (s == 10 && g == 7) { t1 *= (1.0f / D_); pin(t1); t2 *= (1.0f / D_); pin(t2); }
                    if constexpr (s == 10 && g == 8) { t2 = fmaxf(t2 - t1 * t1, 0.f); pin(t2); }
                    if constexpr (s == 11 && g == 6) { rinv = __builtin_amdgcn_rsqf(t2 + 1e-5f); pin(rinv); }
                    if constexpr (s == 11 && g == 8) { mri = -t1 * rinv; pin(mri); }
                    if constexpr (s == 12 && g == 4) *(float*)(smem + l_rs(1, 0) + w * INBOX + lane * 4) = c < 8 ? rinv : mri;
                    if constexpr (s >= 12 && s < 15 && g >= 6 && g < 9 && 3 * (s - 12) + g - 6 < 8) bc2b<3 * (s - 12) + g - 6>(rr_[3 * (s - 12) + g - 6], rm_[3 * (s - 12) + g - 6], rinv, mri);
                    if constexpr (s >= 15 && s < 19 && g >= 5) TT_GAP(5 * (s - 15) + g - 5)
                    site<6000 + 10 * s + g>();
                    SCHED();
                });
            });
            rr[0] = f32x4{rr_[0], rr_[1], rr_[2], rr_[3]}; rr[1] = f32x4{rr_[4], rr_[5], rr_[6], rr_[7]};
            asm volatile("kpf_end2:");
            ACC_FENCE10(acc[0], acc[1]);
            STAMP(13)
        } else {
#pragma unroll
            for (int nb = 0; nb < 5; ++nb) __builtin_amdgcn_raw_buffer_store_b128(__builtin_bit_cast(v4u, xin[nb]), ro, oo + 64 * nb, 0, OUTP);
            STAMP(21)
            STAMP_DUMP()
            asm volatile("kpf_end:");
        }
    };
    layer_body(std::integral_constant<int, 0>{});
    layer_body(std::integral_constant<int, 1>{});
}

extern "C" void kernel_launch(void* const* d_in, const int* in_sizes, int n_in,
                              void* d_out, int out_size, void* d_ws, size_t ws_size,
                              hipStream_t stream) {
    Params p;
    p.x0 = (const u16*)d_in[0];
    p.Lre = (const u16*)d_in[1];
    p.Lim = (const u16*)d_in[2];
    p.Bre = (const u16*)d_in[3];
    p.Bim = (const u16*)d_in[4];
    p.Cre = (const u16*)d_in[5];
    p.Cim = (const u16*)d_in[6];
    p.logdt = (const u16*)d_in[7];
    p.Dskip = (const u16*)d_in[8];
    p.k = (const u16*)d_in[10];
    p.v = (const u16*)d_in[11];
    p.shk = (const u16*)d_in[12];
    p.ssmk = (const u16*)d_in[13];
    p.ssmD = (const u16*)d_in[14];
    p.ws = (char*)d_ws + (size_t)WSOFF * 1048576;
    p.out = (float*)d_out;
    k_main<<<256, 256, 0, stream>>>(p);
}
```

```cpp
#include <hip/hip_runtime.h>
#include <stdint.h>
#include <utility>

#define STAMPS 0

typedef unsigned short u16;
typedef unsigned long long u64;
typedef unsigned v4u __attribute__((ext_vector_type(4)));
typedef unsigned v2u __attribute__((ext_vector_type(2)));
typedef float f32x4 __attribute__((ext_vector_type(4)));
typedef float f32x2 __attribute__((ext_vector_type(2)));
typedef short s16x4 __attribute__((ext_vector_type(4)));
typedef __bf16 bf16x2v __attribute__((ext_vector_type(2)));
#define LAS __attribute__((address_space(3)))
#define DI __device__ __forceinline__

constexpr int S_ = 2048, D_ = 640;
#define KG0 12
#define KRT 12
#define KRS 2
#define KB1 4
#define KMB 1
#define SBT 2
#define LAG 1
#define KR1 5
#define KR2 10
constexpr int RING1 = KR1;
#define KCR1 3
constexpr int kCR1 = KCR1;
#define KXA 10
#define KN3E 17
#define KN3 6
constexpr int kXA = KXA;
constexpr int kN3E = KN3E, kN3 = KN3;
#define KFHK 4
#define KFHP 12
constexpr int kFHK = KFHK, kFHP = KFHP;
#define KGPI 27
#define KGCI 33
#define KFGI 11
#define G2ND 1
#define KGSI 2
constexpr int kGSI = KGSI, kFGI = KFGI, kGPI = KGPI, kGCI = KGCI;

constexpr unsigned OFF_NONCE = 0;
constexpr unsigned OFF_ST = 4096;
constexpr unsigned OFF_FW = OFF_ST + 65536;
constexpr unsigned OFF_FK = OFF_FW + 131072;
constexpr unsigned OFF_FH = OFF_FK + 81920;
constexpr unsigned OFF_FX = OFF_FH + 262144;
constexpr unsigned OFF_FG = OFF_FX + 131072;
constexpr unsigned OFF_EG = OFF_FG + 98304;
constexpr unsigned OFF_SG = OFF_EG + 327680;
constexpr unsigned OFF_PS = OFF_SG + 524288;
constexpr unsigned OFF_KVT = OFF_PS + 131072;
constexpr unsigned OFF_GT = OFF_KVT + 2621440;
constexpr unsigned OFF_W2F = OFF_GT + 2621440;
constexpr unsigned OFF_X2F = OFF_W2F + 1638400;
constexpr unsigned OFF_DUM = OFF_X2F + 2621440;
constexpr unsigned OFF_HF = OFF_DUM + 163840;
constexpr unsigned WS_BYTES = OFF_HF + 2 * 5242880;
constexpr unsigned kNone = 0x40000000u;
constexpr unsigned OFF_DBG = 0x1800000;
#define MAP 0
#define TILES() const int xq = b & 7, j = b >> 3, mt2 = MAP ? 8 * (xq >> 1) + (j >> 2) : 4 * xq + (j >> 3), nt2 = MAP ? 4 * (xq & 1) + (j & 3) : j & 7, mt1 = mt2 >> 1, \
    nt1 = MAP ? 8 * (xq & 1) + (j & 7) : j & 15, i32 = 16 * (mt2 & 1) + mt1;

constexpr int SC = 102400;
constexpr int REGSZ = 13056;
constexpr int L_XR = SC;
constexpr int L_CO = SC + 12288;
constexpr int L_KB = SC + 51712;
constexpr int L_TAB = SC + 55808;
constexpr int L_CW = SC + 39168;
constexpr int INBOX = 15360;
constexpr int l_rs(int l, int w) { return l ? SC + w * INBOX : L_KB + w * 256; }
constexpr int l_sg(int l, int w) { return l ? SC + w * INBOX + 256 : SC + (w == 0 ? 43264 : w == 1 ? 46080 : w == 2 ? 52736 : 56320); }
constexpr int l_tb(int l, int w) { return l ? SC + w * INBOX + 2048 : SC + 40960 + w * 5120; }
constexpr int LDS_BYTES = 163840;
constexpr int CV_E = 4608;
constexpr int CV_O = CV_E + (1056 + 15) * 4;

struct Params {
    const u16 *x0, *Lre, *Lim, *Bre, *Bim, *Cre, *Cim, *logdt, *Dskip, *k, *v, *shk, *ssmk, *ssmD;
    char* ws;
    float* out;
};

#define MF "v_mfma_f32_16x16x32_bf16 "
DI void mf(f32x4& c, v4u a, v4u b) { asm(MF "%0, %1, %2, %0" : "+v"(c) : "v"(a), "v"(b)); }
DI void mf0(f32x4& c, v4u a, v4u b) { asm(MF "%0, %1, %2, 0" : "=&v"(c) : "v"(a), "v"(b)); }
constexpr int W2R = 56;
#define WA(n) "a[%c" #n ":%c" #n "+3]"
template <int R> DI void mfa(f32x4& c, v4u a) { asm(MF "%0, " WA(2) ", %1, %0" : "+v"(c) : "v"(a), "i"(W2R + 4 * R)); }
template <int R> DI void mfa0(f32x4& c, v4u a) { asm(MF "%0, " WA(2) ", %1, 0" : "=&v"(c) : "v"(a), "i"(W2R + 4 * R)); }
#define ACC_FENCE10(c, d) asm volatile("" : "+v"(c[0]), "+v"(c[1]), "+v"(c[2]), "+v"(c[3]), "+v"(c[4]), "+v"(d[0]), "+v"(d[1]), "+v"(d[2]), "+v"(d[3]), "+v"(d[4]))
#define ACC_TIE5(c) asm volatile("" : "+v"(c[0]), "+v"(c[1]), "+v"(c[2]), "+v"(c[3]), "+v"(c[4]))
#define QP " quad_perm:[1,0,3,2] row_mask:0xf bank_mask:0xf"
DI void cstep(float (&t)[5], const float (&h)[5], const float (&lr)[5], const float (&ls)[5], float b0, float b1, float b2, float b3, float b4) {
    asm("v_fma_f32 %0, %10, %5, %20\n\tv_fma_f32 %1, %11, %6, %21\n\tv_fma_f32 %2, %12, %7, %22\n\tv_fma_f32 %3, %13, %8, %23\n\tv_fma_f32 %4, %14, %9, %24\n\t"
        "v_fmac_f32_dpp %0, %5, %15" QP "\n\tv_fmac_f32_dpp %1, %6, %16" QP "\n\tv_fmac_f32_dpp %2, %7, %17" QP "\n\tv_fmac_f32_dpp %3, %8, %18" QP "\n\tv_fmac_f32_dpp %4, %9, %19" QP
        : "=&v"(t[0]), "=&v"(t[1]), "=&v"(t[2]), "=&v"(t[3]), "=&v"(t[4])
        : "v"(h[0]), "v"(h[1]), "v"(h[2]), "v"(h[3]), "v"(h[4]), "v"(lr[0]), "v"(lr[1]), "v"(lr[2]), "v"(lr[3]), "v"(lr[4]),
          "v"(ls[0]), "v"(ls[1]), "v"(ls[2]), "v"(ls[3]), "v"(ls[4]), "v"(b0), "v"(b1), "v"(b2), "v"(b3), "v"(b4));
}
DI void cstep3(float* t, const float* h, const float* lr, const float* ls, const float* b) {
    asm("v_fma_f32 %0, %6, %3, %12\n\tv_fma_f32 %1, %7, %4, %13\n\tv_fma_f32 %2, %8, %5, %14\n\t"
        "v_fmac_f32_dpp %0, %3, %9" QP "\n\tv_fmac_f32_dpp %1, %4, %10" QP "\n\tv_fmac_f32_dpp %2, %5, %11" QP
        : "=&v"(t[0]), "=&v"(t[1]), "=&v"(t[2])
        : "v"(h[0]), "v"(h[1]), "v"(h[2]), "v"(lr[0]), "v"(lr[1]), "v"(lr[2]), "v"(ls[0]), "v"(ls[1]), "v"(ls[2]), "v"(b[0]), "v"(b[1]), "v"(b[2]));
}
template <int o> DI void chain24(float (*t)[3], const float (*b)[5], const float* lr, const float* ls) {
    asm("v_fma_f32 %0, %18, %16, %8\n\t" "v_fma_f32 %1, %19, %17, %9\n\t" "v_fmac_f32_dpp %0, %16, %20" QP "\n\t" "v_fmac_f32_dpp %1, %17, %21" QP "\n\t"
        "v_fma_f32 %2, %18, %0, %10\n\t" "v_fma_f32 %3, %19, %1, %11\n\t" "v_fmac_f32_dpp %2, %0, %20" QP "\n\t" "v_fmac_f32_dpp %3, %1, %21" QP "\n\t"
        "v_fma_f32 %4, %18, %2, %12\n\t" "v_fma_f32 %5, %19, %3, %13\n\t" "v_fmac_f32_dpp %4, %2, %20" QP "\n\t" "v_fmac_f32_dpp %5, %3, %21" QP "\n\t"
        "v_fma_f32 %6, %18, %4, %14\n\t" "v_fma_f32 %7, %19, %5, %15\n\t" "v_fmac_f32_dpp %6, %4, %20" QP "\n\t" "v_fmac_f32_dpp %7, %5, %21" QP "\n\t"
        : "=&v"(t[1][0]), "=&v"(t[1][1]), "=&v"(t[2][0]), "=&v"(t[2][1]), "=&v"(t[3][0]), "=&v"(t[3][1]), "=&v"(t[4][0]), "=&v"(t[4][1])
        : "v"(b[0][o + 0]), "v"(b[0][o + 1]), "v"(b[1][o + 0]), "v"(b[1][o + 1]), "v"(b[2][o + 0]), "v"(b[2][o + 1]), "v"(b[3][o + 0]), "v"(b[3][o + 1]), "v"(t[0][0]), "v"(t[0][1]), "v"(lr[o + 0]), "v"(lr[o + 1]), "v"(ls[o + 0]), "v"(ls[o + 1]));
}
template <int o> DI void chain33(float (*t)[3], const float (*b)[5], const float* lr, const float* ls) {
    asm("v_fma_f32 %0, %21, %18, %9\n\t" "v_fma_f32 %1, %22, %19, %10\n\t" "v_fma_f32 %2, %23, %20, %11\n\t" "v_fmac_f32_dpp %0, %18, %24" QP "\n\t" "v_fmac_f32_dpp %1, %19, %25" QP "\n\t" "v_fmac_f32_dpp %2, %20, %26" QP "\n\t"
        "v_fma_f32 %3, %21, %0, %12\n\t" "v_fma_f32 %4, %22, %1, %13\n\t" "v_fma_f32 %5, %23, %2, %14\n\t" "v_fmac_f32_dpp %3, %0, %24" QP "\n\t" "v_fmac_f32_dpp %4, %1, %25" QP "\n\t" "v_fmac_f32_dpp %5, %2, %26" QP "\n\t"
        "v_fma_f32 %6, %21, %3, %15\n\t" "v_fma_f32 %7, %22, %4, %16\n\t" "v_fma_f32 %8, %23, %5, %17\n\t" "v_fmac_f32_dpp %6, %3, %24" QP "\n\t" "v_fmac_f32_dpp %7, %4, %25" QP "\n\t" "v_fmac_f32_dpp %8, %5, %26" QP "\n\t"
        : "=&v"(t[1][0]), "=&v"(t[1][1]), "=&v"(t[1][2]), "=&v"(t[2][0]), "=&v"(t[2][1]), "=&v"(t[2][2]), "=&v"(t[3][0]), "=&v"(t[3][1]), "=&v"(t[3][2])
        : "v"(b[0][o + 0]), "v"(b[0][o + 1]), "v"(b[0][o + 2]), "v"(b[1][o + 0]), "v"(b[1][o + 1]), "v"(b[1][o + 2]), "v"(b[2][o + 0]), "v"(b[2][o + 1]), "v"(b[2][o + 2]), "v"(t[0][0]), "v"(t[0][1]), "v"(t[0][2]), "v"(lr[o + 0]), "v"(lr[o + 1]), "v"(lr[o + 2]), "v"(ls[o + 0]), "v"(ls[o + 1]), "v"(ls[o + 2]));
}
template <int o> DI void chain32(float (*t)[3], const float (*b)[5], const float* lr, const float* ls) {
    asm("v_fma_f32 %0, %15, %12, %6\n\t" "v_fma_f32 %1, %16, %13, %7\n\t" "v_fma_f32 %2, %17, %14, %8\n\t" "v_fmac_f32_dpp %0, %12, %18" QP "\n\t" "v_fmac_f32_dpp %1, %13, %19" QP "\n\t" "v_fmac_f32_dpp %2, %14, %20" QP "\n\t"
        "v_fma_f32 %3, %15, %0, %9\n\t" "v_fma_f32 %4, %16, %1, %10\n\t" "v_fma_f32 %5, %17, %2, %11\n\t" "v_fmac_f32_dpp %3, %0, %18" QP "\n\t" "v_fmac_f32_dpp %4, %1, %19" QP "\n\t" "v_fmac_f32_dpp %5, %2, %20" QP "\n\t"
        : "=&v"(t[1][0]), "=&v"(t[1][1]), "=&v"(t[1][2]), "=&v"(t[2][0]), "=&v"(t[2][1]), "=&v"(t[2][2])
        : "v"(b[0][o + 0]), "v"(b[0][o + 1]), "v"(b[0][o + 2]), "v"(b[1][o + 0]), "v"(b[1][o + 1]), "v"(b[1][o + 2]), "v"(t[0][0]), "v"(t[0][1]), "v"(t[0][2]), "v"(lr[o + 0]), "v"(lr[o + 1]), "v"(lr[o + 2]), "v"(ls[o + 0]), "v"(ls[o + 1]), "v"(ls[o + 2]));
}
DI void pstep(f32x2& xr, f32x2& xs, f32x2 pr, f32x2 ps, f32x2 m) {
    asm("v_pk_mul_f32 %0, %2, %4 op_sel_hi:[1,0]\n\tv_pk_mul_f32 %1, %3, %4 op_sel_hi:[1,0]\n\t"
        "v_pk_fma_f32 %0, %3, %4, %0 op_sel:[0,1,0] neg_lo:[1,0,0] neg_hi:[1,0,0]\n\tv_pk_fma_f32 %1, %2, %4, %1 op_sel:[0,1,0]"
        : "=&v"(xr), "=&v"(xs) : "v"(pr), "v"(ps), "v"(m));
}
DI void padd(f32x2 (&h)[4], const f32x2 (&pr)[2], const f32x2 (&ps)[2], f32x2 c) {
    f32x2 d;
    asm("v_pk_mul_f32 %4, %6, %9 op_sel:[1,0]\n\tv_pk_fma_f32 %0, %5, %9, %0 op_sel_hi:[1,0,1]\n\tv_pk_fma_f32 %1, %6, %9, %1 op_sel_hi:[1,0,1]\n\t"
        "v_pk_fma_f32 %4, %8, %9, %4 op_sel:[1,1,0] op_sel_hi:[1,0,1] neg_hi:[1,0,0]\n\tv_pk_fma_f32 %0, %7, %9, %0 op_sel:[0,1,0]\n\tv_pk_fma_f32 %1, %8, %9, %1 op_sel:[0,1,0]\n\t"
        "v_pk_fma_f32 %2, %5, %4, %2 op_sel_hi:[1,0,1]\n\tv_pk_fma_f32 %3, %6, %4, %3 op_sel_hi:[1,0,1]\n\tv_pk_fma_f32 %2, %7, %4, %2 op_sel:[0,1,0]\n\tv_pk_fma_f32 %3, %8, %4, %3 op_sel:[0,1,0]"
        : "+v"(h[0]), "+v"(h[1]), "+v"(h[2]), "+v"(h[3]), "=&v"(d) : "v"(pr[0]), "v"(pr[1]), "v"(ps[0]), "v"(ps[1]), "v"(c));
}
DI void cstepi(float& b0, float& b1, float& b2, float& b3, float& b4, float h0, float h1, float h2, float h3, float h4, const float (&lr)[5], const float (&ls)[5]) {
    asm("v_fmac_f32 %0, %10, %5\n\tv_fmac_f32 %1, %11, %6\n\tv_fmac_f32 %2, %12, %7\n\tv_fmac_f32 %3, %13, %8\n\tv_fmac_f32 %4, %14, %9\n\t"
        "v_fmac_f32_dpp %0, %5, %15" QP "\n\tv_fmac_f32_dpp %1, %6, %16" QP "\n\tv_fmac_f32_dpp %2, %7, %17" QP "\n\tv_fmac_f32_dpp %3, %8, %18" QP "\n\tv_fmac_f32_dpp %4, %9, %19" QP
        : "+v"(b0), "+v"(b1), "+v"(b2), "+v"(b3), "+v"(b4)
        : "v"(h0), "v"(h1), "v"(h2), "v"(h3), "v"(h4), "v"(lr[0]), "v"(lr[1]), "v"(lr[2]), "v"(lr[3]), "v"(lr[4]), "v"(ls[0]), "v"(ls[1]), "v"(ls[2]), "v"(ls[3]), "v"(ls[4]));
}
#define UNDEF8(c) asm volatile("" : "=v"(c[0]), "=v"(c[1]), "=v"(c[2]), "=v"(c[3]), "=v"(c[4]), "=v"(c[5]), "=v"(c[6]), "=v"(c[7]))
#define PIN5(t) asm volatile("s_nop 1" : "+v"(t[0]), "+v"(t[1]), "+v"(t[2]), "+v"(t[3]), "+v"(t[4]))
DI unsigned cvtpk(float lo, float hi) {
    f32x2 v = {lo, hi};
    return __builtin_bit_cast(unsigned, __builtin_convertvector(v, bf16x2v));
}
DI float lo16f(unsigned u) { return __uint_as_float(u << 16); }
DI float hi16f(unsigned u) { return __uint_as_float(u & 0xffff0000u); }
DI float rbf(float v) { return lo16f(cvtpk(v, 0.f)); }
template <int CTRL> DI float dpp(float x) {
    return __int_as_float(__builtin_amdgcn_update_dpp(0, __float_as_int(x), CTRL, 0xF, 0xF, true));
}
DI float rowsum16(float x) {
    x += dpp<0x128>(x); x += dpp<0x124>(x); x += dpp<0x122>(x); x += dpp<0x121>(x);
    return x;
}
DI float xsum32(float x) { auto r = __builtin_amdgcn_permlane32_swap(__float_as_uint(x), __float_as_uint(x), false, false); return __uint_as_float(r[0]) + __uint_as_float(r[1]); }
DI void xsum2(float& a, float& d) {
    auto r = __builtin_amdgcn_permlane16_swap(__float_as_uint(a), __float_as_uint(d), false, false);
    const float s = __uint_as_float(r[0]) + __uint_as_float(r[1]);
    const float t = xsum32(s);
    auto u = __builtin_amdgcn_permlane16_swap(__float_as_uint(t), __float_as_uint(t), false, false);
    a = __uint_as_float(u[0]); d = __uint_as_float(u[1]);
}
DI float xsum2h(float a, float d) {
    auto r = __builtin_amdgcn_permlane16_swap(__float_as_uint(a), __float_as_uint(d), false, false);
    return xsum32(__uint_as_float(r[0]) + __uint_as_float(r[1]));
}
DI float hsum4(f32x4 v) { return (v.x + v.y) + (v.z + v.w); }
typedef __amdgpu_buffer_rsrc_t rsrc_t;
constexpr int SC1 = 16;
DI v4u ld128(rsrc_t rs, unsigned off) { return __builtin_amdgcn_raw_buffer_load_b128(rs, off, 0, SC1); }
DI v4u ld128s(rsrc_t rs, unsigned off, unsigned soff) { return __builtin_amdgcn_raw_buffer_load_b128(rs, off, soff, SC1); }
DI v2u ld64(rsrc_t rs, unsigned off) { return __builtin_amdgcn_raw_buffer_load_b64(rs, off, 0, SC1); }
DI void st128(v4u v, rsrc_t rs, unsigned off) { __builtin_amdgcn_raw_buffer_store_b128(v, rs, off, 0, SC1); }
DI void st64(v2u v, rsrc_t rs, unsigned off) { __builtin_amdgcn_raw_buffer_store_b64(v, rs, off, 0, SC1); }
#define LGKM0() { __builtin_amdgcn_s_waitcnt(0xC07F); asm volatile("" ::: "memory"); }
#define DRAIN() asm volatile("s_waitcnt vmcnt(0)" ::: "memory")
#define ORDER() __builtin_amdgcn_fence(__ATOMIC_ACQUIRE, "wavefront")
#define SCHED() __builtin_amdgcn_sched_barrier(0)
constexpr unsigned kSpinCap = 1u << 17;
#define WAVE_WAIT(...)                                                      \
    {                                                                       \
        unsigned spins_ = dead ? kSpinCap : 0u;                             \
        _Pragma("nounroll") for (;;) {                                      \
            asm volatile("" ::: "memory");                                  \
            bool ok;                                                        \
            __VA_ARGS__;                                                    \
            if (__builtin_amdgcn_ballot_w64(!ok) == 0ull) break;            \
            if (++spins_ > kSpinCap) { dead = true; break; }                \
            __builtin_amdgcn_s_sleep(1);                                    \
        }                                                                   \
        ORDER();                                                            \
    }
#define KPD1 2
#define KPDS 6
#define KPDS0 0
#define KPF0 14
#define KPFX 6
#define KPF1 4
constexpr int PD1 = KPD1, PDS = KPDS, PDS0 = KPDS0;
#define PL(r) "global_load_dwordx2 " r ", %5, %6 sc1\n\t"
#define PS_(r) "s_waitcnt vmcnt(1)\n\tv_cmp_ne_u64 vcc, %7, " r "\n\ts_cbranch_vccz 2f\n\t" PL(r)
template <int D0> DI void pipe_flag(v2u (&t)[4], unsigned off, const void* base, u64 tag, bool& dead) {
    unsigned n = dead ? 0u : kSpinCap;
    asm volatile("s_sleep %8\n\t" PL("%0") "s_sleep %9\n\t" PL("%1")
                 "1:\n\t" PS_("%0") PS_("%1") "s_sub_u32 %4, %4, 1\n\ts_cbranch_scc0 1b\n2:"
                 : "=&v"(t[0]), "=&v"(t[1]), "=&v"(t[2]), "=&v"(t[3]), "+s"(n) : "v"(off), "s"(base), "s"(tag), "i"(D0), "i"(KPF1) : "memory", "vcc", "scc");
    dead = __builtin_amdgcn_readfirstlane(n) == ~0u;
}
#define WAVE_RETRY(OK, ...) { if (__builtin_expect(__builtin_amdgcn_ballot_w64(!(OK)) != 0ull, 0)) WAVE_WAIT({ __VA_ARGS__; ok = OK; }) ORDER(); }
#define STAMP(i)
#define STAMP_DUMP()

#define KLB0 0
#define KLB1 12
DI void site() { asm volatile(""); }
template <class F, int... I> DI void static_for_impl(std::integer_sequence<int, I...>, F&& f) { (f(std::integral_constant<int, I>{}), ...); }
template <int N, class F> DI void static_for(F&& f) { static_for_impl(std::make_integer_sequence<int, N>{}, f); }
#define IC(n) std::integral_constant<int, (n)>{}
#define LAMBDA(v) [&](auto v) __attribute__((always_inline))
#define PK2 " op_sel:[1,1,0] op_sel_hi:[1,0,1] neg_lo:[1,0,0]"
DI f32x2 cmul(f32x2 a, f32x2 b) {
    f32x2 t;
    asm("v_pk_mul_f32 %0, %1, %2 op_sel_hi:[0,1]\n\ts_nop 0\n\tv_pk_fma_f32 %0, %1, %2, %0" PK2 "\n\ts_nop 0" : "=&v"(t) : "v"(a), "v"(b));
    return t;
}
DI f32x2 cfma(f32x2 a, f32x2 b, f32x2 c) {
    f32x2 t;
    asm("v_pk_fma_f32 %0, %1, %2, %3 op_sel_hi:[0,1,1]\n\ts_nop 0\n\tv_pk_fma_f32 %0, %1, %2, %0" PK2 "\n\ts_nop 0" : "=&v"(t) : "v"(a), "v"(b), "v"(c));
    return t;
}
DI void cfma4(f32x2 (&t)[4], f32x2 a, f32x2 b0, f32x2 b1, f32x2 b2, f32x2 b3, f32x2 c0, f32x2 c1, f32x2 c2, f32x2 c3) {
    asm("v_pk_fma_f32 %0, %4, %5, %9 op_sel_hi:[0,1,1]\n\tv_pk_fma_f32 %1, %4, %6, %10 op_sel_hi:[0,1,1]\n\tv_pk_fma_f32 %2, %4, %7, %11 op_sel_hi:[0,1,1]\n\tv_pk_fma_f32 %3, %4, %8, %12 op_sel_hi:[0,1,1]\n\t"
        "v_pk_fma_f32 %0, %4, %5, %0" PK2 "\n\tv_pk_fma_f32 %1, %4, %6, %1" PK2 "\n\tv_pk_fma_f32 %2, %4, %7, %2" PK2 "\n\tv_pk_fma_f32 %3, %4, %8, %3" PK2 "\n\ts_nop 0"
        : "=&v"(t[0]), "=&v"(t[1]), "=&v"(t[2]), "=&v"(t[3]) : "v"(a), "v"(b0), "v"(b1), "v"(b2), "v"(b3), "v"(c0), "v"(c1), "v"(c2), "v"(c3));
}
DI void gacc2(f32x2& h0, f32x2& h1, f32x2 w0, f32x2 w1, v4u e0, v4u e1) {
    const v2u a0 = {e0.x, e0.y}, b0 = {e0.z, e0.w}, a1 = {e1.x, e1.y}, b1 = {e1.z, e1.w};
    asm("v_pk_fma_f32 %0, %4, %2, %0 op_sel_hi:[0,1,1]\n\tv_pk_fma_f32 %1, %6, %3, %1 op_sel_hi:[0,1,1]\n\t"
        "v_pk_fma_f32 %0, %5, %2, %0 op_sel:[0,1,0] op_sel_hi:[0,0,1] neg_lo:[1,0,0]\n\tv_pk_fma_f32 %1, %7, %3, %1 op_sel:[0,1,0] op_sel_hi:[0,0,1] neg_lo:[1,0,0]"
        : "+v"(h0), "+v"(h1) : "v"(w0), "v"(w1), "v"(a0), "v"(b0), "v"(a1), "v"(b1));
}
DI s16x4 trread(char* smem, int byteoff) {
    return __builtin_amdgcn_ds_read_tr16_b64_v4i16((LAS s16x4*)(smem + byteoff));
}

#define CV_LOAD_A(A, a, rr_, hf) { const unsigned* ap_ = (const unsigned*)(smem + ab[(a) >> 1] + (((a) & 1) ? 0 : 512) + 64 * (7 - (rr_)) + 8 * (hf)); A[a][2 * (hf)] = ap_[0]; A[a][2 * (hf) + 1] = ap_[1]; }
#define CV_LOAD_B(B, e, rr_) { B[e] = *(const v4u*)(smem + bb + 64 * (7 - (rr_)) + 512 * (e)); }
constexpr int cv_pair(int lo, int hi, int n) {
    for (int a = 0; a < 8; ++a)
        for (int e = 0; e < 8; ++e)
            if (a + e >= lo && a + e < hi && n-- == 0) return 8 * a + e;
    return -1;
}
template <int R, int PF, bool OWN, class F>
DI void conv_round(f32x4 (&ca)[8], v4u (&A)[8], const v4u (&B)[8], v4u (&An)[8], v4u (&Bn)[8], const char* smem, const int (&ab)[4], int bb, F&& fill) {
    static_for<(R == 7 ? KG0 : 36)>(LAMBDA(g_) {
        constexpr int g = g_.value, a = cv_pair(0, 8, g) / 8, e = cv_pair(0, 8, g) % 8;
        mf(ca[a + e], A[a], B[e]);
        SCHED();
        if constexpr (OWN && g < 12) { CV_LOAD_A(A, 2 + g / 2, R, g & 1) SCHED(); }
        if constexpr (g < 4 * PF && (g & 3) != 3) { if constexpr ((g & 3) == 2) CV_LOAD_B(Bn, g >> 2, R + 1) else CV_LOAD_A(An, g >> 2, R + 1, g & 1) SCHED(); }
        site();
        if constexpr (R == 7) SCHED();
        if constexpr ((g & 3) == 3 && g < 28) { fill(std::integral_constant<int, 7 * R + g / 4>{}); SCHED(); }
    });
}
template <int R0, int R1, class F, class M>
DI void conv_part(f32x4 (&ca)[8], const char* smem, const int (&ab)[4], int bb, F&& fill, M&& mid, v4u (&A)[2][8], v4u (&B)[2][8]) {
    if constexpr (R0 > 0) { CV_LOAD_A(A[0], 0, R0, 0) CV_LOAD_A(A[0], 0, R0, 1) SCHED(); }
    static_for<8>(LAMBDA(a_) { constexpr int a = a_.value; CV_LOAD_B(B[0], a, R0) SCHED(); });
    if constexpr (R0 > 0) { CV_LOAD_A(A[0], 1, R0, 0) CV_LOAD_A(A[0], 1, R0, 1) SCHED(); }
    else {
        CV_LOAD_A(A[0], 0, 0, 0) CV_LOAD_A(A[0], 0, 0, 1) SCHED();
        const v4u ad = mid();
        SCHED();
        static_for<8>(LAMBDA(e_) {
            constexpr int e = e_.value;
            mf0(ca[e], ad, B[0][e]); SCHED();
            if constexpr (e < 7) { CV_LOAD_A(A[0], e + 1, 0, 0) CV_LOAD_A(A[0], e + 1, 0, 1) SCHED(); }
        });
    }
    static_for<R1 - R0>(LAMBDA(r_) {
        constexpr int r = r_.value;
        conv_round<R0 + r, (R0 + r + 1 < R1 ? 8 : 0), (r == 0 && R0 > 0)>(ca, A[r & 1], B[r & 1], A[(r & 1) ^ 1], B[(r & 1) ^ 1], smem, ab, bb, fill);
    });
}
template <int IMM> DI void gld(v4u& dst, unsigned voff, const void* sbase) {
    asm volatile("global_load_dwordx4 %0, %1, %2 offset:%3" : "=v"(dst) : "v"(voff), "s"(sbase), "i"(IMM));
}
#define OUTP 16
#define NTM 31
template <int IMM, int BIT> DI void gldn(v4u& dst, unsigned voff, const void* sbase) {
    if constexpr ((NTM >> BIT) & 1) asm volatile("global_load_dwordx4 %0, %1, %2 offset:%3 nt" : "=v"(dst) : "v"(voff), "s"(sbase), "i"(IMM));
    else gld<IMM>(dst, voff, sbase);
}
DI void gldu16(unsigned& dst, unsigned voff, const void* sbase) { asm volatile("global_load_ushort %0, %1, %2" : "=v"(dst) : "v"(voff), "s"(sbase)); }
template <int IMM> DI void gld64(v4u& dst, const void* a) {
    if constexpr ((NTM >> 5) & 1) asm volatile("global_load_dwordx4 %0, %1, off offset:%2 nt" : "=v"(dst) : "v"(a), "i"(IMM));
    else asm volatile("global_load_dwordx4 %0, %1, off offset:%2" : "=v"(dst) : "v"(a), "i"(IMM));
}
template <int V> DI void SETM0(unsigned ldsbase) { asm volatile("s_add_u32 m0, %0, %1" ::"s"(ldsbase), "i"(V)); }
template <int GOFF> DI void GLD0(unsigned voff, const char* sbase) { asm volatile("global_load_lds_dwordx4 %0, %1 offset:%2" ::"v"(voff), "s"(sbase), "i"(GOFF) : "memory"); }
template <int GOFF, int V> DI void GLD1(unsigned voff, const char* sbase, unsigned ldsbase) {
    asm volatile("global_load_lds_dwordx4 %0, %1 offset:%3\n\ts_add_u32 m0, %2, %4" ::"v"(voff), "s"(sbase), "s"(ldsbase), "i"(GOFF), "i"(V) : "memory");
}
constexpr int dma_goff(int it) { return it < 40 ? (it >> 2) * 128 : ((it - 100) / 5) * 256; }
constexpr int dma_loff(int it) { return it < 40 ? L_XR + ((it >> 2) % 3) * 4096 + (it & 3) * 1024 : ((it - 100) % 5) * 20480 + ((it - 100) / 5) * 2048; }
#define KFD0 10
#define KNF 13
#define KNW 11
#define KW2S 17
#define KW2M 0x4A
#define KFWP 14
constexpr int kW2S = KW2S, kW2M = KW2M;
constexpr int NW2P = (20 - kW2S) * __builtin_popcount(kW2M);
constexpr int P1_PRE = 34;
constexpr int p1_pre(int i) { return i < 5 ? 100 + i : i < 13 ? i - 5 : i < 18 ? 105 + (i - 13) : i < 22 ? 8 + (i - 18) : i < 27 ? 110 + (i - 22) : 400; }
constexpr int p1_item(int s, int g) {
    if (s == 7 && g == 0) return 324;
    if (g == 9) return s < 10 ? 115 + s : s == 11 ? 202 : s == 12 ? 203 : -1;
    if (s == KFWP && g == 1) return 208;
    if (s >= kW2S && ((kW2M >> g) & 1)) return 500 + (s - kW2S) * __builtin_popcount(kW2M) + __builtin_popcount(kW2M & ((1 << g) - 1));
    if ((s & 1) == 0 && s <= 12) return g >= 5 ? 4 * (s / 2 + 3) + (g - 5) : -1;
    if (s == 14 && g == 5) return 207;
    return -1;
}
constexpr int p1_cnt(int s, int g) {
    int n = P1_PRE;
    for (int t = 0; t <= s; ++t)
        for (int u = 0; u < 10; ++u) {
            if (t == s && u >= g) break;
            if (p1_item(t, u) >= 0 && p1_item(t, u) != 319) ++n;
        }
    return n;
}
constexpr int p1_pos(int code) {
    for (int i = 0; i < P1_PRE; ++i) if (p1_pre(i) == code) return i;
    for (int t = 0; t < 20; ++t)
        for (int u = 0; u < 10; ++u) if (p1_item(t, u) == code) return p1_cnt(t, u);
    return -1000;
}
constexpr int p1_cap(int s, int g) {
    int u = (s < 19 && g < 5) + (g == 9 && s < 19);
    if ((s & 1) && s < 19 && g >= 5 && g < 9) u += 1 + (g == 5);
    if ((s & 1) == 0 && g == 4) u += 1;
    const int it = p1_item(s, g), nt = g < 9 ? p1_item(s, g + 1) : -1;
    u += (it >= 0 && it < 500 && it != 208) + (nt >= 0 && nt < 200);
    if ((s == 18 && g == 1) || (s == 19 && g == 2) || (g == 9 && ((s & 3) == 2 || s == 17))) u += 2;
    return u < 2 ? 2 - u : 0;
}
constexpr int p1_slot(int s0, int s, int g, int gmax) {
    int n = 0;
    for (int t = s0; t <= s; ++t)
        for (int u = 0; u < gmax && !(t == s && u >= g); ++u) n += p1_cap(t, u);
    return n;
}
constexpr int p1_lim(int n) { return n > 63 ? 63 : n; }
template <int IMM> DI void gld4s(v4u& dst, unsigned voff, const void* sbase) { asm volatile("global_load_dwordx4 %0, %1, %2 offset:%3 sc1" : "=v"(dst) : "v"(voff), "s"(sbase), "i"(IMM)); }
template <int IMM> DI void gld2s(v2u& dst, unsigned voff, const void* sbase) { asm volatile("global_load_dwordx2 %0, %1, %2 offset:%3 sc1" : "=v"(dst) : "v"(voff), "s"(sbase), "i"(IMM)); }
template <class T> DI void pin(T& v) { asm volatile("" : "+v"(v)); }
DI void mul2(float& a, float& b, float x, float y, float s) { asm volatile("v_mul_f32 %0, %2, %4\n\tv_mul_f32 %1, %3, %4" : "=&v"(a), "=&v"(b) : "v"(x), "v"(y), "v"(s)); }
DI void rot1(unsigned& a, unsigned x) { asm volatile("v_alignbit_b32 %0, %1, %1, 16" : "=v"(a) : "v"(x)); }
DI void mix1(unsigned& a, unsigned x, unsigned y) { asm volatile("v_bfi_b32 %0, %3, %1, %2" : "=v"(a) : "v"(x), "v"(y), "s"(0xffffu)); }
DI void ror1(unsigned& o, unsigned prev) { asm volatile("v_mov_b32_dpp %0, %1 wave_ror:1 row_mask:0xf bank_mask:0xf" : "=v"(o) : "v"(prev)); }
DI void shr1b(unsigned& o, unsigned x) { asm volatile("v_mov_b32_dpp %0, %1 wave_shr:1 row_mask:0xf bank_mask:0xf" : "+v"(o) : "v"(x)); }
DI void xor1(float& a, unsigned x, unsigned s) { asm volatile("v_xor_b32 %0, %1, %2" : "=v"(a) : "v"(x), "v"(s)); }
template <int G> DI void bc1(float& a, unsigned x) { asm volatile("v_mov_b32_dpp %0, %1 row_newbcast:%2 row_mask:0xf bank_mask:0xf" : "=v"(a) : "v"(x), "i"(G)); }
template <int G> DI void mulb(float& o, unsigned x, float y) { asm volatile("v_mul_f32_dpp %0, %1, %2 row_newbcast:%3 row_mask:0xf bank_mask:0xf" : "=v"(o) : "v"(x), "v"(y), "i"(G)); }
template <int N> DI void lgkm() { __builtin_amdgcn_s_waitcnt(0xC07F | (N << 8)); asm volatile("" ::: "memory"); }
DI void unp2(float& a, float& b, unsigned x) { asm volatile("v_lshlrev_b32 %0, 16, %2\n\tv_and_b32 %1, 0xffff0000, %2" : "=&v"(a), "=&v"(b) : "v"(x)); }
#define GL(a) "global_load_dwordx4 a[" #a ":" #a "+3], %7, %8 sc1\n\tglobal_load_dwordx4 a[" #a "+4:" #a "+7], %7, %8 offset:256 sc1\n\t"
#define AR(d, a, o) "v_accvgpr_read_b32 " d ", a[" #a "+" #o "]\n\t"
#define GS_(a) "s_waitcnt vmcnt(4)\n\t" AR("%2", a, 1) AR("%3", a, 3) AR("%4", a, 5) AR("%5", a, 7) AR("%0", a, 0) AR("%1", a, 2)                              \
    "v_xor_b32 %2, %9, %2\n\tv_bitop3_b32 %4, %4, %9, %2 bitop3:0xbe\n\tv_bitop3_b32 %4, %3, %9, %4 bitop3:0xbe\n\tv_bitop3_b32 %4, %5, %9, %4 bitop3:0xbe\n\t" \
    AR("%2", a, 4) AR("%3", a, 6) "v_cmp_ne_u32 vcc, 0, %4\n\ts_cbranch_vccz 2f\n\t" GL(a)
template <int D0> DI void pipe_gran(float& a, float& d, unsigned off, const void* base, unsigned tag, bool& dead) {
    unsigned n = dead ? 0u : kSpinCap, y, z;
    float a1, d1;
    asm volatile("s_sleep %10\n\t" GL(32) "s_sleep %11\n\t" GL(40) "s_sleep %11\n\t" GL(48)
                 "1:\n\t" GS_(32) GS_(40) GS_(48) "s_sub_u32 %6, %6, 1\n\ts_cbranch_scc0 1b\n2:"
                 : "=&v"(a), "=&v"(d), "=&v"(a1), "=&v"(d1), "=&v"(y), "=&v"(z), "+s"(n) : "v"(off), "s"(base), "s"(tag), "i"(D0), "i"(PD1) : "memory", "vcc", "scc");
    dead = __builtin_amdgcn_readfirstlane(n) == ~0u;
    a += a1; d += d1;
}
DI void fma2(float& a, float& b, float x, float y, float s, float t, float u, float v) {
    asm volatile("v_fma_f32 %0, %2, %4, %6\n\tv_fma_f32 %1, %3, %5, %7" : "=&v"(a), "=&v"(b) : "v"(x), "v"(y), "v"(s), "v"(t), "v"(u), "v"(v));
}
DI void cvt2(unsigned& a, unsigned& b, f32x4 v) { asm volatile("v_cvt_pk_bf16_f32 %0, %2, %3\n\tv_cvt_pk_bf16_f32 %1, %4, %5" : "=&v"(a), "=&v"(b) : "v"(v.x), "v"(v.y), "v"(v.z), "v"(v.w)); }
DI void swp2n(unsigned& a, unsigned& b, unsigned& c, unsigned& d) { asm volatile("v_permlane16_swap_b32 %0, %1\n\tv_permlane16_swap_b32 %2, %3" : "+v"(a), "+v"(b), "+v"(c), "+v"(d)); }
template <int G> DI void bc2b(float& a, float& b, float x, float y) {
    asm volatile("v_mov_b32_dpp %0, %2 row_newbcast:%4 row_mask:0xf bank_mask:0xf\n\tv_mov_b32_dpp %1, %3 row_newbcast:%4 row_mask:0xf bank_mask:0xf" : "=&v"(a), "=&v"(b) : "v"(x), "v"(y), "i"(G));
}
template <class T> DI void pins(T& v) { asm volatile("" : "+s"(v)); }
template <int I> DI void gld_w2(unsigned voff, const void* sbase) {
    asm volatile("global_load_dwordx4 " WA(2) ", %0, %1 offset:%3 sc1" ::"v"(voff), "s"(sbase), "i"(W2R + 4 * I), "i"((I % 5) * 1024 - 2048));
}
DI void undef4(v4u (&x)[4]) { asm volatile("" : "=v"(x[0]), "=v"(x[1]), "=v"(x[2]), "=v"(x[3])); }
DI void wait1(v2u& v) { asm volatile("s_waitcnt vmcnt(0)" : "+v"(v)::"memory"); }
DI void ld_ur(unsigned o, const void* base) {
    asm volatile("global_load_dwordx4 a[40:40+3], %0, %1 sc1\n\tglobal_load_dwordx4 a[44:44+3], %0, %1 offset:1024 sc1\n\t"
                 "global_load_dwordx4 a[48:48+3], %0, %1 offset:2048 sc1\n\tglobal_load_dwordx4 a[52:52+3], %0, %1 offset:3072 sc1" ::"v"(o), "s"(base));
}
DI void st_ur(unsigned a) {
    asm volatile("s_waitcnt vmcnt(%1)\n\tds_write_b128 %0, a[40:40+3] offset:480\n\tds_write_b128 %0, a[44:44+3] offset:1504\n\t"
                 "ds_write_b128 %0, a[48:48+3] offset:2528\n\tds_write_b128 %0, a[52:52+3] offset:3552" ::"v"(a), "i"(2 * __builtin_popcount(kW2M)) : "memory");
}
template <int A, int IMM> DI void lda4(unsigned o, const void* base) { asm volatile("global_load_dwordx4 a[%c2:%c2+3], %0, %1 offset:%3" ::"v"(o), "s"(base), "i"(A), "i"(IMM)); }
template <int A, int IMM> DI void lda2(unsigned o, const void* base) { asm volatile("global_load_dwordx2 a[%c2:%c2+1], %0, %1 offset:%3" ::"v"(o), "s"(base), "i"(A), "i"(IMM)); }
template <int A> DI void rda(unsigned& v) { asm volatile("v_accvgpr_read_b32 %0, a[%c1+0]" : "=v"(v) : "i"(A)); }
DI void poll1(unsigned a, const void* base) { asm volatile("global_load_dwordx2 a[36:36+1], %0, %1 sc1" ::"v"(a), "s"(base)); }
DI void poll2(unsigned a, unsigned b, const void* base) { asm volatile("global_load_dwordx2 a[36:36+1], %0, %2 sc1\n\tglobal_load_dwordx2 a[36+2:36+3], %1, %2 sc1" ::"v"(a), "v"(b), "s"(base)); }
DI void touch_(unsigned a, const void* base) { asm volatile("global_load_dword a[32+1], %0, %1" ::"v"(a), "s"(base)); }
template <int N> DI void wait_st(v2u& a, v4u& b) { asm volatile("s_waitcnt vmcnt(%2)" : "+v"(a), "+v"(b) : "i"(N) : "memory"); }
DI void undef3(v4u (&x)[3]) { asm volatile("" : "=v"(x[0]), "=v"(x[1]), "=v"(x[2])); }
template <int IMM> DI void gld4t(v4u& dst, unsigned voff, const void* sbase) { asm volatile("global_load_dwordx4 %0, %1, %2 offset:%3 sc1" : "+v"(dst) : "v"(voff), "s"(sbase), "i"(IMM)); }
template <int N> DI void vm_wait() { asm volatile("s_waitcnt vmcnt(%0)" ::"i"(N) : "memory"); }
DI float dot2a(unsigned a, unsigned b, float c) {
    return __builtin_amdgcn_fdot2_f32_bf16(__builtin_bit_cast(bf16x2v, a), __builtin_bit_cast(bf16x2v, b), c, false);
}
DI void disc(unsigned l0, unsigned l1, unsigned l2, f32x2& lam, f32x2& co) {
    const float dt = rbf(__builtin_amdgcn_exp2f(lo16f(l2) * 1.44269504088896f));
    const float lre = lo16f(l0), lim = lo16f(l1);
    const float e_ = __builtin_amdgcn_exp2f(dt * lre * 1.44269504088896f);
    const float rv = __builtin_amdgcn_fractf(dt * lim * 0.159154943091895f);
    lam = f32x2{e_ * __builtin_amdgcn_cosf(rv), e_ * __builtin_amdgcn_sinf(rv)};
    const float nr = lam.x - 1.0f, ni = lam.y, id = __builtin_amdgcn_rcpf(lre * lre + lim * lim);
    co = f32x2{(nr * lre + ni * lim) * id, (ni * lre - nr * lim) * id};
}
DI f32x4 unpk4(v2u u) { return f32x4{lo16f(u.x), hi16f(u.x), lo16f(u.y), hi16f(u.y)}; }
#define D2 "v_dot2_f32_bf16 "
#define CP "v_cvt_pk_bf16_f32 "
DI void d2x4(unsigned* o, unsigned a0, unsigned a1, unsigned a2, unsigned a3, unsigned b0, unsigned b1, unsigned b2, unsigned b3) {
    asm volatile(D2 "%0, %4, %8, 0\n\t" D2 "%1, %5, %9, 0\n\t" D2 "%2, %6, %10, 0\n\t" D2 "%3, %7, %11, 0"
                 : "=&v"(o[0]), "=&v"(o[1]), "=&v"(o[2]), "=&v"(o[3]) : "v"(a0), "v"(a1), "v"(a2), "v"(a3), "v"(b0), "v"(b1), "v"(b2), "v"(b3));
}

__global__ __launch_bounds__(256, 1) __attribute__((aligned(4096))) void k_main(Params p) {
    __shared__ __attribute__((aligned(16))) char smem[LDS_BYTES];
    const rsrc_t rs = __builtin_amdgcn_make_buffer_rsrc((void*)p.ws, 0, WS_BYTES, 0x00020000);
    const rsrc_t ro = __builtin_amdgcn_make_buffer_rsrc((void*)p.out, 0, S_ * D_ * 4, 0x00020000);
    const int w0 = __builtin_amdgcn_readfirstlane(threadIdx.x >> 6), b0 = blockIdx.x;
    bool dead = false;
    asm volatile("" ::: "a255");
    float SW[5];
    f32x4 xin[5];
    v2u gt[5], dsk[5];
    f32x4 acc[2][5];
    f32x4 rr[2];
    float tt[2][5][4];
    float lr[5], ls[5], lqr[5], lqs[5];
    f32x2 l8, l32, l128;
    bool kearly;
    unsigned sdq;
    int sgn_, ab[4], bb, zpa, uwa, spa, tbw, tbr, cwa, spl, dst[3];
    unsigned sgo, sgs, glo, gso, fgo, fgo1;
    int srca;
    unsigned adm, dso, w2o, wro, fwo, xso, fho1, fho, hso, eo, es, ho[4];
    bool cv0;
    int mt1_;
    auto addrs = [&](auto layer_, int lane, int w, int b) __attribute__((always_inline)) {
        constexpr int layer = layer_.value;
        const int c = lane & 15, q = lane >> 4;
        TILES()
        static_assert(l_sg(0, 0) == SC + 0xA900 && l_sg(0, 1) == SC + 0xB400 && l_sg(0, 2) == SC + 0xCE00 && l_sg(0, 3) == SC + 0xDC00, "");
        sgn_ = (layer ? l_sg(1, 0) + w * INBOX : SC + (int)((0xDC00CE00B400A900ull >> (16 * w)) & 0xffff)) + q * 320 + c * 4;
        const int pl = lane < 40 ? lane : 39, tbb = l_tb(layer, 0) + w * (layer ? INBOX : 5120);
        spl = SC + pl * 8;
        eo = OFF_EG + (unsigned)(layer * 163840 + nt1 * 640 + pl * 16);
        es = eo + (unsigned)(mt1 * 10240);
        spa = l_rs(layer, 0) + (2 * (mt2 & 1) + (w >> 1)) * (layer ? INBOX : 256) + (2 * (w & 1) + (c >> 3)) * 64 + (c & 7) * 4;
        tbw = tbb + c * 64 + ((q ^ ((c >> 1) & 3)) * 16);
        tbr = tbb + (2 * (c >> 2) + (q & 1)) * 64 + (((2 * (q >> 1) + ((c >> 1) & 1)) ^ (c >> 2)) * 16) + (c & 1) * 8;
        hso = OFF_HF + (unsigned)(layer * 5242880 + ((8 * mt1 + 2 * w + (q >> 1)) * 160 + 10 * nt1 + 5 * (q & 1)) * 256 + c * 16);
        cwa = L_CW + c * 4;
        fho1 = lane ? kNone : OFF_FH + (unsigned)((((layer * 32 + 2 * mt1 + (w >> 1)) * 4 + (nt1 >> 2)) * 8 + (nt1 & 3) * 2 + (w & 1)) * 128);
        fho = OFF_FH + (unsigned)(((layer * 32 + mt2) * 4 + w) * 1024 + (lane & 7) * 128);
        pin(sgn_); pin(spl); pin(eo); pin(es); pin(spa); pin(tbw); pin(tbr); pin(hso); pin(cwa); pin(fho1); pin(fho);
        const unsigned hb_ = OFF_HF + (unsigned)(layer * 5242880 + (4 * mt2 * 160 + 40 * w) * 256);
#pragma unroll
        for (int m = 0; m < 4; ++m) { ho[m] = hb_ + (unsigned)(((m + 1 + w) & 3) * 40960 + lane * 16); pin(ho[m]); }
#pragma unroll
        for (int m = 0; m < 3; ++m) { dst[m] = SC + ((w + 1 + m) & 3) * INBOX + m * 5120 + lane * 16; pin(dst[m]); }
        mt1_ = mt1;
        sgo = OFF_SG + (unsigned)(((layer * 32 + mt2) * 4 + w) * 2048 + c * 16);
        sgs = q < 2 ? sgo + (unsigned)(nt2 * 256 + q * 8) : kNone;
        sgo += (unsigned)(q * 512);
        pin(sgo); pin(sgs);
        if constexpr (layer == 0) {
            srca = SC + w * INBOX + lane * 16;
            glo = OFF_GT + (unsigned)((80 * nt2 + (lane >> 1)) * 4096 + (64 * mt2 + 16 * w) * 2 + (lane & 1) * 16);
            gso = cv0 ? OFF_GT + (unsigned)((80 * nt2 + i32 + 32 * w) * 4096 + (q & 1) * 512 + c * 32 + (q >> 1) * 16) : kNone;
            fgo = OFF_FG + (unsigned)((nt2 * 96 + (lane & 31)) * 128);
            fgo1 = w == 0 ? OFF_FG + (unsigned)((nt2 * 96 + i32) * 128) : kNone;
            pin(srca); pin(glo); pin(gso); pin(fgo); pin(fgo1);
            const int r0 = SC + (w < 3 ? w : 0) * REGSZ, y0 = 2031 + 8 * q - c, l34 = lane < 33 ? lane : 33;
#pragma unroll
            for (int i = 0; i < 4; ++i) { ab[i] = r0 + ((y0 & 1) ? CV_O : CV_E) + ((y0 + 1) >> 1) * 4 - 448 - 512 - 1024 * i; pin(ab[i]); }
            bb = r0 + 32 * c + 16 * q;
            zpa = w < 3 ? r0 + l34 * 16 + (l34 >= 30 ? CV_E + 4096 - 480 : 0) + (l34 >= 32 ? CV_O + 4100 - 32 - CV_E - 4096 : 0) : SC + 47360 + lane * 16;
            uwa = r0 + lane * 16;
            xso = (unsigned)((64 * mt2 + 16 * w + c) * D_ + 80 * nt2 + 16 * (q & 1) + 8 * (q >> 1)) * 2u;
            dso = (unsigned)(160 * nt2 + 8 * q);
            adm = (unsigned)((c & 1) * 16 + (q == 2 + (c >> 3) ? 256 << ((c & 7) >> 1) : 0));
            pin(adm);
            pin(bb); pin(zpa); pin(uwa); pin(xso); pin(dso);
        }
        SCHED();
    };
    unsigned tag0, tag1;
#define FLAG(off) { if (lane == 0) st64(v2u{tag0, tag1}, rs, off); }
#define ISTAG(f) (((f.x ^ tag0) | (f.y ^ tag1)) == 0u)
#define KW_REQ() { if (cv) ld_ur(wro, p.ws + OFF_KVT); static_for<NW2P>(LAMBDA(i_) { gld_w2<i_.value>(w2o, p.ws + 2048 + (i_.value / 5) * 5120); }); }
#define FW_OK() { if (__builtin_expect(!kearly, 0)) { WAVE_WAIT({ const v2u fw_ = ld64(rs, fwo); ok = ISTAG(fw_); }) KW_REQ() } }
#define TT_GAP(n) { constexpr int mb_ = (n) / 10, nb_ = ((n) % 10) / 2, e_ = ((n) & 1) * 2; \
        mul2(tt[mb_][nb_][e_], tt[mb_][nb_][e_ + 1], rm_[4 * mb_ + e_], rm_[4 * mb_ + e_ + 1], SW[nb_]); }
    {
        const int tid = threadIdx.x, lane = tid & 63, w = w0, b = b0;
        const int c = lane & 15, q = lane >> 4;
        TILES()
        STAMP(0)
        const unsigned lds0 = (unsigned)(size_t)(LAS char*)smem;
        v2u nonce;
        {
            u64 pc;
            int len, len2;
            asm volatile("s_getpc_b64 %0\nkpf_begin:\n\ts_mov_b32 %1, kpf_end-kpf_begin\n\ts_mov_b32 %2, kpf_end2-kpf_begin" : "=s"(pc), "=s"(len), "=s"(len2));
            len = len > len2 ? len : len2;
            const int o0 = (((j & 15) * 4 + w) + 64 * (lane >> 3)) * 128;
            touch_((unsigned)(o0 < len ? o0 : 0), (const void*)pc);
        }
        const bool cv = w < 2 || (w == 2 && i32 < 16);
        const int chn = 80 * nt2 + i32 + 32 * (cv ? w : 0);
        const bool kw = w < 2 || (w == 2 && i32 >= 16);
        const int kp = 16 * nt2 + mt1, ku = 5 * kp + (i32 < 16 ? w & 1 : 2 + (w < 3 ? w : 0)), kc = ku >> 6, kr = ku & 63;
        const int lc = cv ? lane : 8;
        int kwi = 2 + (i32 >> 4) - w;
        v4u ax[15], sa[5], cr[2], wr[4];
        unsigned uL[3];
        const int cu_ = (b < 200 ? b : 0) * 4 + w, cdb = cu_ / 20, ccg = cu_ % 20;
        {
            int ca_ = 32 * ccg, cb_ = 40 * nt1 + 10 * w - 32;
            asm volatile("" : "+s"(ca_), "+s"(cb_), "+s"(kwi));
            unsigned cpo = (unsigned)((lane < 32 ? ca_ : cb_) + (lane < 41 ? lane : 41)) * 2u;
            unsigned kso = (unsigned)(64 * kc + 8 * (lane & 7)) * 2u, ko = (unsigned)(32 * kr + 4 * (lane >> 3)) * 1280u + kso, kh = ko - 3840u;
            kso *= 4u;
            pin(cpo); pin(kso); pin(ko); pin(kh);
            SCHED();
            gldu16(uL[0], cpo, p.Lre); gldu16(uL[1], cpo, p.Lim); gldu16(uL[2], cpo, p.logdt);
            const v4u rk = {(unsigned)(u64)p.k, (unsigned)((u64)p.k >> 32) & 0xffffu, S_ * D_ * 2u, 0x00020000u};
#define L4(d, v, s_, o, f) "global_load_dwordx4 %" #d ", %" #v ", %" #s_ " offset:" #o f "\n\t"
#define B4(d, o) "buffer_load_dwordx4 %" #d ", %17, %21, 0 offen offset:" #o " nt\n\t"
            asm volatile("s_cmp_lt_i32 %22, 1\n\ts_cbranch_scc1 Lrole2\n\t"
                         L4(3, 15, 18, 0, " nt") L4(4, 15, 18, 1280, " nt") L4(5, 15, 18, 2560, " nt") L4(6, 15, 18, 3840, " nt")
                         L4(7, 15, 20, 0, " nt") L4(8, 15, 20, 1280, " nt") L4(9, 15, 20, 2560, " nt") L4(10, 15, 20, 3840, " nt")
                         B4(0, 0) B4(1, 1280) B4(2, 2560) L4(11, 16, 19, 0, "") L4(12, 16, 19, 16, "") L4(13, 16, 19, 32, "") L4(14, 16, 19, 48, "")
                         "\nLrole2:"
                         : "=&v"(ax[0]), "=&v"(ax[1]), "=&v"(ax[2]), "=&v"(ax[3]), "=&v"(ax[4]), "=&v"(ax[5]), "=&v"(ax[6]), "=&v"(ax[7]), "=&v"(ax[8]), "=&v"(ax[9]),
                           "=&v"(ax[10]), "=&v"(ax[11]), "=&v"(ax[12]), "=&v"(ax[13]), "=&v"(ax[14])
                         : "v"(ko), "v"(kso), "v"(kh), "s"(p.k), "s"(p.shk), "s"(p.v), "s"(rk), "s"(kwi) : "scc");
            SCHED();
        }
        const char* ssrc;
        {
            u64 pa = (u64)(p.x0 + (128 * mt1 + 8 * nt1 + 2 * w) * D_), pb = (u64)(((mt1 & 1) ? p.Bim : p.Bre) + (40 * nt1 + (mt1 >> 1)) * D_);
            asm volatile("" : "+s"(pa), "+s"(pb));
            const int nbx = 2 * w + q - 2, r = q < 2 ? q : 8 * (nbx < 5 ? nbx : 4);
            ssrc = (const char*)(q < 2 ? pa : pb) + (unsigned)(r * 1280 + c * 16);
            pin(ssrc);
        }
        SCHED();
        STAMP(22)
        const int row8 = lane >> 3;
        const unsigned wvo = (unsigned)(40 * nt1 + row8) * 1280u + (unsigned)(((lane & 7) ^ ((w & 1) | ((row8 >> 1) << 1))) * 16 + (w >> 1) * 128);
        const char* const wbase = (const char*)((w & 1) ? p.Bim : p.Bre);
        const unsigned wl = lds0 + (unsigned)(((w & 1) * 10 + (w >> 1)) * 1024);
        const unsigned xvo0 = (unsigned)(8 * (row8 >> 1) + 2 * (row8 & 1)) * 1280u + (unsigned)(((lane & 7) ^ ((row8 >> 1) << 1)) * 16);
        const unsigned xvo1 = (xvo0 ^ 16u) + 1280u;
        const char* const xbase = (const char*)p.x0 + (size_t)(128 * mt1 + 32 * w) * 1280;
        const unsigned xl = lds0 + (unsigned)(w * REGSZ);
        unsigned xv[4] = {xvo0, xvo1, xvo0 + 5120u, xvo1 + 5120u}, wv[5];
#pragma unroll
        for (int i = 0; i < 5; ++i) { wv[i] = wvo + i * 10240u; pin(wv[i]); if (i < 4) pin(xv[i]); }
#define DMA_M0(it) SETM0<dma_loff(it) - dma_goff(it)>((it) < 40 ? xl : wl)
#define DMA_LD1(it, nx_) GLD1<dma_goff(it), dma_loff(nx_) - dma_goff(nx_)>((it) < 40 ? xv[(it) & 3] : wv[(it) < 40 ? 0 : ((it) - 100) % 5], (it) < 40 ? xbase : wbase, (nx_) < 40 ? xl : wl)
#define DMA_LD(it) GLD0<dma_goff(it)>((it) < 40 ? xv[(it) & 3] : wv[(it) < 40 ? 0 : ((it) - 100) % 5], (it) < 40 ? xbase : wbase)
        const int swz = (c & 1) | ((c >> 2) << 1);
        int w1lane0 = (c & 1) * 10240 + (c >> 1) * 128 + ((q ^ swz) * 16);
        int w1lane1 = (c & 1) * 10240 + (c >> 1) * 128 + (((4 + q) ^ swz) * 16);
        int xlane0 = L_XR + w * REGSZ + (c & 1) * 1024 + (c >> 1) * 128 + ((q ^ swz) * 16);
        int xlane1 = L_XR + w * REGSZ + (c & 1) * 1024 + (c >> 1) * 128 + (((4 + q) ^ swz) * 16);
        unsigned cvo = (unsigned)((16 * cdb + c) * D_ + 32 * ccg + 8 * q) * 2u, for0, for1;
        {
            const int G = 4 * ccg + q, ch = 10 * (G / 5) + G % 5, ci = ch + 5;
            const unsigned fo = OFF_W2F + (unsigned)(((cdb / 5) * 200 + cdb % 5) * 1024 + c * 16);
            for0 = fo + (unsigned)((ch >> 2) * 5120 + (ch & 3) * 256); for1 = fo + (unsigned)((ci >> 2) * 5120 + (ci & 3) * 256);
        }
        unsigned sto = OFF_ST + (unsigned)((mt1 * 16 + 4 * w + q) * 256 + c * 8), swo = OFF_ST + (unsigned)(((lane >> 2) * 16 + nt1) * 256 + 128 + (lane & 3) * 16);
        unsigned kvo = OFF_KVT + (unsigned)((64 * kc + 8 * (lane >> 3) + ((lane >> 2) & 1)) * 4096 + (32 * kr + 8 * (lane & 3)) * 2);
        int kbw = L_KB + w * 1024 + (lane & 7) * 128 + (((lane >> 3) * 8) ^ ((lane & 7) * 16));
        int kbr = L_KB + w * 1024 + (lane >> 3) * 128 + (((lane & 7) * 16) ^ ((lane >> 3) * 16));
        const unsigned dum = kNone;
        if (!kw) kvo = dum;
        if (b >= 200) { for0 = dum; for1 = dum; cvo = (unsigned)((b * 4 + w) * 128); }
        wro = (unsigned)(chn * 4096 + lc * 16);
        cv0 = cv;
        pin(kvo); pin(kbw); pin(kbr);
        int w1hi0 = w1lane0 + 61440, w1hi1 = w1lane1 + 61440;
        pin(w1hi0); pin(w1hi1);
        pin(w1lane0); pin(w1lane1); pin(xlane0); pin(xlane1); pin(cvo); pin(for0); pin(for1); pin(sto); pin(swo); pin(wro);
        SCHED();
        auto pre = LAMBDA(i_) {
            constexpr int it = p1_pre(i_.value);
            SCHED();
            DMA_M0(it);
            asm volatile("s_nop 0");
            DMA_LD(it);
            SCHED();
        };
        {
            int f0 = 2 * ((chn & ~63) / 5), f1 = 2 * ((chn | 63) / 5) + 1, nk = cv ? 28 : 0;
            asm volatile("" : "+s"(f0), "+s"(f1), "+s"(nk));
            const int l10 = lane - 32 < 0 || lane - 32 > 9 ? 0 : lane - 32;
            fwo = lane < nk ? OFF_FK + (unsigned)(f0 + lane < f1 ? f0 + lane : f1) * 128u : OFF_FW + (unsigned)(25 * nt2 + w + 5 * (l10 >> 1) + (l10 & 1)) * 128u;
        }
        w2o = OFF_W2F + (unsigned)((nt2 * 40 + 10 * w) * 5120 + lane * 16);
        int ime = SC + (w < 3 ? w * REGSZ + CV_E : 43264) + 1008 - 16 * lane, imo = SC + (w < 3 ? w * REGSZ + CV_O + 4 : 47360) + 1008 - 16 * lane, im3 = w < 3 ? ime + 3072 : imo;
        int tca = L_TAB + (c >> 1) * 136, tcb = tca + 8 + 32 * w + 8 * q, tcl = L_TAB + (lane < 40 ? lane : 39) * 136, coa = L_CO + w * REGSZ + q * 64, rsa = l_rs(0, 0) + w * 256 + lane * 4;
        unsigned sgn = (c & 1) ? 0u : 0x80000000u;
        unsigned fko1 = w ? kNone : lane < 32 ? OFF_FK + (unsigned)((2 * kp + (i32 >> 4)) * 128) : OFF_FW + (unsigned)(b * 128);
        pin(fwo); pin(w2o); pin(fko1); pin(ime); pin(imo); pin(im3); pin(tca); pin(tcb); pin(tcl); pin(coa); pin(rsa); pin(sgn);
        SCHED();
        auto cw_ = [&]() __attribute__((always_inline)) {
            gldn<0, 2>(cr[0], cvo, p.Cre); gldn<0, 2>(cr[1], cvo, p.Cim);
            static_for<4>(LAMBDA(i) { gldn<i.value * 1024, 1>(wr[i.value], wro, p.ssmk); });
        };
        STAMP(33)
        gld2s<0>(nonce, 0u, p.ws + OFF_NONCE);
        asm volatile("s_waitcnt vmcnt(16)\n\ts_cmp_gt_i32 %3, 0\n\ts_cbranch_scc1 Lrole3\n\ts_waitcnt vmcnt(1)\nLrole3:" : "+v"(uL[0]), "+v"(uL[1]), "+v"(uL[2]) : "s"(kwi) : "memory", "scc");
        {
            f32x2 lam, co, lp[16];
            disc(uL[0], uL[1], uL[2], lam, co);
            if (lane < 32) *(f32x2*)(smem + L_CO + w * REGSZ + lane * 8) = 2.0f * co;
            lp[0] = cmul(lam, lam); lp[0] = cmul(lp[0], lp[0]); lp[1] = cmul(lp[0], lp[0]);
            lp[0] = f32x2{1.f, 0.f};
            lp[2] = cmul(lp[1], lp[1]); lp[3] = cmul(lp[2], lp[1]); lp[4] = cmul(lp[2], lp[2]);
            const f32x2 z2 = {0.f, 0.f};
            f32x2 t4[4];
            cfma4(t4, lp[4], lp[1], lp[2], lp[3], lp[4], z2, z2, z2, z2);
            lp[5] = t4[0]; lp[6] = t4[1]; lp[7] = t4[2]; lp[8] = t4[3];
            cfma4(t4, lp[8], lp[1], lp[2], lp[3], lp[4], z2, z2, z2, z2);
            lp[9] = t4[0]; lp[10] = t4[1]; lp[11] = t4[2]; lp[12] = t4[3];
            cfma4(t4, lp[8], lp[5], lp[6], lp[7], lp[7], z2, z2, z2, z2);
            lp[13] = t4[0]; lp[14] = t4[1]; lp[15] = t4[2];
            if (lane >= 32 && lane < 42) {
                f32x2* tl = (f32x2*)(smem + L_TAB + (10 * w + lane - 32) * 136);
                tl[0] = lam;
#pragma unroll
                for (int i = 0; i < 16; ++i) tl[1 + i] = lp[i];
            }
        }
        SCHED();
        STAMP(35)
        static_for<5>(LAMBDA(i) { gld64<256 * i.value>(sa[i.value], ssrc); });
        cw_();
        addrs(std::integral_constant<int, 0>{}, lane, w, b);
        v4u kvt[4];
        unsigned one2 = 0x3F803F80u, kq[12];
        float kf_[8];
        pin(one2);
        auto kvf = LAMBDA(n_) {
            constexpr int n = n_.value, h = n / 5, u = n % 5;
            if constexpr (u == 0) {
#pragma unroll
                for (int r = 0; r < 6; ++r) { kq[r] = __builtin_amdgcn_perm(ax[r][h], ax[r + 1][h], 0x05040100u); kq[6 + r] = __builtin_amdgcn_perm(ax[r][h], ax[r + 1][h], 0x07060302u); }
                if constexpr (h > 0) kvt[h - 1] = *(const v4u*)(smem + kbr);
            } else if constexpr (u == 1) {
                unsigned t[8];
                d2x4(t, kq[0], kq[1], kq[2], kq[3], ax[11 + h][1], ax[11 + h][1], ax[11 + h][1], ax[11 + h][1]);
                d2x4(t + 4, kq[6], kq[7], kq[8], kq[9], ax[11 + h][3], ax[11 + h][3], ax[11 + h][3], ax[11 + h][3]);
#pragma unroll
                for (int r = 0; r < 8; ++r) kf_[r] = __uint_as_float(t[r]);
            } else if constexpr (u == 2) {
#pragma unroll
                for (int r = 0; r < 8; ++r) kf_[r] = dot2a(kq[r + 2 * (r >> 2) + 2], ax[11 + h][2 * (r >> 2)], kf_[r]);
            } else if constexpr (u == 3) {
#pragma unroll
                for (int r = 0; r < 4; ++r) { kq[r] = ax[7 + r][h] << 16; kq[4 + r] = ax[7 + r][h] & 0xffff0000u; pin(kq[r]); pin(kq[4 + r]); }
            } else {
#pragma unroll
                for (int r = 0; r < 8; ++r) kf_[r] *= __uint_as_float(kq[r]);
                *(v2u*)(smem + kbw) = v2u{cvtpk(kf_[0], kf_[1]), cvtpk(kf_[2], kf_[3])};
                *(v2u*)(smem + (kbw ^ 64)) = v2u{cvtpk(kf_[4], kf_[5]), cvtpk(kf_[6], kf_[7])};
                if constexpr (h == 3) kvt[3] = *(const v4u*)(smem + kbr);
            }
            SCHED();
        };
        asm volatile("s_waitcnt vmcnt(%15)" : "+v"(ax[0]), "+v"(ax[1]), "+v"(ax[2]), "+v"(ax[3]), "+v"(ax[4]), "+v"(ax[5]), "+v"(ax[6]), "+v"(ax[7]), "+v"(ax[8]), "+v"(ax[9]),
                     "+v"(ax[10]), "+v"(ax[11]), "+v"(ax[12]), "+v"(ax[13]), "+v"(ax[14]) : "i"(11) : "memory");
        pin(nonce);
        STAMP(30)
        unsigned tch = 0u;
        constexpr int NF = KNF;
        static_assert(NF < 27, "");
        if (kw) {
            {
                static_for<20>(LAMBDA(n_) {
                    constexpr int n = n_.value, DN = 20 - KFD0;
                    site(); SCHED();
                    kvf(n_);
                    if constexpr (n >= KFD0) static_for<(n - KFD0 + 1) * NF / (DN ? DN : 1) - (n - KFD0) * NF / (DN ? DN : 1)>(LAMBDA(i_) { pre(std::integral_constant<int, (n - KFD0) * NF / (DN ? DN : 1) + i_.value>{}); });
                });
                if constexpr (KFD0 == 20) static_for<NF>(pre);
            }
        } else {
            if (w == 3) {
            const int l = lane, l3 = l & 3;
            unsigned a = OFF_SG + (unsigned)((((((l >> 3) & 1) * 32 + mt2) * 4 + ((l >> 1) & 3)) * 8 + nt2) * 256 + (l & 1) * 128);
            a = l >= 16 ? OFF_EG + (unsigned)((l >= 21 ? 163840 - 640 : 0) + (mt1 * 16 + nt1) * 640 + (l < 26 ? l - 16 : 5) * 128) : a;
            a = l >= 28 ? OFF_PS + (unsigned)(((mt2 * 4 + l3) * 8 + nt2) * 128) : a;
            a = l >= 32 ? OFF_FH + (unsigned)((((((l >> 2) & 1) * 32 + 2 * mt1 + (l3 >> 1)) * 4 + (nt1 >> 2)) * 8 + (nt1 & 3) * 2 + (l3 & 1)) * 128) : a;
            a = l >= 40 ? OFF_FX + (unsigned)(((mt1 * 4 + 2 * (mt2 & 1) + (l3 >> 1)) * 16 + nt2 * 2 + (l3 & 1)) * 128) : a;
            a = l >= 48 ? OFF_FG + (unsigned)((nt2 * 96 + i32) * 128) : a;
            const unsigned big = (l & 7) < 2 ? OFF_HF + (unsigned)((l & 1) * 5242880 + (8 * mt1 * 160 + 10 * nt1) * 256) : (l & 7) == 2 ? OFF_X2F + (unsigned)(2 * mt2 * 40960 + 2560 * nt2)
                               : (l & 7) < 5 ? OFF_KVT + (unsigned)((l & 1) * 2621440 + (80 * nt2 + i32) * 4096) : OFF_W2F + (unsigned)(nt2 * 204800 + ((l & 7) - 5) * 98304);
            a = l >= 56 ? big : a;
            tch = a;
        }
            vm_wait<KNW>();
            static_for<NF>(pre);
            undef4(kvt);
        }
        tag0 = __builtin_amdgcn_readfirstlane(nonce.x) + 1u; tag1 = __builtin_amdgcn_readfirstlane(nonce.y) + (tag0 == 0u ? 1u : 0u);
        SCHED();
        STAMP(34)
        v4u orr, oi;
        v2u gr;
        unsigned gro;
        {
            asm volatile("s_waitcnt vmcnt(%7)" : "+v"(cr[0]), "+v"(cr[1]), "+v"(sa[0]), "+v"(sa[1]), "+v"(sa[2]), "+v"(sa[3]), "+v"(sa[4]) : "i"(4 + NF) : "memory");
#pragma unroll
            for (int e = 0; e < 4; ++e) {
                const f32x4 co = *(const f32x4*)(smem + coa + e * 16);
                const f32x2 o0 = lo16f(cr[0][e]) * f32x2{co.x, -co.y} - lo16f(cr[1][e]) * f32x2{co.y, co.x};
                const f32x2 o1 = hi16f(cr[0][e]) * f32x2{co.z, -co.w} - hi16f(cr[1][e]) * f32x2{co.w, co.z};
                orr[e] = cvtpk(o0.x, o1.x); oi[e] = cvtpk(o0.y, o1.y);
            }
            pin(orr); pin(oi);
            SCHED();
        }
        static_for<(27 - NF) / 3>(LAMBDA(i_) { pre(std::integral_constant<int, NF + i_.value>{}); });
        {
            float s1[2] = {0.f, 0.f}, s2[2] = {0.f, 0.f};
#pragma unroll
            for (int i = 0; i < 5; ++i)
#pragma unroll
                for (int e = 0; e < 4; ++e) { s1[e & 1] = dot2a(sa[i][e], one2, s1[e & 1]); s2[e & 1] = dot2a(sa[i][e], sa[i][e], s2[e & 1]); }
            const float a1 = rowsum16(s1[0] + s1[1]), a2 = rowsum16(s2[0] + s2[1]);
            const float mean = a1 * (1.0f / D_);
            const float var = fmaxf(a2 * (1.0f / D_) - mean * mean, 0.f);
            const float ri = __builtin_amdgcn_rcpf(rbf(__builtin_amdgcn_sqrtf(rbf(rbf(var) + 1e-5f))));
            gr = v2u{__float_as_uint(q >= 2 ? a1 : c ? -ri * rbf(mean) : ri), tag0};
            gro = c < 2 ? OFF_ST + (unsigned)((mt1 * 16 + nt1) * 256 + ((q < 2 ? 8 * c : 14) + 2 * w + q) * 8) : kNone;
            pin(gr); pin(gro);
        }
        SCHED();
        STAMP(59)
        {
            static_for<27 - NF - (27 - NF) / 3>(LAMBDA(i_) { pre(std::integral_constant<int, NF + (27 - NF) / 3 + i_.value>{}); });
#pragma unroll
            for (int i = 0; i < 4; ++i) st128(kvt[i], rs, kvo + i * 8192);
            st128(orr, rs, for0); st128(oi, rs, for1);
        }
        st64(gr, rs, gro);
        SCHED();
        STAMP(23)
        asm volatile("s_waitcnt vmcnt(%4) lgkmcnt(0)" : "+v"(wr[0]), "+v"(wr[1]), "+v"(wr[2]), "+v"(wr[3]) : "i"(P1_PRE - 9) : "memory");
        __builtin_amdgcn_s_barrier();
        asm volatile("" ::: "memory");
        STAMP(24)
        v4u wf[2][5], af[2][2][2];
#pragma unroll
        for (int nb = 0; nb < 5; ++nb) wf[0][nb] = *(const v4u*)(smem + w1lane0 + nb * 20480 + (0) * 2048);
#pragma unroll
        for (int i = 0; i < 4; ++i) af[0][i >> 1][i & 1] = *(const v4u*)(smem + ((i >> 1) ? xlane1 : xlane0) + (i & 1) * 2048);
        SCHED();
        const char *kvtb = p.ws + OFF_KVT, *w2b0 = p.ws + 2048;
        pins(kvtb); pins(w2b0);
        v2u sg, tc[10], fw, tg2 = {tag0, tag1};
        pin(tg2);
        v4u sw0;
        unsigned ie[4][4], io[4][4], iw, zq[4];
        float rr_[8];
        auto kstep = LAMBDA(s_) {
            constexpr int s = s_.value, ph = s >> 2, s4 = s & 3, cu = s & 1, nx = cu ^ 1, jj = s >> 1, pp = jj & 1;
            if constexpr (s4 == 3 && ph < 4) {
                constexpr int pw = p1_pos(104 + 5 * (ph + 1)), px = p1_pos(4 * (jj + 1) + 3), pm = pw > px ? pw : px;
                constexpr int vm = p1_lim(p1_cnt(s, 0) - 1 - pm);
                asm volatile("" ::: "memory");
                __builtin_amdgcn_s_waitcnt((vm & 15) | ((vm >> 4) << 14) | 0x70);
                __builtin_amdgcn_s_barrier();
                asm volatile("" ::: "memory");
                STAMP(25 + ph)
                SCHED();
            }
            if constexpr (s == 15) {
                wait_st<p1_cnt(s, 0) - 1 - p1_pos(203)>(sg, sw0);
                WAVE_RETRY((((sg.y ^ tag0) | (sw0.y ^ tag0) | (sw0.w ^ tag0)) == 0u), sg = ld64(rs, sto); sw0 = ld128(rs, swo))
                STAMP(7)
            }
            if constexpr (s == kW2S) { vm_wait<p1_cnt(s, 0) - 1 - p1_pos(208)>(); SCHED(); }
            if constexpr (s == 18) {
                vm_wait<p1_cnt(s, 0) - 1 - (p1_pos(207) > p1_pos(208) ? p1_pos(207) : p1_pos(208))>();
                pin(fw); pin(sdq);
                kearly = __builtin_amdgcn_ballot_w64(!ISTAG(fw)) == 0ull;
                ORDER();
                if (kearly && cv) ld_ur(wro, kvtb);
                SCHED();
            }
            if constexpr (s4 != 3 || ph == 4) lgkm<(cu == 0 && s > 0 ? 3 : s == 17 ? 4 : s == 19 ? 2 : 0)>();
            SCHED();
            static_for<10>(LAMBDA(g_) {
                constexpr int g = g_.value, mb = g / 5, k = g % 5, it = p1_item(s, g), nt_ = g < 9 ? p1_item(s, g + 1) : -1;
                constexpr bool d0 = it >= 0 && it < 200, d1 = nt_ >= 0 && nt_ < 200;
                if constexpr (s == 0) mf0(acc[mb][k], af[pp][cu][mb], wf[cu][k]); else mf(acc[mb][k], af[pp][cu][mb], wf[cu][k]);
                SCHED();
                if constexpr (s < 19) {
                    if constexpr (g < 5) wf[nx][g] = *(const v4u*)(smem + (g < 3 ? (nx ? w1lane1 : w1lane0) : (nx ? w1hi1 : w1hi0)) + (g < 3 ? g : g - 3) * 20480 + ((s + 1) >> 1) * 1024);
                    if constexpr (cu == 1 && g == 5) vm_wait<p1_lim(p1_cnt(s, 5) - 1 - p1_pos(4 * (jj + 1) + 3))>();
                    if constexpr (cu == 1 && g >= 5 && g < 9)
                        af[pp ^ 1][(g - 5) >> 1][(g - 5) & 1] = *(const v4u*)(smem + (((g - 5) >> 1) ? xlane1 : xlane0) + ((jj + 1) % 3) * 4096 + ((g - 5) & 1) * 2048);
                }
                if constexpr (cu == 0 && g == 4 && s > 0) lgkm<(s == 8 || s == 10 || s == 14 ? 8 : 5)>();
                static_for<p1_cap(s, g)>(LAMBDA(e_) {
                    constexpr int na = p1_slot(0, s, g, 10) + e_.value, i = (na / 10) & 3, u = na % 10;
                    static_assert(p1_slot(0, 8, 0, 10) >= 40, "");
                    if constexpr (na < 40) {
                        if constexpr (u == 0) { if constexpr (i > 0) ror1(iw, wr[i - 1].w); else { iw = 0u; pin(iw); } }
                        else if constexpr (u < 5) rot1(ie[i][u - 1], wr[i][4 - u]);
                        else if constexpr (u == 5) shr1b(iw, wr[i].w);
                        else if constexpr (u < 9) mix1(io[i][u - 6], wr[i][9 - u], wr[i][8 - u]);
                        else mix1(io[i][3], wr[i].x, iw);
                    }
                    constexpr int nb = p1_slot(8, s, g, 3) + e_.value;
                    if constexpr (s >= 8 && s < 12 && g < 3) {
                        if constexpr (nb < 10) tc[nb] = *(const v2u*)(smem + (nb < 5 ? tca : tcb) + (nb % 5) * 1088);
                        else if constexpr (nb == 10) { const f32x2* tp = (const f32x2*)(smem + tcl); l8 = tp[2]; l32 = tp[5]; }
                        else if constexpr (nb == 11) l128 = *(const f32x2*)(smem + tcl + 72);
                        static_assert(p1_slot(8, 12, 0, 3) == 12, "");
                    }
                    constexpr int nc = p1_slot(12, s, g, 10) + e_.value;
                    if constexpr (s >= 12 && s < 14 && nc < 10) {
                        if constexpr (nc < 5) { lr[nc] = __uint_as_float(tc[nc].x); xor1(ls[nc], tc[nc].y, sgn); }
                        else { lqr[nc - 5] = __uint_as_float(tc[nc].x); xor1(lqs[nc - 5], tc[nc].y, sgn); }
                        static_assert(p1_slot(12, 14, 0, 10) >= 10, "");
                    }
                    if constexpr (s == 15 && g < 5) SW[g] = __int_as_float(__builtin_amdgcn_ds_bpermute(16 * c + 4 * (g >> 1), (g & 1) ? sw0.z : sw0.x));
                    constexpr bool img = (s == 16 && g >= 5 && g < 9 && e_.value == 0) || (s == 18 && (g == 5 || g == 6) && e_.value == 0);
                    constexpr int nd = p1_slot(15, s, g, 10) + e_.value - 5 - (s > 16 ? 4 : s == 16 && g >= 5 ? (g < 9 ? g - 4 : 4) : 0) - (s > 18 ? 2 : s == 18 && g >= 5 ? (g == 5 ? 1 : 2) : 0);
                    if constexpr (s >= 15 && nd >= 0 && !img) {
                        if constexpr (nd < 4) bc1<nd>(rr_[nd], sg.x);
                        else if constexpr (nd < 44) mulb<8 + (nd - 4) % 8>(tt[(nd - 4) % 8 / 4][(nd - 4) / 8][(nd - 4) % 4], sg.x, SW[(nd - 4) / 8]);
                        else if constexpr (nd < 48) bc1<nd - 40>(rr_[nd - 40], sg.x);
                        static_assert(p1_slot(15, 16, 0, 10) <= 9 && p1_slot(15, 20, 0, 10) >= 59, "");
                    }
                });
                if constexpr (s == 14 && g < 3) *(v4u*)(smem + ime + 1024 * g) = v4u{ie[3 - g][0], ie[3 - g][1], ie[3 - g][2], ie[3 - g][3]};
                if constexpr (s == 16 && g >= 5 && g < 9) *(v4u*)(smem + imo + 1024 * (8 - g)) = v4u{io[g - 5][0], io[g - 5][1], io[g - 5][2], io[g - 5][3]};
                if constexpr (s == 18 && g == 5) *(v4u*)(smem + im3) = v4u{ie[0][0], ie[0][1], ie[0][2], ie[0][3]};
                if constexpr (s == 18 && g == 6) *(unsigned*)(smem + rsa) = sg.x;
                if constexpr (s == 14 && (g == 6 || g == 8)) { zq[g - 6] = 0u; zq[g - 5] = 0u; pin(zq[g - 6]); pin(zq[g - 5]); }
                if constexpr (s == 19 && g == 0) *(v4u*)(smem + zpa) = v4u{zq[0], zq[1], zq[2], zq[3]};
                if constexpr (s == 18 && g == 1) touch_(tch, p.ws);
                if constexpr (s == 19 && g == 2) { if (__builtin_expect(!kearly, 0)) poll1(fwo, p.ws); }
                if constexpr (it >= 0) {
                    if constexpr (d0 && d1) DMA_LD1(it, d1 ? nt_ : 0);
                    else if constexpr (d0) DMA_LD(it);
                    else if constexpr (it == 202) gld2s<0>(sg, sto, p.ws);
                    else if constexpr (it == 203) gld4s<0>(sw0, swo, p.ws);
                    else if constexpr (it == 207) gldu16(sdq, (unsigned)chn * 2u, p.ssmD);
                    else if constexpr (it == 208) gld2s<0>(fw, fwo, p.ws);
                    else if constexpr (it >= 500) gld_w2<it % 50>(w2o, w2b0 + ((it % 50) / 5) * 5120);
                    else { static_assert(p1_pos(19) >= P1_PRE, ""); st64(tg2, rs, fko1); }
                }
                if constexpr (d1 && !d0) DMA_M0(d1 ? nt_ : 0);
                site();
                SCHED();
            });
        };
        static_for<20>(kstep);
        SCHED();
        rr[0] = f32x4{rr_[0], rr_[1], rr_[2], rr_[3]}; rr[1] = f32x4{rr_[4], rr_[5], rr_[6], rr_[7]};
        ACC_FENCE10(acc[0], acc[1]);
        STAMP(1)
    }

    auto layer_body = LAMBDA(layer_) {
        constexpr int layer = layer_.value;
        const int sga = sgn_;
        const bool cv = layer == 0 && cv0;
        v2u fh;
        unsigned ks[10];
        f32x4 ca[8], a2[4][5];
        float rsr, rsm;
        v4u cA[2][8], cB[2][8];
        constexpr int R2 = kCR1, LP = (7 - R2) & 1;
        static_assert(kFHP >= 7 - KG0 / 4 + 1, "");
        constexpr bool TB = (SBT >> layer) & 1;
        float bv[8][5];
        f32x2 pr[5][2], ps[5][2];
        auto powers = [&]() __attribute__((always_inline)) {
            if constexpr (TB) {
#pragma unroll
                for (int nb = 0; nb < 5; ++nb) {
                    const f32x2 m = {lr[nb] * lr[nb] - ls[nb] * ls[nb], 2.0f * lr[nb] * ls[nb]};
                    pr[nb][0] = f32x2{lr[nb], m.x}; ps[nb][0] = f32x2{ls[nb], m.y};
#pragma unroll
                    for (int j = 1; j < 2; ++j) pstep(pr[nb][j], ps[nb][j], pr[nb][j - 1], ps[nb][j - 1], m);
#pragma unroll
                    for (int j = 0; j < 2; ++j) { pin(pr[nb][j]); pin(ps[nb][j]); }
                }
                SCHED();
            }
        };
        {
            float a0[5], a1[5];
            static_for<2>(LAMBDA(m_) {
                constexpr int mb = m_.value;
#pragma unroll
                for (int nb = 0; nb < 5; ++nb) {
                    const f32x4 v = __builtin_elementwise_fma(acc[mb][nb], rr[mb], f32x4{tt[mb][nb][0], tt[mb][nb][1], tt[mb][nb][2], tt[mb][nb][3]});
#pragma unroll
                    for (int i = 0; i < 4; ++i) bv[4 * mb + i][nb] = v[i];
                }
                SCHED();
                if constexpr (TB) {
#pragma unroll
                    for (int i = mb ? 4 : 1; i < 4 + 4 * mb; ++i) cstepi(bv[i][0], bv[i][1], bv[i][2], bv[i][3], bv[i][4], bv[i - 1][0], bv[i - 1][1], bv[i - 1][2], bv[i - 1][3], bv[i - 1][4], lr, ls);
#pragma unroll
                    for (int nb = 0; nb < 5; ++nb) a0[nb] = bv[7][nb];
                }
                if constexpr (mb == 0 && !TB) cstep(a0, bv[0], lr, ls, bv[1][0], bv[1][1], bv[1][2], bv[1][3], bv[1][4]);
#pragma unroll
                for (int i8 = mb ? 4 : 2; i8 < (TB ? 0 : 4 + 4 * mb); i8 += 2) {
                    cstep(a1, a0, lr, ls, bv[i8][0], bv[i8][1], bv[i8][2], bv[i8][3], bv[i8][4]);
                    cstep(a0, a1, lr, ls, bv[i8 + 1][0], bv[i8 + 1][1], bv[i8 + 1][2], bv[i8 + 1][3], bv[i8 + 1][4]);
                }
                SCHED();
            });
#pragma unroll
            for (int nb = 0; nb < 5; ++nb) *(float*)(smem + sga + nb * 64) = a0[nb];
        }
        SCHED();
        const int w_ = w0;
        {
        const int w = w_, mt1 = mt1_;
        if (layer == 0 && __builtin_expect(!kearly, 0)) {
            unsigned f0_, f1_;
            asm volatile("s_waitcnt vmcnt(0)\n\t" AR("%0", 36, 0) AR("%1", 36, 1) : "=v"(f0_), "=v"(f1_)::"memory");
            kearly = __builtin_amdgcn_ballot_w64(((f0_ ^ tag0) | (f1_ ^ tag1)) != 0u) == 0ull;
            ORDER();
            if (kearly) KW_REQ()
        }
        asm volatile("s_waitcnt lgkmcnt(0)" ::: "memory");
        __builtin_amdgcn_s_barrier();
        asm volatile("" ::: "memory");
        STAMP(layer ? 14 : 2)
#pragma unroll
        for (int s = 0; s < 10; ++s) ks[s] = (unsigned)s * 1024u;
        constexpr int NX = 58;
        constexpr int NW2A = NW2P + 7 * kCR1 + kXA, NW3 = NW2P + kN3E + kN3;
        auto w2ld = LAMBDA(i_) {
            constexpr int i = i_.value;
            gld_w2<i>(w2o, p.ws + 2048 + ks[i / 5] * 5u);
        };
        auto w2a = LAMBDA(i_) {
            constexpr int k = i_.value;
            if constexpr (k < kXA) { w2ld(std::integral_constant<int, NW2P + 2 * k>{}); w2ld(std::integral_constant<int, NW2P + 2 * k + 1>{}); }
            else w2ld(std::integral_constant<int, NW2P + kXA + k>{});
        };
        auto w2x = LAMBDA(i_) {
            constexpr int i = i_.value < NX - 50 ? 50 + i_.value : NW2A + i_.value - (NX - 50) < 50 ? NW2A + i_.value - (NX - 50) : 99;
            if constexpr (i < 50) w2ld(std::integral_constant<int, i>{});
            else if constexpr (i < 52) lda4<40 + 4 * (i - 50), (i - 50) * 64>(xso, p.x0);
            else if constexpr (i == 52) lda4<48, 128>(xso - 2u * (unsigned)(bb & 16), p.x0);
            else if constexpr (i < NX) lda2<20 + 2 * (i - 53), 32 * (i - 53)>(dso, p.Dskip);
        };
        constexpr int NB = NX - NW2A, NB1 = KB1 < NB ? KB1 : NB, KPL = 7 * (8 - R2) - kFHP, NFH = NB - NB1 - (KPL - kFHK) > 0 ? NB - NB1 - (KPL - kFHK) : 0;
        auto w2b = LAMBDA(i_) {
            constexpr int k = i_.value - 7 * R2;
            if constexpr (k < NB1) w2x(std::integral_constant<int, k>{});
            if constexpr (k > kFHK) w2x(std::integral_constant<int, NB1 + k - kFHK - 1>{});
            if constexpr (k == kFHK) { vm_wait<NB1>(); st64(v2u{tag0, tag1}, rs, fho1); STAMP(39) }
            if constexpr (k == kFHK - 1) { STAMP(38) }
            if constexpr (k == KPL) poll1(fho, p.ws);
            static_assert(NB1 <= kFHK + 1 && NB1 + 7 * (7 - R2) + KG0 / 4 - kFHK - 1 >= NB && NW2A <= 50 && NW3 <= NW2A && NFH <= NB - NB1, "");
        };
        auto partb = [&]() __attribute__((always_inline)) {
            const float* sp = (const float*)(smem + spa);
            rsr = sp[0]; rsm = sp[8];
            if (layer == 1) { pin(rsr); pin(rsm); }
            if (layer == 1) {
                ACC_TIE5(dsk);
#pragma unroll
                for (int nb = 0; nb < 5; ++nb) { a2[3][nb] = xin[nb] + (xin[nb] * rsr + rsm) * unpk4(dsk[nb]); pin(a2[3][nb]); }
            }
        };
        constexpr int LBW = layer ? 2 : 3;
        if (w == 3) {
            site(); SCHED();
            f32x2 sv[16];
#pragma unroll
            for (int i = 0; i < 16; ++i) sv[i] = *(const f32x2*)(smem + spl + (l_sg(layer, i >> 2) - SC + (i & 3) * 320));
            f32x2 u[4], v[4], x[4];
            cfma4(u, l8, sv[0], sv[4], sv[8], sv[12], sv[1], sv[5], sv[9], sv[13]);
            cfma4(v, l8, u[0], u[1], u[2], u[3], sv[2], sv[6], sv[10], sv[14]);
            cfma4(x, l8, v[0], v[1], v[2], v[3], sv[3], sv[7], sv[11], sv[15]);
            f32x2 P[4][4];
            P[0][0] = f32x2{0.f, 0.f}; P[0][1] = x[0]; P[0][2] = cfma(l32, x[0], x[1]); P[0][3] = cfma(l32, P[0][2], x[2]);
            const f32x2 E = cfma(l32, P[0][3], x[3]);
            st128(v4u{__float_as_uint(E.x), tag0, __float_as_uint(E.y), tag0}, rs, es);
            SCHED();
            if (layer == 0) { FW_OK() static_for<kN3E>(LAMBDA(i_) { w2ld(std::integral_constant<int, NW2P + i_.value>{}); }); }
#pragma unroll
            for (int kk = 1; kk < 4; ++kk) cfma4(P[kk], l8, P[kk - 1][0], P[kk - 1][1], P[kk - 1][2], P[kk - 1][3], sv[kk - 1], sv[kk + 3], sv[kk + 7], sv[kk + 11]);
#pragma unroll
            for (int i = 0; i < 16; ++i) *(f32x2*)(smem + spl + (l_sg(layer, i >> 2) - SC + (i & 3) * 320)) = P[i & 3][i >> 2];
            if (layer == 0) partb();
            if (layer == 1) powers();
        }
        if (w == LBW) {
            site(); SCHED();
            f32x2 wg[17], t4[4];
            unsigned ea[15], tg[15];
            const f32x2 z2 = {0.f, 0.f};
            wg[1] = cmul(l128, l128); wg[2] = cmul(wg[1], wg[1]); wg[3] = cmul(wg[2], wg[1]); wg[4] = cmul(wg[2], wg[2]);
#pragma unroll
            for (int i = 4; i < 16; i += 4) {
                cfma4(t4, wg[i], wg[1], wg[2], wg[3], wg[4], z2, z2, z2, z2);
#pragma unroll
                for (int e = 0; e < 4; ++e) wg[i + 1 + e] = t4[e];
            }
#pragma unroll
            for (int i = 0; i < 15; ++i) {
                const int ch = mt1 - 1 - i;
                if (i) pin(wg[i]);
                ea[i] = ch < 0 ? kNone : (unsigned)ch * 10240u; tg[i] = ch < 0 ? 0u : tag0;
                pins(ea[i]); pins(tg[i]);
            }
            powers();
            f32x2 H0, H1;
            __builtin_amdgcn_s_sleep(layer ? KLB1 : KLB0);
            unsigned spins = dead ? kSpinCap : 0u;
            bool miss;
#pragma nounroll
            do {
                asm volatile("" ::: "memory");
                v4u eg[15];
#pragma unroll
                for (int i = 0; i < 15; ++i) eg[i] = ld128s(rs, eo, ea[i]);
                SCHED();
                unsigned x = __builtin_amdgcn_bitop3_b32(eg[0].y, eg[0].w, tg[0], 0x7E);
                H0 = f32x2{__uint_as_float(eg[0].x), __uint_as_float(eg[0].z)}; H1 = f32x2{0.f, 0.f};
#pragma unroll
                for (int i = 1; i < 15; i += 2) {
                    x |= __builtin_amdgcn_bitop3_b32(eg[i].y, eg[i].w, tg[i], 0x7E) | __builtin_amdgcn_bitop3_b32(eg[i + 1].y, eg[i + 1].w, tg[i + 1], 0x7E);
                    pin(x);
                    gacc2(H0, H1, wg[i], wg[i + 1], eg[i], eg[i + 1]);
                    SCHED();
                }
                miss = __builtin_amdgcn_ballot_w64(x != 0u) != 0ull;
            } while (miss && ++spins <= kSpinCap);
            dead = dead || miss;
            ORDER();
            *(f32x2*)(smem + spl + (L_CW - SC)) = H0 + H1;
            STAMP(layer ? 36 : 37)
            if (layer == 0) {
                static_for<NW3 - NW2P - kN3E>(LAMBDA(i_) { w2ld(std::integral_constant<int, NW2P + kN3E + i_.value>{}); });
            }
        }
        int w2_ = w;
        pins(w2_);
        if (layer == 0) UNDEF8(ca);
        if (layer == 0 && w2_ != LBW) {
            if (cv) {
                site(); SCHED();
                FW_OK()
                st_ur((unsigned)(size_t)(LAS char*)smem + (unsigned)uwa);
                STAMP(32)
                conv_part<0, kCR1>(ca, smem, ab, bb, w2a, [&]() __attribute__((always_inline)) {
                    v4u ad;
                    const unsigned dv = sdq << (adm & 16u);
#pragma unroll
                    for (int i = 0; i < 4; ++i) ad[i] = dv & (unsigned)__builtin_amdgcn_sbfe((int)adm, 8 + i, 1);
                    asm volatile("s_nop 1" : "+v"(ad));
                    return ad;
                }, cA, cB);
                partb();
                powers();
            } else {
                FW_OK()
                static_for<NW2A - NW2P>(LAMBDA(i_) { w2ld(std::integral_constant<int, NW2P + i_.value>{}); __builtin_amdgcn_s_sleep(1); });
                partb();
                powers();
            }
        }
        if (layer == 1 && w < LBW) { site(); SCHED(); powers(); }
        asm volatile("s_waitcnt lgkmcnt(0)" ::: "memory");
        __builtin_amdgcn_s_barrier();
        asm volatile("" ::: "memory");
        STAMP(layer ? 15 : 4)
        {
            float cw[5], ex[5];
#pragma unroll
            for (int nb = 0; nb < 5; ++nb) { cw[nb] = *(const float*)(smem + cwa + nb * 64); ex[nb] = *(const float*)(smem + sga + nb * 64); }
            v2u ta[5], td[5];
            if constexpr (TB) {
                cstepi(ex[0], ex[1], ex[2], ex[3], ex[4], cw[0], cw[1], cw[2], cw[3], cw[4], lqr, lqs);
                f32x2 cs[5];
                PIN5(ex);
#pragma unroll
                for (int nb = 0; nb < 5; ++nb) { cs[nb] = f32x2{ex[nb], dpp<0xB1>(ex[nb])}; pin(cs[nb]); }
                SCHED();
                static_for<5>(LAMBDA(n_) {
                    constexpr int nb = n_.value;
                    f32x2 h[4] = {{bv[0][nb], bv[1][nb]}, {bv[2][nb], bv[3][nb]}, {bv[4][nb], bv[5][nb]}, {bv[6][nb], bv[7][nb]}};
                    padd(h, pr[nb], ps[nb], cs[nb]);
                    *(v4u*)(smem + tbw + nb * 1024) = v4u{cvtpk(h[0].x, h[0].y), cvtpk(h[1].x, h[1].y), cvtpk(h[2].x, h[2].y), cvtpk(h[3].x, h[3].y)};
                    ta[nb] = __builtin_bit_cast(v2u, trread(smem, tbr + nb * 1024)); td[nb] = __builtin_bit_cast(v2u, trread(smem, tbr + nb * 1024 + 512));
                    SCHED();
                    if constexpr (nb >= LAG) { st128(v4u{ta[nb - LAG].x, ta[nb - LAG].y, td[nb - LAG].x, td[nb - LAG].y}, rs, hso + (nb - LAG) * 256); SCHED(); }
                });
            } else
            static_for<2>(LAMBDA(h_) {
                constexpr int n0 = 2 * h_.value;
                float hv[9][3];
                cstep3(hv[0], cw + n0, lqr + n0, lqs + n0, ex + n0);
                if constexpr (n0 == 0) {
                    chain24<0>(hv, bv, lr, ls);
                    chain24<0>(hv + 4, bv + 4, lr, ls);
                } else {
                    chain33<2>(hv, bv, lr, ls);
                    SCHED();
#pragma unroll
                    for (int rd = 0; rd < 2; ++rd) st128(v4u{ta[rd].x, ta[rd].y, td[rd].x, td[rd].y}, rs, hso + rd * 256);
                    SCHED();
                    chain33<2>(hv + 3, bv + 3, lr, ls);
                    chain32<2>(hv + 6, bv + 6, lr, ls);
                }
#pragma unroll
                for (int e = 0; e < (n0 ? 3 : 2); ++e) {
                    v4u o;
                    o.x = cvtpk(hv[1][e], hv[2][e]); o.y = cvtpk(hv[3][e], hv[4][e]);
                    o.z = cvtpk(hv[5][e], hv[6][e]); o.w = cvtpk(hv[7][e], hv[8][e]);
                    *(v4u*)(smem + tbw + (n0 + e) * 1024) = o;
                }
#pragma unroll
                for (int rd = n0; rd < (n0 ? 5 : 2); ++rd) { ta[rd] = __builtin_bit_cast(v2u, trread(smem, tbr + rd * 1024)); td[rd] = __builtin_bit_cast(v2u, trread(smem, tbr + rd * 1024 + 512)); }
                SCHED();
            });
#pragma unroll
            for (int rd = TB ? 5 - LAG : 2; rd < 5; ++rd) st128(v4u{ta[rd].x, ta[rd].y, td[rd].x, td[rd].y}, rs, hso + rd * 256);
            SCHED();
        }
#pragma unroll
        for (int s = 0; s < 10; ++s) pins(ks[s]);
        if (layer == 1) { partb(); SCHED(); DRAIN(); st64(v2u{tag0, tag1}, rs, fho1); }
        STAMP(layer ? 16 : 5)
        if (cv) {
            if constexpr (layer == 0) site();
            SCHED();
            conv_part<kCR1, 8>(ca, smem, ab, bb, w2b, []() {}, cA, cB);
        } else if (layer == 0) {
            static_for<NB1>(LAMBDA(i_) { w2x(i_); __builtin_amdgcn_s_sleep(1); });
            vm_wait<NB1>();
            st64(v2u{tag0, tag1}, rs, fho1);
            static_for<NB - NB1 - NFH>(LAMBDA(i_) { w2x(std::integral_constant<int, NB1 + i_.value>{}); __builtin_amdgcn_s_sleep(1); });
            if (w == 3) static_for<NW2A - NW3>(LAMBDA(i_) { w2ld(std::integral_constant<int, NW3 + i_.value>{}); __builtin_amdgcn_s_sleep(1); });
            poll1(fho, p.ws);
            static_for<NFH>(LAMBDA(i_) { w2x(std::integral_constant<int, NB - NFH + i_.value>{}); __builtin_amdgcn_s_sleep(1); });
            static_assert(NFH <= NB - (NX - 50), "");
            UNDEF8(cA[LP]); UNDEF8(cB[LP]);
        }
        if (layer == 0) { unsigned f0_, f1_; asm volatile("s_waitcnt vmcnt(%2)\n\t" AR("%0", 36, 0) AR("%1", 36, 1) : "=v"(f0_), "=v"(f1_) : "i"(NFH) : "memory"); fh = v2u{f0_, f1_}; }
        STAMP(layer ? 29 : 6)
        }
        unsigned g[8][2];

            v2u pt[4];
            if (layer == 0) WAVE_RETRY(ISTAG(fh), fh = ld64(rs, fho))
            else { pipe_flag<KPF0>(pt, fho, p.ws, (u64)tag0 | ((u64)tag1 << 32), dead); ORDER(); }
            STAMP(layer ? 17 : 31)
            site(); SCHED();
            constexpr int RING2 = layer ? KR2 : KRT;
            v4u ar[RING2];
            constexpr int NRT = layer ? RING2 : (36 - KG0) / KRS + 2 < KRT ? (36 - KG0) / KRS + 2 : KRT;
            unsigned xs_[3][4];
            auto gate = LAMBDA(n_) {
                constexpr int n = n_.value;
                if constexpr (n >= 100) swp2n(g[2 * (n - 100)][0], g[2 * (n - 100) + 1][0], g[2 * (n - 100)][1], g[2 * (n - 100) + 1][1]);
                else if constexpr (n >= 0) cvt2(g[n][0], g[n][1], ca[n]);
            };
            if constexpr (layer == 0) {
#pragma unroll
                for (int i = 0; i < 2; ++i) { ar[i] = ld128s(rs, ho[0], ks[i]); SCHED(); }
                static_for<36 - KG0>(LAMBDA(n_) {
                    constexpr int n = n_.value, g = KG0 + n;
                    constexpr int a = g < 8 ? 0 : g < 15 ? 1 : g < 21 ? 2 : g < 26 ? 3 : g < 30 ? 4 : g < 33 ? 5 : g < 35 ? 6 : 7;
                    constexpr int e = g - (a * 8 - a * (a - 1) / 2), i = 2 + n / KRS;
                    mf(ca[a + e], cA[LP][a], cB[LP][e]);
                    SCHED();
                    if constexpr (n % KRS == 0 && i < NRT) ar[i] = ld128s(rs, ho[i / 10], ks[i % 10]);
                    if constexpr (n < 12) rda<40 + n>(xs_[n >> 2][n & 3]);
                    constexpr int gt_[12] = {0, 1, 2, -1, 100, -1, 3, -1, -1, 101, 4, -1};
                    if constexpr (g >= 24) gate(IC(gt_[g < 24 ? 0 : g - 24]));
                    site();
                    SCHED();
                });
                static_assert(KG0 <= 15 && NRT >= 12, "");
            } else {
#pragma unroll
                for (int i = 0; i < RING2; ++i) { ar[i] = ld128s(rs, ho[i / 10], ks[i % 10]); SCHED(); }
            }
        v4u gl[3];
        v2u f0;
        bool gearly = false;
        {
            const char* src = smem + srca;
            const char *glb1 = p.ws + 32 * 4096, *glb2 = p.ws + 48 * 4096;
            float zx[4], zd[4], zt[4], zz[5][4];
            if constexpr (STAMPS == 1) { STAMP(layer ? 50 : 40) }
            static_for<40>(LAMBDA(i_) {
                constexpr int i = i_.value, m = i / 10, s = i % 10;
                static_for<5>(LAMBDA(k_) {
                    constexpr int kk = k_.value, n = 2 * (i - 3) + kk / 2 - 3, nb = n / 8, u = n % 8, pv = i - 1 + RING2;
                    if constexpr (s == 0 && m < 3) mfa0<kk>(a2[m][kk], ar[i % RING2]); else mfa<5 * s + kk>(a2[m][kk], ar[i % RING2]);
                    SCHED();
                    constexpr int gu_[12] = {-1, 5, -1, -1, 102, 6, -1, -1, 7, -1, 103, -1};
                    if constexpr (layer == 0 && 5 * i + kk < 12) gate(IC(gu_[(5 * i + kk) % 12]));
                    if constexpr (layer == 0 && i == 25 && kk == 1) { pins(glb1); pins(glb2); }
                    if constexpr (layer == 0 && i >= 3 && i < 26 && (kk & 1)) {
                        if constexpr (n < 0) swp2n(xs_[n + 3][0], xs_[n + 3][2], xs_[n + 3][1], xs_[n + 3][3]);
                        else if constexpr (nb < 5) {
                            if constexpr (u < 2) unp2(zx[2 * u], zx[2 * u + 1], xs_[nb >> 1][2 * (nb & 1) + u]);
                            else if constexpr (u < 4) unp2(zd[2 * u - 4], zd[2 * u - 3], dsk[nb][u - 2]);
                            else if constexpr (u < 6) fma2(zt[2 * u - 8], zt[2 * u - 7], zx[2 * u - 8], zx[2 * u - 7], rsr, rsr, rsm, rsm);
                            else fma2(zz[nb][2 * u - 12], zz[nb][2 * u - 11], zt[2 * u - 12], zt[2 * u - 11], zd[2 * u - 12], zd[2 * u - 11], zx[2 * u - 12], zx[2 * u - 11]);
                        }
                    }
                    if constexpr (i == 0 && kk == 0 && layer == 1) { pin(pt[0]); pin(pt[1]); pin(pt[2]); pin(pt[3]); }
                    if constexpr (layer == 0 && i < 2 && kk > 0 && 12 + 4 * i + kk - 1 >= NRT && 12 + 4 * i + kk - 1 < RING2) ar[12 + 4 * i + kk - 1] = ld128s(rs, ho[1], ks[2 + 4 * i + kk - 1]);
                    if constexpr (layer == 0 && i == kGSI && kk > 0) st128(v4u{g[2 * kk - 2][0], g[2 * kk - 2][1], g[2 * kk - 1][0], g[2 * kk - 1][1]}, rs, gso + (kk - 1) * 1024);
                    if constexpr (layer == 0 && i > 2 && i <= 7 && kk == (i == kGSI ? 0 : 2)) {
                        { unsigned d0_, d1_; rda<20 + 2 * (i - 3)>(d0_); rda<21 + 2 * (i - 3)>(d1_); dsk[i - 3] = v2u{d0_, d1_}; }
                    }
                    if constexpr (kk == 0 && i > 0 && pv < 40) ar[pv % RING2] = ld128s(rs, ho[pv / 10], ks[pv % 10]);
                    constexpr int s1 = s - 1 - (KMB && layer == 0 && m == 1 ? kFGI - 10 : 0);
                    if constexpr (kk == 2 && m >= 1 && s1 >= 0 && s1 < 5) *(f32x4*)(smem + dst[m - 1] + s1 * 1024) = a2[m - 1][s1];
                    site();
                    if constexpr (i >= 37) a2[i - 37][kk] = *(const f32x4*)(src + (i - 37) * 5120 + kk * 1024);
                    SCHED();
                });
                if constexpr (i == 10 && (layer || !KMB)) { asm volatile("s_waitcnt lgkmcnt(0)" ::: "memory"); __builtin_amdgcn_s_barrier(); asm volatile("" ::: "memory"); }
                if constexpr (i == kFGI && layer == 0) {
                    static_assert(kFGI >= 10 && kFGI <= 14, "");
                    asm volatile("s_waitcnt vmcnt(%0) lgkmcnt(0)" ::"i"(kFGI - kGSI) : "memory");
                    __builtin_amdgcn_s_barrier(); asm volatile("" ::: "memory");
                    st64(v2u{tag0, tag1}, rs, fgo1);
                }
                if constexpr (i == 26 && layer == 0) {
#pragma unroll
                    for (int nb = 0; nb < 5; ++nb) { a2[3][nb] = f32x4{zz[nb][0], zz[nb][1], zz[nb][2], zz[nb][3]}; pin(a2[3][nb]); }
                }
                if constexpr (i == kGPI && layer == 0) gld2s<0>(f0, fgo, p.ws);
                if constexpr (i == kGCI && layer == 0) {
                    wait1(f0);
                    static_for<39 - i>(LAMBDA(r_) { pin(ar[(i + 1 + r_.value) % RING2]); });
                    gearly = __builtin_amdgcn_ballot_w64(!ISTAG(f0)) == 0ull;
                    ORDER();
                    undef3(gl);
                    if (__builtin_expect(gearly, 1)) { gld4t<0>(gl[0], glo, p.ws); gld4t<0>(gl[1], glo, glb1); gld4t<0>(gl[2], glo, glb2); }
                    else if (G2ND) poll2(fgo, fgo, p.ws);
                }
                if constexpr (i == 36) {
                    asm volatile("s_waitcnt lgkmcnt(0)" ::: "memory");
                    __builtin_amdgcn_s_barrier(); asm volatile("" ::: "memory");
                }
                if constexpr (STAMPS == 1 && (i == 0 || i == 9 || i == 10 || i == 15 || i == 25 || i == 35 || i == 36)) { STAMP((layer ? 51 : 41) + (i == 0 ? 0 : i == 9 ? 1 : i == 10 ? 2 : i == 15 ? 3 : i == 25 ? 4 : i == 35 ? 5 : 6)) }
                SCHED();
            });
            STAMP(layer ? 18 : 8)
        }
#pragma unroll
        for (int nb = 0; nb < 5; ++nb) a2[0][nb] = (a2[0][nb] + a2[1][nb]) + a2[2][nb];
        asm volatile("s_nop 1" : "+v"(a2[3][0]), "+v"(a2[3][1]), "+v"(a2[3][2]), "+v"(a2[3][3]), "+v"(a2[3][4]),
                     "+v"(a2[0][0]), "+v"(a2[0][1]), "+v"(a2[0][2]), "+v"(a2[0][3]), "+v"(a2[0][4]));
        float ty;
        {
            f32x4 s1, s2;
#pragma unroll
            for (int nb = 0; nb < 5; ++nb) {
                const f32x4 y = a2[3][nb] + a2[0][nb];
                xin[nb] = y;
                if (nb == 0) { s1 = y; s2 = y * y; } else { s1 += y; s2 += y * y; }
            }
            ty = xsum2h(hsum4(s1), hsum4(s2));
            st64(v2u{__float_as_uint(ty), tag0}, rs, sgs);
        }
        SCHED();
        STAMP(layer ? 19 : 9)
        int lane_o, b_o = b0, w_o = w0;
        asm volatile("v_mbcnt_lo_u32_b32 %0, -1, 0\n\tv_mbcnt_hi_u32_b32 %0, -1, %0" : "=v"(lane_o));
        asm volatile("" : "+s"(b_o), "+s"(w_o));
        const int lane = lane_o, b = b_o, w = w_o;
        const int c = lane & 15, q = lane >> 4;
        TILES()
        if (layer == 0 && __builtin_expect(!gearly, 0)) {
            if (G2ND) {
                unsigned f0_, f1_;
                asm volatile("s_waitcnt vmcnt(1)\n\t" AR("%0", 36, 0) AR("%1", 36, 1) : "=v"(f0_), "=v"(f1_)::"memory");
                f0 = v2u{f0_, f1_};
            }
            WAVE_RETRY(ISTAG(f0), f0 = ld64(rs, fgo))
            gld4t<0>(gl[0], glo, p.ws); gld4t<0>(gl[1], glo, p.ws + 32 * 4096); gld4t<0>(gl[2], glo, p.ws + 48 * 4096);
            asm volatile("s_waitcnt vmcnt(0)" : "+v"(gl[0]), "+v"(gl[1]), "+v"(gl[2])::"memory");
        }
        if (layer == 0) {
            asm volatile("s_waitcnt vmcnt(1)" : "+v"(gl[0]), "+v"(gl[1]), "+v"(gl[2])::"memory");
            char* tb = smem + SC + w * INBOX;
            *(v4u*)(tb + lane * 16) = gl[0]; *(v4u*)(tb + 1024 + lane * 16) = gl[1]; *(v4u*)(tb + 1536 + lane * 16) = gl[2];
#pragma unroll
            for (int nb = 0; nb < 5; ++nb) gt[nb] = __builtin_bit_cast(v2u, trread(tb, (16 * nb + 4 * q + (c >> 2)) * 32 + (c & 3) * 8));
        } else ACC_TIE5(gt);
        SCHED();
        f32x4 gf[5], yg[5];
#pragma unroll
        for (int nb = 0; nb < 5; ++nb) { gf[nb] = unpk4(gt[nb]); yg[nb] = xin[nb] * gf[nb]; pin(gf[nb]); pin(yg[nb]); }
        unsigned xs = OFF_X2F + (unsigned)((2 * mt2 + (w >> 1)) * 40960 + ((c >> 2) & 1) * 20480 + (10 * nt2 + 2 * (q & 1) + (q >> 1)) * 256 + (8 * (w & 1) + 4 * (c >> 3) + (c & 3)) * 16);
        unsigned pso = q < 2 ? OFF_PS + (unsigned)(((mt2 * 4 + w) * 8 + nt2) * 128 + c * 8 + q * 4) : kNone;
        unsigned fxo1 = lane ? kNone : OFF_FX + (unsigned)(((mt1 * 4 + 2 * (mt2 & 1) + (w >> 1)) * 16 + nt2 * 2 + (w & 1)) * 128);
        unsigned oo = (unsigned)(((64 * mt2 + 16 * w + c) * D_ + 80 * nt2 + 4 * q) * 4);
        if (layer == 0) { pin(xs); pin(pso); pin(fxo1); } else pin(oo);
        SCHED();
        {
            float a, d;
            if constexpr (layer == 0) site();
            pipe_gran<layer ? PDS : PDS0>(a, d, sgo, p.ws, tag0, dead);
            STAMP(layer ? 20 : 10)
            xsum2(a, d);
            const float me = a * (1.0f / D_);
            const float ri = __builtin_amdgcn_rsqf(fmaxf(d * (1.0f / D_) - me * me, 0.f) + 1e-5f), mr = -me * ri;
#pragma unroll
            for (int nb = 0; nb < 5; ++nb) xin[nb] = yg[nb] * ri + (gf[nb] * mr + xin[nb]);
        }
        if constexpr (layer == 0) {
            v2u pk[5];
#pragma unroll
            for (int nb = 0; nb < 5; ++nb) { const f32x4 x = xin[nb]; pk[nb] = v2u{cvtpk(x.x, x.y), cvtpk(x.z, x.w)}; }
#pragma unroll
            for (int i = 0; i < 3; ++i) {
                const v2u pa = pk[2 * i], pb = pk[i < 2 ? 2 * i + 1 : 4];
                const auto r0 = __builtin_amdgcn_permlane16_swap(pa.x, pb.x, false, false);
                const auto r1 = __builtin_amdgcn_permlane16_swap(pa.y, pb.y, false, false);
                if (i < 2 || (q & 1) == 0) st128(v4u{r0[0], r1[0], r0[1], r1[1]}, rs, xs + i * 1024);
            }
            {
                f32x4 s1, s2;
#pragma unroll
                for (int nb = 0; nb < 5; ++nb) { const f32x4 x = xin[nb]; if (nb == 0) { s1 = x; s2 = x * x; } else { s1 += x; s2 += x * x; } }
                __builtin_amdgcn_raw_buffer_store_b32(__float_as_uint(xsum2h(hsum4(s1), hsum4(s2))), rs, pso, 0, SC1);
            }
            SCHED();
            int w1lane0 = (c & 1) * 10240 + (c >> 1) * 128 + ((q ^ ((c & 1) | ((c >> 2) << 1))) * 16);
            int w1lane1 = (c & 1) * 10240 + (c >> 1) * 128 + (((4 + q) ^ ((c & 1) | ((c >> 2) << 1))) * 16);
            unsigned fxo = OFF_FX + (unsigned)((mt1 * 4 + w) * 2048 + c * 128);
            unsigned xo = OFF_X2F + (unsigned)((4 * mt1 + w) * 40960 + lane * 16), xo1 = xo + 20480u;
            const int row = 8 * q + (c & 7);
            unsigned po = OFF_PS + (unsigned)((((2 * mt1 + (w >> 1)) * 4 + 2 * (w & 1) + (row >> 4)) * 8 + 4 * (c >> 3)) * 128 + (row & 15) * 8);
            int w1hi0 = w1lane0 + 61440, w1hi1 = w1lane1 + 61440;
            pin(w1lane0); pin(w1lane1); pin(w1hi0); pin(w1hi1); pin(fxo); pin(xo); pin(xo1); pin(po);
            v4u wf[2][5];
#pragma unroll
            for (int nb = 0; nb < 5; ++nb) wf[0][nb] = *(const v4u*)(smem + w1lane0 + nb * 20480);
            v2u tg2 = {tag0, tag1};
            pin(tg2);
            SCHED();
            DRAIN();
            st64(tg2, rs, fxo1);
            if (b == 255 && w == 0) FLAG(OFF_NONCE)
            STAMP(11)
            addrs(std::integral_constant<int, 1>{}, lane, w, b);
            v2u pt[4];
            pipe_flag<KPFX>(pt, fxo, p.ws, (u64)tag0 | ((u64)tag1 << 32), dead);
            ORDER();
            STAMP(12)
            v4u ar[RING1][2];
#pragma unroll
            for (int s = 0; s < RING1; ++s)
#pragma unroll
                for (int mb = 0; mb < 2; ++mb) { ar[s][mb] = ld128s(rs, mb ? xo1 : xo, s * 1024); SCHED(); }
            v2u ps[4];
#pragma unroll
            for (int i = 0; i < 4; ++i) ps[i] = ld64(rs, po + i * 128);
            SCHED();
            float t1, t2, rinv, mri, rr_[8], rm_[8];
            static_for<20>(LAMBDA(s_) {
                constexpr int s = s_.value, cu = s & 1, nx = cu ^ 1;
                LGKM0() SCHED();
                static_for<10>(LAMBDA(g_) {
                    constexpr int g = g_.value, mb = g / 5, k = g % 5;
                    if constexpr (s == 0) mf0(acc[mb][k], ar[0][mb], wf[0][k]); else mf(acc[mb][k], ar[s % RING1][mb], wf[cu][k]);
                    SCHED();
                    if constexpr (s == 0 && g == 0) { pin(pt[0]); pin(pt[1]); pin(pt[2]); pin(pt[3]); }
                    if constexpr (s < 19 && g < 3) wf[nx][g] = *(const v4u*)(smem + (nx ? w1lane1 : w1lane0) + g * 20480 + ((s + 1) >> 1) * 1024);
                    if constexpr (s < 19 && (g == 3 || g == 4)) wf[nx][g] = *(const v4u*)(smem + (nx ? w1hi1 : w1hi0) + (g - 3) * 20480 + ((s + 1) >> 1) * 1024);
                    if constexpr (g == 5 && s + RING1 < 20) ar[s % RING1][0] = ld128s(rs, xo, (s + RING1) * 1024);
                    if constexpr (g == 2 && s > 0 && s - 1 + RING1 < 20) ar[(s - 1) % RING1][1] = ld128s(rs, xo1, (s - 1 + RING1) * 1024);
                    if constexpr (s == 8 && g == 8) { t1 = __uint_as_float(ps[0].x) + __uint_as_float(ps[1].x); pin(t1); t2 = __uint_as_float(ps[0].y) + __uint_as_float(ps[1].y); pin(t2); }
                    if constexpr (s == 9 && g == 6) { t1 += __uint_as_float(ps[2].x); pin(t1); t2 += __uint_as_float(ps[2].y); pin(t2); }
                    if constexpr (s == 9 && g == 7) { t1 += __uint_as_float(ps[3].x); pin(t1); t2 += __uint_as_float(ps[3].y); pin(t2); }
                    if constexpr (s == 10 && g == 6) { t1 += dpp<0x128>(t1); t2 += dpp<0x128>(t2); pin(t1); pin(t2); }
                    if constexpr (s == 10 && g == 7) { t1 *= (1.0f / D_); pin(t1); t2 *= (1.0f / D_); pin(t2); }
                    if constexpr (s == 10 && g == 8) { t2 = fmaxf(t2 - t1 * t1, 0.f); pin(t2); }
                    if constexpr (s == 11 && g == 6) { rinv = __builtin_amdgcn_rsqf(t2 + 1e-5f); pin(rinv); }
                    if constexpr (s == 11 && g == 8) { mri = -t1 * rinv; pin(mri); }
                    if constexpr (s == 12 && g == 4) *(float*)(smem + l_rs(1, 0) + w * INBOX + lane * 4) = c < 8 ? rinv : mri;
                    if constexpr (s >= 12 && s < 15 && g >= 6 && g < 9 && 3 * (s - 12) + g - 6 < 8) bc2b<3 * (s - 12) + g - 6>(rr_[3 * (s - 12) + g - 6], rm_[3 * (s - 12) + g - 6], rinv, mri);
                    if constexpr (s >= 15 && s < 19 && g >= 5) TT_GAP(5 * (s - 15) + g - 5)
                    site();
                    SCHED();
                });
            });
            rr[0] = f32x4{rr_[0], rr_[1], rr_[2], rr_[3]}; rr[1] = f32x4{rr_[4], rr_[5], rr_[6], rr_[7]};
            asm volatile("kpf_end2:");
            ACC_FENCE10(acc[0], acc[1]);
            STAMP(13)
        } else {
#pragma unroll
            for (int nb = 0; nb < 5; ++nb) __builtin_amdgcn_raw_buffer_store_b128(__builtin_bit_cast(v4u, xin[nb]), ro, oo + 64 * nb, 0, OUTP);
            STAMP(21)
            STAMP_DUMP()
            asm volatile("kpf_end:");
        }
    };
    layer_body(std::integral_constant<int, 0>{});
    layer_body(std::integral_constant<int, 1>{});
}

extern "C" void kernel_launch(void* const* d_in, const int* in_sizes, int n_in,
                              void* d_out, int out_size, void* d_ws, size_t ws_size,
                              hipStream_t stream) {
    Params p;
    p.x0 = (const u16*)d_in[0];
    p.Lre = (const u16*)d_in[1];
    p.Lim = (const u16*)d_in[2];
    p.Bre = (const u16*)d_in[3];
    p.Bim = (const u16*)d_in[4];
    p.Cre = (const u16*)d_in[5];
    p.Cim = (const u16*)d_in[6];
    p.logdt = (const u16*)d_in[7];
    p.Dskip = (const u16*)d_in[8];
    p.k = (const u16*)d_in[10];
    p.v = (const u16*)d_in[11];
    p.shk = (const u16*)d_in[12];
    p.ssmk = (const u16*)d_in[13];
    p.ssmD = (const u16*)d_in[14];
    p.ws = (char*)d_ws;
    p.out = (float*)d_out;
    k_main<<<256, 256, 0, stream>>>(p);
}
```

```cpp
#include <hip/hip_runtime.h>
#include <stdint.h>
#include <utility>

#define STAMPS 0

typedef unsigned short u16;
typedef unsigned long long u64;
typedef unsigned v4u __attribute__((ext_vector_type(4)));
typedef unsigned v2u __attribute__((ext_vector_type(2)));
typedef float f32x4 __attribute__((ext_vector_type(4)));
typedef float f32x2 __attribute__((ext_vector_type(2)));
typedef short s16x4 __attribute__((ext_vector_type(4)));
typedef __bf16 bf16x2v __attribute__((ext_vector_type(2)));
#define LAS __attribute__((address_space(3)))
#define DI __device__ __forceinline__

constexpr int S_ = 2048, D_ = 640;
#define KG0 12
#define KRT 12
#define KRS 2
#define KB1 4
#define KMB 1
#define SBT 2
#define LAG 1
#define OTR 1
#define KR1 5
#define KR2 10
constexpr int RING1 = KR1;
#define KCR1 3
constexpr int kCR1 = KCR1;
#define KXA 10
#define KN3E 17
#define KN3 6
constexpr int kXA = KXA;
constexpr int kN3E = KN3E, kN3 = KN3;
#define KFHK 4
#define KFHP 12
constexpr int kFHK = KFHK, kFHP = KFHP;
#define KGPI 27
#define KGCI 33
#define KFGI 11
#define G2ND 1
#define KGSI 2
constexpr int kGSI = KGSI, kFGI = KFGI, kGPI = KGPI, kGCI = KGCI;

constexpr unsigned OFF_NONCE = 0;
constexpr unsigned OFF_ST = 4096;
constexpr unsigned OFF_FW = OFF_ST + 65536;
constexpr unsigned OFF_FK = OFF_FW + 131072;
constexpr unsigned OFF_FH = OFF_FK + 81920;
constexpr unsigned OFF_FX = OFF_FH + 262144;
constexpr unsigned OFF_FG = OFF_FX + 131072;
constexpr unsigned OFF_EG = OFF_FG + 98304;
constexpr unsigned OFF_SG = OFF_EG + 327680;
constexpr unsigned OFF_PS = OFF_SG + 524288;
constexpr unsigned OFF_KVT = OFF_PS + 131072;
constexpr unsigned OFF_GT = OFF_KVT + 2621440;
constexpr unsigned OFF_W2F = OFF_GT + 2621440;
constexpr unsigned OFF_X2F = OFF_W2F + 1638400;
constexpr unsigned OFF_DUM = OFF_X2F + 2621440;
constexpr unsigned OFF_HF = OFF_DUM + 163840;
constexpr unsigned WS_BYTES = OFF_HF + 2 * 5242880;
constexpr unsigned kNone = 0x40000000u;
constexpr unsigned OFF_DBG = 0x1800000;
#define MAP 0
#define TILES() const int xq = b & 7, j = b >> 3, mt2 = MAP ? 8 * (xq >> 1) + (j >> 2) : 4 * xq + (j >> 3), nt2 = MAP ? 4 * (xq & 1) + (j & 3) : j & 7, mt1 = mt2 >> 1, \
    nt1 = MAP ? 8 * (xq & 1) + (j & 7) : j & 15, i32 = 16 * (mt2 & 1) + mt1;

constexpr int SC = 102400;
constexpr int REGSZ = 13056;
constexpr int L_XR = SC;
constexpr int L_CO = SC + 12288;
constexpr int L_KB = SC + 51712;
constexpr int L_TAB = SC + 55808;
constexpr int L_CW = SC + 39168;
constexpr int INBOX = 15360;
constexpr int l_rs(int l, int w) { return l ? SC + w * INBOX : L_KB + w * 256; }
constexpr int l_sg(int l, int w) { return l ? SC + w * INBOX + 256 : SC + (w == 0 ? 43264 : w == 1 ? 46080 : w == 2 ? 52736 : 56320); }
constexpr int l_tb(int l, int w) { return l ? SC + w * INBOX + 2048 : SC + 40960 + w * 5120; }
constexpr int LDS_BYTES = 163840;
constexpr int CV_E = 4608;
constexpr int CV_O = CV_E + (1056 + 15) * 4;

struct Params {
    const u16 *x0, *Lre, *Lim, *Bre, *Bim, *Cre, *Cim, *logdt, *Dskip, *k, *v, *shk, *ssmk, *ssmD;
    char* ws;
    float* out;
};

#define MF "v_mfma_f32_16x16x32_bf16 "
DI void mf(f32x4& c, v4u a, v4u b) { asm(MF "%0, %1, %2, %0" : "+v"(c) : "v"(a), "v"(b)); }
DI void mf0(f32x4& c, v4u a, v4u b) { asm(MF "%0, %1, %2, 0" : "=&v"(c) : "v"(a), "v"(b)); }
constexpr int W2R = 56;
#define WA(n) "a[%c" #n ":%c" #n "+3]"
template <int R> DI void mfa(f32x4& c, v4u a) { asm(MF "%0, " WA(2) ", %1, %0" : "+v"(c) : "v"(a), "i"(W2R + 4 * R)); }
template <int R> DI void mfa0(f32x4& c, v4u a) { asm(MF "%0, " WA(2) ", %1, 0" : "=&v"(c) : "v"(a), "i"(W2R + 4 * R)); }
#define ACC_FENCE10(c, d) asm volatile("" : "+v"(c[0]), "+v"(c[1]), "+v"(c[2]), "+v"(c[3]), "+v"(c[4]), "+v"(d[0]), "+v"(d[1]), "+v"(d[2]), "+v"(d[3]), "+v"(d[4]))
#define ACC_TIE5(c) asm volatile("" : "+v"(c[0]), "+v"(c[1]), "+v"(c[2]), "+v"(c[3]), "+v"(c[4]))
#define QP " quad_perm:[1,0,3,2] row_mask:0xf bank_mask:0xf"
DI void cstep(float (&t)[5], const float (&h)[5], const float (&lr)[5], const float (&ls)[5], float b0, float b1, float b2, float b3, float b4) {
    asm("v_fma_f32 %0, %10, %5, %20\n\tv_fma_f32 %1, %11, %6, %21\n\tv_fma_f32 %2, %12, %7, %22\n\tv_fma_f32 %3, %13, %8, %23\n\tv_fma_f32 %4, %14, %9, %24\n\t"
        "v_fmac_f32_dpp %0, %5, %15" QP "\n\tv_fmac_f32_dpp %1, %6, %16" QP "\n\tv_fmac_f32_dpp %2, %7, %17" QP "\n\tv_fmac_f32_dpp %3, %8, %18" QP "\n\tv_fmac_f32_dpp %4, %9, %19" QP
        : "=&v"(t[0]), "=&v"(t[1]), "=&v"(t[2]), "=&v"(t[3]), "=&v"(t[4])
        : "v"(h[0]), "v"(h[1]), "v"(h[2]), "v"(h[3]), "v"(h[4]), "v"(lr[0]), "v"(lr[1]), "v"(lr[2]), "v"(lr[3]), "v"(lr[4]),
          "v"(ls[0]), "v"(ls[1]), "v"(ls[2]), "v"(ls[3]), "v"(ls[4]), "v"(b0), "v"(b1), "v"(b2), "v"(b3), "v"(b4));
}
DI void cstep3(float* t, const float* h, const float* lr, const float* ls, const float* b) {
    asm("v_fma_f32 %0, %6, %3, %12\n\tv_fma_f32 %1, %7, %4, %13\n\tv_fma_f32 %2, %8, %5, %14\n\t"
        "v_fmac_f32_dpp %0, %3, %9" QP "\n\tv_fmac_f32_dpp %1, %4, %10" QP "\n\tv_fmac_f32_dpp %2, %5, %11" QP
        : "=&v"(t[0]), "=&v"(t[1]), "=&v"(t[2])
        : "v"(h[0]), "v"(h[1]), "v"(h[2]), "v"(lr[0]), "v"(lr[1]), "v"(lr[2]), "v"(ls[0]), "v"(ls[1]), "v"(ls[2]), "v"(b[0]), "v"(b[1]), "v"(b[2]));
}
template <int o> DI void chain24(float (*t)[3], const float (*b)[5], const float* lr, const float* ls) {
    asm("v_fma_f32 %0, %18, %16, %8\n\t" "v_fma_f32 %1, %19, %17, %9\n\t" "v_fmac_f32_dpp %0, %16, %20" QP "\n\t" "v_fmac_f32_dpp %1, %17, %21" QP "\n\t"
        "v_fma_f32 %2, %18, %0, %10\n\t" "v_fma_f32 %3, %19, %1, %11\n\t" "v_fmac_f32_dpp %2, %0, %20" QP "\n\t" "v_fmac_f32_dpp %3, %1, %21" QP "\n\t"
        "v_fma_f32 %4, %18, %2, %12\n\t" "v_fma_f32 %5, %19, %3, %13\n\t" "v_fmac_f32_dpp %4, %2, %20" QP "\n\t" "v_fmac_f32_dpp %5, %3, %21" QP "\n\t"
        "v_fma_f32 %6, %18, %4, %14\n\t" "v_fma_f32 %7, %19, %5, %15\n\t" "v_fmac_f32_dpp %6, %4, %20" QP "\n\t" "v_fmac_f32_dpp %7, %5, %21" QP "\n\t"
        : "=&v"(t[1][0]), "=&v"(t[1][1]), "=&v"(t[2][0]), "=&v"(t[2][1]), "=&v"(t[3][0]), "=&v"(t[3][1]), "=&v"(t[4][0]), "=&v"(t[4][1])
        : "v"(b[0][o + 0]), "v"(b[0][o + 1]), "v"(b[1][o + 0]), "v"(b[1][o + 1]), "v"(b[2][o + 0]), "v"(b[2][o + 1]), "v"(b[3][o + 0]), "v"(b[3][o + 1]), "v"(t[0][0]), "v"(t[0][1]), "v"(lr[o + 0]), "v"(lr[o + 1]), "v"(ls[o + 0]), "v"(ls[o + 1]));
}
template <int o> DI void chain33(float (*t)[3], const float (*b)[5], const float* lr, const float* ls) {
    asm("v_fma_f32 %0, %21, %18, %9\n\t" "v_fma_f32 %1, %22, %19, %10\n\t" "v_fma_f32 %2, %23, %20, %11\n\t" "v_fmac_f32_dpp %0, %18, %24" QP "\n\t" "v_fmac_f32_dpp %1, %19, %25" QP "\n\t" "v_fmac_f32_dpp %2, %20, %26" QP "\n\t"
        "v_fma_f32 %3, %21, %0, %12\n\t" "v_fma_f32 %4, %22, %1, %13\n\t" "v_fma_f32 %5, %23, %2, %14\n\t" "v_fmac_f32_dpp %3, %0, %24" QP "\n\t" "v_fmac_f32_dpp %4, %1, %25" QP "\n\t" "v_fmac_f32_dpp %5, %2, %26" QP "\n\t"
        "v_fma_f32 %6, %21, %3, %15\n\t" "v_fma_f32 %7, %22, %4, %16\n\t" "v_fma_f32 %8, %23, %5, %17\n\t" "v_fmac_f32_dpp %6, %3, %24" QP "\n\t" "v_fmac_f32_dpp %7, %4, %25" QP "\n\t" "v_fmac_f32_dpp %8, %5, %26" QP "\n\t"
        : "=&v"(t[1][0]), "=&v"(t[1][1]), "=&v"(t[1][2]), "=&v"(t[2][0]), "=&v"(t[2][1]), "=&v"(t[2][2]), "=&v"(t[3][0]), "=&v"(t[3][1]), "=&v"(t[3][2])
        : "v"(b[0][o + 0]), "v"(b[0][o + 1]), "v"(b[0][o + 2]), "v"(b[1][o + 0]), "v"(b[1][o + 1]), "v"(b[1][o + 2]), "v"(b[2][o + 0]), "v"(b[2][o + 1]), "v"(b[2][o + 2]), "v"(t[0][0]), "v"(t[0][1]), "v"(t[0][2]), "v"(lr[o + 0]), "v"(lr[o + 1]), "v"(lr[o + 2]), "v"(ls[o + 0]), "v"(ls[o + 1]), "v"(ls[o + 2]));
}
template <int o> DI void chain32(float (*t)[3], const float (*b)[5], const float* lr, const float* ls) {
    asm("v_fma_f32 %0, %15, %12, %6\n\t" "v_fma_f32 %1, %16, %13, %7\n\t" "v_fma_f32 %2, %17, %14, %8\n\t" "v_fmac_f32_dpp %0, %12, %18" QP "\n\t" "v_fmac_f32_dpp %1, %13, %19" QP "\n\t" "v_fmac_f32_dpp %2, %14, %20" QP "\n\t"
        "v_fma_f32 %3, %15, %0, %9\n\t" "v_fma_f32 %4, %16, %1, %10\n\t" "v_fma_f32 %5, %17, %2, %11\n\t" "v_fmac_f32_dpp %3, %0, %18" QP "\n\t" "v_fmac_f32_dpp %4, %1, %19" QP "\n\t" "v_fmac_f32_dpp %5, %2, %20" QP "\n\t"
        : "=&v"(t[1][0]), "=&v"(t[1][1]), "=&v"(t[1][2]), "=&v"(t[2][0]), "=&v"(t[2][1]), "=&v"(t[2][2])
        : "v"(b[0][o + 0]), "v"(b[0][o + 1]), "v"(b[0][o + 2]), "v"(b[1][o + 0]), "v"(b[1][o + 1]), "v"(b[1][o + 2]), "v"(t[0][0]), "v"(t[0][1]), "v"(t[0][2]), "v"(lr[o + 0]), "v"(lr[o + 1]), "v"(lr[o + 2]), "v"(ls[o + 0]), "v"(ls[o + 1]), "v"(ls[o + 2]));
}
DI void pstep(f32x2& xr, f32x2& xs, f32x2 pr, f32x2 ps, f32x2 m) {
    asm("v_pk_mul_f32 %0, %2, %4 op_sel_hi:[1,0]\n\tv_pk_mul_f32 %1, %3, %4 op_sel_hi:[1,0]\n\t"
        "v_pk_fma_f32 %0, %3, %4, %0 op_sel:[0,1,0] neg_lo:[1,0,0] neg_hi:[1,0,0]\n\tv_pk_fma_f32 %1, %2, %4, %1 op_sel:[0,1,0]"
        : "=&v"(xr), "=&v"(xs) : "v"(pr), "v"(ps), "v"(m));
}
DI void padd(f32x2 (&h)[4], const f32x2 (&pr)[2], const f32x2 (&ps)[2], f32x2 c) {
    f32x2 d;
    asm("v_pk_mul_f32 %4, %6, %9 op_sel:[1,0]\n\tv_pk_fma_f32 %0, %5, %9, %0 op_sel_hi:[1,0,1]\n\tv_pk_fma_f32 %1, %6, %9, %1 op_sel_hi:[1,0,1]\n\t"
        "v_pk_fma_f32 %4, %8, %9, %4 op_sel:[1,1,0] op_sel_hi:[1,0,1] neg_hi:[1,0,0]\n\tv_pk_fma_f32 %0, %7, %9, %0 op_sel:[0,1,0]\n\tv_pk_fma_f32 %1, %8, %9, %1 op_sel:[0,1,0]\n\t"
        "v_pk_fma_f32 %2, %5, %4, %2 op_sel_hi:[1,0,1]\n\tv_pk_fma_f32 %3, %6, %4, %3 op_sel_hi:[1,0,1]\n\tv_pk_fma_f32 %2, %7, %4, %2 op_sel:[0,1,0]\n\tv_pk_fma_f32 %3, %8, %4, %3 op_sel:[0,1,0]"
        : "+v"(h[0]), "+v"(h[1]), "+v"(h[2]), "+v"(h[3]), "=&v"(d) : "v"(pr[0]), "v"(pr[1]), "v"(ps[0]), "v"(ps[1]), "v"(c));
}
DI void cstepi(float& b0, float& b1, float& b2, float& b3, float& b4, float h0, float h1, float h2, float h3, float h4, const float (&lr)[5], const float (&ls)[5]) {
    asm("v_fmac_f32 %0, %10, %5\n\tv_fmac_f32 %1, %11, %6\n\tv_fmac_f32 %2, %12, %7\n\tv_fmac_f32 %3, %13, %8\n\tv_fmac_f32 %4, %14, %9\n\t"
        "v_fmac_f32_dpp %0, %5, %15" QP "\n\tv_fmac_f32_dpp %1, %6, %16" QP "\n\tv_fmac_f32_dpp %2, %7, %17" QP "\n\tv_fmac_f32_dpp %3, %8, %18" QP "\n\tv_fmac_f32_dpp %4, %9, %19" QP
        : "+v"(b0), "+v"(b1), "+v"(b2), "+v"(b3), "+v"(b4)
        : "v"(h0), "v"(h1), "v"(h2), "v"(h3), "v"(h4), "v"(lr[0]), "v"(lr[1]), "v"(lr[2]), "v"(lr[3]), "v"(lr[4]), "v"(ls[0]), "v"(ls[1]), "v"(ls[2]), "v"(ls[3]), "v"(ls[4]));
}
#define UNDEF8(c) asm volatile("" : "=v"(c[0]), "=v"(c[1]), "=v"(c[2]), "=v"(c[3]), "=v"(c[4]), "=v"(c[5]), "=v"(c[6]), "=v"(c[7]))
#define PIN5(t) asm volatile("s_nop 1" : "+v"(t[0]), "+v"(t[1]), "+v"(t[2]), "+v"(t[3]), "+v"(t[4]))
DI unsigned cvtpk(float lo, float hi) {
    f32x2 v = {lo, hi};
    return __builtin_bit_cast(unsigned, __builtin_convertvector(v, bf16x2v));
}
DI float lo16f(unsigned u) { return __uint_as_float(u << 16); }
DI float hi16f(unsigned u) { return __uint_as_float(u & 0xffff0000u); }
DI float rbf(float v) { return lo16f(cvtpk(v, 0.f)); }
template <int CTRL> DI float dpp(float x) {
    return __int_as_float(__builtin_amdgcn_update_dpp(0, __float_as_int(x), CTRL, 0xF, 0xF, true));
}
DI float rowsum16(float x) {
    x += dpp<0x128>(x); x += dpp<0x124>(x); x += dpp<0x122>(x); x += dpp<0x121>(x);
    return x;
}
DI float xsum32(float x) { auto r = __builtin_amdgcn_permlane32_swap(__float_as_uint(x), __float_as_uint(x), false, false); return __uint_as_float(r[0]) + __uint_as_float(r[1]); }
DI void xsum2(float& a, float& d) {
    auto r = __builtin_amdgcn_permlane16_swap(__float_as_uint(a), __float_as_uint(d), false, false);
    const float s = __uint_as_float(r[0]) + __uint_as_float(r[1]);
    const float t = xsum32(s);
    auto u = __builtin_amdgcn_permlane16_swap(__float_as_uint(t), __float_as_uint(t), false, false);
    a = __uint_as_float(u[0]); d = __uint_as_float(u[1]);
}
DI float xsum2h(float a, float d) {
    auto r = __builtin_amdgcn_permlane16_swap(__float_as_uint(a), __float_as_uint(d), false, false);
    return xsum32(__uint_as_float(r[0]) + __uint_as_float(r[1]));
}
DI float hsum4(f32x4 v) { return (v.x + v.y) + (v.z + v.w); }
typedef __amdgpu_buffer_rsrc_t rsrc_t;
constexpr int SC1 = 16;
DI v4u ld128(rsrc_t rs, unsigned off) { return __builtin_amdgcn_raw_buffer_load_b128(rs, off, 0, SC1); }
DI v4u ld128s(rsrc_t rs, unsigned off, unsigned soff) { return __builtin_amdgcn_raw_buffer_load_b128(rs, off, soff, SC1); }
DI v2u ld64(rsrc_t rs, unsigned off) { return __builtin_amdgcn_raw_buffer_load_b64(rs, off, 0, SC1); }
DI void st128(v4u v, rsrc_t rs, unsigned off) { __builtin_amdgcn_raw_buffer_store_b128(v, rs, off, 0, SC1); }
DI void st64(v2u v, rsrc_t rs, unsigned off) { __builtin_amdgcn_raw_buffer_store_b64(v, rs, off, 0, SC1); }
#define LGKM0() { __builtin_amdgcn_s_waitcnt(0xC07F); asm volatile("" ::: "memory"); }
#define DRAIN() asm volatile("s_waitcnt vmcnt(0)" ::: "memory")
#define ORDER() __builtin_amdgcn_fence(__ATOMIC_ACQUIRE, "wavefront")
#define SCHED() __builtin_amdgcn_sched_barrier(0)
constexpr unsigned kSpinCap = 1u << 17;
#define WAVE_WAIT(...)                                                      \
    {                                                                       \
        unsigned spins_ = dead ? kSpinCap : 0u;                             \
        _Pragma("nounroll") for (;;) {                                      \
            asm volatile("" ::: "memory");                                  \
            bool ok;                                                        \
            __VA_ARGS__;                                                    \
            if (__builtin_amdgcn_ballot_w64(!ok) == 0ull) break;            \
            if (++spins_ > kSpinCap) { dead = true; break; }                \
            __builtin_amdgcn_s_sleep(1);                                    \
        }                                                                   \
        ORDER();                                                            \
    }
#define KPD1 2
#define KPDS 2
#define KPDS0 0
#define KPF0 14
#define KPFX 6
#define KPF1 4
constexpr int PD1 = KPD1, PDS = KPDS, PDS0 = KPDS0;
#define PL(r) "global_load_dwordx2 " r ", %5, %6 sc1\n\t"
#define PS_(r) "s_waitcnt vmcnt(1)\n\tv_cmp_ne_u64 vcc, %7, " r "\n\ts_cbranch_vccz 2f\n\t" PL(r)
template <int D0> DI void pipe_flag(v2u (&t)[4], unsigned off, const void* base, u64 tag, bool& dead) {
    unsigned n = dead ? 0u : kSpinCap;
    asm volatile("s_sleep %8\n\t" PL("%0") "s_sleep %9\n\t" PL("%1")
                 "1:\n\t" PS_("%0") PS_("%1") "s_sub_u32 %4, %4, 1\n\ts_cbranch_scc0 1b\n2:"
                 : "=&v"(t[0]), "=&v"(t[1]), "=&v"(t[2]), "=&v"(t[3]), "+s"(n) : "v"(off), "s"(base), "s"(tag), "i"(D0), "i"(KPF1) : "memory", "vcc", "scc");
    dead = __builtin_amdgcn_readfirstlane(n) == ~0u;
}
#define WAVE_RETRY(OK, ...) { if (__builtin_expect(__builtin_amdgcn_ballot_w64(!(OK)) != 0ull, 0)) WAVE_WAIT({ __VA_ARGS__; ok = OK; }) ORDER(); }
#define STAMP(i)
#define STAMP_DUMP()

#define KLB0 0
#define KLB1 12
DI void site() { asm volatile(""); }
template <class F, int... I> DI void static_for_impl(std::integer_sequence<int, I...>, F&& f) { (f(std::integral_constant<int, I>{}), ...); }
template <int N, class F> DI void static_for(F&& f) { static_for_impl(std::make_integer_sequence<int, N>{}, f); }
#define IC(n) std::integral_constant<int, (n)>{}
#define LAMBDA(v) [&](auto v) __attribute__((always_inline))
#define PK2 " op_sel:[1,1,0] op_sel_hi:[1,0,1] neg_lo:[1,0,0]"
DI f32x2 cmul(f32x2 a, f32x2 b) {
    f32x2 t;
    asm("v_pk_mul_f32 %0, %1, %2 op_sel_hi:[0,1]\n\ts_nop 0\n\tv_pk_fma_f32 %0, %1, %2, %0" PK2 "\n\ts_nop 0" : "=&v"(t) : "v"(a), "v"(b));
    return t;
}
DI f32x2 cfma(f32x2 a, f32x2 b, f32x2 c) {
    f32x2 t;
    asm("v_pk_fma_f32 %0, %1, %2, %3 op_sel_hi:[0,1,1]\n\ts_nop 0\n\tv_pk_fma_f32 %0, %1, %2, %0" PK2 "\n\ts_nop 0" : "=&v"(t) : "v"(a), "v"(b), "v"(c));
    return t;
}
DI void cfma4(f32x2 (&t)[4], f32x2 a, f32x2 b0, f32x2 b1, f32x2 b2, f32x2 b3, f32x2 c0, f32x2 c1, f32x2 c2, f32x2 c3) {
    asm("v_pk_fma_f32 %0, %4, %5, %9 op_sel_hi:[0,1,1]\n\tv_pk_fma_f32 %1, %4, %6, %10 op_sel_hi:[0,1,1]\n\tv_pk_fma_f32 %2, %4, %7, %11 op_sel_hi:[0,1,1]\n\tv_pk_fma_f32 %3, %4, %8, %12 op_sel_hi:[0,1,1]\n\t"
        "v_pk_fma_f32 %0, %4, %5, %0" PK2 "\n\tv_pk_fma_f32 %1, %4, %6, %1" PK2 "\n\tv_pk_fma_f32 %2, %4, %7, %2" PK2 "\n\tv_pk_fma_f32 %3, %4, %8, %3" PK2 "\n\ts_nop 0"
        : "=&v"(t[0]), "=&v"(t[1]), "=&v"(t[2]), "=&v"(t[3]) : "v"(a), "v"(b0), "v"(b1), "v"(b2), "v"(b3), "v"(c0), "v"(c1), "v"(c2), "v"(c3));
}
DI void gacc2(f32x2& h0, f32x2& h1, f32x2 w0, f32x2 w1, v4u e0, v4u e1) {
    const v2u a0 = {e0.x, e0.y}, b0 = {e0.z, e0.w}, a1 = {e1.x, e1.y}, b1 = {e1.z, e1.w};
    asm("v_pk_fma_f32 %0, %4, %2, %0 op_sel_hi:[0,1,1]\n\tv_pk_fma_f32 %1, %6, %3, %1 op_sel_hi:[0,1,1]\n\t"
        "v_pk_fma_f32 %0, %5, %2, %0 op_sel:[0,1,0] op_sel_hi:[0,0,1] neg_lo:[1,0,0]\n\tv_pk_fma_f32 %1, %7, %3, %1 op_sel:[0,1,0] op_sel_hi:[0,0,1] neg_lo:[1,0,0]"
        : "+v"(h0), "+v"(h1) : "v"(w0), "v"(w1), "v"(a0), "v"(b0), "v"(a1), "v"(b1));
}
DI s16x4 trread(char* smem, int byteoff) {
    return __builtin_amdgcn_ds_read_tr16_b64_v4i16((LAS s16x4*)(smem + byteoff));
}

#define CV_LOAD_A(A, a, rr_, hf) { const unsigned* ap_ = (const unsigned*)(smem + ab[(a) >> 1] + (((a) & 1) ? 0 : 512) + 64 * (7 - (rr_)) + 8 * (hf)); A[a][2 * (hf)] = ap_[0]; A[a][2 * (hf) + 1] = ap_[1]; }
#define CV_LOAD_B(B, e, rr_) { B[e] = *(const v4u*)(smem + bb + 64 * (7 - (rr_)) + 512 * (e)); }
constexpr int cv_pair(int lo, int hi, int n) {
    for (int a = 0; a < 8; ++a)
        for (int e = 0; e < 8; ++e)
            if (a + e >= lo && a + e < hi && n-- == 0) return 8 * a + e;
    return -1;
}
template <int R, int PF, bool OWN, class F>
DI void conv_round(f32x4 (&ca)[8], v4u (&A)[8], const v4u (&B)[8], v4u (&An)[8], v4u (&Bn)[8], const char* smem, const int (&ab)[4], int bb, F&& fill) {
    static_for<(R == 7 ? KG0 : 36)>(LAMBDA(g_) {
        constexpr int g = g_.value, a = cv_pair(0, 8, g) / 8, e = cv_pair(0, 8, g) % 8;
        mf(ca[a + e], A[a], B[e]);
        SCHED();
        if constexpr (OWN && g < 12) { CV_LOAD_A(A, 2 + g / 2, R, g & 1) SCHED(); }
        if constexpr (g < 4 * PF && (g & 3) != 3) { if constexpr ((g & 3) == 2) CV_LOAD_B(Bn, g >> 2, R + 1) else CV_LOAD_A(An, g >> 2, R + 1, g & 1) SCHED(); }
        site();
        if constexpr (R == 7) SCHED();
        if constexpr ((g & 3) == 3 && g < 28) { fill(std::integral_constant<int, 7 * R + g / 4>{}); SCHED(); }
    });
}
template <int R0, int R1, class F, class M>
DI void conv_part(f32x4 (&ca)[8], const char* smem, const int (&ab)[4], int bb, F&& fill, M&& mid, v4u (&A)[2][8], v4u (&B)[2][8]) {
    if constexpr (R0 > 0) { CV_LOAD_A(A[0], 0, R0, 0) CV_LOAD_A(A[0], 0, R0, 1) SCHED(); }
    static_for<8>(LAMBDA(a_) { constexpr int a = a_.value; CV_LOAD_B(B[0], a, R0) SCHED(); });
    if constexpr (R0 > 0) { CV_LOAD_A(A[0], 1, R0, 0) CV_LOAD_A(A[0], 1, R0, 1) SCHED(); }
    else {
        CV_LOAD_A(A[0], 0, 0, 0) CV_LOAD_A(A[0], 0, 0, 1) SCHED();
        const v4u ad = mid();
        SCHED();
        static_for<8>(LAMBDA(e_) {
            constexpr int e = e_.value;
            mf0(ca[e], ad, B[0][e]); SCHED();
            if constexpr (e < 7) { CV_LOAD_A(A[0], e + 1, 0, 0) CV_LOAD_A(A[0], e + 1, 0, 1) SCHED(); }
        });
    }
    static_for<R1 - R0>(LAMBDA(r_) {
        constexpr int r = r_.value;
        conv_round<R0 + r, (R0 + r + 1 < R1 ? 8 : 0), (r == 0 && R0 > 0)>(ca, A[r & 1], B[r & 1], A[(r & 1) ^ 1], B[(r & 1) ^ 1], smem, ab, bb, fill);
    });
}
template <int IMM> DI void gld(v4u& dst, unsigned voff, const void* sbase) {
    asm volatile("global_load_dwordx4 %0, %1, %2 offset:%3" : "=v"(dst) : "v"(voff), "s"(sbase), "i"(IMM));
}
#define OUTP 16
#define NTM 31
template <int IMM, int BIT> DI void gldn(v4u& dst, unsigned voff, const void* sbase) {
    if constexpr ((NTM >> BIT) & 1) asm volatile("global_load_dwordx4 %0, %1, %2 offset:%3 nt" : "=v"(dst) : "v"(voff), "s"(sbase), "i"(IMM));
    else gld<IMM>(dst, voff, sbase);
}
DI void gldu16(unsigned& dst, unsigned voff, const void* sbase) { asm volatile("global_load_ushort %0, %1, %2" : "=v"(dst) : "v"(voff), "s"(sbase)); }
template <int IMM> DI void gld64(v4u& dst, const void* a) {
    if constexpr ((NTM >> 5) & 1) asm volatile("global_load_dwordx4 %0, %1, off offset:%2 nt" : "=v"(dst) : "v"(a), "i"(IMM));
    else asm volatile("global_load_dwordx4 %0, %1, off offset:%2" : "=v"(dst) : "v"(a), "i"(IMM));
}
template <int V> DI void SETM0(unsigned ldsbase) { asm volatile("s_add_u32 m0, %0, %1" ::"s"(ldsbase), "i"(V)); }
template <int GOFF> DI void GLD0(unsigned voff, const char* sbase) { asm volatile("global_load_lds_dwordx4 %0, %1 offset:%2" ::"v"(voff), "s"(sbase), "i"(GOFF) : "memory"); }
template <int GOFF, int V> DI void GLD1(unsigned voff, const char* sbase, unsigned ldsbase) {
    asm volatile("global_load_lds_dwordx4 %0, %1 offset:%3\n\ts_add_u32 m0, %2, %4" ::"v"(voff), "s"(sbase), "s"(ldsbase), "i"(GOFF), "i"(V) : "memory");
}
constexpr int dma_goff(int it) { return it < 40 ? (it >> 2) * 128 : ((it - 100) / 5) * 256; }
constexpr int dma_loff(int it) { return it < 40 ? L_XR + ((it >> 2) % 3) * 4096 + (it & 3) * 1024 : ((it - 100) % 5) * 20480 + ((it - 100) / 5) * 2048; }
#define KFD0 10
#define KNF 13
#define KNW 11
#define KW2S 17
#define KW2M 0x4A
#define KFWP 14
constexpr int kW2S = KW2S, kW2M = KW2M;
constexpr int NW2P = (20 - kW2S) * __builtin_popcount(kW2M);
constexpr int P1_PRE = 34;
constexpr int p1_pre(int i) { return i < 5 ? 100 + i : i < 13 ? i - 5 : i < 18 ? 105 + (i - 13) : i < 22 ? 8 + (i - 18) : i < 27 ? 110 + (i - 22) : 400; }
constexpr int p1_item(int s, int g) {
    if (s == 7 && g == 0) return 324;
    if (g == 9) return s < 10 ? 115 + s : s == 11 ? 202 : s == 12 ? 203 : -1;
    if (s == KFWP && g == 1) return 208;
    if (s >= kW2S && ((kW2M >> g) & 1)) return 500 + (s - kW2S) * __builtin_popcount(kW2M) + __builtin_popcount(kW2M & ((1 << g) - 1));
    if ((s & 1) == 0 && s <= 12) return g >= 5 ? 4 * (s / 2 + 3) + (g - 5) : -1;
    if (s == 14 && g == 5) return 207;
    return -1;
}
constexpr int p1_cnt(int s, int g) {
    int n = P1_PRE;
    for (int t = 0; t <= s; ++t)
        for (int u = 0; u < 10; ++u) {
            if (t == s && u >= g) break;
            if (p1_item(t, u) >= 0 && p1_item(t, u) != 319) ++n;
        }
    return n;
}
constexpr int p1_pos(int code) {
    for (int i = 0; i < P1_PRE; ++i) if (p1_pre(i) == code) return i;
    for (int t = 0; t < 20; ++t)
        for (int u = 0; u < 10; ++u) if (p1_item(t, u) == code) return p1_cnt(t, u);
    return -1000;
}
constexpr int p1_cap(int s, int g) {
    int u = (s < 19 && g < 5) + (g == 9 && s < 19);
    if ((s & 1) && s < 19 && g >= 5 && g < 9) u += 1 + (g == 5);
    if ((s & 1) == 0 && g == 4) u += 1;
    const int it = p1_item(s, g), nt = g < 9 ? p1_item(s, g + 1) : -1;
    u += (it >= 0 && it < 500 && it != 208) + (nt >= 0 && nt < 200);
    if ((s == 18 && g == 1) || (s == 19 && g == 2) || (g == 9 && ((s & 3) == 2 || s == 17))) u += 2;
    return u < 2 ? 2 - u : 0;
}
constexpr int p1_slot(int s0, int s, int g, int gmax) {
    int n = 0;
    for (int t = s0; t <= s; ++t)
        for (int u = 0; u < gmax && !(t == s && u >= g); ++u) n += p1_cap(t, u);
    return n;
}
constexpr int p1_lim(int n) { return n > 63 ? 63 : n; }
template <int IMM> DI void gld4s(v4u& dst, unsigned voff, const void* sbase) { asm volatile("global_load_dwordx4 %0, %1, %2 offset:%3 sc1" : "=v"(dst) : "v"(voff), "s"(sbase), "i"(IMM)); }
template <int IMM> DI void gld2s(v2u& dst, unsigned voff, const void* sbase) { asm volatile("global_load_dwordx2 %0, %1, %2 offset:%3 sc1" : "=v"(dst) : "v"(voff), "s"(sbase), "i"(IMM)); }
template <class T> DI void pin(T& v) { asm volatile("" : "+v"(v)); }
DI void mul2(float& a, float& b, float x, float y, float s) { asm volatile("v_mul_f32 %0, %2, %4\n\tv_mul_f32 %1, %3, %4" : "=&v"(a), "=&v"(b) : "v"(x), "v"(y), "v"(s)); }
DI void rot1(unsigned& a, unsigned x) { asm volatile("v_alignbit_b32 %0, %1, %1, 16" : "=v"(a) : "v"(x)); }
DI void mix1(unsigned& a, unsigned x, unsigned y) { asm volatile("v_bfi_b32 %0, %3, %1, %2" : "=v"(a) : "v"(x), "v"(y), "s"(0xffffu)); }
DI void ror1(unsigned& o, unsigned prev) { asm volatile("v_mov_b32_dpp %0, %1 wave_ror:1 row_mask:0xf bank_mask:0xf" : "=v"(o) : "v"(prev)); }
DI void shr1b(unsigned& o, unsigned x) { asm volatile("v_mov_b32_dpp %0, %1 wave_shr:1 row_mask:0xf bank_mask:0xf" : "+v"(o) : "v"(x)); }
DI void xor1(float& a, unsigned x, unsigned s) { asm volatile("v_xor_b32 %0, %1, %2" : "=v"(a) : "v"(x), "v"(s)); }
template <int G> DI void bc1(float& a, unsigned x) { asm volatile("v_mov_b32_dpp %0, %1 row_newbcast:%2 row_mask:0xf bank_mask:0xf" : "=v"(a) : "v"(x), "i"(G)); }
template <int G> DI void mulb(float& o, unsigned x, float y) { asm volatile("v_mul_f32_dpp %0, %1, %2 row_newbcast:%3 row_mask:0xf bank_mask:0xf" : "=v"(o) : "v"(x), "v"(y), "i"(G)); }
template <int N> DI void lgkm() { __builtin_amdgcn_s_waitcnt(0xC07F | (N << 8)); asm volatile("" ::: "memory"); }
DI void unp2(float& a, float& b, unsigned x) { asm volatile("v_lshlrev_b32 %0, 16, %2\n\tv_and_b32 %1, 0xffff0000, %2" : "=&v"(a), "=&v"(b) : "v"(x)); }
#define GL(a) "global_load_dwordx4 a[" #a ":" #a "+3], %7, %8 sc1\n\tglobal_load_dwordx4 a[" #a "+4:" #a "+7], %7, %8 offset:256 sc1\n\t"
#define AR(d, a, o) "v_accvgpr_read_b32 " d ", a[" #a "+" #o "]\n\t"
#define GS_(a) "s_waitcnt vmcnt(4)\n\t" AR("%2", a, 1) AR("%3", a, 3) AR("%4", a, 5) AR("%5", a, 7) AR("%0", a, 0) AR("%1", a, 2)                              \
    "v_xor_b32 %2, %9, %2\n\tv_bitop3_b32 %4, %4, %9, %2 bitop3:0xbe\n\tv_bitop3_b32 %4, %3, %9, %4 bitop3:0xbe\n\tv_bitop3_b32 %4, %5, %9, %4 bitop3:0xbe\n\t" \
    AR("%2", a, 4) AR("%3", a, 6) "v_cmp_ne_u32 vcc, 0, %4\n\ts_cbranch_vccz 2f\n\t" GL(a)
template <int D0> DI void pipe_gran(float& a, float& d, unsigned off, const void* base, unsigned tag, bool& dead) {
    unsigned n = dead ? 0u : kSpinCap, y, z;
    float a1, d1;
    asm volatile("s_sleep %10\n\t" GL(32) "s_sleep %11\n\t" GL(40) "s_sleep %11\n\t" GL(48)
                 "1:\n\t" GS_(32) GS_(40) GS_(48) "s_sub_u32 %6, %6, 1\n\ts_cbranch_scc0 1b\n2:"
                 : "=&v"(a), "=&v"(d), "=&v"(a1), "=&v"(d1), "=&v"(y), "=&v"(z), "+s"(n) : "v"(off), "s"(base), "s"(tag), "i"(D0), "i"(PD1) : "memory", "vcc", "scc");
    dead = __builtin_amdgcn_readfirstlane(n) == ~0u;
    a += a1; d += d1;
}
DI void fma2(float& a, float& b, float x, float y, float s, float t, float u, float v) {
    asm volatile("v_fma_f32 %0, %2, %4, %6\n\tv_fma_f32 %1, %3, %5, %7" : "=&v"(a), "=&v"(b) : "v"(x), "v"(y), "v"(s), "v"(t), "v"(u), "v"(v));
}
DI void cvt2(unsigned& a, unsigned& b, f32x4 v) { asm volatile("v_cvt_pk_bf16_f32 %0, %2, %3\n\tv_cvt_pk_bf16_f32 %1, %4, %5" : "=&v"(a), "=&v"(b) : "v"(v.x), "v"(v.y), "v"(v.z), "v"(v.w)); }
DI void swp2n(unsigned& a, unsigned& b, unsigned& c, unsigned& d) { asm volatile("v_permlane16_swap_b32 %0, %1\n\tv_permlane16_swap_b32 %2, %3" : "+v"(a), "+v"(b), "+v"(c), "+v"(d)); }
template <int G> DI void bc2b(float& a, float& b, float x, float y) {
    asm volatile("v_mov_b32_dpp %0, %2 row_newbcast:%4 row_mask:0xf bank_mask:0xf\n\tv_mov_b32_dpp %1, %3 row_newbcast:%4 row_mask:0xf bank_mask:0xf" : "=&v"(a), "=&v"(b) : "v"(x), "v"(y), "i"(G));
}
template <class T> DI void pins(T& v) { asm volatile("" : "+s"(v)); }
template <int I> DI void gld_w2(unsigned voff, const void* sbase) {
    asm volatile("global_load_dwordx4 " WA(2) ", %0, %1 offset:%3 sc1" ::"v"(voff), "s"(sbase), "i"(W2R + 4 * I), "i"((I % 5) * 1024 - 2048));
}
DI void undef4(v4u (&x)[4]) { asm volatile("" : "=v"(x[0]), "=v"(x[1]), "=v"(x[2]), "=v"(x[3])); }
DI void wait1(v2u& v) { asm volatile("s_waitcnt vmcnt(0)" : "+v"(v)::"memory"); }
DI void ld_ur(unsigned o, const void* base) {
    asm volatile("global_load_dwordx4 a[40:40+3], %0, %1 sc1\n\tglobal_load_dwordx4 a[44:44+3], %0, %1 offset:1024 sc1\n\t"
                 "global_load_dwordx4 a[48:48+3], %0, %1 offset:2048 sc1\n\tglobal_load_dwordx4 a[52:52+3], %0, %1 offset:3072 sc1" ::"v"(o), "s"(base));
}
DI void st_ur(unsigned a) {
    asm volatile("s_waitcnt vmcnt(%1)\n\tds_write_b128 %0, a[40:40+3] offset:480\n\tds_write_b128 %0, a[44:44+3] offset:1504\n\t"
                 "ds_write_b128 %0, a[48:48+3] offset:2528\n\tds_write_b128 %0, a[52:52+3] offset:3552" ::"v"(a), "i"(2 * __builtin_popcount(kW2M)) : "memory");
}
template <int A, int IMM> DI void lda4(unsigned o, const void* base) { asm volatile("global_load_dwordx4 a[%c2:%c2+3], %0, %1 offset:%3" ::"v"(o), "s"(base), "i"(A), "i"(IMM)); }
template <int A, int IMM> DI void lda2(unsigned o, const void* base) { asm volatile("global_load_dwordx2 a[%c2:%c2+1], %0, %1 offset:%3" ::"v"(o), "s"(base), "i"(A), "i"(IMM)); }
template <int A> DI void rda(unsigned& v) { asm volatile("v_accvgpr_read_b32 %0, a[%c1+0]" : "=v"(v) : "i"(A)); }
DI void poll1(unsigned a, const void* base) { asm volatile("global_load_dwordx2 a[36:36+1], %0, %1 sc1" ::"v"(a), "s"(base)); }
DI void poll2(unsigned a, unsigned b, const void* base) { asm volatile("global_load_dwordx2 a[36:36+1], %0, %2 sc1\n\tglobal_load_dwordx2 a[36+2:36+3], %1, %2 sc1" ::"v"(a), "v"(b), "s"(base)); }
DI void touch_(unsigned a, const void* base) { asm volatile("global_load_dword a[32+1], %0, %1" ::"v"(a), "s"(base)); }
template <int N> DI void wait_st(v2u& a, v4u& b) { asm volatile("s_waitcnt vmcnt(%2)" : "+v"(a), "+v"(b) : "i"(N) : "memory"); }
DI void undef3(v4u (&x)[3]) { asm volatile("" : "=v"(x[0]), "=v"(x[1]), "=v"(x[2])); }
template <int IMM> DI void gld4t(v4u& dst, unsigned voff, const void* sbase) { asm volatile("global_load_dwordx4 %0, %1, %2 offset:%3 sc1" : "+v"(dst) : "v"(voff), "s"(sbase), "i"(IMM)); }
template <int N> DI void vm_wait() { asm volatile("s_waitcnt vmcnt(%0)" ::"i"(N) : "memory"); }
DI float dot2a(unsigned a, unsigned b, float c) {
    return __builtin_amdgcn_fdot2_f32_bf16(__builtin_bit_cast(bf16x2v, a), __builtin_bit_cast(bf16x2v, b), c, false);
}
DI void disc(unsigned l0, unsigned l1, unsigned l2, f32x2& lam, f32x2& co) {
    const float dt = rbf(__builtin_amdgcn_exp2f(lo16f(l2) * 1.44269504088896f));
    const float lre = lo16f(l0), lim = lo16f(l1);
    const float e_ = __builtin_amdgcn_exp2f(dt * lre * 1.44269504088896f);
    const float rv = __builtin_amdgcn_fractf(dt * lim * 0.159154943091895f);
    lam = f32x2{e_ * __builtin_amdgcn_cosf(rv), e_ * __builtin_amdgcn_sinf(rv)};
    const float nr = lam.x - 1.0f, ni = lam.y, id = __builtin_amdgcn_rcpf(lre * lre + lim * lim);
    co = f32x2{(nr * lre + ni * lim) * id, (ni * lre - nr * lim) * id};
}
DI f32x4 unpk4(v2u u) { return f32x4{lo16f(u.x), hi16f(u.x), lo16f(u.y), hi16f(u.y)}; }
#define D2 "v_dot2_f32_bf16 "
#define CP "v_cvt_pk_bf16_f32 "
DI void d2x4(unsigned* o, unsigned a0, unsigned a1, unsigned a2, unsigned a3, unsigned b0, unsigned b1, unsigned b2, unsigned b3) {
    asm volatile(D2 "%0, %4, %8, 0\n\t" D2 "%1, %5, %9, 0\n\t" D2 "%2, %6, %10, 0\n\t" D2 "%3, %7, %11, 0"
                 : "=&v"(o[0]), "=&v"(o[1]), "=&v"(o[2]), "=&v"(o[3]) : "v"(a0), "v"(a1), "v"(a2), "v"(a3), "v"(b0), "v"(b1), "v"(b2), "v"(b3));
}

__global__ __launch_bounds__(256, 1) __attribute__((aligned(4096))) void k_main(Params p) {
    __shared__ __attribute__((aligned(16))) char smem[LDS_BYTES];
    const rsrc_t rs = __builtin_amdgcn_make_buffer_rsrc((void*)p.ws, 0, WS_BYTES, 0x00020000);
    const rsrc_t ro = __builtin_amdgcn_make_buffer_rsrc((void*)p.out, 0, S_ * D_ * 4, 0x00020000);
    const int w0 = __builtin_amdgcn_readfirstlane(threadIdx.x >> 6), b0 = blockIdx.x;
    bool dead = false;
    asm volatile("" ::: "a255");
    float SW[5];
    f32x4 xin[5];
    v2u gt[5], dsk[5];
    f32x4 acc[2][5];
    f32x4 rr[2];
    float tt[2][5][4];
    float lr[5], ls[5], lqr[5], lqs[5];
    f32x2 l8, l32, l128;
    bool kearly;
    unsigned sdq;
    int sgn_, ab[4], bb, zpa, uwa, spa, tbw, tbr, cwa, spl, dst[3];
    unsigned sgo, sgs, glo, gso, fgo, fgo1;
    int srca;
    unsigned adm, dso, w2o, wro, fwo, xso, fho1, fho, hso, eo, es, ho[4];
    bool cv0;
    int mt1_;
    auto addrs = [&](auto layer_, int lane, int w, int b) __attribute__((always_inline)) {
        constexpr int layer = layer_.value;
        const int c = lane & 15, q = lane >> 4;
        TILES()
        static_assert(l_sg(0, 0) == SC + 0xA900 && l_sg(0, 1) == SC + 0xB400 && l_sg(0, 2) == SC + 0xCE00 && l_sg(0, 3) == SC + 0xDC00, "");
        sgn_ = (layer ? l_sg(1, 0) + w * INBOX : SC + (int)((0xDC00CE00B400A900ull >> (16 * w)) & 0xffff)) + q * 320 + c * 4;
        const int pl = lane < 40 ? lane : 39, tbb = l_tb(layer, 0) + w * (layer ? INBOX : 5120);
        spl = SC + pl * 8;
        eo = OFF_EG + (unsigned)(layer * 163840 + nt1 * 640 + pl * 16);
        es = eo + (unsigned)(mt1 * 10240);
        spa = l_rs(layer, 0) + (2 * (mt2 & 1) + (w >> 1)) * (layer ? INBOX : 256) + (2 * (w & 1) + (c >> 3)) * 64 + (c & 7) * 4;
        tbw = tbb + c * 64 + ((q ^ ((c >> 1) & 3)) * 16);
        tbr = tbb + (2 * (c >> 2) + (q & 1)) * 64 + (((2 * (q >> 1) + ((c >> 1) & 1)) ^ (c >> 2)) * 16) + (c & 1) * 8;
        hso = OFF_HF + (unsigned)(layer * 5242880 + ((8 * mt1 + 2 * w + (q >> 1)) * 160 + 10 * nt1 + 5 * (q & 1)) * 256 + c * 16);
        cwa = L_CW + c * 4;
        fho1 = lane ? kNone : OFF_FH + (unsigned)((((layer * 32 + 2 * mt1 + (w >> 1)) * 4 + (nt1 >> 2)) * 8 + (nt1 & 3) * 2 + (w & 1)) * 128);
        fho = OFF_FH + (unsigned)(((layer * 32 + mt2) * 4 + w) * 1024 + (lane >> 3) * 128);
        pin(sgn_); pin(spl); pin(eo); pin(es); pin(spa); pin(tbw); pin(tbr); pin(hso); pin(cwa); pin(fho1); pin(fho);
        const unsigned hb_ = OFF_HF + (unsigned)(layer * 5242880 + (4 * mt2 * 160 + 40 * w) * 256);
#pragma unroll
        for (int m = 0; m < 4; ++m) { ho[m] = hb_ + (unsigned)(((m + 1 + w) & 3) * 40960 + lane * 16); pin(ho[m]); }
#pragma unroll
        for (int m = 0; m < 3; ++m) { dst[m] = SC + ((w + 1 + m) & 3) * INBOX + m * 5120 + lane * 16; pin(dst[m]); }
        mt1_ = mt1;
        sgo = OFF_SG + (unsigned)(((layer * 32 + mt2) * 4 + w) * 2048 + c * 16);
        sgs = q < 2 ? sgo + (unsigned)(nt2 * 256 + q * 8) : kNone;
        sgo += (unsigned)(q * 512);
        pin(sgo); pin(sgs);
        if constexpr (layer == 0) {
            srca = SC + w * INBOX + lane * 16;
            glo = OFF_GT + (unsigned)((80 * nt2 + (lane >> 1)) * 4096 + (64 * mt2 + 16 * w) * 2 + (lane & 1) * 16);
            gso = cv0 ? OFF_GT + (unsigned)((80 * nt2 + i32 + 32 * w) * 4096 + (q & 1) * 512 + c * 32 + (q >> 1) * 16) : kNone;
            fgo = OFF_FG + (unsigned)((nt2 * 96 + (lane >> 1)) * 128);
            fgo1 = w == 0 ? OFF_FG + (unsigned)((nt2 * 96 + i32) * 128) : kNone;
            pin(srca); pin(glo); pin(gso); pin(fgo); pin(fgo1);
            const int r0 = SC + (w < 3 ? w : 0) * REGSZ, y0 = 2031 + 8 * q - c, l34 = lane < 33 ? lane : 33;
#pragma unroll
            for (int i = 0; i < 4; ++i) { ab[i] = r0 + ((y0 & 1) ? CV_O : CV_E) + ((y0 + 1) >> 1) * 4 - 448 - 512 - 1024 * i; pin(ab[i]); }
            bb = r0 + 32 * c + 16 * q;
            zpa = w < 3 ? r0 + l34 * 16 + (l34 >= 30 ? CV_E + 4096 - 480 : 0) + (l34 >= 32 ? CV_O + 4100 - 32 - CV_E - 4096 : 0) : SC + 47360 + lane * 16;
            uwa = r0 + lane * 16;
            xso = (unsigned)((64 * mt2 + 16 * w + c) * D_ + 80 * nt2 + 16 * (q & 1) + 8 * (q >> 1)) * 2u;
            dso = (unsigned)(160 * nt2 + 8 * q);
            adm = (unsigned)((c & 1) * 16 + (q == 2 + (c >> 3) ? 256 << ((c & 7) >> 1) : 0));
            pin(adm);
            pin(bb); pin(zpa); pin(uwa); pin(xso); pin(dso);
        }
        SCHED();
    };
    unsigned tag0, tag1;
#define FLAG(off) { if (lane == 0) st64(v2u{tag0, tag1}, rs, off); }
#define ISTAG(f) (((f.x ^ tag0) | (f.y ^ tag1)) == 0u)
#define KW_REQ() { if (cv) ld_ur(wro, p.ws + OFF_KVT); static_for<NW2P>(LAMBDA(i_) { gld_w2<i_.value>(w2o, p.ws + 2048 + (i_.value / 5) * 5120); }); }
#define FW_OK() { if (__builtin_expect(!kearly, 0)) { WAVE_WAIT({ const v2u fw_ = ld64(rs, fwo); ok = ISTAG(fw_); }) KW_REQ() } }
#define TT_GAP(n) { constexpr int mb_ = (n) / 10, nb_ = ((n) % 10) / 2, e_ = ((n) & 1) * 2; \
        mul2(tt[mb_][nb_][e_], tt[mb_][nb_][e_ + 1], rm_[4 * mb_ + e_], rm_[4 * mb_ + e_ + 1], SW[nb_]); }
    {
        const int tid = threadIdx.x, lane = tid & 63, w = w0, b = b0;
        const int c = lane & 15, q = lane >> 4;
        TILES()
        STAMP(0)
        const unsigned lds0 = (unsigned)(size_t)(LAS char*)smem;
        v2u nonce;
        {
            u64 pc;
            int len, len2;
            asm volatile("s_getpc_b64 %0\nkpf_begin:\n\ts_mov_b32 %1, kpf_end-kpf_begin\n\ts_mov_b32 %2, kpf_end2-kpf_begin" : "=s"(pc), "=s"(len), "=s"(len2));
            len = len > len2 ? len : len2;
            const int o0 = (((j & 15) * 4 + w) + 64 * (lane >> 3)) * 128;
            touch_((unsigned)(o0 < len ? o0 : 0), (const void*)pc);
        }
        const bool cv = w < 2 || (w == 2 && i32 < 16);
        const int chn = 80 * nt2 + i32 + 32 * (cv ? w : 0);
        const bool kw = w < 2 || (w == 2 && i32 >= 16);
        const int kp = 16 * nt2 + mt1, ku = 5 * kp + (i32 < 16 ? w & 1 : 2 + (w < 3 ? w : 0)), kc = ku >> 6, kr = ku & 63;
        const int lc = cv ? lane : 8;
        int kwi = 2 + (i32 >> 4) - w;
        v4u ax[15], sa[5], cr[2], wr[4];
        unsigned uL[3];
        const int cu_ = (b < 200 ? b : 0) * 4 + w, cdb = cu_ / 20, ccg = cu_ % 20;
        {
            int ca_ = 32 * ccg, cb_ = 40 * nt1 + 10 * w - 32;
            asm volatile("" : "+s"(ca_), "+s"(cb_), "+s"(kwi));
            unsigned cpo = (unsigned)((lane < 32 ? ca_ : cb_) + (lane < 41 ? lane : 41)) * 2u;
            unsigned kso = (unsigned)(64 * kc + 8 * (lane & 7)) * 2u, ko = (unsigned)(32 * kr + 4 * (lane >> 3)) * 1280u + kso, kh = ko - 3840u;
            kso *= 4u;
            pin(cpo); pin(kso); pin(ko); pin(kh);
            SCHED();
            gldu16(uL[0], cpo, p.Lre); gldu16(uL[1], cpo, p.Lim); gldu16(uL[2], cpo, p.logdt);
            const v4u rk = {(unsigned)(u64)p.k, (unsigned)((u64)p.k >> 32) & 0xffffu, S_ * D_ * 2u, 0x00020000u};
#define L4(d, v, s_, o, f) "global_load_dwordx4 %" #d ", %" #v ", %" #s_ " offset:" #o f "\n\t"
#define B4(d, o) "buffer_load_dwordx4 %" #d ", %17, %21, 0 offen offset:" #o " nt\n\t"
            asm volatile("s_cmp_lt_i32 %22, 1\n\ts_cbranch_scc1 Lrole2\n\t"
                         L4(3, 15, 18, 0, " nt") L4(4, 15, 18, 1280, " nt") L4(5, 15, 18, 2560, " nt") L4(6, 15, 18, 3840, " nt")
                         L4(7, 15, 20, 0, " nt") L4(8, 15, 20, 1280, " nt") L4(9, 15, 20, 2560, " nt") L4(10, 15, 20, 3840, " nt")
                         B4(0, 0) B4(1, 1280) B4(2, 2560) L4(11, 16, 19, 0, "") L4(12, 16, 19, 16, "") L4(13, 16, 19, 32, "") L4(14, 16, 19, 48, "")
                         "\nLrole2:"
                         : "=&v"(ax[0]), "=&v"(ax[1]), "=&v"(ax[2]), "=&v"(ax[3]), "=&v"(ax[4]), "=&v"(ax[5]), "=&v"(ax[6]), "=&v"(ax[7]), "=&v"(ax[8]), "=&v"(ax[9]),
                           "=&v"(ax[10]), "=&v"(ax[11]), "=&v"(ax[12]), "=&v"(ax[13]), "=&v"(ax[14])
                         : "v"(ko), "v"(kso), "v"(kh), "s"(p.k), "s"(p.shk), "s"(p.v), "s"(rk), "s"(kwi) : "scc");
            SCHED();
        }
        const char* ssrc;
        {
            u64 pa = (u64)(p.x0 + (128 * mt1 + 8 * nt1 + 2 * w) * D_), pb = (u64)(((mt1 & 1) ? p.Bim : p.Bre) + (40 * nt1 + (mt1 >> 1)) * D_);
            asm volatile("" : "+s"(pa), "+s"(pb));
            const int nbx = 2 * w + q - 2, r = q < 2 ? q : 8 * (nbx < 5 ? nbx : 4);
            ssrc = (const char*)(q < 2 ? pa : pb) + (unsigned)(r * 1280 + c * 16);
            pin(ssrc);
        }
        SCHED();
        STAMP(22)
        const int row8 = lane >> 3;
        const unsigned wvo = (unsigned)(40 * nt1 + row8) * 1280u + (unsigned)(((lane & 7) ^ ((w & 1) | ((row8 >> 1) << 1))) * 16 + (w >> 1) * 128);
        const char* const wbase = (const char*)((w & 1) ? p.Bim : p.Bre);
        const unsigned wl = lds0 + (unsigned)(((w & 1) * 10 + (w >> 1)) * 1024);
        const unsigned xvo0 = (unsigned)(8 * (row8 >> 1) + 2 * (row8 & 1)) * 1280u + (unsigned)(((lane & 7) ^ ((row8 >> 1) << 1)) * 16);
        const unsigned xvo1 = (xvo0 ^ 16u) + 1280u;
        const char* const xbase = (const char*)p.x0 + (size_t)(128 * mt1 + 32 * w) * 1280;
        const unsigned xl = lds0 + (unsigned)(w * REGSZ);
        unsigned xv[4] = {xvo0, xvo1, xvo0 + 5120u, xvo1 + 5120u}, wv[5];
#pragma unroll
        for (int i = 0; i < 5; ++i) { wv[i] = wvo + i * 10240u; pin(wv[i]); if (i < 4) pin(xv[i]); }
#define DMA_M0(it) SETM0<dma_loff(it) - dma_goff(it)>((it) < 40 ? xl : wl)
#define DMA_LD1(it, nx_) GLD1<dma_goff(it), dma_loff(nx_) - dma_goff(nx_)>((it) < 40 ? xv[(it) & 3] : wv[(it) < 40 ? 0 : ((it) - 100) % 5], (it) < 40 ? xbase : wbase, (nx_) < 40 ? xl : wl)
#define DMA_LD(it) GLD0<dma_goff(it)>((it) < 40 ? xv[(it) & 3] : wv[(it) < 40 ? 0 : ((it) - 100) % 5], (it) < 40 ? xbase : wbase)
        const int swz = (c & 1) | ((c >> 2) << 1);
        int w1lane0 = (c & 1) * 10240 + (c >> 1) * 128 + ((q ^ swz) * 16);
        int w1lane1 = (c & 1) * 10240 + (c >> 1) * 128 + (((4 + q) ^ swz) * 16);
        int xlane0 = L_XR + w * REGSZ + (c & 1) * 1024 + (c >> 1) * 128 + ((q ^ swz) * 16);
        int xlane1 = L_XR + w * REGSZ + (c & 1) * 1024 + (c >> 1) * 128 + (((4 + q) ^ swz) * 16);
        unsigned cvo = (unsigned)((16 * cdb + c) * D_ + 32 * ccg + 8 * q) * 2u, for0, for1;
        {
            const int G = 4 * ccg + q, ch = 10 * (G / 5) + G % 5, ci = ch + 5;
            const unsigned fo = OFF_W2F + (unsigned)(((cdb / 5) * 200 + cdb % 5) * 1024 + c * 16);
            for0 = fo + (unsigned)((ch >> 2) * 5120 + (ch & 3) * 256); for1 = fo + (unsigned)((ci >> 2) * 5120 + (ci & 3) * 256);
        }
        unsigned sto = OFF_ST + (unsigned)((mt1 * 16 + 4 * w + q) * 256 + c * 8), swo = OFF_ST + (unsigned)(((lane >> 2) * 16 + nt1) * 256 + 128 + (lane & 3) * 16);
        unsigned kvo = OFF_KVT + (unsigned)((64 * kc + 8 * (lane >> 3) + ((lane >> 2) & 1)) * 4096 + (32 * kr + 8 * (lane & 3)) * 2);
        int kbw = L_KB + w * 1024 + (lane & 7) * 128 + (((lane >> 3) * 8) ^ ((lane & 7) * 16));
        int kbr = L_KB + w * 1024 + (lane >> 3) * 128 + (((lane & 7) * 16) ^ ((lane >> 3) * 16));
        const unsigned dum = kNone;
        if (!kw) kvo = dum;
        if (b >= 200) { for0 = dum; for1 = dum; cvo = (unsigned)((b * 4 + w) * 128); }
        wro = (unsigned)(chn * 4096 + lc * 16);
        cv0 = cv;
        pin(kvo); pin(kbw); pin(kbr);
        int w1hi0 = w1lane0 + 61440, w1hi1 = w1lane1 + 61440;
        pin(w1hi0); pin(w1hi1);
        pin(w1lane0); pin(w1lane1); pin(xlane0); pin(xlane1); pin(cvo); pin(for0); pin(for1); pin(sto); pin(swo); pin(wro);
        SCHED();
        auto pre = LAMBDA(i_) {
            constexpr int it = p1_pre(i_.value);
            SCHED();
            DMA_M0(it);
            asm volatile("s_nop 0");
            DMA_LD(it);
            SCHED();
        };
        {
            int f0 = 2 * ((chn & ~63) / 5), f1 = 2 * ((chn | 63) / 5) + 1, nk = cv ? 28 : 0;
            asm volatile("" : "+s"(f0), "+s"(f1), "+s"(nk));
            const int l10 = lane - 32 < 0 || lane - 32 > 9 ? 0 : lane - 32;
            fwo = lane < nk ? OFF_FK + (unsigned)(f0 + lane < f1 ? f0 + lane : f1) * 128u : OFF_FW + (unsigned)(25 * nt2 + w + 5 * (l10 >> 1) + (l10 & 1)) * 128u;
        }
        w2o = OFF_W2F + (unsigned)((nt2 * 40 + 10 * w) * 5120 + lane * 16);
        int ime = SC + (w < 3 ? w * REGSZ + CV_E : 43264) + 1008 - 16 * lane, imo = SC + (w < 3 ? w * REGSZ + CV_O + 4 : 47360) + 1008 - 16 * lane, im3 = w < 3 ? ime + 3072 : imo;
        int tca = L_TAB + (c >> 1) * 136, tcb = tca + 8 + 32 * w + 8 * q, tcl = L_TAB + (lane < 40 ? lane : 39) * 136, coa = L_CO + w * REGSZ + q * 64, rsa = l_rs(0, 0) + w * 256 + lane * 4;
        unsigned sgn = (c & 1) ? 0u : 0x80000000u;
        unsigned fko1 = w ? kNone : lane < 32 ? OFF_FK + (unsigned)((2 * kp + (i32 >> 4)) * 128) : OFF_FW + (unsigned)(b * 128);
        pin(fwo); pin(w2o); pin(fko1); pin(ime); pin(imo); pin(im3); pin(tca); pin(tcb); pin(tcl); pin(coa); pin(rsa); pin(sgn);
        SCHED();
        auto cw_ = [&]() __attribute__((always_inline)) {
            gldn<0, 2>(cr[0], cvo, p.Cre); gldn<0, 2>(cr[1], cvo, p.Cim);
            static_for<4>(LAMBDA(i) { gldn<i.value * 1024, 1>(wr[i.value], wro, p.ssmk); });
        };
        STAMP(33)
        gld2s<0>(nonce, 0u, p.ws + OFF_NONCE);
        asm volatile("s_waitcnt vmcnt(16)\n\ts_cmp_gt_i32 %3, 0\n\ts_cbranch_scc1 Lrole3\n\ts_waitcnt vmcnt(1)\nLrole3:" : "+v"(uL[0]), "+v"(uL[1]), "+v"(uL[2]) : "s"(kwi) : "memory", "scc");
        {
            f32x2 lam, co, lp[16];
            disc(uL[0], uL[1], uL[2], lam, co);
            if (lane < 32) *(f32x2*)(smem + L_CO + w * REGSZ + lane * 8) = 2.0f * co;
            lp[0] = cmul(lam, lam); lp[0] = cmul(lp[0], lp[0]); lp[1] = cmul(lp[0], lp[0]);
            lp[0] = f32x2{1.f, 0.f};
            lp[2] = cmul(lp[1], lp[1]); lp[3] = cmul(lp[2], lp[1]); lp[4] = cmul(lp[2], lp[2]);
            const f32x2 z2 = {0.f, 0.f};
            f32x2 t4[4];
            cfma4(t4, lp[4], lp[1], lp[2], lp[3], lp[4], z2, z2, z2, z2);
            lp[5] = t4[0]; lp[6] = t4[1]; lp[7] = t4[2]; lp[8] = t4[3];
            cfma4(t4, lp[8], lp[1], lp[2], lp[3], lp[4], z2, z2, z2, z2);
            lp[9] = t4[0]; lp[10] = t4[1]; lp[11] = t4[2]; lp[12] = t4[3];
            cfma4(t4, lp[8], lp[5], lp[6], lp[7], lp[7], z2, z2, z2, z2);
            lp[13] = t4[0]; lp[14] = t4[1]; lp[15] = t4[2];
            if (lane >= 32 && lane < 42) {
                f32x2* tl = (f32x2*)(smem + L_TAB + (10 * w + lane - 32) * 136);
                tl[0] = lam;
#pragma unroll
                for (int i = 0; i < 16; ++i) tl[1 + i] = lp[i];
            }
        }
        SCHED();
        STAMP(35)
        static_for<5>(LAMBDA(i) { gld64<256 * i.value>(sa[i.value], ssrc); });
        cw_();
        addrs(std::integral_constant<int, 0>{}, lane, w, b);
        v4u kvt[4];
        unsigned one2 = 0x3F803F80u, kq[12];
        float kf_[8];
        pin(one2);
        auto kvf = LAMBDA(n_) {
            constexpr int n = n_.value, h = n / 5, u = n % 5;
            if constexpr (u == 0) {
#pragma unroll
                for (int r = 0; r < 6; ++r) { kq[r] = __builtin_amdgcn_perm(ax[r][h], ax[r + 1][h], 0x05040100u); kq[6 + r] = __builtin_amdgcn_perm(ax[r][h], ax[r + 1][h], 0x07060302u); }
                if constexpr (h > 0) kvt[h - 1] = *(const v4u*)(smem + kbr);
            } else if constexpr (u == 1) {
                unsigned t[8];
                d2x4(t, kq[0], kq[1], kq[2], kq[3], ax[11 + h][1], ax[11 + h][1], ax[11 + h][1], ax[11 + h][1]);
                d2x4(t + 4, kq[6], kq[7], kq[8], kq[9], ax[11 + h][3], ax[11 + h][3], ax[11 + h][3], ax[11 + h][3]);
#pragma unroll
                for (int r = 0; r < 8; ++r) kf_[r] = __uint_as_float(t[r]);
            } else if constexpr (u == 2) {
#pragma unroll
                for (int r = 0; r < 8; ++r) kf_[r] = dot2a(kq[r + 2 * (r >> 2) + 2], ax[11 + h][2 * (r >> 2)], kf_[r]);
            } else if constexpr (u == 3) {
#pragma unroll
                for (int r = 0; r < 4; ++r) { kq[r] = ax[7 + r][h] << 16; kq[4 + r] = ax[7 + r][h] & 0xffff0000u; pin(kq[r]); pin(kq[4 + r]); }
            } else {
#pragma unroll
                for (int r = 0; r < 8; ++r) kf_[r] *= __uint_as_float(kq[r]);
                *(v2u*)(smem + kbw) = v2u{cvtpk(kf_[0], kf_[1]), cvtpk(kf_[2], kf_[3])};
                *(v2u*)(smem + (kbw ^ 64)) = v2u{cvtpk(kf_[4], kf_[5]), cvtpk(kf_[6], kf_[7])};
                if constexpr (h == 3) kvt[3] = *(const v4u*)(smem + kbr);
            }
            SCHED();
        };
        asm volatile("s_waitcnt vmcnt(%15)" : "+v"(ax[0]), "+v"(ax[1]), "+v"(ax[2]), "+v"(ax[3]), "+v"(ax[4]), "+v"(ax[5]), "+v"(ax[6]), "+v"(ax[7]), "+v"(ax[8]), "+v"(ax[9]),
                     "+v"(ax[10]), "+v"(ax[11]), "+v"(ax[12]), "+v"(ax[13]), "+v"(ax[14]) : "i"(11) : "memory");
        pin(nonce);
        STAMP(30)
        unsigned tch = 0u;
        constexpr int NF = KNF;
        static_assert(NF < 27, "");
        if (kw) {
            {
                static_for<20>(LAMBDA(n_) {
                    constexpr int n = n_.value, DN = 20 - KFD0;
                    site(); SCHED();
                    kvf(n_);
                    if constexpr (n >= KFD0) static_for<(n - KFD0 + 1) * NF / (DN ? DN : 1) - (n - KFD0) * NF / (DN ? DN : 1)>(LAMBDA(i_) { pre(std::integral_constant<int, (n - KFD0) * NF / (DN ? DN : 1) + i_.value>{}); });
                });
                if constexpr (KFD0 == 20) static_for<NF>(pre);
            }
        } else {
            if (w == 3) {
            const int l = lane, l3 = l & 3;
            unsigned a = OFF_SG + (unsigned)((((((l >> 3) & 1) * 32 + mt2) * 4 + ((l >> 1) & 3)) * 8 + nt2) * 256 + (l & 1) * 128);
            a = l >= 16 ? OFF_EG + (unsigned)((l >= 21 ? 163840 - 640 : 0) + (mt1 * 16 + nt1) * 640 + (l < 26 ? l - 16 : 5) * 128) : a;
            a = l >= 28 ? OFF_PS + (unsigned)(((mt2 * 4 + l3) * 8 + nt2) * 128) : a;
            a = l >= 32 ? OFF_FH + (unsigned)((((((l >> 2) & 1) * 32 + 2 * mt1 + (l3 >> 1)) * 4 + (nt1 >> 2)) * 8 + (nt1 & 3) * 2 + (l3 & 1)) * 128) : a;
            a = l >= 40 ? OFF_FX + (unsigned)(((mt1 * 4 + 2 * (mt2 & 1) + (l3 >> 1)) * 16 + nt2 * 2 + (l3 & 1)) * 128) : a;
            a = l >= 48 ? OFF_FG + (unsigned)((nt2 * 96 + i32) * 128) : a;
            const unsigned big = (l & 7) < 2 ? OFF_HF + (unsigned)((l & 1) * 5242880 + (8 * mt1 * 160 + 10 * nt1) * 256) : (l & 7) == 2 ? OFF_X2F + (unsigned)(2 * mt2 * 40960 + 2560 * nt2)
                               : (l & 7) < 5 ? OFF_KVT + (unsigned)((l & 1) * 2621440 + (80 * nt2 + i32) * 4096) : OFF_W2F + (unsigned)(nt2 * 204800 + ((l & 7) - 5) * 98304);
            a = l >= 56 ? big : a;
            tch = a;
        }
            vm_wait<KNW>();
            static_for<NF>(pre);
            undef4(kvt);
        }
        tag0 = __builtin_amdgcn_readfirstlane(nonce.x) + 1u; tag1 = __builtin_amdgcn_readfirstlane(nonce.y) + (tag0 == 0u ? 1u : 0u);
        SCHED();
        STAMP(34)
        v4u orr, oi;
        v2u gr;
        unsigned gro;
        {
            asm volatile("s_waitcnt vmcnt(%7)" : "+v"(cr[0]), "+v"(cr[1]), "+v"(sa[0]), "+v"(sa[1]), "+v"(sa[2]), "+v"(sa[3]), "+v"(sa[4]) : "i"(4 + NF) : "memory");
#pragma unroll
            for (int e = 0; e < 4; ++e) {
                const f32x4 co = *(const f32x4*)(smem + coa + e * 16);
                const f32x2 o0 = lo16f(cr[0][e]) * f32x2{co.x, -co.y} - lo16f(cr[1][e]) * f32x2{co.y, co.x};
                const f32x2 o1 = hi16f(cr[0][e]) * f32x2{co.z, -co.w} - hi16f(cr[1][e]) * f32x2{co.w, co.z};
                orr[e] = cvtpk(o0.x, o1.x); oi[e] = cvtpk(o0.y, o1.y);
            }
            pin(orr); pin(oi);
            SCHED();
        }
        static_for<(27 - NF) / 3>(LAMBDA(i_) { pre(std::integral_constant<int, NF + i_.value>{}); });
        {
            float s1[2] = {0.f, 0.f}, s2[2] = {0.f, 0.f};
#pragma unroll
            for (int i = 0; i < 5; ++i)
#pragma unroll
                for (int e = 0; e < 4; ++e) { s1[e & 1] = dot2a(sa[i][e], one2, s1[e & 1]); s2[e & 1] = dot2a(sa[i][e], sa[i][e], s2[e & 1]); }
            const float a1 = rowsum16(s1[0] + s1[1]), a2 = rowsum16(s2[0] + s2[1]);
            const float mean = a1 * (1.0f / D_);
            const float var = fmaxf(a2 * (1.0f / D_) - mean * mean, 0.f);
            const float ri = __builtin_amdgcn_rcpf(rbf(__builtin_amdgcn_sqrtf(rbf(rbf(var) + 1e-5f))));
            gr = v2u{__float_as_uint(q >= 2 ? a1 : c ? -ri * rbf(mean) : ri), tag0};
            gro = c < 2 ? OFF_ST + (unsigned)((mt1 * 16 + nt1) * 256 + ((q < 2 ? 8 * c : 14) + 2 * w + q) * 8) : kNone;
            pin(gr); pin(gro);
        }
        SCHED();
        STAMP(59)
        {
            static_for<27 - NF - (27 - NF) / 3>(LAMBDA(i_) { pre(std::integral_constant<int, NF + (27 - NF) / 3 + i_.value>{}); });
#pragma unroll
            for (int i = 0; i < 4; ++i) st128(kvt[i], rs, kvo + i * 8192);
            st128(orr, rs, for0); st128(oi, rs, for1);
        }
        st64(gr, rs, gro);
        SCHED();
        STAMP(23)
        asm volatile("s_waitcnt vmcnt(%4) lgkmcnt(0)" : "+v"(wr[0]), "+v"(wr[1]), "+v"(wr[2]), "+v"(wr[3]) : "i"(P1_PRE - 9) : "memory");
        __builtin_amdgcn_s_barrier();
        asm volatile("" ::: "memory");
        STAMP(24)
        v4u wf[2][5], af[2][2][2];
#pragma unroll
        for (int nb = 0; nb < 5; ++nb) wf[0][nb] = *(const v4u*)(smem + w1lane0 + nb * 20480 + (0) * 2048);
#pragma unroll
        for (int i = 0; i < 4; ++i) af[0][i >> 1][i & 1] = *(const v4u*)(smem + ((i >> 1) ? xlane1 : xlane0) + (i & 1) * 2048);
        SCHED();
        const char *kvtb = p.ws + OFF_KVT, *w2b0 = p.ws + 2048;
        pins(kvtb); pins(w2b0);
        v2u sg, tc[10], fw, tg2 = {tag0, tag1};
        pin(tg2);
        v4u sw0;
        unsigned ie[4][4], io[4][4], iw, zq[4];
        float rr_[8];
        auto kstep = LAMBDA(s_) {
            constexpr int s = s_.value, ph = s >> 2, s4 = s & 3, cu = s & 1, nx = cu ^ 1, jj = s >> 1, pp = jj & 1;
            if constexpr (s4 == 3 && ph < 4) {
                constexpr int pw = p1_pos(104 + 5 * (ph + 1)), px = p1_pos(4 * (jj + 1) + 3), pm = pw > px ? pw : px;
                constexpr int vm = p1_lim(p1_cnt(s, 0) - 1 - pm);
                asm volatile("" ::: "memory");
                __builtin_amdgcn_s_waitcnt((vm & 15) | ((vm >> 4) << 14) | 0x70);
                __builtin_amdgcn_s_barrier();
                asm volatile("" ::: "memory");
                STAMP(25 + ph)
                SCHED();
            }
            if constexpr (s == 15) {
                wait_st<p1_cnt(s, 0) - 1 - p1_pos(203)>(sg, sw0);
                WAVE_RETRY((((sg.y ^ tag0) | (sw0.y ^ tag0) | (sw0.w ^ tag0)) == 0u), sg = ld64(rs, sto); sw0 = ld128(rs, swo))
                STAMP(7)
            }
            if constexpr (s == kW2S) { vm_wait<p1_cnt(s, 0) - 1 - p1_pos(208)>(); SCHED(); }
            if constexpr (s == 18) {
                vm_wait<p1_cnt(s, 0) - 1 - (p1_pos(207) > p1_pos(208) ? p1_pos(207) : p1_pos(208))>();
                pin(fw); pin(sdq);
                kearly = __builtin_amdgcn_ballot_w64(!ISTAG(fw)) == 0ull;
                ORDER();
                if (kearly && cv) ld_ur(wro, kvtb);
                SCHED();
            }
            if constexpr (s4 != 3 || ph == 4) lgkm<(cu == 0 && s > 0 ? 3 : s == 17 ? 4 : s == 19 ? 2 : 0)>();
            SCHED();
            static_for<10>(LAMBDA(g_) {
                constexpr int g = g_.value, mb = g / 5, k = g % 5, it = p1_item(s, g), nt_ = g < 9 ? p1_item(s, g + 1) : -1;
                constexpr bool d0 = it >= 0 && it < 200, d1 = nt_ >= 0 && nt_ < 200;
                if constexpr (s == 0) mf0(acc[mb][k], af[pp][cu][mb], wf[cu][k]); else mf(acc[mb][k], af[pp][cu][mb], wf[cu][k]);
                SCHED();
                if constexpr (s < 19) {
                    if constexpr (g < 5) wf[nx][g] = *(const v4u*)(smem + (g < 3 ? (nx ? w1lane1 : w1lane0) : (nx ? w1hi1 : w1hi0)) + (g < 3 ? g : g - 3) * 20480 + ((s + 1) >> 1) * 1024);
                    if constexpr (cu == 1 && g == 5) vm_wait<p1_lim(p1_cnt(s, 5) - 1 - p1_pos(4 * (jj + 1) + 3))>();
                    if constexpr (cu == 1 && g >= 5 && g < 9)
                        af[pp ^ 1][(g - 5) >> 1][(g - 5) & 1] = *(const v4u*)(smem + (((g - 5) >> 1) ? xlane1 : xlane0) + ((jj + 1) % 3) * 4096 + ((g - 5) & 1) * 2048);
                }
                if constexpr (cu == 0 && g == 4 && s > 0) lgkm<(s == 8 || s == 10 || s == 14 ? 8 : 5)>();
                static_for<p1_cap(s, g)>(LAMBDA(e_) {
                    constexpr int na = p1_slot(0, s, g, 10) + e_.value, i = (na / 10) & 3, u = na % 10;
                    static_assert(p1_slot(0, 8, 0, 10) >= 40, "");
                    if constexpr (na < 40) {
                        if constexpr (u == 0) { if constexpr (i > 0) ror1(iw, wr[i - 1].w); else { iw = 0u; pin(iw); } }
                        else if constexpr (u < 5) rot1(ie[i][u - 1], wr[i][4 - u]);
                        else if constexpr (u == 5) shr1b(iw, wr[i].w);
                        else if constexpr (u < 9) mix1(io[i][u - 6], wr[i][9 - u], wr[i][8 - u]);
                        else mix1(io[i][3], wr[i].x, iw);
                    }
                    constexpr int nb = p1_slot(8, s, g, 3) + e_.value;
                    if constexpr (s >= 8 && s < 12 && g < 3) {
                        if constexpr (nb < 10) tc[nb] = *(const v2u*)(smem + (nb < 5 ? tca : tcb) + (nb % 5) * 1088);
                        else if constexpr (nb == 10) { const f32x2* tp = (const f32x2*)(smem + tcl); l8 = tp[2]; l32 = tp[5]; }
                        else if constexpr (nb == 11) l128 = *(const f32x2*)(smem + tcl + 72);
                        static_assert(p1_slot(8, 12, 0, 3) == 12, "");
                    }
                    constexpr int nc = p1_slot(12, s, g, 10) + e_.value;
                    if constexpr (s >= 12 && s < 14 && nc < 10) {
                        if constexpr (nc < 5) { lr[nc] = __uint_as_float(tc[nc].x); xor1(ls[nc], tc[nc].y, sgn); }
                        else { lqr[nc - 5] = __uint_as_float(tc[nc].x); xor1(lqs[nc - 5], tc[nc].y, sgn); }
                        static_assert(p1_slot(12, 14, 0, 10) >= 10, "");
                    }
                    if constexpr (s == 15 && g < 5) SW[g] = __int_as_float(__builtin_amdgcn_ds_bpermute(16 * c + 4 * (g >> 1), (g & 1) ? sw0.z : sw0.x));
                    constexpr bool img = (s == 16 && g >= 5 && g < 9 && e_.value == 0) || (s == 18 && (g == 5 || g == 6) && e_.value == 0);
                    constexpr int nd = p1_slot(15, s, g, 10) + e_.value - 5 - (s > 16 ? 4 : s == 16 && g >= 5 ? (g < 9 ? g - 4 : 4) : 0) - (s > 18 ? 2 : s == 18 && g >= 5 ? (g == 5 ? 1 : 2) : 0);
                    if constexpr (s >= 15 && nd >= 0 && !img) {
                        if constexpr (nd < 4) bc1<nd>(rr_[nd], sg.x);
                        else if constexpr (nd < 44) mulb<8 + (nd - 4) % 8>(tt[(nd - 4) % 8 / 4][(nd - 4) / 8][(nd - 4) % 4], sg.x, SW[(nd - 4) / 8]);
                        else if constexpr (nd < 48) bc1<nd - 40>(rr_[nd - 40], sg.x);
                        static_assert(p1_slot(15, 16, 0, 10) <= 9 && p1_slot(15, 20, 0, 10) >= 59, "");
                    }
                });
                if constexpr (s == 14 && g < 3) *(v4u*)(smem + ime + 1024 * g) = v4u{ie[3 - g][0], ie[3 - g][1], ie[3 - g][2], ie[3 - g][3]};
                if constexpr (s == 16 && g >= 5 && g < 9) *(v4u*)(smem + imo + 1024 * (8 - g)) = v4u{io[g - 5][0], io[g - 5][1], io[g - 5][2], io[g - 5][3]};
                if constexpr (s == 18 && g == 5) *(v4u*)(smem + im3) = v4u{ie[0][0], ie[0][1], ie[0][2], ie[0][3]};
                if constexpr (s == 18 && g == 6) *(unsigned*)(smem + rsa) = sg.x;
                if constexpr (s == 14 && (g == 6 || g == 8)) { zq[g - 6] = 0u; zq[g - 5] = 0u; pin(zq[g - 6]); pin(zq[g - 5]); }
                if constexpr (s == 19 && g == 0) *(v4u*)(smem + zpa) = v4u{zq[0], zq[1], zq[2], zq[3]};
                if constexpr (s == 18 && g == 1) touch_(tch, p.ws);
                if constexpr (s == 19 && g == 2) { if (__builtin_expect(!kearly, 0)) poll1(fwo, p.ws); }
                if constexpr (it >= 0) {
                    if constexpr (d0 && d1) DMA_LD1(it, d1 ? nt_ : 0);
                    else if constexpr (d0) DMA_LD(it);
                    else if constexpr (it == 202) gld2s<0>(sg, sto, p.ws);
                    else if constexpr (it == 203) gld4s<0>(sw0, swo, p.ws);
                    else if constexpr (it == 207) gldu16(sdq, (unsigned)chn * 2u, p.ssmD);
                    else if constexpr (it == 208) gld2s<0>(fw, fwo, p.ws);
                    else if constexpr (it >= 500) gld_w2<it % 50>(w2o, w2b0 + ((it % 50) / 5) * 5120);
                    else { static_assert(p1_pos(19) >= P1_PRE, ""); st64(tg2, rs, fko1); }
                }
                if constexpr (d1 && !d0) DMA_M0(d1 ? nt_ : 0);
                site();
                SCHED();
            });
        };
        static_for<20>(kstep);
        SCHED();
        rr[0] = f32x4{rr_[0], rr_[1], rr_[2], rr_[3]}; rr[1] = f32x4{rr_[4], rr_[5], rr_[6], rr_[7]};
        ACC_FENCE10(acc[0], acc[1]);
        STAMP(1)
    }

    auto layer_body = LAMBDA(layer_) {
        constexpr int layer = layer_.value;
        const int sga = sgn_;
        const bool cv = layer == 0 && cv0;
        v2u fh;
        unsigned ks[10];
        f32x4 ca[8], a2[4][5];
        float rsr, rsm;
        int otw, otr, ots;
        v4u cA[2][8], cB[2][8];
        constexpr int R2 = kCR1, LP = (7 - R2) & 1;
        static_assert(kFHP >= 7 - KG0 / 4 + 1, "");
        constexpr bool TB = (SBT >> layer) & 1;
        float bv[8][5];
        f32x2 pr[5][2], ps[5][2];
        auto powers = [&]() __attribute__((always_inline)) {
            if constexpr (TB) {
#pragma unroll
                for (int nb = 0; nb < 5; ++nb) {
                    const f32x2 m = {lr[nb] * lr[nb] - ls[nb] * ls[nb], 2.0f * lr[nb] * ls[nb]};
                    pr[nb][0] = f32x2{lr[nb], m.x}; ps[nb][0] = f32x2{ls[nb], m.y};
#pragma unroll
                    for (int j = 1; j < 2; ++j) pstep(pr[nb][j], ps[nb][j], pr[nb][j - 1], ps[nb][j - 1], m);
#pragma unroll
                    for (int j = 0; j < 2; ++j) { pin(pr[nb][j]); pin(ps[nb][j]); }
                }
                SCHED();
            }
        };
        {
            float a0[5], a1[5];
            static_for<2>(LAMBDA(m_) {
                constexpr int mb = m_.value;
#pragma unroll
                for (int nb = 0; nb < 5; ++nb) {
                    const f32x4 v = __builtin_elementwise_fma(acc[mb][nb], rr[mb], f32x4{tt[mb][nb][0], tt[mb][nb][1], tt[mb][nb][2], tt[mb][nb][3]});
#pragma unroll
                    for (int i = 0; i < 4; ++i) bv[4 * mb + i][nb] = v[i];
                }
                SCHED();
                if constexpr (TB) {
#pragma unroll
                    for (int i = mb ? 4 : 1; i < 4 + 4 * mb; ++i) cstepi(bv[i][0], bv[i][1], bv[i][2], bv[i][3], bv[i][4], bv[i - 1][0], bv[i - 1][1], bv[i - 1][2], bv[i - 1][3], bv[i - 1][4], lr, ls);
#pragma unroll
                    for (int nb = 0; nb < 5; ++nb) a0[nb] = bv[7][nb];
                }
                if constexpr (mb == 0 && !TB) cstep(a0, bv[0], lr, ls, bv[1][0], bv[1][1], bv[1][2], bv[1][3], bv[1][4]);
#pragma unroll
                for (int i8 = mb ? 4 : 2; i8 < (TB ? 0 : 4 + 4 * mb); i8 += 2) {
                    cstep(a1, a0, lr, ls, bv[i8][0], bv[i8][1], bv[i8][2], bv[i8][3], bv[i8][4]);
                    cstep(a0, a1, lr, ls, bv[i8 + 1][0], bv[i8 + 1][1], bv[i8 + 1][2], bv[i8 + 1][3], bv[i8 + 1][4]);
                }
                SCHED();
            });
#pragma unroll
            for (int nb = 0; nb < 5; ++nb) *(float*)(smem + sga + nb * 64) = a0[nb];
        }
        SCHED();
        const int w_ = w0;
        {
        const int w = w_, mt1 = mt1_;
        if (layer == 0 && __builtin_expect(!kearly, 0)) {
            unsigned f0_, f1_;
            asm volatile("s_waitcnt vmcnt(0)\n\t" AR("%0", 36, 0) AR("%1", 36, 1) : "=v"(f0_), "=v"(f1_)::"memory");
            kearly = __builtin_amdgcn_ballot_w64(((f0_ ^ tag0) | (f1_ ^ tag1)) != 0u) == 0ull;
            ORDER();
            if (kearly) KW_REQ()
        }
        asm volatile("s_waitcnt lgkmcnt(0)" ::: "memory");
        __builtin_amdgcn_s_barrier();
        asm volatile("" ::: "memory");
        STAMP(layer ? 14 : 2)
#pragma unroll
        for (int s = 0; s < 10; ++s) ks[s] = (unsigned)s * 1024u;
        constexpr int NX = 58;
        constexpr int NW2A = NW2P + 7 * kCR1 + kXA, NW3 = NW2P + kN3E + kN3;
        auto w2ld = LAMBDA(i_) {
            constexpr int i = i_.value;
            gld_w2<i>(w2o, p.ws + 2048 + ks[i / 5] * 5u);
        };
        auto w2a = LAMBDA(i_) {
            constexpr int k = i_.value;
            if constexpr (k < kXA) { w2ld(std::integral_constant<int, NW2P + 2 * k>{}); w2ld(std::integral_constant<int, NW2P + 2 * k + 1>{}); }
            else w2ld(std::integral_constant<int, NW2P + kXA + k>{});
        };
        auto w2x = LAMBDA(i_) {
            constexpr int i = i_.value < NX - 50 ? 50 + i_.value : NW2A + i_.value - (NX - 50) < 50 ? NW2A + i_.value - (NX - 50) : 99;
            if constexpr (i < 50) w2ld(std::integral_constant<int, i>{});
            else if constexpr (i < 52) lda4<40 + 4 * (i - 50), (i - 50) * 64>(xso, p.x0);
            else if constexpr (i == 52) lda4<48, 128>(xso - 2u * (unsigned)(bb & 16), p.x0);
            else if constexpr (i < NX) lda2<20 + 2 * (i - 53), 32 * (i - 53)>(dso, p.Dskip);
        };
        constexpr int NB = NX - NW2A, NB1 = KB1 < NB ? KB1 : NB, KPL = 7 * (8 - R2) - kFHP, NFH = NB - NB1 - (KPL - kFHK) > 0 ? NB - NB1 - (KPL - kFHK) : 0;
        auto w2b = LAMBDA(i_) {
            constexpr int k = i_.value - 7 * R2;
            if constexpr (k < NB1) w2x(std::integral_constant<int, k>{});
            if constexpr (k > kFHK) w2x(std::integral_constant<int, NB1 + k - kFHK - 1>{});
            if constexpr (k == kFHK) { vm_wait<NB1>(); st64(v2u{tag0, tag1}, rs, fho1); STAMP(39) }
            if constexpr (k == kFHK - 1) { STAMP(38) }
            if constexpr (k == KPL) poll1(fho, p.ws);
            static_assert(NB1 <= kFHK + 1 && NB1 + 7 * (7 - R2) + KG0 / 4 - kFHK - 1 >= NB && NW2A <= 50 && NW3 <= NW2A && NFH <= NB - NB1, "");
        };
        auto partb = [&]() __attribute__((always_inline)) {
            const float* sp = (const float*)(smem + spa);
            rsr = sp[0]; rsm = sp[8];
            if (layer == 1) { pin(rsr); pin(rsm); }
            if (layer == 1) {
                ACC_TIE5(dsk);
#pragma unroll
                for (int nb = 0; nb < 5; ++nb) { a2[3][nb] = xin[nb] + (xin[nb] * rsr + rsm) * unpk4(dsk[nb]); pin(a2[3][nb]); }
            }
        };
        constexpr int LBW = layer ? 2 : 3;
        if (w == 3) {
            site(); SCHED();
            f32x2 sv[16];
#pragma unroll
            for (int i = 0; i < 16; ++i) sv[i] = *(const f32x2*)(smem + spl + (l_sg(layer, i >> 2) - SC + (i & 3) * 320));
            f32x2 u[4], v[4], x[4];
            cfma4(u, l8, sv[0], sv[4], sv[8], sv[12], sv[1], sv[5], sv[9], sv[13]);
            cfma4(v, l8, u[0], u[1], u[2], u[3], sv[2], sv[6], sv[10], sv[14]);
            cfma4(x, l8, v[0], v[1], v[2], v[3], sv[3], sv[7], sv[11], sv[15]);
            f32x2 P[4][4];
            P[0][0] = f32x2{0.f, 0.f}; P[0][1] = x[0]; P[0][2] = cfma(l32, x[0], x[1]); P[0][3] = cfma(l32, P[0][2], x[2]);
            const f32x2 E = cfma(l32, P[0][3], x[3]);
            st128(v4u{__float_as_uint(E.x), tag0, __float_as_uint(E.y), tag0}, rs, es);
            SCHED();
            if (layer == 0) { FW_OK() static_for<kN3E>(LAMBDA(i_) { w2ld(std::integral_constant<int, NW2P + i_.value>{}); }); }
#pragma unroll
            for (int kk = 1; kk < 4; ++kk) cfma4(P[kk], l8, P[kk - 1][0], P[kk - 1][1], P[kk - 1][2], P[kk - 1][3], sv[kk - 1], sv[kk + 3], sv[kk + 7], sv[kk + 11]);
#pragma unroll
            for (int i = 0; i < 16; ++i) *(f32x2*)(smem + spl + (l_sg(layer, i >> 2) - SC + (i & 3) * 320)) = P[i & 3][i >> 2];
            if (layer == 0) partb();
            if (layer == 1) powers();
        }
        if (w == LBW) {
            site(); SCHED();
            f32x2 wg[17], t4[4];
            unsigned ea[15], tg[15];
            const f32x2 z2 = {0.f, 0.f};
            wg[1] = cmul(l128, l128); wg[2] = cmul(wg[1], wg[1]); wg[3] = cmul(wg[2], wg[1]); wg[4] = cmul(wg[2], wg[2]);
#pragma unroll
            for (int i = 4; i < 16; i += 4) {
                cfma4(t4, wg[i], wg[1], wg[2], wg[3], wg[4], z2, z2, z2, z2);
#pragma unroll
                for (int e = 0; e < 4; ++e) wg[i + 1 + e] = t4[e];
            }
#pragma unroll
            for (int i = 0; i < 15; ++i) {
                const int ch = mt1 - 1 - i;
                if (i) pin(wg[i]);
                ea[i] = ch < 0 ? kNone : (unsigned)ch * 10240u; tg[i] = ch < 0 ? 0u : tag0;
                pins(ea[i]); pins(tg[i]);
            }
            powers();
            f32x2 H0, H1;
            __builtin_amdgcn_s_sleep(layer ? KLB1 : KLB0);
            unsigned spins = dead ? kSpinCap : 0u;
            bool miss;
#pragma nounroll
            do {
                asm volatile("" ::: "memory");
                v4u eg[15];
#pragma unroll
                for (int i = 0; i < 15; ++i) eg[i] = ld128s(rs, eo, ea[i]);
                SCHED();
                unsigned x = __builtin_amdgcn_bitop3_b32(eg[0].y, eg[0].w, tg[0], 0x7E);
                H0 = f32x2{__uint_as_float(eg[0].x), __uint_as_float(eg[0].z)}; H1 = f32x2{0.f, 0.f};
#pragma unroll
                for (int i = 1; i < 15; i += 2) {
                    x |= __builtin_amdgcn_bitop3_b32(eg[i].y, eg[i].w, tg[i], 0x7E) | __builtin_amdgcn_bitop3_b32(eg[i + 1].y, eg[i + 1].w, tg[i + 1], 0x7E);
                    pin(x);
                    gacc2(H0, H1, wg[i], wg[i + 1], eg[i], eg[i + 1]);
                    SCHED();
                }
                miss = __builtin_amdgcn_ballot_w64(x != 0u) != 0ull;
            } while (miss && ++spins <= kSpinCap);
            dead = dead || miss;
            ORDER();
            *(f32x2*)(smem + spl + (L_CW - SC)) = H0 + H1;
            STAMP(layer ? 36 : 37)
            if (layer == 0) {
                static_for<NW3 - NW2P - kN3E>(LAMBDA(i_) { w2ld(std::integral_constant<int, NW2P + kN3E + i_.value>{}); });
            }
        }
        int w2_ = w;
        pins(w2_);
        if (layer == 0) UNDEF8(ca);
        if (layer == 0 && w2_ != LBW) {
            if (cv) {
                site(); SCHED();
                FW_OK()
                st_ur((unsigned)(size_t)(LAS char*)smem + (unsigned)uwa);
                STAMP(32)
                conv_part<0, kCR1>(ca, smem, ab, bb, w2a, [&]() __attribute__((always_inline)) {
                    v4u ad;
                    const unsigned dv = sdq << (adm & 16u);
#pragma unroll
                    for (int i = 0; i < 4; ++i) ad[i] = dv & (unsigned)__builtin_amdgcn_sbfe((int)adm, 8 + i, 1);
                    asm volatile("s_nop 1" : "+v"(ad));
                    return ad;
                }, cA, cB);
                partb();
                powers();
            } else {
                FW_OK()
                static_for<NW2A - NW2P>(LAMBDA(i_) { w2ld(std::integral_constant<int, NW2P + i_.value>{}); __builtin_amdgcn_s_sleep(1); });
                partb();
                powers();
            }
        }
        if (layer == 1 && w < LBW) { site(); SCHED(); powers(); }
        asm volatile("s_waitcnt lgkmcnt(0)" ::: "memory");
        __builtin_amdgcn_s_barrier();
        asm volatile("" ::: "memory");
        STAMP(layer ? 15 : 4)
        {
            float cw[5], ex[5];
#pragma unroll
            for (int nb = 0; nb < 5; ++nb) { cw[nb] = *(const float*)(smem + cwa + nb * 64); ex[nb] = *(const float*)(smem + sga + nb * 64); }
            v2u ta[5], td[5];
            if constexpr (TB) {
                cstepi(ex[0], ex[1], ex[2], ex[3], ex[4], cw[0], cw[1], cw[2], cw[3], cw[4], lqr, lqs);
                f32x2 cs[5];
                PIN5(ex);
#pragma unroll
                for (int nb = 0; nb < 5; ++nb) { cs[nb] = f32x2{ex[nb], dpp<0xB1>(ex[nb])}; pin(cs[nb]); }
                SCHED();
                static_for<5>(LAMBDA(n_) {
                    constexpr int nb = n_.value;
                    f32x2 h[4] = {{bv[0][nb], bv[1][nb]}, {bv[2][nb], bv[3][nb]}, {bv[4][nb], bv[5][nb]}, {bv[6][nb], bv[7][nb]}};
                    padd(h, pr[nb], ps[nb], cs[nb]);
                    *(v4u*)(smem + tbw + nb * 1024) = v4u{cvtpk(h[0].x, h[0].y), cvtpk(h[1].x, h[1].y), cvtpk(h[2].x, h[2].y), cvtpk(h[3].x, h[3].y)};
                    ta[nb] = __builtin_bit_cast(v2u, trread(smem, tbr + nb * 1024)); td[nb] = __builtin_bit_cast(v2u, trread(smem, tbr + nb * 1024 + 512));
                    SCHED();
                    if constexpr (nb >= LAG) { st128(v4u{ta[nb - LAG].x, ta[nb - LAG].y, td[nb - LAG].x, td[nb - LAG].y}, rs, hso + (nb - LAG) * 256); SCHED(); }
                });
            } else
            static_for<2>(LAMBDA(h_) {
                constexpr int n0 = 2 * h_.value;
                float hv[9][3];
                cstep3(hv[0], cw + n0, lqr + n0, lqs + n0, ex + n0);
                if constexpr (n0 == 0) {
                    chain24<0>(hv, bv, lr, ls);
                    chain24<0>(hv + 4, bv + 4, lr, ls);
                } else {
                    chain33<2>(hv, bv, lr, ls);
                    SCHED();
#pragma unroll
                    for (int rd = 0; rd < 2; ++rd) st128(v4u{ta[rd].x, ta[rd].y, td[rd].x, td[rd].y}, rs, hso + rd * 256);
                    SCHED();
                    chain33<2>(hv + 3, bv + 3, lr, ls);
                    chain32<2>(hv + 6, bv + 6, lr, ls);
                }
#pragma unroll
                for (int e = 0; e < (n0 ? 3 : 2); ++e) {
                    v4u o;
                    o.x = cvtpk(hv[1][e], hv[2][e]); o.y = cvtpk(hv[3][e], hv[4][e]);
                    o.z = cvtpk(hv[5][e], hv[6][e]); o.w = cvtpk(hv[7][e], hv[8][e]);
                    *(v4u*)(smem + tbw + (n0 + e) * 1024) = o;
                }
#pragma unroll
                for (int rd = n0; rd < (n0 ? 5 : 2); ++rd) { ta[rd] = __builtin_bit_cast(v2u, trread(smem, tbr + rd * 1024)); td[rd] = __builtin_bit_cast(v2u, trread(smem, tbr + rd * 1024 + 512)); }
                SCHED();
            });
#pragma unroll
            for (int rd = TB ? 5 - LAG : 2; rd < 5; ++rd) st128(v4u{ta[rd].x, ta[rd].y, td[rd].x, td[rd].y}, rs, hso + rd * 256);
            SCHED();
        }
#pragma unroll
        for (int s = 0; s < 10; ++s) pins(ks[s]);
        if constexpr (layer == 1) {
            partb();
            SCHED();
            if constexpr (OTR) {
                const int ln = (srca >> 4) & 63, od = (b0 >> 3) & 1, r0 = srca - 16 * ln;
                otw = (ln & 15) * 336 + (ln >> 4) * 16; otr = (ln & 12) * 336 + ((ln >> 4) + od) * 64 + (ln & 3) * 16; ots = otw + (od ? 0 : 256);
                ACC_TIE5(gt);
#pragma unroll
                for (int nb = 0; nb < 5; ++nb) *(v2u*)(smem + r0 + 8192 + otw / 2 + nb * 32) = gt[nb];
#pragma unroll
                for (int nb = 0; nb < 5; ++nb) gt[nb] = *(const v2u*)(smem + r0 + 8192 + (nb < 4 ? otr / 2 + nb * 168 : ots / 2));
                otw += r0; otr += r0; ots += r0;
                pin(otw); pin(otr); pin(ots);
                ACC_TIE5(gt);
                static_assert(MAP == 0 && (SC & 1023) == 0 && (INBOX & 1023) == 0, "");
                SCHED();
            }
            DRAIN();
            st64(v2u{tag0, tag1}, rs, fho1);
        }
        STAMP(layer ? 16 : 5)
        if (cv) {
            if constexpr (layer == 0) site();
            SCHED();
            conv_part<kCR1, 8>(ca, smem, ab, bb, w2b, []() {}, cA, cB);
        } else if (layer == 0) {
            static_for<NB1>(LAMBDA(i_) { w2x(i_); __builtin_amdgcn_s_sleep(1); });
            vm_wait<NB1>();
            st64(v2u{tag0, tag1}, rs, fho1);
            static_for<NB - NB1 - NFH>(LAMBDA(i_) { w2x(std::integral_constant<int, NB1 + i_.value>{}); __builtin_amdgcn_s_sleep(1); });
            if (w == 3) static_for<NW2A - NW3>(LAMBDA(i_) { w2ld(std::integral_constant<int, NW3 + i_.value>{}); __builtin_amdgcn_s_sleep(1); });
            poll1(fho, p.ws);
            static_for<NFH>(LAMBDA(i_) { w2x(std::integral_constant<int, NB - NFH + i_.value>{}); __builtin_amdgcn_s_sleep(1); });
            static_assert(NFH <= NB - (NX - 50), "");
            UNDEF8(cA[LP]); UNDEF8(cB[LP]);
        }
        if (layer == 0) { unsigned f0_, f1_; asm volatile("s_waitcnt vmcnt(%2)\n\t" AR("%0", 36, 0) AR("%1", 36, 1) : "=v"(f0_), "=v"(f1_) : "i"(NFH) : "memory"); fh = v2u{f0_, f1_}; }
        STAMP(layer ? 29 : 6)
        }
        unsigned g[8][2];

            v2u pt[4];
            if (layer == 0) WAVE_RETRY(ISTAG(fh), fh = ld64(rs, fho))
            else { pipe_flag<KPF0>(pt, fho, p.ws, (u64)tag0 | ((u64)tag1 << 32), dead); ORDER(); }
            STAMP(layer ? 17 : 31)
            site(); SCHED();
            constexpr int RING2 = layer ? KR2 : KRT;
            v4u ar[RING2];
            constexpr int NRT = layer ? RING2 : (36 - KG0) / KRS + 2 < KRT ? (36 - KG0) / KRS + 2 : KRT;
            unsigned xs_[3][4];
            auto gate = LAMBDA(n_) {
                constexpr int n = n_.value;
                if constexpr (n >= 100) swp2n(g[2 * (n - 100)][0], g[2 * (n - 100) + 1][0], g[2 * (n - 100)][1], g[2 * (n - 100) + 1][1]);
                else if constexpr (n >= 0) cvt2(g[n][0], g[n][1], ca[n]);
            };
            if constexpr (layer == 0) {
#pragma unroll
                for (int i = 0; i < 2; ++i) { ar[i] = ld128s(rs, ho[0], ks[i]); SCHED(); }
                static_for<36 - KG0>(LAMBDA(n_) {
                    constexpr int n = n_.value, g = KG0 + n;
                    constexpr int a = g < 8 ? 0 : g < 15 ? 1 : g < 21 ? 2 : g < 26 ? 3 : g < 30 ? 4 : g < 33 ? 5 : g < 35 ? 6 : 7;
                    constexpr int e = g - (a * 8 - a * (a - 1) / 2), i = 2 + n / KRS;
                    mf(ca[a + e], cA[LP][a], cB[LP][e]);
                    SCHED();
                    if constexpr (n % KRS == 0 && i < NRT) ar[i] = ld128s(rs, ho[i / 10], ks[i % 10]);
                    if constexpr (n < 12) rda<40 + n>(xs_[n >> 2][n & 3]);
                    constexpr int gt_[12] = {0, 1, 2, -1, 100, -1, 3, -1, -1, 101, 4, -1};
                    if constexpr (g >= 24) gate(IC(gt_[g < 24 ? 0 : g - 24]));
                    site();
                    SCHED();
                });
                static_assert(KG0 <= 15 && NRT >= 12, "");
            } else {
#pragma unroll
                for (int i = 0; i < RING2; ++i) { ar[i] = ld128s(rs, ho[i / 10], ks[i % 10]); SCHED(); }
            }
        v4u gl[3];
        v2u f0;
        bool gearly = false;
        {
            const char* src = smem + srca;
            const char *glb1 = p.ws + 32 * 4096, *glb2 = p.ws + 48 * 4096;
            float zx[4], zd[4], zt[4], zz[5][4];
            if constexpr (STAMPS == 1) { STAMP(layer ? 50 : 40) }
            static_for<40>(LAMBDA(i_) {
                constexpr int i = i_.value, m = i / 10, s = i % 10;
                static_for<5>(LAMBDA(k_) {
                    constexpr int kk = k_.value, n = 2 * (i - 3) + kk / 2 - 3, nb = n / 8, u = n % 8, pv = i - 1 + RING2;
                    if constexpr (s == 0 && m < 3) mfa0<kk>(a2[m][kk], ar[i % RING2]); else mfa<5 * s + kk>(a2[m][kk], ar[i % RING2]);
                    SCHED();
                    constexpr int gu_[12] = {-1, 5, -1, -1, 102, 6, -1, -1, 7, -1, 103, -1};
                    if constexpr (layer == 0 && 5 * i + kk < 12) gate(IC(gu_[(5 * i + kk) % 12]));
                    if constexpr (layer == 0 && i == 25 && kk == 1) { pins(glb1); pins(glb2); }
                    if constexpr (layer == 0 && i >= 3 && i < 26 && (kk & 1)) {
                        if constexpr (n < 0) swp2n(xs_[n + 3][0], xs_[n + 3][2], xs_[n + 3][1], xs_[n + 3][3]);
                        else if constexpr (nb < 5) {
                            if constexpr (u < 2) unp2(zx[2 * u], zx[2 * u + 1], xs_[nb >> 1][2 * (nb & 1) + u]);
                            else if constexpr (u < 4) unp2(zd[2 * u - 4], zd[2 * u - 3], dsk[nb][u - 2]);
                            else if constexpr (u < 6) fma2(zt[2 * u - 8], zt[2 * u - 7], zx[2 * u - 8], zx[2 * u - 7], rsr, rsr, rsm, rsm);
                            else fma2(zz[nb][2 * u - 12], zz[nb][2 * u - 11], zt[2 * u - 12], zt[2 * u - 11], zd[2 * u - 12], zd[2 * u - 11], zx[2 * u - 12], zx[2 * u - 11]);
                        }
                    }
                    if constexpr (i == 0 && kk == 0 && layer == 1) { pin(pt[0]); pin(pt[1]); pin(pt[2]); pin(pt[3]); }
                    if constexpr (layer == 0 && i < 2 && kk > 0 && 12 + 4 * i + kk - 1 >= NRT && 12 + 4 * i + kk - 1 < RING2) ar[12 + 4 * i + kk - 1] = ld128s(rs, ho[1], ks[2 + 4 * i + kk - 1]);
                    if constexpr (layer == 0 && i == kGSI && kk > 0) st128(v4u{g[2 * kk - 2][0], g[2 * kk - 2][1], g[2 * kk - 1][0], g[2 * kk - 1][1]}, rs, gso + (kk - 1) * 1024);
                    if constexpr (layer == 0 && i > 2 && i <= 7 && kk == (i == kGSI ? 0 : 2)) {
                        { unsigned d0_, d1_; rda<20 + 2 * (i - 3)>(d0_); rda<21 + 2 * (i - 3)>(d1_); dsk[i - 3] = v2u{d0_, d1_}; }
                    }
                    if constexpr (kk == 0 && i > 0 && pv < 40) ar[pv % RING2] = ld128s(rs, ho[pv / 10], ks[pv % 10]);
                    constexpr int s1 = s - 1 - (KMB && layer == 0 && m == 1 ? kFGI - 10 : 0);
                    if constexpr (kk == 2 && m >= 1 && s1 >= 0 && s1 < 5) *(f32x4*)(smem + dst[m - 1] + s1 * 1024) = a2[m - 1][s1];
                    site();
                    if constexpr (i >= 37) a2[i - 37][kk] = *(const f32x4*)(src + (i - 37) * 5120 + kk * 1024);
                    SCHED();
                });
                if constexpr (i == 10 && (layer || !KMB)) { asm volatile("s_waitcnt lgkmcnt(0)" ::: "memory"); __builtin_amdgcn_s_barrier(); asm volatile("" ::: "memory"); }
                if constexpr (i == kFGI && layer == 0) {
                    static_assert(kFGI >= 10 && kFGI <= 14, "");
                    asm volatile("s_waitcnt vmcnt(%0) lgkmcnt(0)" ::"i"(kFGI - kGSI) : "memory");
                    __builtin_amdgcn_s_barrier(); asm volatile("" ::: "memory");
                    st64(v2u{tag0, tag1}, rs, fgo1);
                }
                if constexpr (i == 26 && layer == 0) {
#pragma unroll
                    for (int nb = 0; nb < 5; ++nb) { a2[3][nb] = f32x4{zz[nb][0], zz[nb][1], zz[nb][2], zz[nb][3]}; pin(a2[3][nb]); }
                }
                if constexpr (i == kGPI && layer == 0) gld2s<0>(f0, fgo, p.ws);
                if constexpr (i == kGCI && layer == 0) {
                    wait1(f0);
                    static_for<39 - i>(LAMBDA(r_) { pin(ar[(i + 1 + r_.value) % RING2]); });
                    gearly = __builtin_amdgcn_ballot_w64(!ISTAG(f0)) == 0ull;
                    ORDER();
                    undef3(gl);
                    if (__builtin_expect(gearly, 1)) { gld4t<0>(gl[0], glo, p.ws); gld4t<0>(gl[1], glo, glb1); gld4t<0>(gl[2], glo, glb2); }
                    else if (G2ND) poll2(fgo, fgo, p.ws);
                }
                if constexpr (i == 36) {
                    asm volatile("s_waitcnt lgkmcnt(0)" ::: "memory");
                    __builtin_amdgcn_s_barrier(); asm volatile("" ::: "memory");
                }
                if constexpr (STAMPS == 1 && (i == 0 || i == 9 || i == 10 || i == 15 || i == 25 || i == 35 || i == 36)) { STAMP((layer ? 51 : 41) + (i == 0 ? 0 : i == 9 ? 1 : i == 10 ? 2 : i == 15 ? 3 : i == 25 ? 4 : i == 35 ? 5 : 6)) }
                SCHED();
            });
            STAMP(layer ? 18 : 8)
        }
#pragma unroll
        for (int nb = 0; nb < 5; ++nb) a2[0][nb] = (a2[0][nb] + a2[1][nb]) + a2[2][nb];
        asm volatile("s_nop 1" : "+v"(a2[3][0]), "+v"(a2[3][1]), "+v"(a2[3][2]), "+v"(a2[3][3]), "+v"(a2[3][4]),
                     "+v"(a2[0][0]), "+v"(a2[0][1]), "+v"(a2[0][2]), "+v"(a2[0][3]), "+v"(a2[0][4]));
        float ty;
        {
            f32x4 s1, s2;
#pragma unroll
            for (int nb = 0; nb < 5; ++nb) {
                const f32x4 y = a2[3][nb] + a2[0][nb];
                xin[nb] = y;
                if (nb == 0) { s1 = y; s2 = y * y; } else { s1 += y; s2 += y * y; }
            }
            ty = xsum2h(hsum4(s1), hsum4(s2));
            st64(v2u{__float_as_uint(ty), tag0}, rs, sgs);
        }
        SCHED();
        STAMP(layer ? 19 : 9)
        if constexpr (layer == 1 && OTR) {
#pragma unroll
            for (int nb = 0; nb < 5; ++nb) *(f32x4*)(smem + otw + nb * 64) = xin[nb];
#pragma unroll
            for (int nb = 0; nb < 5; ++nb) xin[nb] = *(const f32x4*)(smem + (nb < 4 ? otr + nb * 336 : ots));
            SCHED();
        }
        int lane_o, b_o = b0, w_o = w0;
        asm volatile("v_mbcnt_lo_u32_b32 %0, -1, 0\n\tv_mbcnt_hi_u32_b32 %0, -1, %0" : "=v"(lane_o));
        asm volatile("" : "+s"(b_o), "+s"(w_o));
        const int lane = lane_o, b = b_o, w = w_o;
        const int c = lane & 15, q = lane >> 4;
        TILES()
        if (layer == 0 && __builtin_expect(!gearly, 0)) {
            if (G2ND) {
                unsigned f0_, f1_;
                asm volatile("s_waitcnt vmcnt(1)\n\t" AR("%0", 36, 0) AR("%1", 36, 1) : "=v"(f0_), "=v"(f1_)::"memory");
                f0 = v2u{f0_, f1_};
            }
            WAVE_RETRY(ISTAG(f0), f0 = ld64(rs, fgo))
            gld4t<0>(gl[0], glo, p.ws); gld4t<0>(gl[1], glo, p.ws + 32 * 4096); gld4t<0>(gl[2], glo, p.ws + 48 * 4096);
            asm volatile("s_waitcnt vmcnt(0)" : "+v"(gl[0]), "+v"(gl[1]), "+v"(gl[2])::"memory");
        }
        if (layer == 0) {
            asm volatile("s_waitcnt vmcnt(1)" : "+v"(gl[0]), "+v"(gl[1]), "+v"(gl[2])::"memory");
            char* tb = smem + SC + w * INBOX;
            *(v4u*)(tb + lane * 16) = gl[0]; *(v4u*)(tb + 1024 + lane * 16) = gl[1]; *(v4u*)(tb + 1536 + lane * 16) = gl[2];
#pragma unroll
            for (int nb = 0; nb < 5; ++nb) gt[nb] = __builtin_bit_cast(v2u, trread(tb, (16 * nb + 4 * q + (c >> 2)) * 32 + (c & 3) * 8));
        } else ACC_TIE5(gt);
        SCHED();
        f32x4 gf[5], yg[5];
#pragma unroll
        for (int nb = 0; nb < 5; ++nb) { gf[nb] = unpk4(gt[nb]); yg[nb] = xin[nb] * gf[nb]; pin(gf[nb]); pin(yg[nb]); }
        unsigned xs = OFF_X2F + (unsigned)((2 * mt2 + (w >> 1)) * 40960 + ((c >> 2) & 1) * 20480 + (10 * nt2 + 2 * (q & 1) + (q >> 1)) * 256 + (8 * (w & 1) + 4 * (c >> 3) + (c & 3)) * 16);
        unsigned pso = q < 2 ? OFF_PS + (unsigned)(((mt2 * 4 + w) * 8 + nt2) * 128 + c * 8 + q * 4) : kNone;
        unsigned fxo1 = lane ? kNone : OFF_FX + (unsigned)(((mt1 * 4 + 2 * (mt2 & 1) + (w >> 1)) * 16 + nt2 * 2 + (w & 1)) * 128);
        unsigned oo = (unsigned)(((64 * mt2 + 16 * w + c) * D_ + 80 * nt2 + 4 * q) * 4);
        unsigned o4 = (unsigned)(((64 * mt2 + 16 * w + (c & 12)) * D_ + 80 * nt2 + 16 * (q + (nt2 & 1)) + 4 * (c & 3)) * 4);
        if (OTR) oo += (nt2 & 1) ? 0u : 256u;
        if (layer == 0) { pin(xs); pin(pso); pin(fxo1); } else { pin(oo); if (OTR) pin(o4); }
        SCHED();
        {
            float a, d;
            if constexpr (layer == 0) site();
            pipe_gran<layer ? PDS : PDS0>(a, d, sgo, p.ws, tag0, dead);
            STAMP(layer ? 20 : 10)
            xsum2(a, d);
            const float me = a * (1.0f / D_);
            const float ri = __builtin_amdgcn_rsqf(fmaxf(d * (1.0f / D_) - me * me, 0.f) + 1e-5f), mr = -me * ri;
            if constexpr (layer == 1 && OTR) {
                static_for<5>(LAMBDA(j_) {
                    constexpr int jj = (j_.value + 4) % 5;
                    const float rj = jj < 4 ? dpp<0x55 * (jj & 3)>(ri) : ri, mj = jj < 4 ? dpp<0x55 * (jj & 3)>(mr) : mr;
                    const f32x4 o = yg[jj] * rj + (gf[jj] * mj + xin[jj]);
                    __builtin_amdgcn_raw_buffer_store_b128(__builtin_bit_cast(v4u, o), ro, jj < 4 ? o4 : oo, jj < 4 ? jj * 2560 : 0, OUTP);
                });
            } else {
#pragma unroll
                for (int nb = 0; nb < 5; ++nb) xin[nb] = yg[nb] * ri + (gf[nb] * mr + xin[nb]);
            }
        }
        if constexpr (layer == 0) {
            v2u pk[5];
#pragma unroll
            for (int nb = 0; nb < 5; ++nb) { const f32x4 x = xin[nb]; pk[nb] = v2u{cvtpk(x.x, x.y), cvtpk(x.z, x.w)}; }
#pragma unroll
            for (int i = 0; i < 3; ++i) {
                const v2u pa = pk[2 * i], pb = pk[i < 2 ? 2 * i + 1 : 4];
                const auto r0 = __builtin_amdgcn_permlane16_swap(pa.x, pb.x, false, false);
                const auto r1 = __builtin_amdgcn_permlane16_swap(pa.y, pb.y, false, false);
                if (i < 2 || (q & 1) == 0) st128(v4u{r0[0], r1[0], r0[1], r1[1]}, rs, xs + i * 1024);
            }
            {
                f32x4 s1, s2;
#pragma unroll
                for (int nb = 0; nb < 5; ++nb) { const f32x4 x = xin[nb]; if (nb == 0) { s1 = x; s2 = x * x; } else { s1 += x; s2 += x * x; } }
                __builtin_amdgcn_raw_buffer_store_b32(__float_as_uint(xsum2h(hsum4(s1), hsum4(s2))), rs, pso, 0, SC1);
            }
            SCHED();
            int w1lane0 = (c & 1) * 10240 + (c >> 1) * 128 + ((q ^ ((c & 1) | ((c >> 2) << 1))) * 16);
            int w1lane1 = (c & 1) * 10240 + (c >> 1) * 128 + (((4 + q) ^ ((c & 1) | ((c >> 2) << 1))) * 16);
            unsigned fxo = OFF_FX + (unsigned)((mt1 * 4 + w) * 2048 + (lane >> 2) * 128);
            unsigned xo = OFF_X2F + (unsigned)((4 * mt1 + w) * 40960 + lane * 16), xo1 = xo + 20480u;
            const int row = 8 * q + (c & 7);
            unsigned po = OFF_PS + (unsigned)((((2 * mt1 + (w >> 1)) * 4 + 2 * (w & 1) + (row >> 4)) * 8 + 4 * (c >> 3)) * 128 + (row & 15) * 8);
            int w1hi0 = w1lane0 + 61440, w1hi1 = w1lane1 + 61440;
            pin(w1lane0); pin(w1lane1); pin(w1hi0); pin(w1hi1); pin(fxo); pin(xo); pin(xo1); pin(po);
            v4u wf[2][5];
#pragma unroll
            for (int nb = 0; nb < 5; ++nb) wf[0][nb] = *(const v4u*)(smem + w1lane0 + nb * 20480);
            v2u tg2 = {tag0, tag1};
            pin(tg2);
            SCHED();
            DRAIN();
            st64(tg2, rs, fxo1);
            if (b == 255 && w == 0) FLAG(OFF_NONCE)
            STAMP(11)
            addrs(std::integral_constant<int, 1>{}, lane, w, b);
            v2u pt[4];
            pipe_flag<KPFX>(pt, fxo, p.ws, (u64)tag0 | ((u64)tag1 << 32), dead);
            ORDER();
            STAMP(12)
            v4u ar[RING1][2];
#pragma unroll
            for (int s = 0; s < RING1; ++s)
#pragma unroll
                for (int mb = 0; mb < 2; ++mb) { ar[s][mb] = ld128s(rs, mb ? xo1 : xo, s * 1024); SCHED(); }
            v2u ps[4];
#pragma unroll
            for (int i = 0; i < 4; ++i) ps[i] = ld64(rs, po + i * 128);
            SCHED();
            float t1, t2, rinv, mri, rr_[8], rm_[8];
            static_for<20>(LAMBDA(s_) {
                constexpr int s = s_.value, cu = s & 1, nx = cu ^ 1;
                LGKM0() SCHED();
                static_for<10>(LAMBDA(g_) {
                    constexpr int g = g_.value, mb = g / 5, k = g % 5;
                    if constexpr (s == 0) mf0(acc[mb][k], ar[0][mb], wf[0][k]); else mf(acc[mb][k], ar[s % RING1][mb], wf[cu][k]);
                    SCHED();
                    if constexpr (s == 0 && g == 0) { pin(pt[0]); pin(pt[1]); pin(pt[2]); pin(pt[3]); }
                    if constexpr (s < 19 && g < 3) wf[nx][g] = *(const v4u*)(smem + (nx ? w1lane1 : w1lane0) + g * 20480 + ((s + 1) >> 1) * 1024);
                    if constexpr (s < 19 && (g == 3 || g == 4)) wf[nx][g] = *(const v4u*)(smem + (nx ? w1hi1 : w1hi0) + (g - 3) * 20480 + ((s + 1) >> 1) * 1024);
                    if constexpr (g == 5 && s + RING1 < 20) ar[s % RING1][0] = ld128s(rs, xo, (s + RING1) * 1024);
                    if constexpr (g == 2 && s > 0 && s - 1 + RING1 < 20) ar[(s - 1) % RING1][1] = ld128s(rs, xo1, (s - 1 + RING1) * 1024);
                    if constexpr (s == 8 && g == 8) { t1 = __uint_as_float(ps[0].x) + __uint_as_float(ps[1].x); pin(t1); t2 = __uint_as_float(ps[0].y) + __uint_as_float(ps[1].y); pin(t2); }
                    if constexpr (s == 9 && g == 6) { t1 += __uint_as_float(ps[2].x); pin(t1); t2 += __uint_as_float(ps[2].y); pin(t2); }
                    if constexpr (s == 9 && g == 7) { t1 += __uint_as_float(ps[3].x); pin(t1); t2 += __uint_as_float(ps[3].y); pin(t2); }
                    if constexpr (s == 10 && g == 6) { t1 += dpp<0x128>(t1); t2 += dpp<0x128>(t2); pin(t1); pin(t2); }
                    if constexpr (s == 10 && g == 7) { t1 *= (1.0f / D_); pin(t1); t2 *= (1.0f / D_); pin(t2); }
                    if constexpr (s == 10 && g == 8) { t2 = fmaxf(t2 - t1 * t1, 0.f); pin(t2); }
                    if constexpr (s == 11 && g == 6) { rinv = __builtin_amdgcn_rsqf(t2 + 1e-5f); pin(rinv); }
                    if constexpr (s == 11 && g == 8) { mri = -t1 * rinv; pin(mri); }
                    if constexpr (s == 12 && g == 4) *(float*)(smem + l_rs(1, 0) + w * INBOX + lane * 4) = c < 8 ? rinv : mri;
                    if constexpr (s >= 12 && s < 15 && g >= 6 && g < 9 && 3 * (s - 12) + g - 6 < 8) bc2b<3 * (s - 12) + g - 6>(rr_[3 * (s - 12) + g - 6], rm_[3 * (s - 12) + g - 6], rinv, mri);
                    if constexpr (s >= 15 && s < 19 && g >= 5) TT_GAP(5 * (s - 15) + g - 5)
                    site();
                    SCHED();
                });
            });
            rr[0] = f32x4{rr_[0], rr_[1], rr_[2], rr_[3]}; rr[1] = f32x4{rr_[4], rr_[5], rr_[6], rr_[7]};
            asm volatile("kpf_end2:");
            ACC_FENCE10(acc[0], acc[1]);
            STAMP(13)
        } else {
#pragma unroll
            for (int nb = 0; nb < (OTR ? 0 : 5); ++nb) __builtin_amdgcn_raw_buffer_store_b128(__builtin_bit_cast(v4u, xin[nb]), ro, oo + 64 * nb, 0, OUTP);
            STAMP(21)
            STAMP_DUMP()
            asm volatile("kpf_end:");
        }
    };
    layer_body(std::integral_constant<int, 0>{});
    layer_body(std::integral_constant<int, 1>{});
}

extern "C" void kernel_launch(void* const* d_in, const int* in_sizes, int n_in,
                              void* d_out, int out_size, void* d_ws, size_t ws_size,
                              hipStream_t stream) {
    Params p;
    p.x0 = (const u16*)d_in[0];
    p.Lre = (const u16*)d_in[1];
    p.Lim = (const u16*)d_in[2];
    p.Bre = (const u16*)d_in[3];
    p.Bim = (const u16*)d_in[4];
    p.Cre = (const u16*)d_in[5];
    p.Cim = (const u16*)d_in[6];
    p.logdt = (const u16*)d_in[7];
    p.Dskip = (const u16*)d_in[8];
    p.k = (const u16*)d_in[10];
    p.v = (const u16*)d_in[11];
    p.shk = (const u16*)d_in[12];
    p.ssmk = (const u16*)d_in[13];
    p.ssmD = (const u16*)d_in[14];
    p.ws = (char*)d_ws;
    p.out = (float*)d_out;
    k_main<<<256, 256, 0, stream>>>(p);
}
```

```cpp
#include <hip/hip_runtime.h>
#include <stdint.h>
#include <utility>

#define STAMPS 0

typedef unsigned short u16;
typedef unsigned long long u64;
typedef unsigned v4u __attribute__((ext_vector_type(4)));
typedef unsigned v2u __attribute__((ext_vector_type(2)));
typedef float f32x4 __attribute__((ext_vector_type(4)));
typedef float f32x2 __attribute__((ext_vector_type(2)));
typedef short s16x4 __attribute__((ext_vector_type(4)));
typedef __bf16 bf16x2v __attribute__((ext_vector_type(2)));
#define LAS __attribute__((address_space(3)))
#define DI __device__ __forceinline__

constexpr int S_ = 2048, D_ = 640;
#define KG0 12
#define KRT 12
#define KRS 2
#define KB1 4
#define KMB 1
#define SBT 2
#define LAG 1
#define OTR 1
#define XI 1
#define GW 0
#define KR1 5
#define KR2 10
constexpr int RING1 = KR1;
#define KCR1 3
constexpr int kCR1 = KCR1;
#define KXA 10
#define KN3E 17
#define KN3 6
constexpr int kXA = KXA;
constexpr int kN3E = KN3E, kN3 = KN3;
#define KFHK 4
#define KFHP 12
constexpr int kFHK = KFHK, kFHP = KFHP;
#define KGPI 27
#define KGCI 33
#define KFGI 11
#define G2ND 1
#define KGSI 2
constexpr int kGSI = KGSI, kFGI = KFGI, kGPI = KGPI, kGCI = KGCI;

constexpr unsigned OFF_NONCE = 0;
constexpr unsigned OFF_ST = 4096;
constexpr unsigned OFF_FW = OFF_ST + 65536;
constexpr unsigned OFF_FK = OFF_FW + 131072;
constexpr unsigned OFF_FH = OFF_FK + 81920;
constexpr unsigned OFF_FX = OFF_FH + 262144;
constexpr unsigned OFF_FG = OFF_FX + 131072;
constexpr unsigned OFF_EG = OFF_FG + 98304;
constexpr unsigned OFF_SG = OFF_EG + 327680;
constexpr unsigned OFF_PS = OFF_SG + 524288;
constexpr unsigned OFF_KVT = OFF_PS + 131072;
constexpr unsigned OFF_GT = OFF_KVT + 2621440;
constexpr unsigned OFF_W2F = OFF_GT + 2621440;
constexpr unsigned OFF_X2F = OFF_W2F + 1638400;
constexpr unsigned OFF_DUM = OFF_X2F + 2621440;
constexpr unsigned OFF_HF = OFF_DUM + 163840;
constexpr unsigned WS_BYTES = OFF_HF + 2 * 5242880;
constexpr unsigned kNone = 0x40000000u;
constexpr unsigned OFF_DBG = 0x1800000;
#define MAP 0
#define TILES() const int xq = b & 7, j = b >> 3, mt2 = MAP ? 8 * (xq >> 1) + (j >> 2) : 4 * xq + (j >> 3), nt2 = MAP ? 4 * (xq & 1) + (j & 3) : j & 7, mt1 = mt2 >> 1, \
    nt1 = MAP ? 8 * (xq & 1) + (j & 7) : j & 15, i32 = 16 * (mt2 & 1) + mt1;

constexpr int SC = 102400;
constexpr int REGSZ = 13056;
constexpr int L_XR = SC;
constexpr int L_CO = SC + 12288;
constexpr int L_KB = SC + 51712;
constexpr int L_TAB = SC + 55808;
constexpr int L_CW = SC + 39168;
constexpr int INBOX = 15360;
constexpr int l_rs(int l, int w) { return l ? SC + w * INBOX : L_KB + w * 256; }
constexpr int l_sg(int l, int w) { return l ? SC + w * INBOX + 256 : SC + (w == 0 ? 43264 : w == 1 ? 46080 : w == 2 ? 52736 : 56320); }
constexpr int l_tb(int l, int w) { return l ? SC + w * INBOX + 2048 : SC + 40960 + w * 5120; }
constexpr int LDS_BYTES = 163840;
constexpr int CV_E = 4608;
constexpr int CV_O = CV_E + (1056 + 15) * 4;

struct Params {
    const u16 *x0, *Lre, *Lim, *Bre, *Bim, *Cre, *Cim, *logdt, *Dskip, *k, *v, *shk, *ssmk, *ssmD;
    char* ws;
    float* out;
};

#define MF "v_mfma_f32_16x16x32_bf16 "
DI void mf(f32x4& c, v4u a, v4u b) { asm(MF "%0, %1, %2, %0" : "+v"(c) : "v"(a), "v"(b)); }
DI void mf0(f32x4& c, v4u a, v4u b) { asm(MF "%0, %1, %2, 0" : "=&v"(c) : "v"(a), "v"(b)); }
constexpr int W2R = 56;
#define WA(n) "a[%c" #n ":%c" #n "+3]"
template <int R> DI void mfa(f32x4& c, v4u a) { asm(MF "%0, " WA(2) ", %1, %0" : "+v"(c) : "v"(a), "i"(W2R + 4 * R)); }
template <int R> DI void mfa0(f32x4& c, v4u a) { asm(MF "%0, " WA(2) ", %1, 0" : "=&v"(c) : "v"(a), "i"(W2R + 4 * R)); }
#define ACC_FENCE10(c, d) asm volatile("" : "+v"(c[0]), "+v"(c[1]), "+v"(c[2]), "+v"(c[3]), "+v"(c[4]), "+v"(d[0]), "+v"(d[1]), "+v"(d[2]), "+v"(d[3]), "+v"(d[4]))
#define ACC_TIE5(c) asm volatile("" : "+v"(c[0]), "+v"(c[1]), "+v"(c[2]), "+v"(c[3]), "+v"(c[4]))
#define QP " quad_perm:[1,0,3,2] row_mask:0xf bank_mask:0xf"
DI void cstep(float (&t)[5], const float (&h)[5], const float (&lr)[5], const float (&ls)[5], float b0, float b1, float b2, float b3, float b4) {
    asm("v_fma_f32 %0, %10, %5, %20\n\tv_fma_f32 %1, %11, %6, %21\n\tv_fma_f32 %2, %12, %7, %22\n\tv_fma_f32 %3, %13, %8, %23\n\tv_fma_f32 %4, %14, %9, %24\n\t"
        "v_fmac_f32_dpp %0, %5, %15" QP "\n\tv_fmac_f32_dpp %1, %6, %16" QP "\n\tv_fmac_f32_dpp %2, %7, %17" QP "\n\tv_fmac_f32_dpp %3, %8, %18" QP "\n\tv_fmac_f32_dpp %4, %9, %19" QP
        : "=&v"(t[0]), "=&v"(t[1]), "=&v"(t[2]), "=&v"(t[3]), "=&v"(t[4])
        : "v"(h[0]), "v"(h[1]), "v"(h[2]), "v"(h[3]), "v"(h[4]), "v"(lr[0]), "v"(lr[1]), "v"(lr[2]), "v"(lr[3]), "v"(lr[4]),
          "v"(ls[0]), "v"(ls[1]), "v"(ls[2]), "v"(ls[3]), "v"(ls[4]), "v"(b0), "v"(b1), "v"(b2), "v"(b3), "v"(b4));
}
DI void cstep3(float* t, const float* h, const float* lr, const float* ls, const float* b) {
    asm("v_fma_f32 %0, %6, %3, %12\n\tv_fma_f32 %1, %7, %4, %13\n\tv_fma_f32 %2, %8, %5, %14\n\t"
        "v_fmac_f32_dpp %0, %3, %9" QP "\n\tv_fmac_f32_dpp %1, %4, %10" QP "\n\tv_fmac_f32_dpp %2, %5, %11" QP
        : "=&v"(t[0]), "=&v"(t[1]), "=&v"(t[2])
        : "v"(h[0]), "v"(h[1]), "v"(h[2]), "v"(lr[0]), "v"(lr[1]), "v"(lr[2]), "v"(ls[0]), "v"(ls[1]), "v"(ls[2]), "v"(b[0]), "v"(b[1]), "v"(b[2]));
}
template <int o> DI void chain24(float (*t)[3], const float (*b)[5], const float* lr, const float* ls) {
    asm("v_fma_f32 %0, %18, %16, %8\n\t" "v_fma_f32 %1, %19, %17, %9\n\t" "v_fmac_f32_dpp %0, %16, %20" QP "\n\t" "v_fmac_f32_dpp %1, %17, %21" QP "\n\t"
        "v_fma_f32 %2, %18, %0, %10\n\t" "v_fma_f32 %3, %19, %1, %11\n\t" "v_fmac_f32_dpp %2, %0, %20" QP "\n\t" "v_fmac_f32_dpp %3, %1, %21" QP "\n\t"
        "v_fma_f32 %4, %18, %2, %12\n\t" "v_fma_f32 %5, %19, %3, %13\n\t" "v_fmac_f32_dpp %4, %2, %20" QP "\n\t" "v_fmac_f32_dpp %5, %3, %21" QP "\n\t"
        "v_fma_f32 %6, %18, %4, %14\n\t" "v_fma_f32 %7, %19, %5, %15\n\t" "v_fmac_f32_dpp %6, %4, %20" QP "\n\t" "v_fmac_f32_dpp %7, %5, %21" QP "\n\t"
        : "=&v"(t[1][0]), "=&v"(t[1][1]), "=&v"(t[2][0]), "=&v"(t[2][1]), "=&v"(t[3][0]), "=&v"(t[3][1]), "=&v"(t[4][0]), "=&v"(t[4][1])
        : "v"(b[0][o + 0]), "v"(b[0][o + 1]), "v"(b[1][o + 0]), "v"(b[1][o + 1]), "v"(b[2][o + 0]), "v"(b[2][o + 1]), "v"(b[3][o + 0]), "v"(b[3][o + 1]), "v"(t[0][0]), "v"(t[0][1]), "v"(lr[o + 0]), "v"(lr[o + 1]), "v"(ls[o + 0]), "v"(ls[o + 1]));
}
template <int o> DI void chain33(float (*t)[3], const float (*b)[5], const float* lr, const float* ls) {
    asm("v_fma_f32 %0, %21, %18, %9\n\t" "v_fma_f32 %1, %22, %19, %10\n\t" "v_fma_f32 %2, %23, %20, %11\n\t" "v_fmac_f32_dpp %0, %18, %24" QP "\n\t" "v_fmac_f32_dpp %1, %19, %25" QP "\n\t" "v_fmac_f32_dpp %2, %20, %26" QP "\n\t"
        "v_fma_f32 %3, %21, %0, %12\n\t" "v_fma_f32 %4, %22, %1, %13\n\t" "v_fma_f32 %5, %23, %2, %14\n\t" "v_fmac_f32_dpp %3, %0, %24" QP "\n\t" "v_fmac_f32_dpp %4, %1, %25" QP "\n\t" "v_fmac_f32_dpp %5, %2, %26" QP "\n\t"
        "v_fma_f32 %6, %21, %3, %15\n\t" "v_fma_f32 %7, %22, %4, %16\n\t" "v_fma_f32 %8, %23, %5, %17\n\t" "v_fmac_f32_dpp %6, %3, %24" QP "\n\t" "v_fmac_f32_dpp %7, %4, %25" QP "\n\t" "v_fmac_f32_dpp %8, %5, %26" QP "\n\t"
        : "=&v"(t[1][0]), "=&v"(t[1][1]), "=&v"(t[1][2]), "=&v"(t[2][0]), "=&v"(t[2][1]), "=&v"(t[2][2]), "=&v"(t[3][0]), "=&v"(t[3][1]), "=&v"(t[3][2])
        : "v"(b[0][o + 0]), "v"(b[0][o + 1]), "v"(b[0][o + 2]), "v"(b[1][o + 0]), "v"(b[1][o + 1]), "v"(b[1][o + 2]), "v"(b[2][o + 0]), "v"(b[2][o + 1]), "v"(b[2][o + 2]), "v"(t[0][0]), "v"(t[0][1]), "v"(t[0][2]), "v"(lr[o + 0]), "v"(lr[o + 1]), "v"(lr[o + 2]), "v"(ls[o + 0]), "v"(ls[o + 1]), "v"(ls[o + 2]));
}
template <int o> DI void chain32(float (*t)[3], const float (*b)[5], const float* lr, const float* ls) {
    asm("v_fma_f32 %0, %15, %12, %6\n\t" "v_fma_f32 %1, %16, %13, %7\n\t" "v_fma_f32 %2, %17, %14, %8\n\t" "v_fmac_f32_dpp %0, %12, %18" QP "\n\t" "v_fmac_f32_dpp %1, %13, %19" QP "\n\t" "v_fmac_f32_dpp %2, %14, %20" QP "\n\t"
        "v_fma_f32 %3, %15, %0, %9\n\t" "v_fma_f32 %4, %16, %1, %10\n\t" "v_fma_f32 %5, %17, %2, %11\n\t" "v_fmac_f32_dpp %3, %0, %18" QP "\n\t" "v_fmac_f32_dpp %4, %1, %19" QP "\n\t" "v_fmac_f32_dpp %5, %2, %20" QP "\n\t"
        : "=&v"(t[1][0]), "=&v"(t[1][1]), "=&v"(t[1][2]), "=&v"(t[2][0]), "=&v"(t[2][1]), "=&v"(t[2][2])
        : "v"(b[0][o + 0]), "v"(b[0][o + 1]), "v"(b[0][o + 2]), "v"(b[1][o + 0]), "v"(b[1][o + 1]), "v"(b[1][o + 2]), "v"(t[0][0]), "v"(t[0][1]), "v"(t[0][2]), "v"(lr[o + 0]), "v"(lr[o + 1]), "v"(lr[o + 2]), "v"(ls[o + 0]), "v"(ls[o + 1]), "v"(ls[o + 2]));
}
DI void pstep(f32x2& xr, f32x2& xs, f32x2 pr, f32x2 ps, f32x2 m) {
    asm("v_pk_mul_f32 %0, %2, %4 op_sel_hi:[1,0]\n\tv_pk_mul_f32 %1, %3, %4 op_sel_hi:[1,0]\n\t"
        "v_pk_fma_f32 %0, %3, %4, %0 op_sel:[0,1,0] neg_lo:[1,0,0] neg_hi:[1,0,0]\n\tv_pk_fma_f32 %1, %2, %4, %1 op_sel:[0,1,0]"
        : "=&v"(xr), "=&v"(xs) : "v"(pr), "v"(ps), "v"(m));
}
DI void padd(f32x2 (&h)[4], const f32x2 (&pr)[2], const f32x2 (&ps)[2], f32x2 c) {
    f32x2 d;
    asm("v_pk_mul_f32 %4, %6, %9 op_sel:[1,0]\n\tv_pk_fma_f32 %0, %5, %9, %0 op_sel_hi:[1,0,1]\n\tv_pk_fma_f32 %1, %6, %9, %1 op_sel_hi:[1,0,1]\n\t"
        "v_pk_fma_f32 %4, %8, %9, %4 op_sel:[1,1,0] op_sel_hi:[1,0,1] neg_hi:[1,0,0]\n\tv_pk_fma_f32 %0, %7, %9, %0 op_sel:[0,1,0]\n\tv_pk_fma_f32 %1, %8, %9, %1 op_sel:[0,1,0]\n\t"
        "v_pk_fma_f32 %2, %5, %4, %2 op_sel_hi:[1,0,1]\n\tv_pk_fma_f32 %3, %6, %4, %3 op_sel_hi:[1,0,1]\n\tv_pk_fma_f32 %2, %7, %4, %2 op_sel:[0,1,0]\n\tv_pk_fma_f32 %3, %8, %4, %3 op_sel:[0,1,0]"
        : "+v"(h[0]), "+v"(h[1]), "+v"(h[2]), "+v"(h[3]), "=&v"(d) : "v"(pr[0]), "v"(pr[1]), "v"(ps[0]), "v"(ps[1]), "v"(c));
}
DI void cstepi(float& b0, float& b1, float& b2, float& b3, float& b4, float h0, float h1, float h2, float h3, float h4, const float (&lr)[5], const float (&ls)[5]) {
    asm("v_fmac_f32 %0, %10, %5\n\tv_fmac_f32 %1, %11, %6\n\tv_fmac_f32 %2, %12, %7\n\tv_fmac_f32 %3, %13, %8\n\tv_fmac_f32 %4, %14, %9\n\t"
        "v_fmac_f32_dpp %0, %5, %15" QP "\n\tv_fmac_f32_dpp %1, %6, %16" QP "\n\tv_fmac_f32_dpp %2, %7, %17" QP "\n\tv_fmac_f32_dpp %3, %8, %18" QP "\n\tv_fmac_f32_dpp %4, %9, %19" QP
        : "+v"(b0), "+v"(b1), "+v"(b2), "+v"(b3), "+v"(b4)
        : "v"(h0), "v"(h1), "v"(h2), "v"(h3), "v"(h4), "v"(lr[0]), "v"(lr[1]), "v"(lr[2]), "v"(lr[3]), "v"(lr[4]), "v"(ls[0]), "v"(ls[1]), "v"(ls[2]), "v"(ls[3]), "v"(ls[4]));
}
#define UNDEF8(c) asm volatile("" : "=v"(c[0]), "=v"(c[1]), "=v"(c[2]), "=v"(c[3]), "=v"(c[4]), "=v"(c[5]), "=v"(c[6]), "=v"(c[7]))
#define PIN5(t) asm volatile("s_nop 1" : "+v"(t[0]), "+v"(t[1]), "+v"(t[2]), "+v"(t[3]), "+v"(t[4]))
DI unsigned cvtpk(float lo, float hi) {
    f32x2 v = {lo, hi};
    return __builtin_bit_cast(unsigned, __builtin_convertvector(v, bf16x2v));
}
DI float lo16f(unsigned u) { return __uint_as_float(u << 16); }
DI float hi16f(unsigned u) { return __uint_as_float(u & 0xffff0000u); }
DI float rbf(float v) { return lo16f(cvtpk(v, 0.f)); }
template <int CTRL> DI float dpp(float x) {
    return __int_as_float(__builtin_amdgcn_update_dpp(0, __float_as_int(x), CTRL, 0xF, 0xF, true));
}
DI float rowsum16(float x) {
    x += dpp<0x128>(x); x += dpp<0x124>(x); x += dpp<0x122>(x); x += dpp<0x121>(x);
    return x;
}
DI float xsum32(float x) { auto r = __builtin_amdgcn_permlane32_swap(__float_as_uint(x), __float_as_uint(x), false, false); return __uint_as_float(r[0]) + __uint_as_float(r[1]); }
DI void xsum2(float& a, float& d) {
    auto r = __builtin_amdgcn_permlane16_swap(__float_as_uint(a), __float_as_uint(d), false, false);
    const float s = __uint_as_float(r[0]) + __uint_as_float(r[1]);
    const float t = xsum32(s);
    auto u = __builtin_amdgcn_permlane16_swap(__float_as_uint(t), __float_as_uint(t), false, false);
    a = __uint_as_float(u[0]); d = __uint_as_float(u[1]);
}
DI float xsum2h(float a, float d) {
    auto r = __builtin_amdgcn_permlane16_swap(__float_as_uint(a), __float_as_uint(d), false, false);
    return xsum32(__uint_as_float(r[0]) + __uint_as_float(r[1]));
}
DI float hsum4(f32x4 v) { return (v.x + v.y) + (v.z + v.w); }
typedef __amdgpu_buffer_rsrc_t rsrc_t;
constexpr int SC1 = 16;
DI v4u ld128(rsrc_t rs, unsigned off) { return __builtin_amdgcn_raw_buffer_load_b128(rs, off, 0, SC1); }
DI v4u ld128s(rsrc_t rs, unsigned off, unsigned soff) { return __builtin_amdgcn_raw_buffer_load_b128(rs, off, soff, SC1); }
DI v2u ld64(rsrc_t rs, unsigned off) { return __builtin_amdgcn_raw_buffer_load_b64(rs, off, 0, SC1); }
DI void st128(v4u v, rsrc_t rs, unsigned off) { __builtin_amdgcn_raw_buffer_store_b128(v, rs, off, 0, SC1); }
DI void st64(v2u v, rsrc_t rs, unsigned off) { __builtin_amdgcn_raw_buffer_store_b64(v, rs, off, 0, SC1); }
#define LGKM0() { __builtin_amdgcn_s_waitcnt(0xC07F); asm volatile("" ::: "memory"); }
#define DRAIN() asm volatile("s_waitcnt vmcnt(0)" ::: "memory")
#define ORDER() __builtin_amdgcn_fence(__ATOMIC_ACQUIRE, "wavefront")
#define SCHED() __builtin_amdgcn_sched_barrier(0)
constexpr unsigned kSpinCap = 1u << 17;
#define WAVE_WAIT(...)                                                      \
    {                                                                       \
        unsigned spins_ = dead ? kSpinCap : 0u;                             \
        _Pragma("nounroll") for (;;) {                                      \
            asm volatile("" ::: "memory");                                  \
            bool ok;                                                        \
            __VA_ARGS__;                                                    \
            if (__builtin_amdgcn_ballot_w64(!ok) == 0ull) break;            \
            if (++spins_ > kSpinCap) { dead = true; break; }                \
            __builtin_amdgcn_s_sleep(1);                                    \
        }                                                                   \
        ORDER();                                                            \
    }
#define KPD1 2
#define KPDS 2
#define KPDS0 0
#define KPF0 14
#define KPFX 6
#define KPF1 4
constexpr int PD1 = KPD1, PDS = KPDS, PDS0 = KPDS0;
#define PL(r) "global_load_dwordx2 " r ", %5, %6 sc1\n\t"
#define PS_(r) "s_waitcnt vmcnt(1)\n\tv_cmp_ne_u64 vcc, %7, " r "\n\ts_cbranch_vccz 2f\n\t" PL(r)
template <int D0> DI void pipe_flag(v2u (&t)[4], unsigned off, const void* base, u64 tag, bool& dead) {
    unsigned n = dead ? 0u : kSpinCap;
    asm volatile("s_sleep %8\n\t" PL("%0") "s_sleep %9\n\t" PL("%1")
                 "1:\n\t" PS_("%0") PS_("%1") "s_sub_u32 %4, %4, 1\n\ts_cbranch_scc0 1b\n2:"
                 : "=&v"(t[0]), "=&v"(t[1]), "=&v"(t[2]), "=&v"(t[3]), "+s"(n) : "v"(off), "s"(base), "s"(tag), "i"(D0), "i"(KPF1) : "memory", "vcc", "scc");
    dead = __builtin_amdgcn_readfirstlane(n) == ~0u;
}
#define WAVE_RETRY(OK, ...) { if (__builtin_expect(__builtin_amdgcn_ballot_w64(!(OK)) != 0ull, 0)) WAVE_WAIT({ __VA_ARGS__; ok = OK; }) ORDER(); }
#define STAMP(i)
#define STAMP_DUMP()

#define KLB0 0
#define KLB1 12
DI void site() { asm volatile(""); }
template <class F, int... I> DI void static_for_impl(std::integer_sequence<int, I...>, F&& f) { (f(std::integral_constant<int, I>{}), ...); }
template <int N, class F> DI void static_for(F&& f) { static_for_impl(std::make_integer_sequence<int, N>{}, f); }
#define IC(n) std::integral_constant<int, (n)>{}
#define LAMBDA(v) [&](auto v) __attribute__((always_inline))
#define PK2 " op_sel:[1,1,0] op_sel_hi:[1,0,1] neg_lo:[1,0,0]"
DI f32x2 cmul(f32x2 a, f32x2 b) {
    f32x2 t;
    asm("v_pk_mul_f32 %0, %1, %2 op_sel_hi:[0,1]\n\ts_nop 0\n\tv_pk_fma_f32 %0, %1, %2, %0" PK2 "\n\ts_nop 0" : "=&v"(t) : "v"(a), "v"(b));
    return t;
}
DI f32x2 cfma(f32x2 a, f32x2 b, f32x2 c) {
    f32x2 t;
    asm("v_pk_fma_f32 %0, %1, %2, %3 op_sel_hi:[0,1,1]\n\ts_nop 0\n\tv_pk_fma_f32 %0, %1, %2, %0" PK2 "\n\ts_nop 0" : "=&v"(t) : "v"(a), "v"(b), "v"(c));
    return t;
}
DI void cfma4(f32x2 (&t)[4], f32x2 a, f32x2 b0, f32x2 b1, f32x2 b2, f32x2 b3, f32x2 c0, f32x2 c1, f32x2 c2, f32x2 c3) {
    asm("v_pk_fma_f32 %0, %4, %5, %9 op_sel_hi:[0,1,1]\n\tv_pk_fma_f32 %1, %4, %6, %10 op_sel_hi:[0,1,1]\n\tv_pk_fma_f32 %2, %4, %7, %11 op_sel_hi:[0,1,1]\n\tv_pk_fma_f32 %3, %4, %8, %12 op_sel_hi:[0,1,1]\n\t"
        "v_pk_fma_f32 %0, %4, %5, %0" PK2 "\n\tv_pk_fma_f32 %1, %4, %6, %1" PK2 "\n\tv_pk_fma_f32 %2, %4, %7, %2" PK2 "\n\tv_pk_fma_f32 %3, %4, %8, %3" PK2 "\n\ts_nop 0"
        : "=&v"(t[0]), "=&v"(t[1]), "=&v"(t[2]), "=&v"(t[3]) : "v"(a), "v"(b0), "v"(b1), "v"(b2), "v"(b3), "v"(c0), "v"(c1), "v"(c2), "v"(c3));
}
DI void gacc2(f32x2& h0, f32x2& h1, f32x2 w0, f32x2 w1, v4u e0, v4u e1) {
    const v2u a0 = {e0.x, e0.y}, b0 = {e0.z, e0.w}, a1 = {e1.x, e1.y}, b1 = {e1.z, e1.w};
    asm("v_pk_fma_f32 %0, %4, %2, %0 op_sel_hi:[0,1,1]\n\tv_pk_fma_f32 %1, %6, %3, %1 op_sel_hi:[0,1,1]\n\t"
        "v_pk_fma_f32 %0, %5, %2, %0 op_sel:[0,1,0] op_sel_hi:[0,0,1] neg_lo:[1,0,0]\n\tv_pk_fma_f32 %1, %7, %3, %1 op_sel:[0,1,0] op_sel_hi:[0,0,1] neg_lo:[1,0,0]"
        : "+v"(h0), "+v"(h1) : "v"(w0), "v"(w1), "v"(a0), "v"(b0), "v"(a1), "v"(b1));
}
DI s16x4 trread(char* smem, int byteoff) {
    return __builtin_amdgcn_ds_read_tr16_b64_v4i16((LAS s16x4*)(smem + byteoff));
}

#define CV_LOAD_A(A, a, rr_, hf) { const unsigned* ap_ = (const unsigned*)(smem + ab[(a) >> 1] + (((a) & 1) ? 0 : 512) + 64 * (7 - (rr_)) + 8 * (hf)); A[a][2 * (hf)] = ap_[0]; A[a][2 * (hf) + 1] = ap_[1]; }
#define CV_LOAD_B(B, e, rr_) { B[e] = *(const v4u*)(smem + bb + 64 * (7 - (rr_)) + 512 * (e)); }
constexpr int cv_pair(int lo, int hi, int n) {
    for (int a = 0; a < 8; ++a)
        for (int e = 0; e < 8; ++e)
            if (a + e >= lo && a + e < hi && n-- == 0) return 8 * a + e;
    return -1;
}
template <int R, int PF, bool OWN, class F>
DI void conv_round(f32x4 (&ca)[8], v4u (&A)[8], const v4u (&B)[8], v4u (&An)[8], v4u (&Bn)[8], const char* smem, const int (&ab)[4], int bb, F&& fill) {
    static_for<(R == 7 ? KG0 : 36)>(LAMBDA(g_) {
        constexpr int g = g_.value, a = cv_pair(0, 8, g) / 8, e = cv_pair(0, 8, g) % 8;
        mf(ca[a + e], A[a], B[e]);
        SCHED();
        if constexpr (OWN && g < 12) { CV_LOAD_A(A, 2 + g / 2, R, g & 1) SCHED(); }
        if constexpr (g < 4 * PF && (g & 3) != 3) { if constexpr ((g & 3) == 2) CV_LOAD_B(Bn, g >> 2, R + 1) else CV_LOAD_A(An, g >> 2, R + 1, g & 1) SCHED(); }
        site();
        if constexpr (R == 7) SCHED();
        if constexpr ((g & 3) == 3 && g < 28) { fill(std::integral_constant<int, 7 * R + g / 4>{}); SCHED(); }
    });
}
template <int R0, int R1, class F, class M>
DI void conv_part(f32x4 (&ca)[8], const char* smem, const int (&ab)[4], int bb, F&& fill, M&& mid, v4u (&A)[2][8], v4u (&B)[2][8]) {
    if constexpr (R0 > 0) { CV_LOAD_A(A[0], 0, R0, 0) CV_LOAD_A(A[0], 0, R0, 1) SCHED(); }
    static_for<8>(LAMBDA(a_) { constexpr int a = a_.value; CV_LOAD_B(B[0], a, R0) SCHED(); });
    if constexpr (R0 > 0) { CV_LOAD_A(A[0], 1, R0, 0) CV_LOAD_A(A[0], 1, R0, 1) SCHED(); }
    else {
        CV_LOAD_A(A[0], 0, 0, 0) CV_LOAD_A(A[0], 0, 0, 1) SCHED();
        const v4u ad = mid();
        SCHED();
        static_for<8>(LAMBDA(e_) {
            constexpr int e = e_.value;
            mf0(ca[e], ad, B[0][e]); SCHED();
            if constexpr (e < 7) { CV_LOAD_A(A[0], e + 1, 0, 0) CV_LOAD_A(A[0], e + 1, 0, 1) SCHED(); }
        });
    }
    static_for<R1 - R0>(LAMBDA(r_) {
        constexpr int r = r_.value;
        conv_round<R0 + r, (R0 + r + 1 < R1 ? 8 : 0), (r == 0 && R0 > 0)>(ca, A[r & 1], B[r & 1], A[(r & 1) ^ 1], B[(r & 1) ^ 1], smem, ab, bb, fill);
    });
}
template <int IMM> DI void gld(v4u& dst, unsigned voff, const void* sbase) {
    asm volatile("global_load_dwordx4 %0, %1, %2 offset:%3" : "=v"(dst) : "v"(voff), "s"(sbase), "i"(IMM));
}
#define OUTP 16
#define NTM 31
template <int IMM, int BIT> DI void gldn(v4u& dst, unsigned voff, const void* sbase) {
    if constexpr ((NTM >> BIT) & 1) asm volatile("global_load_dwordx4 %0, %1, %2 offset:%3 nt" : "=v"(dst) : "v"(voff), "s"(sbase), "i"(IMM));
    else gld<IMM>(dst, voff, sbase);
}
DI void gldu16(unsigned& dst, unsigned voff, const void* sbase) { asm volatile("global_load_ushort %0, %1, %2" : "=v"(dst) : "v"(voff), "s"(sbase)); }
template <int IMM> DI void gld64(v4u& dst, const void* a) {
    if constexpr ((NTM >> 5) & 1) asm volatile("global_load_dwordx4 %0, %1, off offset:%2 nt" : "=v"(dst) : "v"(a), "i"(IMM));
    else asm volatile("global_load_dwordx4 %0, %1, off offset:%2" : "=v"(dst) : "v"(a), "i"(IMM));
}
template <int V> DI void SETM0(unsigned ldsbase) { asm volatile("s_add_u32 m0, %0, %1" ::"s"(ldsbase), "i"(V)); }
template <int GOFF> DI void GLD0(unsigned voff, const char* sbase) { asm volatile("global_load_lds_dwordx4 %0, %1 offset:%2" ::"v"(voff), "s"(sbase), "i"(GOFF) : "memory"); }
template <int GOFF, int V> DI void GLD1(unsigned voff, const char* sbase, unsigned ldsbase) {
    asm volatile("global_load_lds_dwordx4 %0, %1 offset:%3\n\ts_add_u32 m0, %2, %4" ::"v"(voff), "s"(sbase), "s"(ldsbase), "i"(GOFF), "i"(V) : "memory");
}
constexpr int dma_goff(int it) { return it < 40 ? (it >> 2) * 128 : ((it - 100) / 5) * 256; }
constexpr int dma_loff(int it) { return it < 40 ? L_XR + ((it >> 2) % 3) * 4096 + (it & 3) * 1024 : ((it - 100) % 5) * 20480 + ((it - 100) / 5) * 2048; }
#define KFD0 10
#define KNF 13
#define KNW 11
#define KW2S 17
#define KW2M 0x4A
#define KFWP 14
constexpr int kW2S = KW2S, kW2M = KW2M;
constexpr int NW2P = (20 - kW2S) * __builtin_popcount(kW2M);
constexpr int P1_PRE = 34;
constexpr int p1_pre(int i) { return i < 5 ? 100 + i : i < 13 ? i - 5 : i < 18 ? 105 + (i - 13) : i < 22 ? 8 + (i - 18) : i < 27 ? 110 + (i - 22) : 400; }
constexpr int p1_item(int s, int g) {
    if (s == 7 && g == 0) return 324;
    if (g == 9) return s < 10 ? 115 + s : s == 11 ? 202 : s == 12 ? 203 : -1;
    if (s == KFWP && g == 1) return 208;
    if (s >= kW2S && ((kW2M >> g) & 1)) return 500 + (s - kW2S) * __builtin_popcount(kW2M) + __builtin_popcount(kW2M & ((1 << g) - 1));
    if ((s & 1) == 0 && s <= 12) return g >= 5 ? 4 * (s / 2 + 3) + (g - 5) : -1;
    if (s == 14 && g == 5) return 207;
    return -1;
}
constexpr int p1_cnt(int s, int g) {
    int n = P1_PRE;
    for (int t = 0; t <= s; ++t)
        for (int u = 0; u < 10; ++u) {
            if (t == s && u >= g) break;
            if (p1_item(t, u) >= 0 && p1_item(t, u) != 319) ++n;
        }
    return n;
}
constexpr int p1_pos(int code) {
    for (int i = 0; i < P1_PRE; ++i) if (p1_pre(i) == code) return i;
    for (int t = 0; t < 20; ++t)
        for (int u = 0; u < 10; ++u) if (p1_item(t, u) == code) return p1_cnt(t, u);
    return -1000;
}
constexpr int p1_cap(int s, int g) {
    int u = (s < 19 && g < 5) + (g == 9 && s < 19);
    if ((s & 1) && s < 19 && g >= 5 && g < 9) u += 1 + (g == 5);
    if ((s & 1) == 0 && g == 4) u += 1;
    const int it = p1_item(s, g), nt = g < 9 ? p1_item(s, g + 1) : -1;
    u += (it >= 0 && it < 500 && it != 208) + (nt >= 0 && nt < 200);
    if ((s == 18 && g == 1) || (s == 19 && g == 2) || (g == 9 && ((s & 3) == 2 || s == 17))) u += 2;
    return u < 2 ? 2 - u : 0;
}
constexpr int p1_slot(int s0, int s, int g, int gmax) {
    int n = 0;
    for (int t = s0; t <= s; ++t)
        for (int u = 0; u < gmax && !(t == s && u >= g); ++u) n += p1_cap(t, u);
    return n;
}
constexpr int p1_lim(int n) { return n > 63 ? 63 : n; }
template <int IMM> DI void gld4s(v4u& dst, unsigned voff, const void* sbase) { asm volatile("global_load_dwordx4 %0, %1, %2 offset:%3 sc1" : "=v"(dst) : "v"(voff), "s"(sbase), "i"(IMM)); }
template <int IMM> DI void gld2s(v2u& dst, unsigned voff, const void* sbase) { asm volatile("global_load_dwordx2 %0, %1, %2 offset:%3 sc1" : "=v"(dst) : "v"(voff), "s"(sbase), "i"(IMM)); }
template <class T> DI void pin(T& v) { asm volatile("" : "+v"(v)); }
DI void mul2(float& a, float& b, float x, float y, float s) { asm volatile("v_mul_f32 %0, %2, %4\n\tv_mul_f32 %1, %3, %4" : "=&v"(a), "=&v"(b) : "v"(x), "v"(y), "v"(s)); }
DI void rot1(unsigned& a, unsigned x) { asm volatile("v_alignbit_b32 %0, %1, %1, 16" : "=v"(a) : "v"(x)); }
DI void mix1(unsigned& a, unsigned x, unsigned y) { asm volatile("v_bfi_b32 %0, %3, %1, %2" : "=v"(a) : "v"(x), "v"(y), "s"(0xffffu)); }
DI void ror1(unsigned& o, unsigned prev) { asm volatile("v_mov_b32_dpp %0, %1 wave_ror:1 row_mask:0xf bank_mask:0xf" : "=v"(o) : "v"(prev)); }
DI void shr1b(unsigned& o, unsigned x) { asm volatile("v_mov_b32_dpp %0, %1 wave_shr:1 row_mask:0xf bank_mask:0xf" : "+v"(o) : "v"(x)); }
DI void xor1(float& a, unsigned x, unsigned s) { asm volatile("v_xor_b32 %0, %1, %2" : "=v"(a) : "v"(x), "v"(s)); }
template <int G> DI void bc1(float& a, unsigned x) { asm volatile("v_mov_b32_dpp %0, %1 row_newbcast:%2 row_mask:0xf bank_mask:0xf" : "=v"(a) : "v"(x), "i"(G)); }
template <int G> DI void mulb(float& o, unsigned x, float y) { asm volatile("v_mul_f32_dpp %0, %1, %2 row_newbcast:%3 row_mask:0xf bank_mask:0xf" : "=v"(o) : "v"(x), "v"(y), "i"(G)); }
template <int N> DI void lgkm() { __builtin_amdgcn_s_waitcnt(0xC07F | (N << 8)); asm volatile("" ::: "memory"); }
DI void unp2(float& a, float& b, unsigned x) { asm volatile("v_lshlrev_b32 %0, 16, %2\n\tv_and_b32 %1, 0xffff0000, %2" : "=&v"(a), "=&v"(b) : "v"(x)); }
#define GL(a) "global_load_dwordx4 a[" #a ":" #a "+3], %7, %8 sc1\n\tglobal_load_dwordx4 a[" #a "+4:" #a "+7], %7, %8 offset:256 sc1\n\t"
#define AR(d, a, o) "v_accvgpr_read_b32 " d ", a[" #a "+" #o "]\n\t"
#define GS_(a) "s_waitcnt vmcnt(4)\n\t" AR("%2", a, 1) AR("%3", a, 3) AR("%4", a, 5) AR("%5", a, 7) AR("%0", a, 0) AR("%1", a, 2)                              \
    "v_xor_b32 %2, %9, %2\n\tv_bitop3_b32 %4, %4, %9, %2 bitop3:0xbe\n\tv_bitop3_b32 %4, %3, %9, %4 bitop3:0xbe\n\tv_bitop3_b32 %4, %5, %9, %4 bitop3:0xbe\n\t" \
    AR("%2", a, 4) AR("%3", a, 6) "v_cmp_ne_u32 vcc, 0, %4\n\ts_cbranch_vccz 2f\n\t" GL(a)
template <int D0> DI void pipe_gran(float& a, float& d, unsigned off, const void* base, unsigned tag, bool& dead) {
    unsigned n = dead ? 0u : kSpinCap, y, z;
    float a1, d1;
    asm volatile("s_sleep %10\n\t" GL(32) "s_sleep %11\n\t" GL(40) "s_sleep %11\n\t" GL(48)
                 "1:\n\t" GS_(32) GS_(40) GS_(48) "s_sub_u32 %6, %6, 1\n\ts_cbranch_scc0 1b\n2:"
                 : "=&v"(a), "=&v"(d), "=&v"(a1), "=&v"(d1), "=&v"(y), "=&v"(z), "+s"(n) : "v"(off), "s"(base), "s"(tag), "i"(D0), "i"(PD1) : "memory", "vcc", "scc");
    dead = __builtin_amdgcn_readfirstlane(n) == ~0u;
    a += a1; d += d1;
}
DI void fma2(float& a, float& b, float x, float y, float s, float t, float u, float v) {
    asm volatile("v_fma_f32 %0, %2, %4, %6\n\tv_fma_f32 %1, %3, %5, %7" : "=&v"(a), "=&v"(b) : "v"(x), "v"(y), "v"(s), "v"(t), "v"(u), "v"(v));
}
DI void selhi(float& a, float x) { asm volatile("v_mov_b32_dpp %0, %1 quad_perm:[0,1,2,3] row_mask:0xf bank_mask:0xc" : "+v"(a) : "v"(x)); }
DI void cvt2(unsigned& a, unsigned& b, f32x4 v) { asm volatile("v_cvt_pk_bf16_f32 %0, %2, %3\n\tv_cvt_pk_bf16_f32 %1, %4, %5" : "=&v"(a), "=&v"(b) : "v"(v.x), "v"(v.y), "v"(v.z), "v"(v.w)); }
DI void swp2n(unsigned& a, unsigned& b, unsigned& c, unsigned& d) { asm volatile("v_permlane16_swap_b32 %0, %1\n\tv_permlane16_swap_b32 %2, %3" : "+v"(a), "+v"(b), "+v"(c), "+v"(d)); }
template <int G> DI void bc2b(float& a, float& b, float x, float y) {
    asm volatile("v_mov_b32_dpp %0, %2 row_newbcast:%4 row_mask:0xf bank_mask:0xf\n\tv_mov_b32_dpp %1, %3 row_newbcast:%4 row_mask:0xf bank_mask:0xf" : "=&v"(a), "=&v"(b) : "v"(x), "v"(y), "i"(G));
}
template <class T> DI void pins(T& v) { asm volatile("" : "+s"(v)); }
template <int I> DI void gld_w2(unsigned voff, const void* sbase) {
    asm volatile("global_load_dwordx4 " WA(2) ", %0, %1 offset:%3 sc1" ::"v"(voff), "s"(sbase), "i"(W2R + 4 * I), "i"((I % 5) * 1024 - 2048));
}
DI void undef4(v4u (&x)[4]) { asm volatile("" : "=v"(x[0]), "=v"(x[1]), "=v"(x[2]), "=v"(x[3])); }
DI void wait1(v2u& v) { asm volatile("s_waitcnt vmcnt(0)" : "+v"(v)::"memory"); }
DI void ld_ur(unsigned o, const void* base) {
    asm volatile("global_load_dwordx4 a[40:40+3], %0, %1 sc1\n\tglobal_load_dwordx4 a[44:44+3], %0, %1 offset:1024 sc1\n\t"
                 "global_load_dwordx4 a[48:48+3], %0, %1 offset:2048 sc1\n\tglobal_load_dwordx4 a[52:52+3], %0, %1 offset:3072 sc1" ::"v"(o), "s"(base));
}
DI void st_ur(unsigned a) {
    asm volatile("s_waitcnt vmcnt(%1)\n\tds_write_b128 %0, a[40:40+3] offset:480\n\tds_write_b128 %0, a[44:44+3] offset:1504\n\t"
                 "ds_write_b128 %0, a[48:48+3] offset:2528\n\tds_write_b128 %0, a[52:52+3] offset:3552" ::"v"(a), "i"(2 * __builtin_popcount(kW2M)) : "memory");
}
template <int A, int IMM> DI void lda4(unsigned o, const void* base) { asm volatile("global_load_dwordx4 a[%c2:%c2+3], %0, %1 offset:%3" ::"v"(o), "s"(base), "i"(A), "i"(IMM)); }
template <int A, int IMM> DI void lda2(unsigned o, const void* base) { asm volatile("global_load_dwordx2 a[%c2:%c2+1], %0, %1 offset:%3" ::"v"(o), "s"(base), "i"(A), "i"(IMM)); }
template <int A> DI void rda(unsigned& v) { asm volatile("v_accvgpr_read_b32 %0, a[%c1+0]" : "=v"(v) : "i"(A)); }
DI void poll1(unsigned a, const void* base) { asm volatile("global_load_dwordx2 a[36:36+1], %0, %1 sc1" ::"v"(a), "s"(base)); }
DI void poll2(unsigned a, unsigned b, const void* base) { asm volatile("global_load_dwordx2 a[36:36+1], %0, %2 sc1\n\tglobal_load_dwordx2 a[36+2:36+3], %1, %2 sc1" ::"v"(a), "v"(b), "s"(base)); }
DI void touch_(unsigned a, const void* base) { asm volatile("global_load_dword a[32+1], %0, %1" ::"v"(a), "s"(base)); }
template <int N> DI void wait_st(v2u& a, v4u& b) { asm volatile("s_waitcnt vmcnt(%2)" : "+v"(a), "+v"(b) : "i"(N) : "memory"); }
DI void undef3(v4u (&x)[3]) { asm volatile("" : "=v"(x[0]), "=v"(x[1]), "=v"(x[2])); }
template <int IMM> DI void gld4t(v4u& dst, unsigned voff, const void* sbase) { asm volatile("global_load_dwordx4 %0, %1, %2 offset:%3 sc1" : "+v"(dst) : "v"(voff), "s"(sbase), "i"(IMM)); }
template <int N> DI void vm_wait() { asm volatile("s_waitcnt vmcnt(%0)" ::"i"(N) : "memory"); }
DI float dot2a(unsigned a, unsigned b, float c) {
    return __builtin_amdgcn_fdot2_f32_bf16(__builtin_bit_cast(bf16x2v, a), __builtin_bit_cast(bf16x2v, b), c, false);
}
DI void disc(unsigned l0, unsigned l1, unsigned l2, f32x2& lam, f32x2& co) {
    const float dt = rbf(__builtin_amdgcn_exp2f(lo16f(l2) * 1.44269504088896f));
    const float lre = lo16f(l0), lim = lo16f(l1);
    const float e_ = __builtin_amdgcn_exp2f(dt * lre * 1.44269504088896f);
    const float rv = __builtin_amdgcn_fractf(dt * lim * 0.159154943091895f);
    lam = f32x2{e_ * __builtin_amdgcn_cosf(rv), e_ * __builtin_amdgcn_sinf(rv)};
    const float nr = lam.x - 1.0f, ni = lam.y, id = __builtin_amdgcn_rcpf(lre * lre + lim * lim);
    co = f32x2{(nr * lre + ni * lim) * id, (ni * lre - nr * lim) * id};
}
DI f32x4 unpk4(v2u u) { return f32x4{lo16f(u.x), hi16f(u.x), lo16f(u.y), hi16f(u.y)}; }
#define D2 "v_dot2_f32_bf16 "
#define CP "v_cvt_pk_bf16_f32 "
DI void d2x4(unsigned* o, unsigned a0, unsigned a1, unsigned a2, unsigned a3, unsigned b0, unsigned b1, unsigned b2, unsigned b3) {
    asm volatile(D2 "%0, %4, %8, 0\n\t" D2 "%1, %5, %9, 0\n\t" D2 "%2, %6, %10, 0\n\t" D2 "%3, %7, %11, 0"
                 : "=&v"(o[0]), "=&v"(o[1]), "=&v"(o[2]), "=&v"(o[3]) : "v"(a0), "v"(a1), "v"(a2), "v"(a3), "v"(b0), "v"(b1), "v"(b2), "v"(b3));
}

__global__ __launch_bounds__(256, 1) __attribute__((aligned(4096))) void k_main(Params p) {
    __shared__ __attribute__((aligned(16))) char smem[LDS_BYTES];
    const rsrc_t rs = __builtin_amdgcn_make_buffer_rsrc((void*)p.ws, 0, WS_BYTES, 0x00020000);
    const rsrc_t ro = __builtin_amdgcn_make_buffer_rsrc((void*)p.out, 0, S_ * D_ * 4, 0x00020000);
    const int w0 = __builtin_amdgcn_readfirstlane(threadIdx.x >> 6), b0 = blockIdx.x;
    bool dead = false;
    asm volatile("" ::: "a255");
    float SW[5];
    f32x4 xin[5];
    v2u gt[5], dsk[5];
    f32x4 acc[2][5];
    f32x4 rr[2];
    float tt[2][5][4];
    float lr[5], ls[5], lqr[5], lqs[5];
    f32x2 l8, l32, l128;
    bool kearly;
    unsigned sdq;
    int sgn_, ab[4], bb, zpa, uwa, spa, tbw, tbr, cwa, spl, dst[3];
    unsigned sgo, sgs, glo, gso, fgo, fgo1;
    int srca;
    unsigned adm, dso, w2o, wro, fwo, xso, fho1, fho, hso, eo, es, ho[4];
    bool cv0;
    int mt1_;
    auto addrs = [&](auto layer_, int lane, int w, int b) __attribute__((always_inline)) {
        constexpr int layer = layer_.value;
        const int c = lane & 15, q = lane >> 4;
        TILES()
        static_assert(l_sg(0, 0) == SC + 0xA900 && l_sg(0, 1) == SC + 0xB400 && l_sg(0, 2) == SC + 0xCE00 && l_sg(0, 3) == SC + 0xDC00, "");
        sgn_ = (layer ? l_sg(1, 0) + w * INBOX : SC + (int)((0xDC00CE00B400A900ull >> (16 * w)) & 0xffff)) + q * 320 + c * 4;
        const int pl = lane < 40 ? lane : 39, tbb = l_tb(layer, 0) + w * (layer ? INBOX : 5120);
        spl = SC + pl * 8;
        eo = OFF_EG + (unsigned)(layer * 163840 + nt1 * 640 + pl * 16);
        es = eo + (unsigned)(mt1 * 10240);
        spa = l_rs(layer, 0) + (2 * (mt2 & 1) + (w >> 1)) * (layer ? INBOX : 256) + (2 * (w & 1) + (c >> 3)) * 64 + (c & 7) * 4;
        tbw = tbb + c * 64 + ((q ^ ((c >> 1) & 3)) * 16);
        tbr = tbb + (2 * (c >> 2) + (q & 1)) * 64 + (((2 * (q >> 1) + ((c >> 1) & 1)) ^ (c >> 2)) * 16) + (c & 1) * 8;
        hso = OFF_HF + (unsigned)(layer * 5242880 + ((8 * mt1 + 2 * w + (q >> 1)) * 160 + 10 * nt1 + 5 * (q & 1)) * 256 + c * 16);
        cwa = L_CW + c * 4;
        fho1 = lane ? kNone : OFF_FH + (unsigned)((((layer * 32 + 2 * mt1 + (w >> 1)) * 4 + (nt1 >> 2)) * 8 + (nt1 & 3) * 2 + (w & 1)) * 128);
        fho = OFF_FH + (unsigned)(((layer * 32 + mt2) * 4 + w) * 1024 + (lane >> 3) * 128);
        pin(sgn_); pin(spl); pin(eo); pin(es); pin(spa); pin(tbw); pin(tbr); pin(hso); pin(cwa); pin(fho1); pin(fho);
        const unsigned hb_ = OFF_HF + (unsigned)(layer * 5242880 + (4 * mt2 * 160 + 40 * w) * 256);
#pragma unroll
        for (int m = 0; m < 4; ++m) { ho[m] = hb_ + (unsigned)(((m + 1 + w) & 3) * 40960 + lane * 16); pin(ho[m]); }
#pragma unroll
        for (int m = 0; m < 3; ++m) { dst[m] = SC + ((w + 1 + m) & 3) * INBOX + m * 5120 + lane * 16; pin(dst[m]); }
        mt1_ = mt1;
        sgo = OFF_SG + (unsigned)(((layer * 32 + mt2) * 4 + w) * 2048 + c * 16);
        sgs = q < 2 ? sgo + (unsigned)(nt2 * 256 + q * 8) : kNone;
        sgo += (unsigned)(q * 512);
        pin(sgo); pin(sgs);
        if constexpr (layer == 0) {
            srca = SC + w * INBOX + lane * 16;
            glo = OFF_GT + (unsigned)((80 * nt2 + (lane >> 1)) * 4096 + (64 * mt2 + 16 * w) * 2 + (lane & 1) * 16);
            gso = cv0 ? OFF_GT + (unsigned)((80 * nt2 + i32 + 32 * w) * 4096 + (q & 1) * 512 + c * 32 + (q >> 1) * 16) : kNone;
            fgo = OFF_FG + (unsigned)((nt2 * 96 + (lane >> 1)) * 128);
            fgo1 = w == 0 ? OFF_FG + (unsigned)((nt2 * 96 + i32) * 128) : kNone;
            pin(srca); pin(glo); pin(gso); pin(fgo); pin(fgo1);
            const int r0 = SC + (w < 3 ? w : 0) * REGSZ, y0 = 2031 + 8 * q - c, l34 = lane < 33 ? lane : 33;
#pragma unroll
            for (int i = 0; i < 4; ++i) { ab[i] = r0 + ((y0 & 1) ? CV_O : CV_E) + ((y0 + 1) >> 1) * 4 - 448 - 512 - 1024 * i; pin(ab[i]); }
            bb = r0 + 32 * c + 16 * q;
            zpa = w < 3 ? r0 + l34 * 16 + (l34 >= 30 ? CV_E + 4096 - 480 : 0) + (l34 >= 32 ? CV_O + 4100 - 32 - CV_E - 4096 : 0) : SC + 47360 + lane * 16;
            uwa = r0 + lane * 16;
            xso = (unsigned)((64 * mt2 + 16 * w + c) * D_ + 80 * nt2 + 16 * (q & 1) + 8 * (q >> 1)) * 2u;
            dso = (unsigned)(160 * nt2 + 8 * q);
            adm = (unsigned)((c & 1) * 16 + (q == 2 + (c >> 3) ? 256 << ((c & 7) >> 1) : 0));
            pin(adm);
            pin(bb); pin(zpa); pin(uwa); pin(xso); pin(dso);
        }
        SCHED();
    };
    unsigned tag0, tag1;
#define FLAG(off) { if (lane == 0) st64(v2u{tag0, tag1}, rs, off); }
#define ISTAG(f) (((f.x ^ tag0) | (f.y ^ tag1)) == 0u)
#define KW_REQ() { if (cv) ld_ur(wro, p.ws + OFF_KVT); static_for<NW2P>(LAMBDA(i_) { gld_w2<i_.value>(w2o, p.ws + 2048 + (i_.value / 5) * 5120); }); }
#define FW_OK() { if (__builtin_expect(!kearly, 0)) { WAVE_WAIT({ const v2u fw_ = ld64(rs, fwo); ok = ISTAG(fw_); }) KW_REQ() } }
#define TT_GAP(n) { constexpr int mb_ = (n) / 10, nb_ = ((n) % 10) / 2, e_ = ((n) & 1) * 2; \
        mul2(tt[mb_][nb_][e_], tt[mb_][nb_][e_ + 1], rm_[4 * mb_ + e_], rm_[4 * mb_ + e_ + 1], SW[nb_]); }
    {
        const int tid = threadIdx.x, lane = tid & 63, w = w0, b = b0;
        const int c = lane & 15, q = lane >> 4;
        TILES()
        STAMP(0)
        const unsigned lds0 = (unsigned)(size_t)(LAS char*)smem;
        v2u nonce;
        {
            u64 pc;
            int len, len2;
            asm volatile("s_getpc_b64 %0\nkpf_begin:\n\ts_mov_b32 %1, kpf_end-kpf_begin\n\ts_mov_b32 %2, kpf_end2-kpf_begin" : "=s"(pc), "=s"(len), "=s"(len2));
            len = len > len2 ? len : len2;
            const int o0 = (((j & 15) * 4 + w) + 64 * (lane >> 3)) * 128;
            touch_((unsigned)(o0 < len ? o0 : 0), (const void*)pc);
        }
        const bool cv = w < 2 || (w == 2 && i32 < 16);
        const int chn = 80 * nt2 + i32 + 32 * (cv ? w : 0);
        const bool kw = w < 2 || (w == 2 && i32 >= 16);
        const int kp = 16 * nt2 + mt1, ku = 5 * kp + (i32 < 16 ? w & 1 : 2 + (w < 3 ? w : 0)), kc = ku >> 6, kr = ku & 63;
        const int lc = cv ? lane : 8;
        int kwi = 2 + (i32 >> 4) - w;
        v4u ax[15], sa[5], cr[2], wr[4];
        unsigned uL[3];
        const int cu_ = (b < 200 ? b : 0) * 4 + w, cdb = cu_ / 20, ccg = cu_ % 20;
        {
            int ca_ = 32 * ccg, cb_ = 40 * nt1 + 10 * w - 32;
            asm volatile("" : "+s"(ca_), "+s"(cb_), "+s"(kwi));
            unsigned cpo = (unsigned)((lane < 32 ? ca_ : cb_) + (lane < 41 ? lane : 41)) * 2u;
            unsigned kso = (unsigned)(64 * kc + 8 * (lane & 7)) * 2u, ko = (unsigned)(32 * kr + 4 * (lane >> 3)) * 1280u + kso, kh = ko - 3840u;
            kso *= 4u;
            pin(cpo); pin(kso); pin(ko); pin(kh);
            SCHED();
            gldu16(uL[0], cpo, p.Lre); gldu16(uL[1], cpo, p.Lim); gldu16(uL[2], cpo, p.logdt);
            const v4u rk = {(unsigned)(u64)p.k, (unsigned)((u64)p.k >> 32) & 0xffffu, S_ * D_ * 2u, 0x00020000u};
#define L4(d, v, s_, o, f) "global_load_dwordx4 %" #d ", %" #v ", %" #s_ " offset:" #o f "\n\t"
#define B4(d, o) "buffer_load_dwordx4 %" #d ", %17, %21, 0 offen offset:" #o " nt\n\t"
            asm volatile("s_cmp_lt_i32 %22, 1\n\ts_cbranch_scc1 Lrole2\n\t"
                         L4(3, 15, 18, 0, " nt") L4(4, 15, 18, 1280, " nt") L4(5, 15, 18, 2560, " nt") L4(6, 15, 18, 3840, " nt")
                         L4(7, 15, 20, 0, " nt") L4(8, 15, 20, 1280, " nt") L4(9, 15, 20, 2560, " nt") L4(10, 15, 20, 3840, " nt")
                         B4(0, 0) B4(1, 1280) B4(2, 2560) L4(11, 16, 19, 0, "") L4(12, 16, 19, 16, "") L4(13, 16, 19, 32, "") L4(14, 16, 19, 48, "")
                         "\nLrole2:"
                         : "=&v"(ax[0]), "=&v"(ax[1]), "=&v"(ax[2]), "=&v"(ax[3]), "=&v"(ax[4]), "=&v"(ax[5]), "=&v"(ax[6]), "=&v"(ax[7]), "=&v"(ax[8]), "=&v"(ax[9]),
                           "=&v"(ax[10]), "=&v"(ax[11]), "=&v"(ax[12]), "=&v"(ax[13]), "=&v"(ax[14])
                         : "v"(ko), "v"(kso), "v"(kh), "s"(p.k), "s"(p.shk), "s"(p.v), "s"(rk), "s"(kwi) : "scc");
            SCHED();
        }
        const char* ssrc;
        {
            u64 pa = (u64)(p.x0 + (128 * mt1 + 8 * nt1 + 2 * w) * D_), pb = (u64)(((mt1 & 1) ? p.Bim : p.Bre) + (40 * nt1 + (mt1 >> 1)) * D_);
            asm volatile("" : "+s"(pa), "+s"(pb));
            const int nbx = 2 * w + q - 2, r = q < 2 ? q : 8 * (nbx < 5 ? nbx : 4);
            ssrc = (const char*)(q < 2 ? pa : pb) + (unsigned)(r * 1280 + c * 16);
            pin(ssrc);
        }
        SCHED();
        STAMP(22)
        const int row8 = lane >> 3;
        const unsigned wvo = (unsigned)(40 * nt1 + row8) * 1280u + (unsigned)(((lane & 7) ^ ((w & 1) | ((row8 >> 1) << 1))) * 16 + (w >> 1) * 128);
        const char* const wbase = (const char*)((w & 1) ? p.Bim : p.Bre);
        const unsigned wl = lds0 + (unsigned)(((w & 1) * 10 + (w >> 1)) * 1024);
        const unsigned xvo0 = (unsigned)(8 * (row8 >> 1) + 2 * (row8 & 1)) * 1280u + (unsigned)(((lane & 7) ^ ((row8 >> 1) << 1)) * 16);
        const unsigned xvo1 = (xvo0 ^ 16u) + 1280u;
        const char* const xbase = (const char*)p.x0 + (size_t)(128 * mt1 + 32 * w) * 1280;
        const unsigned xl = lds0 + (unsigned)(w * REGSZ);
        unsigned xv[4] = {xvo0, xvo1, xvo0 + 5120u, xvo1 + 5120u}, wv[5];
#pragma unroll
        for (int i = 0; i < 5; ++i) { wv[i] = wvo + i * 10240u; pin(wv[i]); if (i < 4) pin(xv[i]); }
#define DMA_M0(it) SETM0<dma_loff(it) - dma_goff(it)>((it) < 40 ? xl : wl)
#define DMA_LD1(it, nx_) GLD1<dma_goff(it), dma_loff(nx_) - dma_goff(nx_)>((it) < 40 ? xv[(it) & 3] : wv[(it) < 40 ? 0 : ((it) - 100) % 5], (it) < 40 ? xbase : wbase, (nx_) < 40 ? xl : wl)
#define DMA_LD(it) GLD0<dma_goff(it)>((it) < 40 ? xv[(it) & 3] : wv[(it) < 40 ? 0 : ((it) - 100) % 5], (it) < 40 ? xbase : wbase)
        const int swz = (c & 1) | ((c >> 2) << 1);
        int w1lane0 = (c & 1) * 10240 + (c >> 1) * 128 + ((q ^ swz) * 16);
        int w1lane1 = (c & 1) * 10240 + (c >> 1) * 128 + (((4 + q) ^ swz) * 16);
        int xlane0 = L_XR + w * REGSZ + (c & 1) * 1024 + (c >> 1) * 128 + ((q ^ swz) * 16);
        int xlane1 = L_XR + w * REGSZ + (c & 1) * 1024 + (c >> 1) * 128 + (((4 + q) ^ swz) * 16);
        unsigned cvo = (unsigned)((16 * cdb + c) * D_ + 32 * ccg + 8 * q) * 2u, for0, for1;
        {
            const int G = 4 * ccg + q, ch = 10 * (G / 5) + G % 5, ci = ch + 5;
            const unsigned fo = OFF_W2F + (unsigned)(((cdb / 5) * 200 + cdb % 5) * 1024 + c * 16);
            for0 = fo + (unsigned)((ch >> 2) * 5120 + (ch & 3) * 256); for1 = fo + (unsigned)((ci >> 2) * 5120 + (ci & 3) * 256);
        }
        unsigned sto = OFF_ST + (unsigned)((mt1 * 16 + 4 * w + q) * 256 + c * 8), swo = OFF_ST + (unsigned)(((lane >> 2) * 16 + nt1) * 256 + 128 + (lane & 3) * 16);
        unsigned kvo = OFF_KVT + (unsigned)((64 * kc + 8 * (lane >> 3) + ((lane >> 2) & 1)) * 4096 + (32 * kr + 8 * (lane & 3)) * 2);
        int kbw = L_KB + w * 1024 + (lane & 7) * 128 + (((lane >> 3) * 8) ^ ((lane & 7) * 16));
        int kbr = L_KB + w * 1024 + (lane >> 3) * 128 + (((lane & 7) * 16) ^ ((lane >> 3) * 16));
        const unsigned dum = kNone;
        if (!kw) kvo = dum;
        if (b >= 200) { for0 = dum; for1 = dum; cvo = (unsigned)((b * 4 + w) * 128); }
        wro = (unsigned)(chn * 4096 + lc * 16);
        cv0 = cv;
        pin(kvo); pin(kbw); pin(kbr);
        int w1hi0 = w1lane0 + 61440, w1hi1 = w1lane1 + 61440;
        pin(w1hi0); pin(w1hi1);
        pin(w1lane0); pin(w1lane1); pin(xlane0); pin(xlane1); pin(cvo); pin(for0); pin(for1); pin(sto); pin(swo); pin(wro);
        SCHED();
        auto pre = LAMBDA(i_) {
            constexpr int it = p1_pre(i_.value);
            SCHED();
            DMA_M0(it);
            asm volatile("s_nop 0");
            DMA_LD(it);
            SCHED();
        };
        {
            int f0 = 2 * ((chn & ~63) / 5), f1 = 2 * ((chn | 63) / 5) + 1, nk = cv ? 28 : 0;
            asm volatile("" : "+s"(f0), "+s"(f1), "+s"(nk));
            const int l10 = lane - 32 < 0 || lane - 32 > 9 ? 0 : lane - 32;
            fwo = lane < nk ? OFF_FK + (unsigned)(f0 + lane < f1 ? f0 + lane : f1) * 128u : OFF_FW + (unsigned)(25 * nt2 + w + 5 * (l10 >> 1) + (l10 & 1)) * 128u;
        }
        w2o = OFF_W2F + (unsigned)((nt2 * 40 + 10 * w) * 5120 + lane * 16);
        int ime = SC + (w < 3 ? w * REGSZ + CV_E : 43264) + 1008 - 16 * lane, imo = SC + (w < 3 ? w * REGSZ + CV_O + 4 : 47360) + 1008 - 16 * lane, im3 = w < 3 ? ime + 3072 : imo;
        int tca = L_TAB + (c >> 1) * 136, tcb = tca + 8 + 32 * w + 8 * q, tcl = L_TAB + (lane < 40 ? lane : 39) * 136, coa = L_CO + w * REGSZ + q * 64, rsa = l_rs(0, 0) + w * 256 + lane * 4;
        unsigned sgn = (c & 1) ? 0u : 0x80000000u;
        unsigned fko1 = w ? kNone : lane < 32 ? OFF_FK + (unsigned)((2 * kp + (i32 >> 4)) * 128) : OFF_FW + (unsigned)(b * 128);
        pin(fwo); pin(w2o); pin(fko1); pin(ime); pin(imo); pin(im3); pin(tca); pin(tcb); pin(tcl); pin(coa); pin(rsa); pin(sgn);
        SCHED();
        auto cw_ = [&]() __attribute__((always_inline)) {
            gldn<0, 2>(cr[0], cvo, p.Cre); gldn<0, 2>(cr[1], cvo, p.Cim);
            static_for<4>(LAMBDA(i) { gldn<i.value * 1024, 1>(wr[i.value], wro, p.ssmk); });
        };
        STAMP(33)
        gld2s<0>(nonce, 0u, p.ws + OFF_NONCE);
        asm volatile("s_waitcnt vmcnt(16)\n\ts_cmp_gt_i32 %3, 0\n\ts_cbranch_scc1 Lrole3\n\ts_waitcnt vmcnt(1)\nLrole3:" : "+v"(uL[0]), "+v"(uL[1]), "+v"(uL[2]) : "s"(kwi) : "memory", "scc");
        {
            f32x2 lam, co, lp[16];
            disc(uL[0], uL[1], uL[2], lam, co);
            if (lane < 32) *(f32x2*)(smem + L_CO + w * REGSZ + lane * 8) = 2.0f * co;
            lp[0] = cmul(lam, lam); lp[0] = cmul(lp[0], lp[0]); lp[1] = cmul(lp[0], lp[0]);
            lp[0] = f32x2{1.f, 0.f};
            lp[2] = cmul(lp[1], lp[1]); lp[3] = cmul(lp[2], lp[1]); lp[4] = cmul(lp[2], lp[2]);
            const f32x2 z2 = {0.f, 0.f};
            f32x2 t4[4];
            cfma4(t4, lp[4], lp[1], lp[2], lp[3], lp[4], z2, z2, z2, z2);
            lp[5] = t4[0]; lp[6] = t4[1]; lp[7] = t4[2]; lp[8] = t4[3];
            cfma4(t4, lp[8], lp[1], lp[2], lp[3], lp[4], z2, z2, z2, z2);
            lp[9] = t4[0]; lp[10] = t4[1]; lp[11] = t4[2]; lp[12] = t4[3];
            cfma4(t4, lp[8], lp[5], lp[6], lp[7], lp[7], z2, z2, z2, z2);
            lp[13] = t4[0]; lp[14] = t4[1]; lp[15] = t4[2];
            if (lane >= 32 && lane < 42) {
                f32x2* tl = (f32x2*)(smem + L_TAB + (10 * w + lane - 32) * 136);
                tl[0] = lam;
#pragma unroll
                for (int i = 0; i < 16; ++i) tl[1 + i] = lp[i];
            }
        }
        SCHED();
        STAMP(35)
        static_for<5>(LAMBDA(i) { gld64<256 * i.value>(sa[i.value], ssrc); });
        cw_();
        addrs(std::integral_constant<int, 0>{}, lane, w, b);
        v4u kvt[4];
        unsigned one2 = 0x3F803F80u, kq[12];
        float kf_[8];
        pin(one2);
        auto kvf = LAMBDA(n_) {
            constexpr int n = n_.value, h = n / 5, u = n % 5;
            if constexpr (u == 0) {
#pragma unroll
                for (int r = 0; r < 6; ++r) { kq[r] = __builtin_amdgcn_perm(ax[r][h], ax[r + 1][h], 0x05040100u); kq[6 + r] = __builtin_amdgcn_perm(ax[r][h], ax[r + 1][h], 0x07060302u); }
                if constexpr (h > 0) kvt[h - 1] = *(const v4u*)(smem + kbr);
            } else if constexpr (u == 1) {
                unsigned t[8];
                d2x4(t, kq[0], kq[1], kq[2], kq[3], ax[11 + h][1], ax[11 + h][1], ax[11 + h][1], ax[11 + h][1]);
                d2x4(t + 4, kq[6], kq[7], kq[8], kq[9], ax[11 + h][3], ax[11 + h][3], ax[11 + h][3], ax[11 + h][3]);
#pragma unroll
                for (int r = 0; r < 8; ++r) kf_[r] = __uint_as_float(t[r]);
            } else if constexpr (u == 2) {
#pragma unroll
                for (int r = 0; r < 8; ++r) kf_[r] = dot2a(kq[r + 2 * (r >> 2) + 2], ax[11 + h][2 * (r >> 2)], kf_[r]);
            } else if constexpr (u == 3) {
#pragma unroll
                for (int r = 0; r < 4; ++r) { kq[r] = ax[7 + r][h] << 16; kq[4 + r] = ax[7 + r][h] & 0xffff0000u; pin(kq[r]); pin(kq[4 + r]); }
            } else {
#pragma unroll
                for (int r = 0; r < 8; ++r) kf_[r] *= __uint_as_float(kq[r]);
                *(v2u*)(smem + kbw) = v2u{cvtpk(kf_[0], kf_[1]), cvtpk(kf_[2], kf_[3])};
                *(v2u*)(smem + (kbw ^ 64)) = v2u{cvtpk(kf_[4], kf_[5]), cvtpk(kf_[6], kf_[7])};
                if constexpr (h == 3) kvt[3] = *(const v4u*)(smem + kbr);
            }
            SCHED();
        };
        asm volatile("s_waitcnt vmcnt(%15)" : "+v"(ax[0]), "+v"(ax[1]), "+v"(ax[2]), "+v"(ax[3]), "+v"(ax[4]), "+v"(ax[5]), "+v"(ax[6]), "+v"(ax[7]), "+v"(ax[8]), "+v"(ax[9]),
                     "+v"(ax[10]), "+v"(ax[11]), "+v"(ax[12]), "+v"(ax[13]), "+v"(ax[14]) : "i"(11) : "memory");
        pin(nonce);
        STAMP(30)
        unsigned tch = 0u;
        constexpr int NF = KNF;
        static_assert(NF < 27, "");
        if (kw) {
            {
                static_for<20>(LAMBDA(n_) {
                    constexpr int n = n_.value, DN = 20 - KFD0;
                    site(); SCHED();
                    kvf(n_);
                    if constexpr (n >= KFD0) static_for<(n - KFD0 + 1) * NF / (DN ? DN : 1) - (n - KFD0) * NF / (DN ? DN : 1)>(LAMBDA(i_) { pre(std::integral_constant<int, (n - KFD0) * NF / (DN ? DN : 1) + i_.value>{}); });
                });
                if constexpr (KFD0 == 20) static_for<NF>(pre);
            }
        } else {
            if (w == 3) {
            const int l = lane, l3 = l & 3;
            unsigned a = OFF_SG + (unsigned)((((((l >> 3) & 1) * 32 + mt2) * 4 + ((l >> 1) & 3)) * 8 + nt2) * 256 + (l & 1) * 128);
            a = l >= 16 ? OFF_EG + (unsigned)((l >= 21 ? 163840 - 640 : 0) + (mt1 * 16 + nt1) * 640 + (l < 26 ? l - 16 : 5) * 128) : a;
            a = l >= 28 ? OFF_PS + (unsigned)(((mt2 * 4 + l3) * 8 + nt2) * 128) : a;
            a = l >= 32 ? OFF_FH + (unsigned)((((((l >> 2) & 1) * 32 + 2 * mt1 + (l3 >> 1)) * 4 + (nt1 >> 2)) * 8 + (nt1 & 3) * 2 + (l3 & 1)) * 128) : a;
            a = l >= 40 ? OFF_FX + (unsigned)(((mt1 * 4 + 2 * (mt2 & 1) + (l3 >> 1)) * 16 + nt2 * 2 + (l3 & 1)) * 128) : a;
            a = l >= 48 ? OFF_FG + (unsigned)((nt2 * 96 + i32) * 128) : a;
            const unsigned big = (l & 7) < 2 ? OFF_HF + (unsigned)((l & 1) * 5242880 + (8 * mt1 * 160 + 10 * nt1) * 256) : (l & 7) == 2 ? OFF_X2F + (unsigned)(2 * mt2 * 40960 + 2560 * nt2)
                               : (l & 7) < 5 ? OFF_KVT + (unsigned)((l & 1) * 2621440 + (80 * nt2 + i32) * 4096) : OFF_W2F + (unsigned)(nt2 * 204800 + ((l & 7) - 5) * 98304);
            a = l >= 56 ? big : a;
            tch = a;
        }
            vm_wait<KNW>();
            static_for<NF>(pre);
            undef4(kvt);
        }
        tag0 = __builtin_amdgcn_readfirstlane(nonce.x) + 1u; tag1 = __builtin_amdgcn_readfirstlane(nonce.y) + (tag0 == 0u ? 1u : 0u);
        SCHED();
        STAMP(34)
        v4u orr, oi;
        v2u gr;
        unsigned gro;
        {
            asm volatile("s_waitcnt vmcnt(%7)" : "+v"(cr[0]), "+v"(cr[1]), "+v"(sa[0]), "+v"(sa[1]), "+v"(sa[2]), "+v"(sa[3]), "+v"(sa[4]) : "i"(4 + NF) : "memory");
#pragma unroll
            for (int e = 0; e < 4; ++e) {
                const f32x4 co = *(const f32x4*)(smem + coa + e * 16);
                const f32x2 o0 = lo16f(cr[0][e]) * f32x2{co.x, -co.y} - lo16f(cr[1][e]) * f32x2{co.y, co.x};
                const f32x2 o1 = hi16f(cr[0][e]) * f32x2{co.z, -co.w} - hi16f(cr[1][e]) * f32x2{co.w, co.z};
                orr[e] = cvtpk(o0.x, o1.x); oi[e] = cvtpk(o0.y, o1.y);
            }
            pin(orr); pin(oi);
            SCHED();
        }
        static_for<(27 - NF) / 3>(LAMBDA(i_) { pre(std::integral_constant<int, NF + i_.value>{}); });
        {
            float s1[2] = {0.f, 0.f}, s2[2] = {0.f, 0.f};
#pragma unroll
            for (int i = 0; i < 5; ++i)
#pragma unroll
                for (int e = 0; e < 4; ++e) { s1[e & 1] = dot2a(sa[i][e], one2, s1[e & 1]); s2[e & 1] = dot2a(sa[i][e], sa[i][e], s2[e & 1]); }
            const float a1 = rowsum16(s1[0] + s1[1]), a2 = rowsum16(s2[0] + s2[1]);
            const float mean = a1 * (1.0f / D_);
            const float var = fmaxf(a2 * (1.0f / D_) - mean * mean, 0.f);
            const float ri = __builtin_amdgcn_rcpf(rbf(__builtin_amdgcn_sqrtf(rbf(rbf(var) + 1e-5f))));
            gr = v2u{__float_as_uint(q >= 2 ? a1 : c ? -ri * rbf(mean) : ri), tag0};
            gro = c < 2 ? OFF_ST + (unsigned)((mt1 * 16 + nt1) * 256 + ((q < 2 ? 8 * c : 14) + 2 * w + q) * 8) : kNone;
            pin(gr); pin(gro);
        }
        SCHED();
        STAMP(59)
        {
            static_for<27 - NF - (27 - NF) / 3>(LAMBDA(i_) { pre(std::integral_constant<int, NF + (27 - NF) / 3 + i_.value>{}); });
#pragma unroll
            for (int i = 0; i < 4; ++i) st128(kvt[i], rs, kvo + i * 8192);
            st128(orr, rs, for0); st128(oi, rs, for1);
        }
        st64(gr, rs, gro);
        SCHED();
        STAMP(23)
        asm volatile("s_waitcnt vmcnt(%4) lgkmcnt(0)" : "+v"(wr[0]), "+v"(wr[1]), "+v"(wr[2]), "+v"(wr[3]) : "i"(P1_PRE - 9) : "memory");
        __builtin_amdgcn_s_barrier();
        asm volatile("" ::: "memory");
        STAMP(24)
        v4u wf[2][5], af[2][2][2];
#pragma unroll
        for (int nb = 0; nb < 5; ++nb) wf[0][nb] = *(const v4u*)(smem + w1lane0 + nb * 20480 + (0) * 2048);
#pragma unroll
        for (int i = 0; i < 4; ++i) af[0][i >> 1][i & 1] = *(const v4u*)(smem + ((i >> 1) ? xlane1 : xlane0) + (i & 1) * 2048);
        SCHED();
        const char *kvtb = p.ws + OFF_KVT, *w2b0 = p.ws + 2048;
        pins(kvtb); pins(w2b0);
        v2u sg, tc[10], fw, tg2 = {tag0, tag1};
        pin(tg2);
        v4u sw0;
        unsigned ie[4][4], io[4][4], iw, zq[4];
        float rr_[8];
        auto kstep = LAMBDA(s_) {
            constexpr int s = s_.value, ph = s >> 2, s4 = s & 3, cu = s & 1, nx = cu ^ 1, jj = s >> 1, pp = jj & 1;
            if constexpr (s4 == 3 && ph < 4) {
                constexpr int pw = p1_pos(104 + 5 * (ph + 1)), px = p1_pos(4 * (jj + 1) + 3), pm = pw > px ? pw : px;
                constexpr int vm = p1_lim(p1_cnt(s, 0) - 1 - pm);
                asm volatile("" ::: "memory");
                __builtin_amdgcn_s_waitcnt((vm & 15) | ((vm >> 4) << 14) | 0x70);
                __builtin_amdgcn_s_barrier();
                asm volatile("" ::: "memory");
                STAMP(25 + ph)
                SCHED();
            }
            if constexpr (s == 15) {
                wait_st<p1_cnt(s, 0) - 1 - p1_pos(203)>(sg, sw0);
                WAVE_RETRY((((sg.y ^ tag0) | (sw0.y ^ tag0) | (sw0.w ^ tag0)) == 0u), sg = ld64(rs, sto); sw0 = ld128(rs, swo))
                STAMP(7)
            }
            if constexpr (s == kW2S) { vm_wait<p1_cnt(s, 0) - 1 - p1_pos(208)>(); SCHED(); }
            if constexpr (s == 18) {
                vm_wait<p1_cnt(s, 0) - 1 - (p1_pos(207) > p1_pos(208) ? p1_pos(207) : p1_pos(208))>();
                pin(fw); pin(sdq);
                kearly = __builtin_amdgcn_ballot_w64(!ISTAG(fw)) == 0ull;
                ORDER();
                if (kearly && cv) ld_ur(wro, kvtb);
                SCHED();
            }
            if constexpr (s4 != 3 || ph == 4) lgkm<(cu == 0 && s > 0 ? 3 : s == 17 ? 4 : s == 19 ? 2 : 0)>();
            SCHED();
            static_for<10>(LAMBDA(g_) {
                constexpr int g = g_.value, mb = g / 5, k = g % 5, it = p1_item(s, g), nt_ = g < 9 ? p1_item(s, g + 1) : -1;
                constexpr bool d0 = it >= 0 && it < 200, d1 = nt_ >= 0 && nt_ < 200;
                if constexpr (s == 0) mf0(acc[mb][k], af[pp][cu][mb], wf[cu][k]); else mf(acc[mb][k], af[pp][cu][mb], wf[cu][k]);
                SCHED();
                if constexpr (s < 19) {
                    if constexpr (g < 5) wf[nx][g] = *(const v4u*)(smem + (g < 3 ? (nx ? w1lane1 : w1lane0) : (nx ? w1hi1 : w1hi0)) + (g < 3 ? g : g - 3) * 20480 + ((s + 1) >> 1) * 1024);
                    if constexpr (cu == 1 && g == 5) vm_wait<p1_lim(p1_cnt(s, 5) - 1 - p1_pos(4 * (jj + 1) + 3))>();
                    if constexpr (cu == 1 && g >= 5 && g < 9)
                        af[pp ^ 1][(g - 5) >> 1][(g - 5) & 1] = *(const v4u*)(smem + (((g - 5) >> 1) ? xlane1 : xlane0) + ((jj + 1) % 3) * 4096 + ((g - 5) & 1) * 2048);
                }
                if constexpr (cu == 0 && g == 4 && s > 0) lgkm<(s == 8 || s == 10 || s == 14 ? 8 : 5)>();
                static_for<p1_cap(s, g)>(LAMBDA(e_) {
                    constexpr int na = p1_slot(0, s, g, 10) + e_.value, i = (na / 10) & 3, u = na % 10;
                    static_assert(p1_slot(0, 8, 0, 10) >= 40, "");
                    if constexpr (na < 40) {
                        if constexpr (u == 0) { if constexpr (i > 0) ror1(iw, wr[i - 1].w); else { iw = 0u; pin(iw); } }
                        else if constexpr (u < 5) rot1(ie[i][u - 1], wr[i][4 - u]);
                        else if constexpr (u == 5) shr1b(iw, wr[i].w);
                        else if constexpr (u < 9) mix1(io[i][u - 6], wr[i][9 - u], wr[i][8 - u]);
                        else mix1(io[i][3], wr[i].x, iw);
                    }
                    constexpr int nb = p1_slot(8, s, g, 3) + e_.value;
                    if constexpr (s >= 8 && s < 12 && g < 3) {
                        if constexpr (nb < 10) tc[nb] = *(const v2u*)(smem + (nb < 5 ? tca : tcb) + (nb % 5) * 1088);
                        else if constexpr (nb == 10) { const f32x2* tp = (const f32x2*)(smem + tcl); l8 = tp[2]; l32 = tp[5]; }
                        else if constexpr (nb == 11) l128 = *(const f32x2*)(smem + tcl + 72);
                        static_assert(p1_slot(8, 12, 0, 3) == 12, "");
                    }
                    constexpr int nc = p1_slot(12, s, g, 10) + e_.value;
                    if constexpr (s >= 12 && s < 14 && nc < 10) {
                        if constexpr (nc < 5) { lr[nc] = __uint_as_float(tc[nc].x); xor1(ls[nc], tc[nc].y, sgn); }
                        else { lqr[nc - 5] = __uint_as_float(tc[nc].x); xor1(lqs[nc - 5], tc[nc].y, sgn); }
                        static_assert(p1_slot(12, 14, 0, 10) >= 10, "");
                    }
                    if constexpr (s == 15 && g < 5) SW[g] = __int_as_float(__builtin_amdgcn_ds_bpermute(16 * c + 4 * (g >> 1), (g & 1) ? sw0.z : sw0.x));
                    constexpr bool img = (s == 16 && g >= 5 && g < 9 && e_.value == 0) || (s == 18 && (g == 5 || g == 6) && e_.value == 0);
                    constexpr int nd = p1_slot(15, s, g, 10) + e_.value - 5 - (s > 16 ? 4 : s == 16 && g >= 5 ? (g < 9 ? g - 4 : 4) : 0) - (s > 18 ? 2 : s == 18 && g >= 5 ? (g == 5 ? 1 : 2) : 0);
                    if constexpr (s >= 15 && nd >= 0 && !img) {
                        if constexpr (nd < 4) bc1<nd>(rr_[nd], sg.x);
                        else if constexpr (nd < 44) mulb<8 + (nd - 4) % 8>(tt[(nd - 4) % 8 / 4][(nd - 4) / 8][(nd - 4) % 4], sg.x, SW[(nd - 4) / 8]);
                        else if constexpr (nd < 48) bc1<nd - 40>(rr_[nd - 40], sg.x);
                        static_assert(p1_slot(15, 16, 0, 10) <= 9 && p1_slot(15, 20, 0, 10) >= 59, "");
                    }
                });
                if constexpr (s == 14 && g < 3) *(v4u*)(smem + ime + 1024 * g) = v4u{ie[3 - g][0], ie[3 - g][1], ie[3 - g][2], ie[3 - g][3]};
                if constexpr (s == 16 && g >= 5 && g < 9) *(v4u*)(smem + imo + 1024 * (8 - g)) = v4u{io[g - 5][0], io[g - 5][1], io[g - 5][2], io[g - 5][3]};
                if constexpr (s == 18 && g == 5) *(v4u*)(smem + im3) = v4u{ie[0][0], ie[0][1], ie[0][2], ie[0][3]};
                if constexpr (s == 18 && g == 6) *(unsigned*)(smem + rsa) = sg.x;
                if constexpr (s == 14 && (g == 6 || g == 8)) { zq[g - 6] = 0u; zq[g - 5] = 0u; pin(zq[g - 6]); pin(zq[g - 5]); }
                if constexpr (s == 19 && g == 0) *(v4u*)(smem + zpa) = v4u{zq[0], zq[1], zq[2], zq[3]};
                if constexpr (s == 18 && g == 1) touch_(tch, p.ws);
                if constexpr (s == 19 && g == 2) { if (__builtin_expect(!kearly, 0)) poll1(fwo, p.ws); }
                if constexpr (it >= 0) {
                    if constexpr (d0 && d1) DMA_LD1(it, d1 ? nt_ : 0);
                    else if constexpr (d0) DMA_LD(it);
                    else if constexpr (it == 202) gld2s<0>(sg, sto, p.ws);
                    else if constexpr (it == 203) gld4s<0>(sw0, swo, p.ws);
                    else if constexpr (it == 207) gldu16(sdq, (unsigned)chn * 2u, p.ssmD);
                    else if constexpr (it == 208) gld2s<0>(fw, fwo, p.ws);
                    else if constexpr (it >= 500) gld_w2<it % 50>(w2o, w2b0 + ((it % 50) / 5) * 5120);
                    else { static_assert(p1_pos(19) >= P1_PRE, ""); st64(tg2, rs, fko1); }
                }
                if constexpr (d1 && !d0) DMA_M0(d1 ? nt_ : 0);
                site();
                SCHED();
            });
        };
        static_for<20>(kstep);
        SCHED();
        rr[0] = f32x4{rr_[0], rr_[1], rr_[2], rr_[3]}; rr[1] = f32x4{rr_[4], rr_[5], rr_[6], rr_[7]};
        ACC_FENCE10(acc[0], acc[1]);
        STAMP(1)
    }

    auto layer_body = LAMBDA(layer_) {
        constexpr int layer = layer_.value;
        const int sga = sgn_;
        const bool cv = layer == 0 && cv0;
        v2u fh;
        unsigned ks[10];
        f32x4 ca[8], a2[4][5];
        float rsr, rsm;
        int otw, otr, ots;
        v4u cA[2][8], cB[2][8];
        constexpr int R2 = kCR1, LP = (7 - R2) & 1;
        static_assert(kFHP >= 7 - KG0 / 4 + 1, "");
        constexpr bool TB = (SBT >> layer) & 1;
        float bv[8][5];
        f32x2 pr[5][2], ps[5][2];
        auto powers = [&]() __attribute__((always_inline)) {
            if constexpr (TB) {
#pragma unroll
                for (int nb = 0; nb < 5; ++nb) {
                    const f32x2 m = {lr[nb] * lr[nb] - ls[nb] * ls[nb], 2.0f * lr[nb] * ls[nb]};
                    pr[nb][0] = f32x2{lr[nb], m.x}; ps[nb][0] = f32x2{ls[nb], m.y};
#pragma unroll
                    for (int j = 1; j < 2; ++j) pstep(pr[nb][j], ps[nb][j], pr[nb][j - 1], ps[nb][j - 1], m);
#pragma unroll
                    for (int j = 0; j < 2; ++j) { pin(pr[nb][j]); pin(ps[nb][j]); }
                }
                SCHED();
            }
        };
        {
            float a0[5], a1[5];
            static_for<2>(LAMBDA(m_) {
                constexpr int mb = m_.value;
#pragma unroll
                for (int nb = 0; nb < 5; ++nb) {
                    const f32x4 v = __builtin_elementwise_fma(acc[mb][nb], rr[mb], f32x4{tt[mb][nb][0], tt[mb][nb][1], tt[mb][nb][2], tt[mb][nb][3]});
#pragma unroll
                    for (int i = 0; i < 4; ++i) bv[4 * mb + i][nb] = v[i];
                }
                SCHED();
                if constexpr (TB) {
#pragma unroll
                    for (int i = mb ? 4 : 1; i < 4 + 4 * mb; ++i) cstepi(bv[i][0], bv[i][1], bv[i][2], bv[i][3], bv[i][4], bv[i - 1][0], bv[i - 1][1], bv[i - 1][2], bv[i - 1][3], bv[i - 1][4], lr, ls);
#pragma unroll
                    for (int nb = 0; nb < 5; ++nb) a0[nb] = bv[7][nb];
                }
                if constexpr (mb == 0 && !TB) cstep(a0, bv[0], lr, ls, bv[1][0], bv[1][1], bv[1][2], bv[1][3], bv[1][4]);
#pragma unroll
                for (int i8 = mb ? 4 : 2; i8 < (TB ? 0 : 4 + 4 * mb); i8 += 2) {
                    cstep(a1, a0, lr, ls, bv[i8][0], bv[i8][1], bv[i8][2], bv[i8][3], bv[i8][4]);
                    cstep(a0, a1, lr, ls, bv[i8 + 1][0], bv[i8 + 1][1], bv[i8 + 1][2], bv[i8 + 1][3], bv[i8 + 1][4]);
                }
                SCHED();
            });
#pragma unroll
            for (int nb = 0; nb < 5; ++nb) *(float*)(smem + sga + nb * 64) = a0[nb];
        }
        SCHED();
        const int w_ = w0;
        {
        const int w = w_, mt1 = mt1_;
        if (layer == 0 && __builtin_expect(!kearly, 0)) {
            unsigned f0_, f1_;
            asm volatile("s_waitcnt vmcnt(0)\n\t" AR("%0", 36, 0) AR("%1", 36, 1) : "=v"(f0_), "=v"(f1_)::"memory");
            kearly = __builtin_amdgcn_ballot_w64(((f0_ ^ tag0) | (f1_ ^ tag1)) != 0u) == 0ull;
            ORDER();
            if (kearly) KW_REQ()
        }
        asm volatile("s_waitcnt lgkmcnt(0)" ::: "memory");
        __builtin_amdgcn_s_barrier();
        asm volatile("" ::: "memory");
        STAMP(layer ? 14 : 2)
#pragma unroll
        for (int s = 0; s < 10; ++s) ks[s] = (unsigned)s * 1024u;
        constexpr int NX = 58;
        constexpr int NW2A = NW2P + 7 * kCR1 + kXA, NW3 = NW2P + kN3E + kN3;
        auto w2ld = LAMBDA(i_) {
            constexpr int i = i_.value;
            gld_w2<i>(w2o, p.ws + 2048 + ks[i / 5] * 5u);
        };
        auto w2a = LAMBDA(i_) {
            constexpr int k = i_.value;
            if constexpr (k < kXA) { w2ld(std::integral_constant<int, NW2P + 2 * k>{}); w2ld(std::integral_constant<int, NW2P + 2 * k + 1>{}); }
            else w2ld(std::integral_constant<int, NW2P + kXA + k>{});
        };
        auto w2x = LAMBDA(i_) {
            constexpr int i = i_.value < NX - 50 ? 50 + i_.value : NW2A + i_.value - (NX - 50) < 50 ? NW2A + i_.value - (NX - 50) : 99;
            if constexpr (i < 50) w2ld(std::integral_constant<int, i>{});
            else if constexpr (i < 52) lda4<40 + 4 * (i - 50), (i - 50) * 64>(xso, p.x0);
            else if constexpr (i == 52) lda4<48, 128>(xso - 2u * (unsigned)(bb & 16), p.x0);
            else if constexpr (i < NX) lda2<20 + 2 * (i - 53), 32 * (i - 53)>(dso, p.Dskip);
        };
        constexpr int NB = NX - NW2A, NB1 = KB1 < NB ? KB1 : NB, KPL = 7 * (8 - R2) - kFHP, NFH = NB - NB1 - (KPL - kFHK) > 0 ? NB - NB1 - (KPL - kFHK) : 0;
        auto w2b = LAMBDA(i_) {
            constexpr int k = i_.value - 7 * R2;
            if constexpr (k < NB1) w2x(std::integral_constant<int, k>{});
            if constexpr (k > kFHK) w2x(std::integral_constant<int, NB1 + k - kFHK - 1>{});
            if constexpr (k == kFHK) { vm_wait<NB1>(); st64(v2u{tag0, tag1}, rs, fho1); STAMP(39) }
            if constexpr (k == kFHK - 1) { STAMP(38) }
            if constexpr (k == KPL) poll1(fho, p.ws);
            static_assert(NB1 <= kFHK + 1 && NB1 + 7 * (7 - R2) + KG0 / 4 - kFHK - 1 >= NB && NW2A <= 50 && NW3 <= NW2A && NFH <= NB - NB1, "");
        };
        auto partb = [&]() __attribute__((always_inline)) {
            const float* sp = (const float*)(smem + spa);
            rsr = sp[0]; rsm = sp[8];
            if (layer == 1) { pin(rsr); pin(rsm); }
            if (layer == 1) {
                ACC_TIE5(dsk);
#pragma unroll
                for (int nb = 0; nb < 5; ++nb) { a2[3][nb] = xin[nb] + (xin[nb] * rsr + rsm) * unpk4(dsk[nb]); pin(a2[3][nb]); }
            }
        };
        constexpr int LBW = layer ? 2 : 3;
        if (w == 3) {
            site(); SCHED();
            f32x2 sv[16];
#pragma unroll
            for (int i = 0; i < 16; ++i) sv[i] = *(const f32x2*)(smem + spl + (l_sg(layer, i >> 2) - SC + (i & 3) * 320));
            f32x2 u[4], v[4], x[4];
            cfma4(u, l8, sv[0], sv[4], sv[8], sv[12], sv[1], sv[5], sv[9], sv[13]);
            cfma4(v, l8, u[0], u[1], u[2], u[3], sv[2], sv[6], sv[10], sv[14]);
            cfma4(x, l8, v[0], v[1], v[2], v[3], sv[3], sv[7], sv[11], sv[15]);
            f32x2 P[4][4];
            P[0][0] = f32x2{0.f, 0.f}; P[0][1] = x[0]; P[0][2] = cfma(l32, x[0], x[1]); P[0][3] = cfma(l32, P[0][2], x[2]);
            const f32x2 E = cfma(l32, P[0][3], x[3]);
            st128(v4u{__float_as_uint(E.x), tag0, __float_as_uint(E.y), tag0}, rs, es);
            SCHED();
            if (layer == 0) { FW_OK() static_for<kN3E>(LAMBDA(i_) { w2ld(std::integral_constant<int, NW2P + i_.value>{}); }); }
#pragma unroll
            for (int kk = 1; kk < 4; ++kk) cfma4(P[kk], l8, P[kk - 1][0], P[kk - 1][1], P[kk - 1][2], P[kk - 1][3], sv[kk - 1], sv[kk + 3], sv[kk + 7], sv[kk + 11]);
#pragma unroll
            for (int i = 0; i < 16; ++i) *(f32x2*)(smem + spl + (l_sg(layer, i >> 2) - SC + (i & 3) * 320)) = P[i & 3][i >> 2];
            if (layer == 0) partb();
            if (layer == 1) powers();
        }
        if (w == LBW) {
            site(); SCHED();
            f32x2 wg[17], t4[4];
            unsigned ea[15], tg[15];
            const f32x2 z2 = {0.f, 0.f};
            wg[1] = cmul(l128, l128); wg[2] = cmul(wg[1], wg[1]); wg[3] = cmul(wg[2], wg[1]); wg[4] = cmul(wg[2], wg[2]);
#pragma unroll
            for (int i = 4; i < 16; i += 4) {
                cfma4(t4, wg[i], wg[1], wg[2], wg[3], wg[4], z2, z2, z2, z2);
#pragma unroll
                for (int e = 0; e < 4; ++e) wg[i + 1 + e] = t4[e];
            }
#pragma unroll
            for (int i = 0; i < 15; ++i) {
                const int ch = mt1 - 1 - i;
                if (i) pin(wg[i]);
                ea[i] = ch < 0 ? kNone : (unsigned)ch * 10240u; tg[i] = ch < 0 ? 0u : tag0;
                pins(ea[i]); pins(tg[i]);
            }
            powers();
            f32x2 H0, H1;
            __builtin_amdgcn_s_sleep(layer ? KLB1 : KLB0);
            unsigned spins = dead ? kSpinCap : 0u;
            bool miss;
#pragma nounroll
            do {
                asm volatile("" ::: "memory");
                v4u eg[15];
#pragma unroll
                for (int i = 0; i < 15; ++i) eg[i] = ld128s(rs, eo, ea[i]);
                SCHED();
                unsigned x = __builtin_amdgcn_bitop3_b32(eg[0].y, eg[0].w, tg[0], 0x7E);
                H0 = f32x2{__uint_as_float(eg[0].x), __uint_as_float(eg[0].z)}; H1 = f32x2{0.f, 0.f};
#pragma unroll
                for (int i = 1; i < 15; i += 2) {
                    x |= __builtin_amdgcn_bitop3_b32(eg[i].y, eg[i].w, tg[i], 0x7E) | __builtin_amdgcn_bitop3_b32(eg[i + 1].y, eg[i + 1].w, tg[i + 1], 0x7E);
                    pin(x);
                    gacc2(H0, H1, wg[i], wg[i + 1], eg[i], eg[i + 1]);
                    SCHED();
                }
                miss = __builtin_amdgcn_ballot_w64(x != 0u) != 0ull;
            } while (miss && ++spins <= kSpinCap);
            dead = dead || miss;
            ORDER();
            *(f32x2*)(smem + spl + (L_CW - SC)) = H0 + H1;
            STAMP(layer ? 36 : 37)
            if (layer == 0) {
                static_for<NW3 - NW2P - kN3E>(LAMBDA(i_) { w2ld(std::integral_constant<int, NW2P + kN3E + i_.value>{}); });
            }
        }
        int w2_ = w;
        pins(w2_);
        if (layer == 0) UNDEF8(ca);
        if (layer == 0 && w2_ != LBW) {
            if (cv) {
                site(); SCHED();
                FW_OK()
                st_ur((unsigned)(size_t)(LAS char*)smem + (unsigned)uwa);
                STAMP(32)
                conv_part<0, kCR1>(ca, smem, ab, bb, w2a, [&]() __attribute__((always_inline)) {
                    v4u ad;
                    const unsigned dv = sdq << (adm & 16u);
#pragma unroll
                    for (int i = 0; i < 4; ++i) ad[i] = dv & (unsigned)__builtin_amdgcn_sbfe((int)adm, 8 + i, 1);
                    asm volatile("s_nop 1" : "+v"(ad));
                    return ad;
                }, cA, cB);
                partb();
                powers();
            } else {
                FW_OK()
                static_for<NW2A - NW2P>(LAMBDA(i_) { w2ld(std::integral_constant<int, NW2P + i_.value>{}); __builtin_amdgcn_s_sleep(1); });
                partb();
                powers();
            }
        }
        if (layer == 1 && w < LBW) { site(); SCHED(); powers(); }
        asm volatile("s_waitcnt lgkmcnt(0)" ::: "memory");
        __builtin_amdgcn_s_barrier();
        asm volatile("" ::: "memory");
        STAMP(layer ? 15 : 4)
        {
            float cw[5], ex[5];
#pragma unroll
            for (int nb = 0; nb < 5; ++nb) { cw[nb] = *(const float*)(smem + cwa + nb * 64); ex[nb] = *(const float*)(smem + sga + nb * 64); }
            v2u ta[5], td[5];
            if constexpr (TB) {
                cstepi(ex[0], ex[1], ex[2], ex[3], ex[4], cw[0], cw[1], cw[2], cw[3], cw[4], lqr, lqs);
                f32x2 cs[5];
                PIN5(ex);
#pragma unroll
                for (int nb = 0; nb < 5; ++nb) { cs[nb] = f32x2{ex[nb], dpp<0xB1>(ex[nb])}; pin(cs[nb]); }
                SCHED();
                static_for<5>(LAMBDA(n_) {
                    constexpr int nb = n_.value;
                    f32x2 h[4] = {{bv[0][nb], bv[1][nb]}, {bv[2][nb], bv[3][nb]}, {bv[4][nb], bv[5][nb]}, {bv[6][nb], bv[7][nb]}};
                    padd(h, pr[nb], ps[nb], cs[nb]);
                    *(v4u*)(smem + tbw + nb * 1024) = v4u{cvtpk(h[0].x, h[0].y), cvtpk(h[1].x, h[1].y), cvtpk(h[2].x, h[2].y), cvtpk(h[3].x, h[3].y)};
                    ta[nb] = __builtin_bit_cast(v2u, trread(smem, tbr + nb * 1024)); td[nb] = __builtin_bit_cast(v2u, trread(smem, tbr + nb * 1024 + 512));
                    SCHED();
                    if constexpr (nb >= LAG) { st128(v4u{ta[nb - LAG].x, ta[nb - LAG].y, td[nb - LAG].x, td[nb - LAG].y}, rs, hso + (nb - LAG) * 256); SCHED(); }
                });
            } else
            static_for<2>(LAMBDA(h_) {
                constexpr int n0 = 2 * h_.value;
                float hv[9][3];
                cstep3(hv[0], cw + n0, lqr + n0, lqs + n0, ex + n0);
                if constexpr (n0 == 0) {
                    chain24<0>(hv, bv, lr, ls);
                    chain24<0>(hv + 4, bv + 4, lr, ls);
                } else {
                    chain33<2>(hv, bv, lr, ls);
                    SCHED();
#pragma unroll
                    for (int rd = 0; rd < 2; ++rd) st128(v4u{ta[rd].x, ta[rd].y, td[rd].x, td[rd].y}, rs, hso + rd * 256);
                    SCHED();
                    chain33<2>(hv + 3, bv + 3, lr, ls);
                    chain32<2>(hv + 6, bv + 6, lr, ls);
                }
#pragma unroll
                for (int e = 0; e < (n0 ? 3 : 2); ++e) {
                    v4u o;
                    o.x = cvtpk(hv[1][e], hv[2][e]); o.y = cvtpk(hv[3][e], hv[4][e]);
                    o.z = cvtpk(hv[5][e], hv[6][e]); o.w = cvtpk(hv[7][e], hv[8][e]);
                    *(v4u*)(smem + tbw + (n0 + e) * 1024) = o;
                }
#pragma unroll
                for (int rd = n0; rd < (n0 ? 5 : 2); ++rd) { ta[rd] = __builtin_bit_cast(v2u, trread(smem, tbr + rd * 1024)); td[rd] = __builtin_bit_cast(v2u, trread(smem, tbr + rd * 1024 + 512)); }
                SCHED();
            });
#pragma unroll
            for (int rd = TB ? 5 - LAG : 2; rd < 5; ++rd) st128(v4u{ta[rd].x, ta[rd].y, td[rd].x, td[rd].y}, rs, hso + rd * 256);
            SCHED();
        }
#pragma unroll
        for (int s = 0; s < 10; ++s) pins(ks[s]);
        if constexpr (layer == 1) {
            partb();
            SCHED();
            if constexpr (OTR) {
                const int ln = (srca >> 4) & 63, od = (b0 >> 3) & 1, r0 = srca - 16 * ln;
                otw = (ln & 15) * 336 + (ln >> 4) * 16; otr = (ln & 12) * 336 + ((ln >> 4) + od) * 64 + (ln & 3) * 16; ots = otw + (od ? 0 : 256);
                ACC_TIE5(gt);
#pragma unroll
                for (int nb = 0; nb < 5; ++nb) *(v2u*)(smem + r0 + 10240 + otw / 2 + nb * 32) = gt[nb];
#pragma unroll
                for (int nb = 0; nb < 5; ++nb) gt[nb] = *(const v2u*)(smem + r0 + 10240 + (nb < 4 ? otr / 2 + nb * 168 : ots / 2));
                otw += r0; otr += r0; ots += r0;
                pin(otw); pin(otr); pin(ots);
                ACC_TIE5(gt);
                static_assert(MAP == 0 && (SC & 1023) == 0 && (INBOX & 1023) == 0 && L_CW + 320 <= SC + 2 * INBOX + 10240, "");
                SCHED();
            }
            DRAIN();
            st64(v2u{tag0, tag1}, rs, fho1);
        }
        STAMP(layer ? 16 : 5)
        if (cv) {
            if constexpr (layer == 0) site();
            SCHED();
            conv_part<kCR1, 8>(ca, smem, ab, bb, w2b, []() {}, cA, cB);
        } else if (layer == 0) {
            static_for<NB1>(LAMBDA(i_) { w2x(i_); __builtin_amdgcn_s_sleep(1); });
            vm_wait<NB1>();
            st64(v2u{tag0, tag1}, rs, fho1);
            static_for<NB - NB1 - NFH>(LAMBDA(i_) { w2x(std::integral_constant<int, NB1 + i_.value>{}); __builtin_amdgcn_s_sleep(1); });
            if (w == 3) static_for<NW2A - NW3>(LAMBDA(i_) { w2ld(std::integral_constant<int, NW3 + i_.value>{}); __builtin_amdgcn_s_sleep(1); });
            poll1(fho, p.ws);
            static_for<NFH>(LAMBDA(i_) { w2x(std::integral_constant<int, NB - NFH + i_.value>{}); __builtin_amdgcn_s_sleep(1); });
            static_assert(NFH <= NB - (NX - 50), "");
            UNDEF8(cA[LP]); UNDEF8(cB[LP]);
        }
        if (layer == 0) { unsigned f0_, f1_; asm volatile("s_waitcnt vmcnt(%2)\n\t" AR("%0", 36, 0) AR("%1", 36, 1) : "=v"(f0_), "=v"(f1_) : "i"(NFH) : "memory"); fh = v2u{f0_, f1_}; }
        STAMP(layer ? 29 : 6)
        }
        unsigned g[8][2];

            v2u pt[4];
            if (layer == 0) WAVE_RETRY(ISTAG(fh), fh = ld64(rs, fho))
            else { pipe_flag<KPF0>(pt, fho, p.ws, (u64)tag0 | ((u64)tag1 << 32), dead); ORDER(); }
            STAMP(layer ? 17 : 31)
            site(); SCHED();
            constexpr int RING2 = layer ? KR2 : KRT;
            v4u ar[RING2];
            constexpr int NRT = layer ? RING2 : (36 - KG0) / KRS + 2 < KRT ? (36 - KG0) / KRS + 2 : KRT;
            unsigned xs_[3][4];
            auto gate = LAMBDA(n_) {
                constexpr int n = n_.value;
                if constexpr (n >= 100) swp2n(g[2 * (n - 100)][0], g[2 * (n - 100) + 1][0], g[2 * (n - 100)][1], g[2 * (n - 100) + 1][1]);
                else if constexpr (n >= 0) cvt2(g[n][0], g[n][1], ca[n]);
            };
            if constexpr (layer == 0) {
#pragma unroll
                for (int i = 0; i < 2; ++i) { ar[i] = ld128s(rs, ho[0], ks[i]); SCHED(); }
                static_for<36 - KG0>(LAMBDA(n_) {
                    constexpr int n = n_.value, g = KG0 + n;
                    constexpr int a = g < 8 ? 0 : g < 15 ? 1 : g < 21 ? 2 : g < 26 ? 3 : g < 30 ? 4 : g < 33 ? 5 : g < 35 ? 6 : 7;
                    constexpr int e = g - (a * 8 - a * (a - 1) / 2), i = 2 + n / KRS;
                    mf(ca[a + e], cA[LP][a], cB[LP][e]);
                    SCHED();
                    if constexpr (n % KRS == 0 && i < NRT) ar[i] = ld128s(rs, ho[i / 10], ks[i % 10]);
                    if constexpr (n < 12) rda<40 + n>(xs_[n >> 2][n & 3]);
                    constexpr int gt_[12] = {0, 1, 2, -1, 100, -1, 3, -1, -1, 101, 4, -1};
                    if constexpr (g >= 24) gate(IC(gt_[g < 24 ? 0 : g - 24]));
                    site();
                    SCHED();
                });
                static_assert(KG0 <= 15 && NRT >= 12, "");
            } else {
#pragma unroll
                for (int i = 0; i < RING2; ++i) { ar[i] = ld128s(rs, ho[i / 10], ks[i % 10]); SCHED(); }
            }
        v4u gl[3];
        v2u f0;
        bool gearly = false;
        {
            const char* src = smem + srca;
            const char *glb1 = p.ws + 32 * 4096, *glb2 = p.ws + 48 * 4096;
            float zx[4], zd[4], zt[4], zz[5][4];
            if constexpr (STAMPS == 1) { STAMP(layer ? 50 : 40) }
            static_for<40>(LAMBDA(i_) {
                constexpr int i = i_.value, m = i / 10, s = i % 10;
                static_for<5>(LAMBDA(k_) {
                    constexpr int kk = k_.value, n = 2 * (i - 3) + kk / 2 - 3, nb = n / 8, u = n % 8, pv = i - 1 + RING2;
                    if constexpr (s == 0 && m < 3) mfa0<kk>(a2[m][kk], ar[i % RING2]); else mfa<5 * s + kk>(a2[m][kk], ar[i % RING2]);
                    SCHED();
                    constexpr int gu_[12] = {-1, 5, -1, -1, 102, 6, -1, -1, 7, -1, 103, -1};
                    if constexpr (layer == 0 && 5 * i + kk < 12) gate(IC(gu_[(5 * i + kk) % 12]));
                    if constexpr (layer == 0 && i == 25 && kk == 1) { pins(glb1); pins(glb2); }
                    if constexpr (layer == 0 && i >= 3 && i < 26 && (kk & 1)) {
                        if constexpr (n < 0) swp2n(xs_[n + 3][0], xs_[n + 3][2], xs_[n + 3][1], xs_[n + 3][3]);
                        else if constexpr (nb < 5) {
                            if constexpr (u < 2) unp2(zx[2 * u], zx[2 * u + 1], xs_[nb >> 1][2 * (nb & 1) + u]);
                            else if constexpr (u < 4) unp2(zd[2 * u - 4], zd[2 * u - 3], dsk[nb][u - 2]);
                            else if constexpr (u < 6) fma2(zt[2 * u - 8], zt[2 * u - 7], zx[2 * u - 8], zx[2 * u - 7], rsr, rsr, rsm, rsm);
                            else fma2(zz[nb][2 * u - 12], zz[nb][2 * u - 11], zt[2 * u - 12], zt[2 * u - 11], zd[2 * u - 12], zd[2 * u - 11], zx[2 * u - 12], zx[2 * u - 11]);
                        }
                    }
                    if constexpr (i == 0 && kk == 0 && layer == 1) { pin(pt[0]); pin(pt[1]); pin(pt[2]); pin(pt[3]); }
                    if constexpr (layer == 0 && i < 2 && kk > 0 && 12 + 4 * i + kk - 1 >= NRT && 12 + 4 * i + kk - 1 < RING2) ar[12 + 4 * i + kk - 1] = ld128s(rs, ho[1], ks[2 + 4 * i + kk - 1]);
                    if constexpr (layer == 0 && i == kGSI && kk > 0) st128(v4u{g[2 * kk - 2][0], g[2 * kk - 2][1], g[2 * kk - 1][0], g[2 * kk - 1][1]}, rs, gso + (kk - 1) * 1024);
                    if constexpr (layer == 0 && i > 2 && i <= 7 && kk == (i == kGSI ? 0 : 2)) {
                        { unsigned d0_, d1_; rda<20 + 2 * (i - 3)>(d0_); rda<21 + 2 * (i - 3)>(d1_); dsk[i - 3] = v2u{d0_, d1_}; }
                    }
                    if constexpr (kk == 0 && i > 0 && pv < 40) ar[pv % RING2] = ld128s(rs, ho[pv / 10], ks[pv % 10]);
                    constexpr int s1 = s - 1 - (KMB && layer == 0 && m == 1 ? kFGI - 10 : 0);
                    if constexpr (kk == 2 && m >= 1 && s1 >= 0 && s1 < 5) *(f32x4*)(smem + dst[m - 1] + s1 * 1024) = a2[m - 1][s1];
                    site();
                    if constexpr (i >= 37) a2[i - 37][kk] = *(const f32x4*)(src + (i - 37) * 5120 + kk * 1024);
                    SCHED();
                });
                if constexpr (i == 10 && (layer || !KMB)) { asm volatile("s_waitcnt lgkmcnt(0)" ::: "memory"); __builtin_amdgcn_s_barrier(); asm volatile("" ::: "memory"); }
                if constexpr (i == kFGI && layer == 0) {
                    static_assert(kFGI >= 10 && kFGI <= 14, "");
                    asm volatile("s_waitcnt vmcnt(%0) lgkmcnt(0)" ::"i"(kFGI - kGSI) : "memory");
                    __builtin_amdgcn_s_barrier(); asm volatile("" ::: "memory");
                    st64(v2u{tag0, tag1}, rs, fgo1);
                }
                if constexpr (i == 26 && layer == 0) {
#pragma unroll
                    for (int nb = 0; nb < 5; ++nb) { a2[3][nb] = f32x4{zz[nb][0], zz[nb][1], zz[nb][2], zz[nb][3]}; pin(a2[3][nb]); }
                }
                if constexpr (i == kGPI && layer == 0) gld2s<0>(f0, fgo, p.ws);
                if constexpr (i == kGCI && layer == 0) {
                    wait1(f0);
                    static_for<39 - i>(LAMBDA(r_) { pin(ar[(i + 1 + r_.value) % RING2]); });
                    gearly = __builtin_amdgcn_ballot_w64(!ISTAG(f0)) == 0ull;
                    ORDER();
                    undef3(gl);
                    if (__builtin_expect(gearly, 1)) { gld4t<0>(gl[0], glo, p.ws); gld4t<0>(gl[1], glo, glb1); gld4t<0>(gl[2], glo, glb2); }
                    else if (G2ND) poll2(fgo, fgo, p.ws);
                }
                if constexpr (i == 36) {
                    asm volatile("s_waitcnt lgkmcnt(0)" ::: "memory");
                    __builtin_amdgcn_s_barrier(); asm volatile("" ::: "memory");
                }
                if constexpr (STAMPS == 1 && (i == 0 || i == 9 || i == 10 || i == 15 || i == 25 || i == 35 || i == 36)) { STAMP((layer ? 51 : 41) + (i == 0 ? 0 : i == 9 ? 1 : i == 10 ? 2 : i == 15 ? 3 : i == 25 ? 4 : i == 35 ? 5 : 6)) }
                SCHED();
            });
            STAMP(layer ? 18 : 8)
        }
#pragma unroll
        for (int nb = 0; nb < 5; ++nb) a2[0][nb] = (a2[0][nb] + a2[1][nb]) + a2[2][nb];
        asm volatile("s_nop 1" : "+v"(a2[3][0]), "+v"(a2[3][1]), "+v"(a2[3][2]), "+v"(a2[3][3]), "+v"(a2[3][4]),
                     "+v"(a2[0][0]), "+v"(a2[0][1]), "+v"(a2[0][2]), "+v"(a2[0][3]), "+v"(a2[0][4]));
        float ty;
        {
            f32x4 s1, s2;
#pragma unroll
            for (int nb = 0; nb < 5; ++nb) {
                const f32x4 y = a2[3][nb] + a2[0][nb];
                xin[nb] = y;
                if (nb == 0) { s1 = y; s2 = y * y; } else { s1 += y; s2 += y * y; }
            }
            ty = xsum2h(hsum4(s1), hsum4(s2));
            st64(v2u{__float_as_uint(ty), tag0}, rs, sgs);
        }
        SCHED();
        STAMP(layer ? 19 : 9)
        if constexpr (layer == 1 && OTR) {
#pragma unroll
            for (int nb = 0; nb < 5; ++nb) *(f32x4*)(smem + otw + nb * 64) = xin[nb];
#pragma unroll
            for (int nb = 0; nb < 5; ++nb) xin[nb] = *(const f32x4*)(smem + (nb < 4 ? otr + nb * 336 : ots));
            SCHED();
        }
        int lane_o, b_o = b0, w_o = w0;
        asm volatile("v_mbcnt_lo_u32_b32 %0, -1, 0\n\tv_mbcnt_hi_u32_b32 %0, -1, %0" : "=v"(lane_o));
        asm volatile("" : "+s"(b_o), "+s"(w_o));
        const int lane = lane_o, b = b_o, w = w_o;
        const int c = lane & 15, q = lane >> 4;
        TILES()
        if (layer == 0 && __builtin_expect(!gearly, 0)) {
            if (G2ND) {
                unsigned f0_, f1_;
                asm volatile("s_waitcnt vmcnt(1)\n\t" AR("%0", 36, 0) AR("%1", 36, 1) : "=v"(f0_), "=v"(f1_)::"memory");
                f0 = v2u{f0_, f1_};
            }
            STAMP(63)
            WAVE_RETRY(ISTAG(f0), f0 = ld64(rs, fgo))
            gld4t<0>(gl[0], glo, p.ws); gld4t<0>(gl[1], glo, p.ws + 32 * 4096); gld4t<0>(gl[2], glo, p.ws + 48 * 4096);
            asm volatile("s_waitcnt vmcnt(0)" : "+v"(gl[0]), "+v"(gl[1]), "+v"(gl[2])::"memory");
        }
        if (layer == 0) {
            asm volatile("s_waitcnt vmcnt(1)" : "+v"(gl[0]), "+v"(gl[1]), "+v"(gl[2])::"memory");
            STAMP(60)
            char* tb = smem + SC + w * INBOX;
            *(v4u*)(tb + lane * 16) = gl[0]; *(v4u*)(tb + 1024 + lane * 16) = gl[1]; *(v4u*)(tb + 1536 + lane * 16) = gl[2];
#pragma unroll
            for (int nb = 0; nb < 5; ++nb) gt[nb] = __builtin_bit_cast(v2u, trread(tb, (16 * nb + 4 * q + (c >> 2)) * 32 + (c & 3) * 8));
        } else ACC_TIE5(gt);
        SCHED();
        f32x4 gf[5], yg[5];
#pragma unroll
        for (int nb = 0; nb < 5; ++nb) { gf[nb] = unpk4(gt[nb]); yg[nb] = xin[nb] * gf[nb]; pin(gf[nb]); pin(yg[nb]); }
        unsigned xs = OFF_X2F + (unsigned)((2 * mt2 + (w >> 1)) * 40960 + ((c >> 2) & 1) * 20480 + (10 * nt2 + 2 * (q & 1) + (q >> 1)) * 256 + (8 * (w & 1) + 4 * (c >> 3) + (c & 3)) * 16);
        unsigned pso = q < 2 ? OFF_PS + (unsigned)(((mt2 * 4 + w) * 8 + nt2) * 128 + c * 8 + q * 4) : kNone;
        unsigned fxo1 = lane ? kNone : OFF_FX + (unsigned)(((mt1 * 4 + 2 * (mt2 & 1) + (w >> 1)) * 16 + nt2 * 2 + (w & 1)) * 128);
        unsigned oo = (unsigned)(((64 * mt2 + 16 * w + c) * D_ + 80 * nt2 + 4 * q) * 4);
        unsigned o4 = (unsigned)(((64 * mt2 + 16 * w + (c & 12)) * D_ + 80 * nt2 + 16 * (q + (nt2 & 1)) + 4 * (c & 3)) * 4);
        if (OTR) oo += (nt2 & 1) ? 0u : 256u;
        unsigned xs2 = (q & 1) ? kNone : xs + 2048u;
        if (layer == 0) { pin(xs); pin(xs2); pin(pso); pin(fxo1); } else { pin(oo); if (OTR) pin(o4); }
        SCHED();
        {
            float a, d;
            STAMP(layer ? 62 : 61)
            if constexpr (layer == 0) site();
            pipe_gran<layer ? PDS : PDS0>(a, d, sgo, p.ws, tag0, dead);
            STAMP(layer ? 20 : 10)
            xsum2(a, d);
            const float me = a * (1.0f / D_);
            const float ri = __builtin_amdgcn_rsqf(fmaxf(d * (1.0f / D_) - me * me, 0.f) + 1e-5f), mr = -me * ri;
            if constexpr (layer == 1 && OTR) {
                static_for<5>(LAMBDA(j_) {
                    constexpr int jj = (j_.value + 4) % 5;
                    const float rj = jj < 4 ? dpp<0x55 * (jj & 3)>(ri) : ri, mj = jj < 4 ? dpp<0x55 * (jj & 3)>(mr) : mr;
                    const f32x4 o = yg[jj] * rj + (gf[jj] * mj + xin[jj]);
                    __builtin_amdgcn_raw_buffer_store_b128(__builtin_bit_cast(v4u, o), ro, jj < 4 ? o4 : oo, jj < 4 ? jj * 2560 : 0, OUTP);
                });
            } else if constexpr (layer == 0 && XI) {
#pragma unroll
                for (int i = 0; i < 3; ++i) {
                    v2u pk[2];
#pragma unroll
                    for (int e = 0; e < (i < 2 ? 2 : 1); ++e) {
                        const f32x4 x = yg[2 * i + e] * ri + (gf[2 * i + e] * mr + xin[2 * i + e]);
                        xin[2 * i + e] = x;
                        pk[e] = v2u{cvtpk(x.x, x.y), cvtpk(x.z, x.w)};
                    }
                    const v2u pa = pk[0], pb = pk[i < 2];
                    const auto r0 = __builtin_amdgcn_permlane16_swap(pa.x, pb.x, false, false);
                    const auto r1 = __builtin_amdgcn_permlane16_swap(pa.y, pb.y, false, false);
                    st128(v4u{r0[0], r1[0], r0[1], r1[1]}, rs, i < 2 ? xs + i * 1024 : xs2);
                    SCHED();
                }
            } else {
#pragma unroll
                for (int nb = 0; nb < 5; ++nb) xin[nb] = yg[nb] * ri + (gf[nb] * mr + xin[nb]);
            }
        }
        if constexpr (layer == 0) {
            v2u pk[5];
#pragma unroll
            for (int nb = 0; nb < 5; ++nb) { const f32x4 x = xin[nb]; pk[nb] = v2u{cvtpk(x.x, x.y), cvtpk(x.z, x.w)}; }
#pragma unroll
            for (int i = 0; i < (XI ? 0 : 3); ++i) {
                const v2u pa = pk[2 * i], pb = pk[i < 2 ? 2 * i + 1 : 4];
                const auto r0 = __builtin_amdgcn_permlane16_swap(pa.x, pb.x, false, false);
                const auto r1 = __builtin_amdgcn_permlane16_swap(pa.y, pb.y, false, false);
                if (i < 2 || (q & 1) == 0) st128(v4u{r0[0], r1[0], r0[1], r1[1]}, rs, xs + i * 1024);
            }
            {
                f32x4 s1, s2;
#pragma unroll
                for (int nb = 0; nb < 5; ++nb) { const f32x4 x = xin[nb]; if (nb == 0) { s1 = x; s2 = x * x; } else { s1 += x; s2 += x * x; } }
                __builtin_amdgcn_raw_buffer_store_b32(__float_as_uint(xsum2h(hsum4(s1), hsum4(s2))), rs, pso, 0, SC1);
            }
            SCHED();
            int w1lane0 = (c & 1) * 10240 + (c >> 1) * 128 + ((q ^ ((c & 1) | ((c >> 2) << 1))) * 16);
            int w1lane1 = (c & 1) * 10240 + (c >> 1) * 128 + (((4 + q) ^ ((c & 1) | ((c >> 2) << 1))) * 16);
            unsigned fxo = OFF_FX + (unsigned)((mt1 * 4 + w) * 2048 + (lane >> 2) * 128);
            unsigned xo = OFF_X2F + (unsigned)((4 * mt1 + w) * 40960 + lane * 16), xo1 = xo + 20480u;
            const int row = 8 * q + (c & 7);
            unsigned po = OFF_PS + (unsigned)((((2 * mt1 + (w >> 1)) * 4 + 2 * (w & 1) + (row >> 4)) * 8 + 4 * (c >> 3)) * 128 + (row & 15) * 8);
            int w1hi0 = w1lane0 + 61440, w1hi1 = w1lane1 + 61440;
            int rsw = l_rs(1, 0) + w * INBOX + lane * 4;
            if (GW) pin(rsw);
            pin(w1lane0); pin(w1lane1); pin(w1hi0); pin(w1hi1); pin(fxo); pin(xo); pin(xo1); pin(po);
            v4u wf[2][5];
#pragma unroll
            for (int nb = 0; nb < 5; ++nb) wf[0][nb] = *(const v4u*)(smem + w1lane0 + nb * 20480);
            v2u tg2 = {tag0, tag1};
            pin(tg2);
            SCHED();
            DRAIN();
            st64(tg2, rs, fxo1);
            if (b == 255 && w == 0) FLAG(OFF_NONCE)
            STAMP(11)
            addrs(std::integral_constant<int, 1>{}, lane, w, b);
            v2u pt[4];
            pipe_flag<KPFX>(pt, fxo, p.ws, (u64)tag0 | ((u64)tag1 << 32), dead);
            ORDER();
            STAMP(12)
            v4u ar[RING1][2];
#pragma unroll
            for (int s = 0; s < RING1; ++s)
#pragma unroll
                for (int mb = 0; mb < 2; ++mb) { ar[s][mb] = ld128s(rs, mb ? xo1 : xo, s * 1024); SCHED(); }
            v2u ps[4];
#pragma unroll
            for (int i = 0; i < 4; ++i) ps[i] = ld64(rs, po + i * 128);
            SCHED();
            float t1, t2, rinv, mri, rsel, rr_[8], rm_[8];
            static_for<20>(LAMBDA(s_) {
                constexpr int s = s_.value, cu = s & 1, nx = cu ^ 1;
                LGKM0() SCHED();
                static_for<10>(LAMBDA(g_) {
                    constexpr int g = g_.value, mb = g / 5, k = g % 5;
                    if constexpr (s == 0) mf0(acc[mb][k], ar[0][mb], wf[0][k]); else mf(acc[mb][k], ar[s % RING1][mb], wf[cu][k]);
                    SCHED();
                    if constexpr (s == 0 && g == 0) { pin(pt[0]); pin(pt[1]); pin(pt[2]); pin(pt[3]); }
                    if constexpr (s < 19 && g < 3) wf[nx][g] = *(const v4u*)(smem + (nx ? w1lane1 : w1lane0) + g * 20480 + ((s + 1) >> 1) * 1024);
                    if constexpr (s < 19 && (g == 3 || g == 4)) wf[nx][g] = *(const v4u*)(smem + (nx ? w1hi1 : w1hi0) + (g - 3) * 20480 + ((s + 1) >> 1) * 1024);
                    if constexpr (g == 5 && s + RING1 < 20) ar[s % RING1][0] = ld128s(rs, xo, (s + RING1) * 1024);
                    if constexpr (g == 2 && s > 0 && s - 1 + RING1 < 20) ar[(s - 1) % RING1][1] = ld128s(rs, xo1, (s - 1 + RING1) * 1024);
                    constexpr int z = GW ? s + 3 : s;
                    if constexpr (z == 8 && g == 8) { t1 = __uint_as_float(ps[0].x) + __uint_as_float(ps[1].x); pin(t1); t2 = __uint_as_float(ps[0].y) + __uint_as_float(ps[1].y); pin(t2); }
                    if constexpr (z == 9 && g == 6) { t1 += __uint_as_float(ps[2].x); pin(t1); t2 += __uint_as_float(ps[2].y); pin(t2); }
                    if constexpr (z == 9 && g == 7) { t1 += __uint_as_float(ps[3].x); pin(t1); t2 += __uint_as_float(ps[3].y); pin(t2); }
                    if constexpr (z == 10 && g == 6) { t1 += dpp<0x128>(t1); t2 += dpp<0x128>(t2); pin(t1); pin(t2); }
                    if constexpr (z == 10 && g == 7) { t1 *= (1.0f / D_); pin(t1); t2 *= (1.0f / D_); pin(t2); }
                    if constexpr (z == 10 && g == 8) { t2 = fmaxf(t2 - t1 * t1, 0.f); pin(t2); }
                    if constexpr (z == 11 && g == 6) { rinv = __builtin_amdgcn_rsqf(t2 + 1e-5f); pin(rinv); }
                    if constexpr (z == 11 && g == 8) { mri = -t1 * rinv; pin(mri); }
                    if constexpr (!GW && s == 12 && g == 4) *(float*)(smem + l_rs(1, 0) + w * INBOX + lane * 4) = c < 8 ? rinv : mri;
                    if constexpr (GW && s == 19 && g == 1) selhi(rsel, mri);
                    if constexpr (GW && s == 19 && g == 0) { rsel = rinv; pin(rsel); }
                    if constexpr (GW && s == 19 && g == 2) *(float*)(smem + rsw) = rsel;
                    if constexpr (z >= 12 && z < 15 && g >= 6 && g < 9 && 3 * (z - 12) + g - 6 < 8) bc2b<3 * (z - 12) + g - 6>(rr_[3 * (z - 12) + g - 6], rm_[3 * (z - 12) + g - 6], rinv, mri);
                    if constexpr (!GW && s >= 15 && s < 19 && g >= 5) TT_GAP(5 * (s - 15) + g - 5)
                    if constexpr (GW && s >= 12 && s < 19 && g >= 6 && g < 9 && 3 * (s - 12) + g - 6 < 20) TT_GAP(3 * (s - 12) + g - 6)
                    site();
                    SCHED();
                });
            });
            rr[0] = f32x4{rr_[0], rr_[1], rr_[2], rr_[3]}; rr[1] = f32x4{rr_[4], rr_[5], rr_[6], rr_[7]};
            asm volatile("kpf_end2:");
            ACC_FENCE10(acc[0], acc[1]);
            STAMP(13)
        } else {
#pragma unroll
            for (int nb = 0; nb < (OTR ? 0 : 5); ++nb) __builtin_amdgcn_raw_buffer_store_b128(__builtin_bit_cast(v4u, xin[nb]), ro, oo + 64 * nb, 0, OUTP);
            STAMP(21)
            STAMP_DUMP()
            asm volatile("kpf_end:");
        }
    };
    layer_body(std::integral_constant<int, 0>{});
    layer_body(std::integral_constant<int, 1>{});
}

extern "C" void kernel_launch(void* const* d_in, const int* in_sizes, int n_in,
                              void* d_out, int out_size, void* d_ws, size_t ws_size,
                              hipStream_t stream) {
    Params p;
    p.x0 = (const u16*)d_in[0];
    p.Lre = (const u16*)d_in[1];
    p.Lim = (const u16*)d_in[2];
    p.Bre = (const u16*)d_in[3];
    p.Bim = (const u16*)d_in[4];
    p.Cre = (const u16*)d_in[5];
    p.Cim = (const u16*)d_in[6];
    p.logdt = (const u16*)d_in[7];
    p.Dskip = (const u16*)d_in[8];
    p.k = (const u16*)d_in[10];
    p.v = (const u16*)d_in[11];
    p.shk = (const u16*)d_in[12];
    p.ssmk = (const u16*)d_in[13];
    p.ssmD = (const u16*)d_in[14];
    p.ws = (char*)d_ws;
    p.out = (float*)d_out;
    k_main<<<256, 256, 0, stream>>>(p);
}
```

```cpp
#include <hip/hip_runtime.h>
#include <stdint.h>
#include <utility>

#define STAMPS 0

typedef unsigned short u16;
typedef unsigned long long u64;
typedef unsigned v4u __attribute__((ext_vector_type(4)));
typedef unsigned v2u __attribute__((ext_vector_type(2)));
typedef float f32x4 __attribute__((ext_vector_type(4)));
typedef float f32x2 __attribute__((ext_vector_type(2)));
typedef short s16x4 __attribute__((ext_vector_type(4)));
typedef __bf16 bf16x2v __attribute__((ext_vector_type(2)));
#define LAS __attribute__((address_space(3)))
#define DI __device__ __forceinline__

constexpr int S_ = 2048, D_ = 640;
#define KG0 12
#define KRT 12
#define KRS 2
#define KB1 4
#define KMB 1
#define SBT 2
#define LAG 1
#define OTR 1
#define XI 1
#define GW 0
#define PT 1
#define KR1 5
#define KR2 10
constexpr int RING1 = KR1;
#define KCR1 3
constexpr int kCR1 = KCR1;
#define KXA 10
#define KN3E 17
#define KN3 6
constexpr int kXA = KXA;
constexpr int kN3E = KN3E, kN3 = KN3;
#define KFHK 4
#define KFHP 12
constexpr int kFHK = KFHK, kFHP = KFHP;
#define KGPI 27
#define KGCI 33
#define KFGI 11
#define G2ND 1
#define KGSI 2
constexpr int kGSI = KGSI, kFGI = KFGI, kGPI = KGPI, kGCI = KGCI;

constexpr unsigned OFF_NONCE = 0;
constexpr unsigned OFF_ST = 4096;
constexpr unsigned OFF_FW = OFF_ST + 65536;
constexpr unsigned OFF_FK = OFF_FW + 131072;
constexpr unsigned OFF_FH = OFF_FK + 81920;
constexpr unsigned OFF_FX = OFF_FH + 262144;
constexpr unsigned OFF_FG = OFF_FX + 131072;
constexpr unsigned OFF_EG = OFF_FG + 98304;
constexpr unsigned OFF_SG = OFF_EG + 327680;
constexpr unsigned OFF_PS = OFF_SG + 524288;
constexpr unsigned OFF_KVT = OFF_PS + 131072;
constexpr unsigned OFF_GT = OFF_KVT + 2621440;
constexpr unsigned OFF_W2F = OFF_GT + 2621440;
constexpr unsigned OFF_X2F = OFF_W2F + 1638400;
constexpr unsigned OFF_DUM = OFF_X2F + 2621440;
constexpr unsigned OFF_HF = OFF_DUM + 163840;
constexpr unsigned WS_BYTES = OFF_HF + 2 * 5242880;
constexpr unsigned kNone = 0x40000000u;
constexpr unsigned OFF_DBG = 0x1800000;
#define MAP 0
#define TILES() const int xq = b & 7, j = b >> 3, mt2 = MAP ? 8 * (xq >> 1) + (j >> 2) : 4 * xq + (j >> 3), nt2 = MAP ? 4 * (xq & 1) + (j & 3) : j & 7, mt1 = mt2 >> 1, \
    nt1 = MAP ? 8 * (xq & 1) + (j & 7) : j & 15, i32 = 16 * (mt2 & 1) + mt1;

constexpr int SC = 102400;
constexpr int REGSZ = 13056;
constexpr int L_XR = SC;
constexpr int L_CO = SC + 12288;
constexpr int L_KB = SC + 51712;
constexpr int L_TAB = SC + 55808;
constexpr int L_CW = SC + 39168;
constexpr int INBOX = 15360;
constexpr int l_rs(int l, int w) { return l ? SC + w * INBOX : L_KB + w * 256; }
constexpr int l_sg(int l, int w) { return l ? SC + w * INBOX + 256 : SC + (w == 0 ? 43264 : w == 1 ? 46080 : w == 2 ? 52736 : 56320); }
constexpr int l_tb(int l, int w) { return l ? SC + w * INBOX + 2048 : SC + 40960 + w * 5120; }
constexpr int LDS_BYTES = 163840;
constexpr int CV_E = 4608;
constexpr int CV_O = CV_E + (1056 + 15) * 4;

struct Params {
    const u16 *x0, *Lre, *Lim, *Bre, *Bim, *Cre, *Cim, *logdt, *Dskip, *k, *v, *shk, *ssmk, *ssmD;
    char* ws;
    float* out;
};

#define MF "v_mfma_f32_16x16x32_bf16 "
DI void mf(f32x4& c, v4u a, v4u b) { asm(MF "%0, %1, %2, %0" : "+v"(c) : "v"(a), "v"(b)); }
DI void mf0(f32x4& c, v4u a, v4u b) { asm(MF "%0, %1, %2, 0" : "=&v"(c) : "v"(a), "v"(b)); }
constexpr int W2R = 56;
#define WA(n) "a[%c" #n ":%c" #n "+3]"
template <int R> DI void mfa(f32x4& c, v4u a) { asm(MF "%0, " WA(2) ", %1, %0" : "+v"(c) : "v"(a), "i"(W2R + 4 * R)); }
template <int R> DI void mfa0(f32x4& c, v4u a) { asm(MF "%0, " WA(2) ", %1, 0" : "=&v"(c) : "v"(a), "i"(W2R + 4 * R)); }
#define ACC_FENCE10(c, d) asm volatile("" : "+v"(c[0]), "+v"(c[1]), "+v"(c[2]), "+v"(c[3]), "+v"(c[4]), "+v"(d[0]), "+v"(d[1]), "+v"(d[2]), "+v"(d[3]), "+v"(d[4]))
#define ACC_TIE5(c) asm volatile("" : "+v"(c[0]), "+v"(c[1]), "+v"(c[2]), "+v"(c[3]), "+v"(c[4]))
#define QP " quad_perm:[1,0,3,2] row_mask:0xf bank_mask:0xf"
DI void cstep(float (&t)[5], const float (&h)[5], const float (&lr)[5], const float (&ls)[5], float b0, float b1, float b2, float b3, float b4) {
    asm("v_fma_f32 %0, %10, %5, %20\n\tv_fma_f32 %1, %11, %6, %21\n\tv_fma_f32 %2, %12, %7, %22\n\tv_fma_f32 %3, %13, %8, %23\n\tv_fma_f32 %4, %14, %9, %24\n\t"
        "v_fmac_f32_dpp %0, %5, %15" QP "\n\tv_fmac_f32_dpp %1, %6, %16" QP "\n\tv_fmac_f32_dpp %2, %7, %17" QP "\n\tv_fmac_f32_dpp %3, %8, %18" QP "\n\tv_fmac_f32_dpp %4, %9, %19" QP
        : "=&v"(t[0]), "=&v"(t[1]), "=&v"(t[2]), "=&v"(t[3]), "=&v"(t[4])
        : "v"(h[0]), "v"(h[1]), "v"(h[2]), "v"(h[3]), "v"(h[4]), "v"(lr[0]), "v"(lr[1]), "v"(lr[2]), "v"(lr[3]), "v"(lr[4]),
          "v"(ls[0]), "v"(ls[1]), "v"(ls[2]), "v"(ls[3]), "v"(ls[4]), "v"(b0), "v"(b1), "v"(b2), "v"(b3), "v"(b4));
}
DI void cstep3(float* t, const float* h, const float* lr, const float* ls, const float* b) {
    asm("v_fma_f32 %0, %6, %3, %12\n\tv_fma_f32 %1, %7, %4, %13\n\tv_fma_f32 %2, %8, %5, %14\n\t"
        "v_fmac_f32_dpp %0, %3, %9" QP "\n\tv_fmac_f32_dpp %1, %4, %10" QP "\n\tv_fmac_f32_dpp %2, %5, %11" QP
        : "=&v"(t[0]), "=&v"(t[1]), "=&v"(t[2])
        : "v"(h[0]), "v"(h[1]), "v"(h[2]), "v"(lr[0]), "v"(lr[1]), "v"(lr[2]), "v"(ls[0]), "v"(ls[1]), "v"(ls[2]), "v"(b[0]), "v"(b[1]), "v"(b[2]));
}
template <int o> DI void chain24(float (*t)[3], const float (*b)[5], const float* lr, const float* ls) {
    asm("v_fma_f32 %0, %18, %16, %8\n\t" "v_fma_f32 %1, %19, %17, %9\n\t" "v_fmac_f32_dpp %0, %16, %20" QP "\n\t" "v_fmac_f32_dpp %1, %17, %21" QP "\n\t"
        "v_fma_f32 %2, %18, %0, %10\n\t" "v_fma_f32 %3, %19, %1, %11\n\t" "v_fmac_f32_dpp %2, %0, %20" QP "\n\t" "v_fmac_f32_dpp %3, %1, %21" QP "\n\t"
        "v_fma_f32 %4, %18, %2, %12\n\t" "v_fma_f32 %5, %19, %3, %13\n\t" "v_fmac_f32_dpp %4, %2, %20" QP "\n\t" "v_fmac_f32_dpp %5, %3, %21" QP "\n\t"
        "v_fma_f32 %6, %18, %4, %14\n\t" "v_fma_f32 %7, %19, %5, %15\n\t" "v_fmac_f32_dpp %6, %4, %20" QP "\n\t" "v_fmac_f32_dpp %7, %5, %21" QP "\n\t"
        : "=&v"(t[1][0]), "=&v"(t[1][1]), "=&v"(t[2][0]), "=&v"(t[2][1]), "=&v"(t[3][0]), "=&v"(t[3][1]), "=&v"(t[4][0]), "=&v"(t[4][1])
        : "v"(b[0][o + 0]), "v"(b[0][o + 1]), "v"(b[1][o + 0]), "v"(b[1][o + 1]), "v"(b[2][o + 0]), "v"(b[2][o + 1]), "v"(b[3][o + 0]), "v"(b[3][o + 1]), "v"(t[0][0]), "v"(t[0][1]), "v"(lr[o + 0]), "v"(lr[o + 1]), "v"(ls[o + 0]), "v"(ls[o + 1]));
}
template <int o> DI void chain33(float (*t)[3], const float (*b)[5], const float* lr, const float* ls) {
    asm("v_fma_f32 %0, %21, %18, %9\n\t" "v_fma_f32 %1, %22, %19, %10\n\t" "v_fma_f32 %2, %23, %20, %11\n\t" "v_fmac_f32_dpp %0, %18, %24" QP "\n\t" "v_fmac_f32_dpp %1, %19, %25" QP "\n\t" "v_fmac_f32_dpp %2, %20, %26" QP "\n\t"
        "v_fma_f32 %3, %21, %0, %12\n\t" "v_fma_f32 %4, %22, %1, %13\n\t" "v_fma_f32 %5, %23, %2, %14\n\t" "v_fmac_f32_dpp %3, %0, %24" QP "\n\t" "v_fmac_f32_dpp %4, %1, %25" QP "\n\t" "v_fmac_f32_dpp %5, %2, %26" QP "\n\t"
        "v_fma_f32 %6, %21, %3, %15\n\t" "v_fma_f32 %7, %22, %4, %16\n\t" "v_fma_f32 %8, %23, %5, %17\n\t" "v_fmac_f32_dpp %6, %3, %24" QP "\n\t" "v_fmac_f32_dpp %7, %4, %25" QP "\n\t" "v_fmac_f32_dpp %8, %5, %26" QP "\n\t"
        : "=&v"(t[1][0]), "=&v"(t[1][1]), "=&v"(t[1][2]), "=&v"(t[2][0]), "=&v"(t[2][1]), "=&v"(t[2][2]), "=&v"(t[3][0]), "=&v"(t[3][1]), "=&v"(t[3][2])
        : "v"(b[0][o + 0]), "v"(b[0][o + 1]), "v"(b[0][o + 2]), "v"(b[1][o + 0]), "v"(b[1][o + 1]), "v"(b[1][o + 2]), "v"(b[2][o + 0]), "v"(b[2][o + 1]), "v"(b[2][o + 2]), "v"(t[0][0]), "v"(t[0][1]), "v"(t[0][2]), "v"(lr[o + 0]), "v"(lr[o + 1]), "v"(lr[o + 2]), "v"(ls[o + 0]), "v"(ls[o + 1]), "v"(ls[o + 2]));
}
template <int o> DI void chain32(float (*t)[3], const float (*b)[5], const float* lr, const float* ls) {
    asm("v_fma_f32 %0, %15, %12, %6\n\t" "v_fma_f32 %1, %16, %13, %7\n\t" "v_fma_f32 %2, %17, %14, %8\n\t" "v_fmac_f32_dpp %0, %12, %18" QP "\n\t" "v_fmac_f32_dpp %1, %13, %19" QP "\n\t" "v_fmac_f32_dpp %2, %14, %20" QP "\n\t"
        "v_fma_f32 %3, %15, %0, %9\n\t" "v_fma_f32 %4, %16, %1, %10\n\t" "v_fma_f32 %5, %17, %2, %11\n\t" "v_fmac_f32_dpp %3, %0, %18" QP "\n\t" "v_fmac_f32_dpp %4, %1, %19" QP "\n\t" "v_fmac_f32_dpp %5, %2, %20" QP "\n\t"
        : "=&v"(t[1][0]), "=&v"(t[1][1]), "=&v"(t[1][2]), "=&v"(t[2][0]), "=&v"(t[2][1]), "=&v"(t[2][2])
        : "v"(b[0][o + 0]), "v"(b[0][o + 1]), "v"(b[0][o + 2]), "v"(b[1][o + 0]), "v"(b[1][o + 1]), "v"(b[1][o + 2]), "v"(t[0][0]), "v"(t[0][1]), "v"(t[0][2]), "v"(lr[o + 0]), "v"(lr[o + 1]), "v"(lr[o + 2]), "v"(ls[o + 0]), "v"(ls[o + 1]), "v"(ls[o + 2]));
}
DI void pstep(f32x2& xr, f32x2& xs, f32x2 pr, f32x2 ps, f32x2 m) {
    asm("v_pk_mul_f32 %0, %2, %4 op_sel_hi:[1,0]\n\tv_pk_mul_f32 %1, %3, %4 op_sel_hi:[1,0]\n\t"
        "v_pk_fma_f32 %0, %3, %4, %0 op_sel:[0,1,0] neg_lo:[1,0,0] neg_hi:[1,0,0]\n\tv_pk_fma_f32 %1, %2, %4, %1 op_sel:[0,1,0]"
        : "=&v"(xr), "=&v"(xs) : "v"(pr), "v"(ps), "v"(m));
}
DI void padd(f32x2 (&h)[4], const f32x2 (&pr)[2], const f32x2 (&ps)[2], f32x2 c) {
    f32x2 d;
    asm("v_pk_mul_f32 %4, %6, %9 op_sel:[1,0]\n\tv_pk_fma_f32 %0, %5, %9, %0 op_sel_hi:[1,0,1]\n\tv_pk_fma_f32 %1, %6, %9, %1 op_sel_hi:[1,0,1]\n\t"
        "v_pk_fma_f32 %4, %8, %9, %4 op_sel:[1,1,0] op_sel_hi:[1,0,1] neg_hi:[1,0,0]\n\tv_pk_fma_f32 %0, %7, %9, %0 op_sel:[0,1,0]\n\tv_pk_fma_f32 %1, %8, %9, %1 op_sel:[0,1,0]\n\t"
        "v_pk_fma_f32 %2, %5, %4, %2 op_sel_hi:[1,0,1]\n\tv_pk_fma_f32 %3, %6, %4, %3 op_sel_hi:[1,0,1]\n\tv_pk_fma_f32 %2, %7, %4, %2 op_sel:[0,1,0]\n\tv_pk_fma_f32 %3, %8, %4, %3 op_sel:[0,1,0]"
        : "+v"(h[0]), "+v"(h[1]), "+v"(h[2]), "+v"(h[3]), "=&v"(d) : "v"(pr[0]), "v"(pr[1]), "v"(ps[0]), "v"(ps[1]), "v"(c));
}
DI void cstepi(float& b0, float& b1, float& b2, float& b3, float& b4, float h0, float h1, float h2, float h3, float h4, const float (&lr)[5], const float (&ls)[5]) {
    asm("v_fmac_f32 %0, %10, %5\n\tv_fmac_f32 %1, %11, %6\n\tv_fmac_f32 %2, %12, %7\n\tv_fmac_f32 %3, %13, %8\n\tv_fmac_f32 %4, %14, %9\n\t"
        "v_fmac_f32_dpp %0, %5, %15" QP "\n\tv_fmac_f32_dpp %1, %6, %16" QP "\n\tv_fmac_f32_dpp %2, %7, %17" QP "\n\tv_fmac_f32_dpp %3, %8, %18" QP "\n\tv_fmac_f32_dpp %4, %9, %19" QP
        : "+v"(b0), "+v"(b1), "+v"(b2), "+v"(b3), "+v"(b4)
        : "v"(h0), "v"(h1), "v"(h2), "v"(h3), "v"(h4), "v"(lr[0]), "v"(lr[1]), "v"(lr[2]), "v"(lr[3]), "v"(lr[4]), "v"(ls[0]), "v"(ls[1]), "v"(ls[2]), "v"(ls[3]), "v"(ls[4]));
}
#define UNDEF8(c) asm volatile("" : "=v"(c[0]), "=v"(c[1]), "=v"(c[2]), "=v"(c[3]), "=v"(c[4]), "=v"(c[5]), "=v"(c[6]), "=v"(c[7]))
#define PIN5(t) asm volatile("s_nop 1" : "+v"(t[0]), "+v"(t[1]), "+v"(t[2]), "+v"(t[3]), "+v"(t[4]))
DI unsigned cvtpk(float lo, float hi) {
    f32x2 v = {lo, hi};
    return __builtin_bit_cast(unsigned, __builtin_convertvector(v, bf16x2v));
}
DI float lo16f(unsigned u) { return __uint_as_float(u << 16); }
DI float hi16f(unsigned u) { return __uint_as_float(u & 0xffff0000u); }
DI float rbf(float v) { return lo16f(cvtpk(v, 0.f)); }
template <int CTRL> DI float dpp(float x) {
    return __int_as_float(__builtin_amdgcn_update_dpp(0, __float_as_int(x), CTRL, 0xF, 0xF, true));
}
DI float rowsum16(float x) {
    x += dpp<0x128>(x); x += dpp<0x124>(x); x += dpp<0x122>(x); x += dpp<0x121>(x);
    return x;
}
DI float xsum32(float x) { auto r = __builtin_amdgcn_permlane32_swap(__float_as_uint(x), __float_as_uint(x), false, false); return __uint_as_float(r[0]) + __uint_as_float(r[1]); }
DI void xsum2(float& a, float& d) {
    auto r = __builtin_amdgcn_permlane16_swap(__float_as_uint(a), __float_as_uint(d), false, false);
    const float s = __uint_as_float(r[0]) + __uint_as_float(r[1]);
    const float t = xsum32(s);
    auto u = __builtin_amdgcn_permlane16_swap(__float_as_uint(t), __float_as_uint(t), false, false);
    a = __uint_as_float(u[0]); d = __uint_as_float(u[1]);
}
DI float xsum2h(float a, float d) {
    auto r = __builtin_amdgcn_permlane16_swap(__float_as_uint(a), __float_as_uint(d), false, false);
    return xsum32(__uint_as_float(r[0]) + __uint_as_float(r[1]));
}
DI float hsum4(f32x4 v) { return (v.x + v.y) + (v.z + v.w); }
typedef __amdgpu_buffer_rsrc_t rsrc_t;
constexpr int SC1 = 16;
DI v4u ld128(rsrc_t rs, unsigned off) { return __builtin_amdgcn_raw_buffer_load_b128(rs, off, 0, SC1); }
DI v4u ld128s(rsrc_t rs, unsigned off, unsigned soff) { return __builtin_amdgcn_raw_buffer_load_b128(rs, off, soff, SC1); }
DI v2u ld64(rsrc_t rs, unsigned off) { return __builtin_amdgcn_raw_buffer_load_b64(rs, off, 0, SC1); }
DI void st128(v4u v, rsrc_t rs, unsigned off) { __builtin_amdgcn_raw_buffer_store_b128(v, rs, off, 0, SC1); }
DI void st64(v2u v, rsrc_t rs, unsigned off) { __builtin_amdgcn_raw_buffer_store_b64(v, rs, off, 0, SC1); }
#define LGKM0() { __builtin_amdgcn_s_waitcnt(0xC07F); asm volatile("" ::: "memory"); }
#define DRAIN() asm volatile("s_waitcnt vmcnt(0)" ::: "memory")
#define ORDER() __builtin_amdgcn_fence(__ATOMIC_ACQUIRE, "wavefront")
#define SCHED() __builtin_amdgcn_sched_barrier(0)
constexpr unsigned kSpinCap = 1u << 17;
#define WAVE_WAIT(...)                                                      \
    {                                                                       \
        unsigned spins_ = dead ? kSpinCap : 0u;                             \
        _Pragma("nounroll") for (;;) {                                      \
            asm volatile("" ::: "memory");                                  \
            bool ok;                                                        \
            __VA_ARGS__;                                                    \
            if (__builtin_amdgcn_ballot_w64(!ok) == 0ull) break;            \
            if (++spins_ > kSpinCap) { dead = true; break; }                \
            __builtin_amdgcn_s_sleep(1);                                    \
        }                                                                   \
        ORDER();                                                            \
    }
#define KPD1 2
#define KPDS 2
#define KPDS0 0
#define KPF0 14
#define KPFX 6
#define KPF1 4
constexpr int PD1 = KPD1, PDS = KPDS, PDS0 = KPDS0;
#define PL(r) "global_load_dwordx2 " r ", %5, %6 sc1\n\t"
#define PS_(r) "s_waitcnt vmcnt(1)\n\tv_cmp_ne_u64 vcc, %7, " r "\n\ts_cbranch_vccz 2f\n\t" PL(r)
template <int D0> DI void pipe_flag(v2u (&t)[4], unsigned off, const void* base, u64 tag, bool& dead) {
    unsigned n = dead ? 0u : kSpinCap;
    asm volatile("s_sleep %8\n\t" PL("%0") "s_sleep %9\n\t" PL("%1")
                 "1:\n\t" PS_("%0") PS_("%1") "s_sub_u32 %4, %4, 1\n\ts_cbranch_scc0 1b\n2:"
                 : "=&v"(t[0]), "=&v"(t[1]), "=&v"(t[2]), "=&v"(t[3]), "+s"(n) : "v"(off), "s"(base), "s"(tag), "i"(D0), "i"(KPF1) : "memory", "vcc", "scc");
    dead = __builtin_amdgcn_readfirstlane(n) == ~0u;
}
#define WAVE_RETRY(OK, ...) { if (__builtin_expect(__builtin_amdgcn_ballot_w64(!(OK)) != 0ull, 0)) WAVE_WAIT({ __VA_ARGS__; ok = OK; }) ORDER(); }
#define STAMP(i)
#define STAMP_DUMP()

#define KLB0 0
#define KLB1 12
DI void site() { asm volatile(""); }
template <class F, int... I> DI void static_for_impl(std::integer_sequence<int, I...>, F&& f) { (f(std::integral_constant<int, I>{}), ...); }
template <int N, class F> DI void static_for(F&& f) { static_for_impl(std::make_integer_sequence<int, N>{}, f); }
#define IC(n) std::integral_constant<int, (n)>{}
#define LAMBDA(v) [&](auto v) __attribute__((always_inline))
#define PK2 " op_sel:[1,1,0] op_sel_hi:[1,0,1] neg_lo:[1,0,0]"
DI f32x2 cmul(f32x2 a, f32x2 b) {
    f32x2 t;
    asm("v_pk_mul_f32 %0, %1, %2 op_sel_hi:[0,1]\n\ts_nop 0\n\tv_pk_fma_f32 %0, %1, %2, %0" PK2 "\n\ts_nop 0" : "=&v"(t) : "v"(a), "v"(b));
    return t;
}
DI f32x2 cfma(f32x2 a, f32x2 b, f32x2 c) {
    f32x2 t;
    asm("v_pk_fma_f32 %0, %1, %2, %3 op_sel_hi:[0,1,1]\n\ts_nop 0\n\tv_pk_fma_f32 %0, %1, %2, %0" PK2 "\n\ts_nop 0" : "=&v"(t) : "v"(a), "v"(b), "v"(c));
    return t;
}
DI void cfma4(f32x2 (&t)[4], f32x2 a, f32x2 b0, f32x2 b1, f32x2 b2, f32x2 b3, f32x2 c0, f32x2 c1, f32x2 c2, f32x2 c3) {
    asm("v_pk_fma_f32 %0, %4, %5, %9 op_sel_hi:[0,1,1]\n\tv_pk_fma_f32 %1, %4, %6, %10 op_sel_hi:[0,1,1]\n\tv_pk_fma_f32 %2, %4, %7, %11 op_sel_hi:[0,1,1]\n\tv_pk_fma_f32 %3, %4, %8, %12 op_sel_hi:[0,1,1]\n\t"
        "v_pk_fma_f32 %0, %4, %5, %0" PK2 "\n\tv_pk_fma_f32 %1, %4, %6, %1" PK2 "\n\tv_pk_fma_f32 %2, %4, %7, %2" PK2 "\n\tv_pk_fma_f32 %3, %4, %8, %3" PK2 "\n\ts_nop 0"
        : "=&v"(t[0]), "=&v"(t[1]), "=&v"(t[2]), "=&v"(t[3]) : "v"(a), "v"(b0), "v"(b1), "v"(b2), "v"(b3), "v"(c0), "v"(c1), "v"(c2), "v"(c3));
}
DI void gacc2(f32x2& h0, f32x2& h1, f32x2 w0, f32x2 w1, v4u e0, v4u e1) {
    const v2u a0 = {e0.x, e0.y}, b0 = {e0.z, e0.w}, a1 = {e1.x, e1.y}, b1 = {e1.z, e1.w};
    asm("v_pk_fma_f32 %0, %4, %2, %0 op_sel_hi:[0,1,1]\n\tv_pk_fma_f32 %1, %6, %3, %1 op_sel_hi:[0,1,1]\n\t"
        "v_pk_fma_f32 %0, %5, %2, %0 op_sel:[0,1,0] op_sel_hi:[0,0,1] neg_lo:[1,0,0]\n\tv_pk_fma_f32 %1, %7, %3, %1 op_sel:[0,1,0] op_sel_hi:[0,0,1] neg_lo:[1,0,0]"
        : "+v"(h0), "+v"(h1) : "v"(w0), "v"(w1), "v"(a0), "v"(b0), "v"(a1), "v"(b1));
}
DI s16x4 trread(char* smem, int byteoff) {
    return __builtin_amdgcn_ds_read_tr16_b64_v4i16((LAS s16x4*)(smem + byteoff));
}

#define CV_LOAD_A(A, a, rr_, hf) { const unsigned* ap_ = (const unsigned*)(smem + ab[(a) >> 1] + (((a) & 1) ? 0 : 512) + 64 * (7 - (rr_)) + 8 * (hf)); A[a][2 * (hf)] = ap_[0]; A[a][2 * (hf) + 1] = ap_[1]; }
#define CV_LOAD_B(B, e, rr_) { B[e] = *(const v4u*)(smem + bb + 64 * (7 - (rr_)) + 512 * (e)); }
constexpr int cv_pair(int lo, int hi, int n) {
    for (int a = 0; a < 8; ++a)
        for (int e = 0; e < 8; ++e)
            if (a + e >= lo && a + e < hi && n-- == 0) return 8 * a + e;
    return -1;
}
template <int R, int PF, bool OWN, class F>
DI void conv_round(f32x4 (&ca)[8], v4u (&A)[8], const v4u (&B)[8], v4u (&An)[8], v4u (&Bn)[8], const char* smem, const int (&ab)[4], int bb, F&& fill) {
    static_for<(R == 7 ? KG0 : 36)>(LAMBDA(g_) {
        constexpr int g = g_.value, a = cv_pair(0, 8, g) / 8, e = cv_pair(0, 8, g) % 8;
        mf(ca[a + e], A[a], B[e]);
        SCHED();
        if constexpr (OWN && g < 12) { CV_LOAD_A(A, 2 + g / 2, R, g & 1) SCHED(); }
        if constexpr (g < 4 * PF && (g & 3) != 3) { if constexpr ((g & 3) == 2) CV_LOAD_B(Bn, g >> 2, R + 1) else CV_LOAD_A(An, g >> 2, R + 1, g & 1) SCHED(); }
        site();
        if constexpr (R == 7) SCHED();
        if constexpr ((g & 3) == 3 && g < 28) { fill(std::integral_constant<int, 7 * R + g / 4>{}); SCHED(); }
    });
}
template <int R0, int R1, class F, class M>
DI void conv_part(f32x4 (&ca)[8], const char* smem, const int (&ab)[4], int bb, F&& fill, M&& mid, v4u (&A)[2][8], v4u (&B)[2][8]) {
    if constexpr (R0 > 0) { CV_LOAD_A(A[0], 0, R0, 0) CV_LOAD_A(A[0], 0, R0, 1) SCHED(); }
    static_for<8>(LAMBDA(a_) { constexpr int a = a_.value; CV_LOAD_B(B[0], a, R0) SCHED(); });
    if constexpr (R0 > 0) { CV_LOAD_A(A[0], 1, R0, 0) CV_LOAD_A(A[0], 1, R0, 1) SCHED(); }
    else {
        CV_LOAD_A(A[0], 0, 0, 0) CV_LOAD_A(A[0], 0, 0, 1) SCHED();
        const v4u ad = mid();
        SCHED();
        static_for<8>(LAMBDA(e_) {
            constexpr int e = e_.value;
            mf0(ca[e], ad, B[0][e]); SCHED();
            if constexpr (e < 7) { CV_LOAD_A(A[0], e + 1, 0, 0) CV_LOAD_A(A[0], e + 1, 0, 1) SCHED(); }
        });
    }
    static_for<R1 - R0>(LAMBDA(r_) {
        constexpr int r = r_.value;
        conv_round<R0 + r, (R0 + r + 1 < R1 ? 8 : 0), (r == 0 && R0 > 0)>(ca, A[r & 1], B[r & 1], A[(r & 1) ^ 1], B[(r & 1) ^ 1], smem, ab, bb, fill);
    });
}
template <int IMM> DI void gld(v4u& dst, unsigned voff, const void* sbase) {
    asm volatile("global_load_dwordx4 %0, %1, %2 offset:%3" : "=v"(dst) : "v"(voff), "s"(sbase), "i"(IMM));
}
#define OUTP 16
#define NTM 31
template <int IMM, int BIT> DI void gldn(v4u& dst, unsigned voff, const void* sbase) {
    if constexpr ((NTM >> BIT) & 1) asm volatile("global_load_dwordx4 %0, %1, %2 offset:%3 nt" : "=v"(dst) : "v"(voff), "s"(sbase), "i"(IMM));
    else gld<IMM>(dst, voff, sbase);
}
DI void gldu16(unsigned& dst, unsigned voff, const void* sbase) { asm volatile("global_load_ushort %0, %1, %2" : "=v"(dst) : "v"(voff), "s"(sbase)); }
template <int IMM> DI void gld64(v4u& dst, const void* a) {
    if constexpr ((NTM >> 5) & 1) asm volatile("global_load_dwordx4 %0, %1, off offset:%2 nt" : "=v"(dst) : "v"(a), "i"(IMM));
    else asm volatile("global_load_dwordx4 %0, %1, off offset:%2" : "=v"(dst) : "v"(a), "i"(IMM));
}
template <int V> DI void SETM0(unsigned ldsbase) { asm volatile("s_add_u32 m0, %0, %1" ::"s"(ldsbase), "i"(V)); }
template <int GOFF> DI void GLD0(unsigned voff, const char* sbase) { asm volatile("global_load_lds_dwordx4 %0, %1 offset:%2" ::"v"(voff), "s"(sbase), "i"(GOFF) : "memory"); }
template <int GOFF, int V> DI void GLD1(unsigned voff, const char* sbase, unsigned ldsbase) {
    asm volatile("global_load_lds_dwordx4 %0, %1 offset:%3\n\ts_add_u32 m0, %2, %4" ::"v"(voff), "s"(sbase), "s"(ldsbase), "i"(GOFF), "i"(V) : "memory");
}
constexpr int dma_goff(int it) { return it < 40 ? (it >> 2) * 128 : ((it - 100) / 5) * 256; }
constexpr int dma_loff(int it) { return it < 40 ? L_XR + ((it >> 2) % 3) * 4096 + (it & 3) * 1024 : ((it - 100) % 5) * 20480 + ((it - 100) / 5) * 2048; }
#define KFD0 10
#define KNF 13
#define KNW 11
#define KW2S 17
#define KW2M 0x4A
#define KFWP 14
constexpr int kW2S = KW2S, kW2M = KW2M;
constexpr int NW2P = (20 - kW2S) * __builtin_popcount(kW2M);
constexpr int P1_PRE = 34;
constexpr int p1_pre(int i) { return i < 5 ? 100 + i : i < 13 ? i - 5 : i < 18 ? 105 + (i - 13) : i < 22 ? 8 + (i - 18) : i < 27 ? 110 + (i - 22) : 400; }
constexpr int p1_item(int s, int g) {
    if (s == 7 && g == 0) return 324;
    if (g == 9) return s < 10 ? 115 + s : s == 11 ? 202 : s == 12 ? 203 : -1;
    if (s == KFWP && g == 1) return 208;
    if (s >= kW2S && ((kW2M >> g) & 1)) return 500 + (s - kW2S) * __builtin_popcount(kW2M) + __builtin_popcount(kW2M & ((1 << g) - 1));
    if ((s & 1) == 0 && s <= 12) return g >= 5 ? 4 * (s / 2 + 3) + (g - 5) : -1;
    if (s == 14 && g == 5) return 207;
    return -1;
}
constexpr int p1_cnt(int s, int g) {
    int n = P1_PRE;
    for (int t = 0; t <= s; ++t)
        for (int u = 0; u < 10; ++u) {
            if (t == s && u >= g) break;
            if (p1_item(t, u) >= 0 && p1_item(t, u) != 319) ++n;
        }
    return n;
}
constexpr int p1_pos(int code) {
    for (int i = 0; i < P1_PRE; ++i) if (p1_pre(i) == code) return i;
    for (int t = 0; t < 20; ++t)
        for (int u = 0; u < 10; ++u) if (p1_item(t, u) == code) return p1_cnt(t, u);
    return -1000;
}
constexpr int p1_cap(int s, int g) {
    int u = (s < 19 && g < 5) + (g == 9 && s < 19);
    if ((s & 1) && s < 19 && g >= 5 && g < 9) u += 1 + (g == 5);
    if ((s & 1) == 0 && g == 4) u += 1;
    const int it = p1_item(s, g), nt = g < 9 ? p1_item(s, g + 1) : -1;
    u += (it >= 0 && it < 500 && it != 208) + (nt >= 0 && nt < 200);
    if ((s == 18 && g == 1) || (s == 19 && g == 2) || (g == 9 && ((s & 3) == 2 || s == 17))) u += 2;
    return u < 2 ? 2 - u : 0;
}
constexpr int p1_slot(int s0, int s, int g, int gmax) {
    int n = 0;
    for (int t = s0; t <= s; ++t)
        for (int u = 0; u < gmax && !(t == s && u >= g); ++u) n += p1_cap(t, u);
    return n;
}
constexpr int p1_lim(int n) { return n > 63 ? 63 : n; }
template <int IMM> DI void gld4s(v4u& dst, unsigned voff, const void* sbase) { asm volatile("global_load_dwordx4 %0, %1, %2 offset:%3 sc1" : "=v"(dst) : "v"(voff), "s"(sbase), "i"(IMM)); }
template <int IMM> DI void gld2s(v2u& dst, unsigned voff, const void* sbase) { asm volatile("global_load_dwordx2 %0, %1, %2 offset:%3 sc1" : "=v"(dst) : "v"(voff), "s"(sbase), "i"(IMM)); }
template <class T> DI void pin(T& v) { asm volatile("" : "+v"(v)); }
DI void mul2(float& a, float& b, float x, float y, float s) { asm volatile("v_mul_f32 %0, %2, %4\n\tv_mul_f32 %1, %3, %4" : "=&v"(a), "=&v"(b) : "v"(x), "v"(y), "v"(s)); }
DI void rot1(unsigned& a, unsigned x) { asm volatile("v_alignbit_b32 %0, %1, %1, 16" : "=v"(a) : "v"(x)); }
DI void mix1(unsigned& a, unsigned x, unsigned y) { asm volatile("v_bfi_b32 %0, %3, %1, %2" : "=v"(a) : "v"(x), "v"(y), "s"(0xffffu)); }
DI void ror1(unsigned& o, unsigned prev) { asm volatile("v_mov_b32_dpp %0, %1 wave_ror:1 row_mask:0xf bank_mask:0xf" : "=v"(o) : "v"(prev)); }
DI void shr1b(unsigned& o, unsigned x) { asm volatile("v_mov_b32_dpp %0, %1 wave_shr:1 row_mask:0xf bank_mask:0xf" : "+v"(o) : "v"(x)); }
DI void xor1(float& a, unsigned x, unsigned s) { asm volatile("v_xor_b32 %0, %1, %2" : "=v"(a) : "v"(x), "v"(s)); }
template <int G> DI void bc1(float& a, unsigned x) { asm volatile("v_mov_b32_dpp %0, %1 row_newbcast:%2 row_mask:0xf bank_mask:0xf" : "=v"(a) : "v"(x), "i"(G)); }
template <int G> DI void mulb(float& o, unsigned x, float y) { asm volatile("v_mul_f32_dpp %0, %1, %2 row_newbcast:%3 row_mask:0xf bank_mask:0xf" : "=v"(o) : "v"(x), "v"(y), "i"(G)); }
template <int N> DI void lgkm() { __builtin_amdgcn_s_waitcnt(0xC07F | (N << 8)); asm volatile("" ::: "memory"); }
DI void unp2(float& a, float& b, unsigned x) { asm volatile("v_lshlrev_b32 %0, 16, %2\n\tv_and_b32 %1, 0xffff0000, %2" : "=&v"(a), "=&v"(b) : "v"(x)); }
#define GL(a) "global_load_dwordx4 a[" #a ":" #a "+3], %7, %8 sc1\n\tglobal_load_dwordx4 a[" #a "+4:" #a "+7], %7, %8 offset:256 sc1\n\t"
#define AR(d, a, o) "v_accvgpr_read_b32 " d ", a[" #a "+" #o "]\n\t"
#define GS_(a) "s_waitcnt vmcnt(4)\n\t" AR("%2", a, 1) AR("%3", a, 3) AR("%4", a, 5) AR("%5", a, 7) AR("%0", a, 0) AR("%1", a, 2)                              \
    "v_xor_b32 %2, %9, %2\n\tv_bitop3_b32 %4, %4, %9, %2 bitop3:0xbe\n\tv_bitop3_b32 %4, %3, %9, %4 bitop3:0xbe\n\tv_bitop3_b32 %4, %5, %9, %4 bitop3:0xbe\n\t" \
    AR("%2", a, 4) AR("%3", a, 6) "v_cmp_ne_u32 vcc, 0, %4\n\ts_cbranch_vccz 2f\n\t" GL(a)
template <int D0> DI void pipe_gran(float& a, float& d, unsigned off, const void* base, unsigned tag, bool& dead) {
    unsigned n = dead ? 0u : kSpinCap, y, z;
    float a1, d1;
    asm volatile("s_sleep %10\n\t" GL(32) "s_sleep %11\n\t" GL(40) "s_sleep %11\n\t" GL(48)
                 "1:\n\t" GS_(32) GS_(40) GS_(48) "s_sub_u32 %6, %6, 1\n\ts_cbranch_scc0 1b\n2:"
                 : "=&v"(a), "=&v"(d), "=&v"(a1), "=&v"(d1), "=&v"(y), "=&v"(z), "+s"(n) : "v"(off), "s"(base), "s"(tag), "i"(D0), "i"(PD1) : "memory", "vcc", "scc");
    dead = __builtin_amdgcn_readfirstlane(n) == ~0u;
    a += a1; d += d1;
}
DI void fma2(float& a, float& b, float x, float y, float s, float t, float u, float v) {
    asm volatile("v_fma_f32 %0, %2, %4, %6\n\tv_fma_f32 %1, %3, %5, %7" : "=&v"(a), "=&v"(b) : "v"(x), "v"(y), "v"(s), "v"(t), "v"(u), "v"(v));
}
DI void selhi(float& a, float x) { asm volatile("v_mov_b32_dpp %0, %1 quad_perm:[0,1,2,3] row_mask:0xf bank_mask:0xc" : "+v"(a) : "v"(x)); }
DI void cvt2(unsigned& a, unsigned& b, f32x4 v) { asm volatile("v_cvt_pk_bf16_f32 %0, %2, %3\n\tv_cvt_pk_bf16_f32 %1, %4, %5" : "=&v"(a), "=&v"(b) : "v"(v.x), "v"(v.y), "v"(v.z), "v"(v.w)); }
DI void swp2n(unsigned& a, unsigned& b, unsigned& c, unsigned& d) { asm volatile("v_permlane16_swap_b32 %0, %1\n\tv_permlane16_swap_b32 %2, %3" : "+v"(a), "+v"(b), "+v"(c), "+v"(d)); }
template <int G> DI void bc2b(float& a, float& b, float x, float y) {
    asm volatile("v_mov_b32_dpp %0, %2 row_newbcast:%4 row_mask:0xf bank_mask:0xf\n\tv_mov_b32_dpp %1, %3 row_newbcast:%4 row_mask:0xf bank_mask:0xf" : "=&v"(a), "=&v"(b) : "v"(x), "v"(y), "i"(G));
}
template <class T> DI void pins(T& v) { asm volatile("" : "+s"(v)); }
template <int I> DI void gld_w2(unsigned voff, const void* sbase) {
    asm volatile("global_load_dwordx4 " WA(2) ", %0, %1 offset:%3 sc1" ::"v"(voff), "s"(sbase), "i"(W2R + 4 * I), "i"((I % 5) * 1024 - 2048));
}
DI void undef4(v4u (&x)[4]) { asm volatile("" : "=v"(x[0]), "=v"(x[1]), "=v"(x[2]), "=v"(x[3])); }
DI void wait1(v2u& v) { asm volatile("s_waitcnt vmcnt(0)" : "+v"(v)::"memory"); }
DI void ld_ur(unsigned o, const void* base) {
    asm volatile("global_load_dwordx4 a[40:40+3], %0, %1 sc1\n\tglobal_load_dwordx4 a[44:44+3], %0, %1 offset:1024 sc1\n\t"
                 "global_load_dwordx4 a[48:48+3], %0, %1 offset:2048 sc1\n\tglobal_load_dwordx4 a[52:52+3], %0, %1 offset:3072 sc1" ::"v"(o), "s"(base));
}
DI void st_ur(unsigned a) {
    asm volatile("s_waitcnt vmcnt(%1)\n\tds_write_b128 %0, a[40:40+3] offset:480\n\tds_write_b128 %0, a[44:44+3] offset:1504\n\t"
                 "ds_write_b128 %0, a[48:48+3] offset:2528\n\tds_write_b128 %0, a[52:52+3] offset:3552" ::"v"(a), "i"(2 * __builtin_popcount(kW2M)) : "memory");
}
template <int A, int IMM> DI void lda4(unsigned o, const void* base) { asm volatile("global_load_dwordx4 a[%c2:%c2+3], %0, %1 offset:%3" ::"v"(o), "s"(base), "i"(A), "i"(IMM)); }
template <int A, int IMM> DI void lda2(unsigned o, const void* base) { asm volatile("global_load_dwordx2 a[%c2:%c2+1], %0, %1 offset:%3" ::"v"(o), "s"(base), "i"(A), "i"(IMM)); }
template <int A> DI void rda(unsigned& v) { asm volatile("v_accvgpr_read_b32 %0, a[%c1+0]" : "=v"(v) : "i"(A)); }
DI void poll1(unsigned a, const void* base) { asm volatile("global_load_dwordx2 a[36:36+1], %0, %1 sc1" ::"v"(a), "s"(base)); }
DI void poll2(unsigned a, unsigned b, const void* base) { asm volatile("global_load_dwordx2 a[36:36+1], %0, %2 sc1\n\tglobal_load_dwordx2 a[36+2:36+3], %1, %2 sc1" ::"v"(a), "v"(b), "s"(base)); }
DI void touch_(unsigned a, const void* base) { asm volatile("global_load_dword a[32+1], %0, %1" ::"v"(a), "s"(base)); }
template <int N> DI void wait_st(v2u& a, v4u& b) { asm volatile("s_waitcnt vmcnt(%2)" : "+v"(a), "+v"(b) : "i"(N) : "memory"); }
DI void undef3(v4u (&x)[3]) { asm volatile("" : "=v"(x[0]), "=v"(x[1]), "=v"(x[2])); }
template <int IMM> DI void gld4t(v4u& dst, unsigned voff, const void* sbase) { asm volatile("global_load_dwordx4 %0, %1, %2 offset:%3 sc1" : "+v"(dst) : "v"(voff), "s"(sbase), "i"(IMM)); }
template <int N> DI void vm_wait() { asm volatile("s_waitcnt vmcnt(%0)" ::"i"(N) : "memory"); }
DI float dot2a(unsigned a, unsigned b, float c) {
    return __builtin_amdgcn_fdot2_f32_bf16(__builtin_bit_cast(bf16x2v, a), __builtin_bit_cast(bf16x2v, b), c, false);
}
DI void disc(unsigned l0, unsigned l1, unsigned l2, f32x2& lam, f32x2& co) {
    const float dt = rbf(__builtin_amdgcn_exp2f(lo16f(l2) * 1.44269504088896f));
    const float lre = lo16f(l0), lim = lo16f(l1);
    const float e_ = __builtin_amdgcn_exp2f(dt * lre * 1.44269504088896f);
    const float rv = __builtin_amdgcn_fractf(dt * lim * 0.159154943091895f);
    lam = f32x2{e_ * __builtin_amdgcn_cosf(rv), e_ * __builtin_amdgcn_sinf(rv)};
    const float nr = lam.x - 1.0f, ni = lam.y, id = __builtin_amdgcn_rcpf(lre * lre + lim * lim);
    co = f32x2{(nr * lre + ni * lim) * id, (ni * lre - nr * lim) * id};
}
DI f32x4 unpk4(v2u u) { return f32x4{lo16f(u.x), hi16f(u.x), lo16f(u.y), hi16f(u.y)}; }
#define D2 "v_dot2_f32_bf16 "
#define CP "v_cvt_pk_bf16_f32 "
DI void d2x4(unsigned* o, unsigned a0, unsigned a1, unsigned a2, unsigned a3, unsigned b0, unsigned b1, unsigned b2, unsigned b3) {
    asm volatile(D2 "%0, %4, %8, 0\n\t" D2 "%1, %5, %9, 0\n\t" D2 "%2, %6, %10, 0\n\t" D2 "%3, %7, %11, 0"
                 : "=&v"(o[0]), "=&v"(o[1]), "=&v"(o[2]), "=&v"(o[3]) : "v"(a0), "v"(a1), "v"(a2), "v"(a3), "v"(b0), "v"(b1), "v"(b2), "v"(b3));
}

__global__ __launch_bounds__(256, 1) __attribute__((aligned(4096))) void k_main(Params p) {
    __shared__ __attribute__((aligned(16))) char smem[LDS_BYTES];
    const rsrc_t rs = __builtin_amdgcn_make_buffer_rsrc((void*)p.ws, 0, WS_BYTES, 0x00020000);
    const rsrc_t ro = __builtin_amdgcn_make_buffer_rsrc((void*)p.out, 0, S_ * D_ * 4, 0x00020000);
    const int w0 = __builtin_amdgcn_readfirstlane(threadIdx.x >> 6), b0 = blockIdx.x;
    bool dead = false;
    asm volatile("" ::: "a255");
    float SW[5];
    f32x4 xin[5];
    v2u gt[5], dsk[5];
    f32x4 acc[2][5];
    f32x4 rr[2];
    float tt[2][5][4];
    float lr[5], ls[5], lqr[5], lqs[5];
    f32x2 l8, l32, l128;
    bool kearly;
    unsigned sdq;
    int sgn_, ab[4], bb, zpa, uwa, spa, tbw, tbr, cwa, spl, dst[3];
    unsigned sgo, sgs, glo, gso, fgo, fgo1;
    int srca;
    unsigned adm, dso, w2o, wro, fwo, xso, fho1, fho, hso, eo, es, ho[4];
    bool cv0;
    int mt1_;
    auto addrs = [&](auto layer_, int lane, int w, int b) __attribute__((always_inline)) {
        constexpr int layer = layer_.value;
        const int c = lane & 15, q = lane >> 4;
        TILES()
        static_assert(l_sg(0, 0) == SC + 0xA900 && l_sg(0, 1) == SC + 0xB400 && l_sg(0, 2) == SC + 0xCE00 && l_sg(0, 3) == SC + 0xDC00, "");
        sgn_ = (layer ? l_sg(1, 0) + w * INBOX : SC + (int)((0xDC00CE00B400A900ull >> (16 * w)) & 0xffff)) + q * 320 + c * 4;
        const int pl = lane < 40 ? lane : 39, tbb = l_tb(layer, 0) + w * (layer ? INBOX : 5120);
        spl = SC + pl * 8;
        eo = OFF_EG + (unsigned)(layer * 163840 + nt1 * 640 + pl * 16);
        es = eo + (unsigned)(mt1 * 10240);
        spa = l_rs(layer, 0) + (2 * (mt2 & 1) + (w >> 1)) * (layer ? INBOX : 256) + (2 * (w & 1) + (c >> 3)) * 64 + (c & 7) * 4;
        tbw = tbb + c * 64 + ((q ^ ((c >> 1) & 3)) * 16);
        tbr = tbb + (2 * (c >> 2) + (q & 1)) * 64 + (((2 * (q >> 1) + ((c >> 1) & 1)) ^ (c >> 2)) * 16) + (c & 1) * 8;
        hso = OFF_HF + (unsigned)(layer * 5242880 + ((8 * mt1 + 2 * w + (q >> 1)) * 160 + 10 * nt1 + 5 * (q & 1)) * 256 + c * 16);
        cwa = L_CW + c * 4;
        fho1 = lane ? kNone : OFF_FH + (unsigned)((((layer * 32 + 2 * mt1 + (w >> 1)) * 4 + (nt1 >> 2)) * 8 + (nt1 & 3) * 2 + (w & 1)) * 128);
        fho = OFF_FH + (unsigned)(((layer * 32 + mt2) * 4 + w) * 1024 + (lane >> 3) * 128);
        pin(sgn_); pin(spl); pin(eo); pin(es); pin(spa); pin(tbw); pin(tbr); pin(hso); pin(cwa); pin(fho1); pin(fho);
        const unsigned hb_ = OFF_HF + (unsigned)(layer * 5242880 + (4 * mt2 * 160 + 40 * w) * 256);
#pragma unroll
        for (int m = 0; m < 4; ++m) { ho[m] = hb_ + (unsigned)(((m + 1 + w) & 3) * 40960 + lane * 16); pin(ho[m]); }
#pragma unroll
        for (int m = 0; m < 3; ++m) { dst[m] = SC + ((w + 1 + m) & 3) * INBOX + m * 5120 + lane * 16; pin(dst[m]); }
        mt1_ = mt1;
        sgo = OFF_SG + (unsigned)(((layer * 32 + mt2) * 4 + w) * 2048 + c * 16);
        sgs = q < 2 ? sgo + (unsigned)(nt2 * 256 + q * 8) : kNone;
        sgo += (unsigned)(q * 512);
        pin(sgo); pin(sgs);
        if constexpr (layer == 0) {
            srca = SC + w * INBOX + lane * 16;
            glo = OFF_GT + (unsigned)((80 * nt2 + (lane >> 1)) * 4096 + (64 * mt2 + 16 * w) * 2 + (lane & 1) * 16);
            gso = cv0 ? OFF_GT + (unsigned)((80 * nt2 + i32 + 32 * w) * 4096 + (q & 1) * 512 + c * 32 + (q >> 1) * 16) : kNone;
            fgo = OFF_FG + (unsigned)((nt2 * 96 + (lane >> 1)) * 128);
            fgo1 = w == 0 ? OFF_FG + (unsigned)((nt2 * 96 + i32) * 128) : kNone;
            pin(srca); pin(glo); pin(gso); pin(fgo); pin(fgo1);
            const int r0 = SC + (w < 3 ? w : 0) * REGSZ, y0 = 2031 + 8 * q - c, l34 = lane < 33 ? lane : 33;
#pragma unroll
            for (int i = 0; i < 4; ++i) { ab[i] = r0 + ((y0 & 1) ? CV_O : CV_E) + ((y0 + 1) >> 1) * 4 - 448 - 512 - 1024 * i; pin(ab[i]); }
            bb = r0 + 32 * c + 16 * q;
            zpa = w < 3 ? r0 + l34 * 16 + (l34 >= 30 ? CV_E + 4096 - 480 : 0) + (l34 >= 32 ? CV_O + 4100 - 32 - CV_E - 4096 : 0) : SC + 47360 + lane * 16;
            uwa = r0 + lane * 16;
            xso = (unsigned)((64 * mt2 + 16 * w + c) * D_ + 80 * nt2 + 16 * (q & 1) + 8 * (q >> 1)) * 2u;
            dso = (unsigned)(160 * nt2 + 8 * q);
            adm = (unsigned)((c & 1) * 16 + (q == 2 + (c >> 3) ? 256 << ((c & 7) >> 1) : 0));
            pin(adm);
            pin(bb); pin(zpa); pin(uwa); pin(xso); pin(dso);
        }
        SCHED();
    };
    unsigned tag0, tag1;
#define FLAG(off) { if (lane == 0) st64(v2u{tag0, tag1}, rs, off); }
#define ISTAG(f) (((f.x ^ tag0) | (f.y ^ tag1)) == 0u)
#define KW_REQ() { if (cv) ld_ur(wro, p.ws + OFF_KVT); static_for<NW2P>(LAMBDA(i_) { gld_w2<i_.value>(w2o, p.ws + 2048 + (i_.value / 5) * 5120); }); }
#define FW_OK() { if (__builtin_expect(!kearly, 0)) { WAVE_WAIT({ const v2u fw_ = ld64(rs, fwo); ok = ISTAG(fw_); }) KW_REQ() } }
#define TT_GAP(n) { constexpr int mb_ = (n) / 10, nb_ = ((n) % 10) / 2, e_ = ((n) & 1) * 2; \
        mul2(tt[mb_][nb_][e_], tt[mb_][nb_][e_ + 1], rm_[4 * mb_ + e_], rm_[4 * mb_ + e_ + 1], SW[nb_]); }
    {
        const int tid = threadIdx.x, lane = tid & 63, w = w0, b = b0;
        const int c = lane & 15, q = lane >> 4;
        TILES()
        STAMP(0)
        const unsigned lds0 = (unsigned)(size_t)(LAS char*)smem;
        v2u nonce;
        {
            u64 pc;
            int len, len2;
            asm volatile("s_getpc_b64 %0\nkpf_begin:\n\ts_mov_b32 %1, kpf_end-kpf_begin\n\ts_mov_b32 %2, kpf_end2-kpf_begin" : "=s"(pc), "=s"(len), "=s"(len2));
            len = len > len2 ? len : len2;
            const int o0 = (((j & 15) * 4 + w) + 64 * (lane >> 3)) * 128;
            touch_((unsigned)(o0 < len ? o0 : 0), (const void*)pc);
        }
        const bool cv = w < 2 || (w == 2 && i32 < 16);
        const int chn = 80 * nt2 + i32 + 32 * (cv ? w : 0);
        const bool kw = w < 2 || (w == 2 && i32 >= 16);
        const int kp = 16 * nt2 + mt1, ku = 5 * kp + (i32 < 16 ? w & 1 : 2 + (w < 3 ? w : 0)), kc = ku >> 6, kr = ku & 63;
        const int lc = cv ? lane : 8;
        int kwi = 2 + (i32 >> 4) - w;
        v4u ax[15], sa[5], cr[2], wr[4];
        unsigned uL[3];
        const int cu_ = (b < 200 ? b : 0) * 4 + w, cdb = cu_ / 20, ccg = cu_ % 20;
        {
            int ca_ = 32 * ccg, cb_ = 40 * nt1 + 10 * w - 32;
            asm volatile("" : "+s"(ca_), "+s"(cb_), "+s"(kwi));
            unsigned cpo = (unsigned)((lane < 32 ? ca_ : cb_) + (lane < 41 ? lane : 41)) * 2u;
            unsigned kso = (unsigned)(64 * kc + 8 * (lane & 7)) * 2u, ko = (unsigned)(32 * kr + 4 * (lane >> 3)) * 1280u + kso, kh = ko - 3840u;
            kso *= 4u;
            pin(cpo); pin(kso); pin(ko); pin(kh);
            SCHED();
            gldu16(uL[0], cpo, p.Lre); gldu16(uL[1], cpo, p.Lim); gldu16(uL[2], cpo, p.logdt);
            const v4u rk = {(unsigned)(u64)p.k, (unsigned)((u64)p.k >> 32) & 0xffffu, S_ * D_ * 2u, 0x00020000u};
#define L4(d, v, s_, o, f) "global_load_dwordx4 %" #d ", %" #v ", %" #s_ " offset:" #o f "\n\t"
#define B4(d, o) "buffer_load_dwordx4 %" #d ", %17, %21, 0 offen offset:" #o " nt\n\t"
            asm volatile("s_cmp_lt_i32 %22, 1\n\ts_cbranch_scc1 Lrole2\n\t"
                         L4(3, 15, 18, 0, " nt") L4(4, 15, 18, 1280, " nt") L4(5, 15, 18, 2560, " nt") L4(6, 15, 18, 3840, " nt")
                         L4(7, 15, 20, 0, " nt") L4(8, 15, 20, 1280, " nt") L4(9, 15, 20, 2560, " nt") L4(10, 15, 20, 3840, " nt")
                         B4(0, 0) B4(1, 1280) B4(2, 2560) L4(11, 16, 19, 0, "") L4(12, 16, 19, 16, "") L4(13, 16, 19, 32, "") L4(14, 16, 19, 48, "")
                         "\nLrole2:"
                         : "=&v"(ax[0]), "=&v"(ax[1]), "=&v"(ax[2]), "=&v"(ax[3]), "=&v"(ax[4]), "=&v"(ax[5]), "=&v"(ax[6]), "=&v"(ax[7]), "=&v"(ax[8]), "=&v"(ax[9]),
                           "=&v"(ax[10]), "=&v"(ax[11]), "=&v"(ax[12]), "=&v"(ax[13]), "=&v"(ax[14])
                         : "v"(ko), "v"(kso), "v"(kh), "s"(p.k), "s"(p.shk), "s"(p.v), "s"(rk), "s"(kwi) : "scc");
            SCHED();
        }
        if (PT && w == 3 && j < 14) {
            const char* pb_ = j == 0 ? (const char*)p.x0 : j == 1 ? (const char*)p.Bre : j == 2 ? (const char*)p.Bim : j == 3 ? (const char*)p.Cre : j == 4 ? (const char*)p.Cim :
                              j == 5 ? (const char*)p.ssmk : j < 8 ? (const char*)p.out + (j - 6) * 2621440 : p.ws + (j - 8) * 3670016;
            const int st_ = j >= 8 ? 57344 : j >= 1 && j <= 4 ? 12800 : 40960;
            touch_((unsigned)(lane * st_ + xq * 128), pb_);
        }
        const char* ssrc;
        {
            u64 pa = (u64)(p.x0 + (128 * mt1 + 8 * nt1 + 2 * w) * D_), pb = (u64)(((mt1 & 1) ? p.Bim : p.Bre) + (40 * nt1 + (mt1 >> 1)) * D_);
            asm volatile("" : "+s"(pa), "+s"(pb));
            const int nbx = 2 * w + q - 2, r = q < 2 ? q : 8 * (nbx < 5 ? nbx : 4);
            ssrc = (const char*)(q < 2 ? pa : pb) + (unsigned)(r * 1280 + c * 16);
            pin(ssrc);
        }
        SCHED();
        STAMP(22)
        const int row8 = lane >> 3;
        const unsigned wvo = (unsigned)(40 * nt1 + row8) * 1280u + (unsigned)(((lane & 7) ^ ((w & 1) | ((row8 >> 1) << 1))) * 16 + (w >> 1) * 128);
        const char* const wbase = (const char*)((w & 1) ? p.Bim : p.Bre);
        const unsigned wl = lds0 + (unsigned)(((w & 1) * 10 + (w >> 1)) * 1024);
        const unsigned xvo0 = (unsigned)(8 * (row8 >> 1) + 2 * (row8 & 1)) * 1280u + (unsigned)(((lane & 7) ^ ((row8 >> 1) << 1)) * 16);
        const unsigned xvo1 = (xvo0 ^ 16u) + 1280u;
        const char* const xbase = (const char*)p.x0 + (size_t)(128 * mt1 + 32 * w) * 1280;
        const unsigned xl = lds0 + (unsigned)(w * REGSZ);
        unsigned xv[4] = {xvo0, xvo1, xvo0 + 5120u, xvo1 + 5120u}, wv[5];
#pragma unroll
        for (int i = 0; i < 5; ++i) { wv[i] = wvo + i * 10240u; pin(wv[i]); if (i < 4) pin(xv[i]); }
#define DMA_M0(it) SETM0<dma_loff(it) - dma_goff(it)>((it) < 40 ? xl : wl)
#define DMA_LD1(it, nx_) GLD1<dma_goff(it), dma_loff(nx_) - dma_goff(nx_)>((it) < 40 ? xv[(it) & 3] : wv[(it) < 40 ? 0 : ((it) - 100) % 5], (it) < 40 ? xbase : wbase, (nx_) < 40 ? xl : wl)
#define DMA_LD(it) GLD0<dma_goff(it)>((it) < 40 ? xv[(it) & 3] : wv[(it) < 40 ? 0 : ((it) - 100) % 5], (it) < 40 ? xbase : wbase)
        const int swz = (c & 1) | ((c >> 2) << 1);
        int w1lane0 = (c & 1) * 10240 + (c >> 1) * 128 + ((q ^ swz) * 16);
        int w1lane1 = (c & 1) * 10240 + (c >> 1) * 128 + (((4 + q) ^ swz) * 16);
        int xlane0 = L_XR + w * REGSZ + (c & 1) * 1024 + (c >> 1) * 128 + ((q ^ swz) * 16);
        int xlane1 = L_XR + w * REGSZ + (c & 1) * 1024 + (c >> 1) * 128 + (((4 + q) ^ swz) * 16);
        unsigned cvo = (unsigned)((16 * cdb + c) * D_ + 32 * ccg + 8 * q) * 2u, for0, for1;
        {
            const int G = 4 * ccg + q, ch = 10 * (G / 5) + G % 5, ci = ch + 5;
            const unsigned fo = OFF_W2F + (unsigned)(((cdb / 5) * 200 + cdb % 5) * 1024 + c * 16);
            for0 = fo + (unsigned)((ch >> 2) * 5120 + (ch & 3) * 256); for1 = fo + (unsigned)((ci >> 2) * 5120 + (ci & 3) * 256);
        }
        unsigned sto = OFF_ST + (unsigned)((mt1 * 16 + 4 * w + q) * 256 + c * 8), swo = OFF_ST + (unsigned)(((lane >> 2) * 16 + nt1) * 256 + 128 + (lane & 3) * 16);
        unsigned kvo = OFF_KVT + (unsigned)((64 * kc + 8 * (lane >> 3) + ((lane >> 2) & 1)) * 4096 + (32 * kr + 8 * (lane & 3)) * 2);
        int kbw = L_KB + w * 1024 + (lane & 7) * 128 + (((lane >> 3) * 8) ^ ((lane & 7) * 16));
        int kbr = L_KB + w * 1024 + (lane >> 3) * 128 + (((lane & 7) * 16) ^ ((lane >> 3) * 16));
        const unsigned dum = kNone;
        if (!kw) kvo = dum;
        if (b >= 200) { for0 = dum; for1 = dum; cvo = (unsigned)((b * 4 + w) * 128); }
        wro = (unsigned)(chn * 4096 + lc * 16);
        cv0 = cv;
        pin(kvo); pin(kbw); pin(kbr);
        int w1hi0 = w1lane0 + 61440, w1hi1 = w1lane1 + 61440;
        pin(w1hi0); pin(w1hi1);
        pin(w1lane0); pin(w1lane1); pin(xlane0); pin(xlane1); pin(cvo); pin(for0); pin(for1); pin(sto); pin(swo); pin(wro);
        SCHED();
        auto pre = LAMBDA(i_) {
            constexpr int it = p1_pre(i_.value);
            SCHED();
            DMA_M0(it);
            asm volatile("s_nop 0");
            DMA_LD(it);
            SCHED();
        };
        {
            int f0 = 2 * ((chn & ~63) / 5), f1 = 2 * ((chn | 63) / 5) + 1, nk = cv ? 28 : 0;
            asm volatile("" : "+s"(f0), "+s"(f1), "+s"(nk));
            const int l10 = lane - 32 < 0 || lane - 32 > 9 ? 0 : lane - 32;
            fwo = lane < nk ? OFF_FK + (unsigned)(f0 + lane < f1 ? f0 + lane : f1) * 128u : OFF_FW + (unsigned)(25 * nt2 + w + 5 * (l10 >> 1) + (l10 & 1)) * 128u;
        }
        w2o = OFF_W2F + (unsigned)((nt2 * 40 + 10 * w) * 5120 + lane * 16);
        int ime = SC + (w < 3 ? w * REGSZ + CV_E : 43264) + 1008 - 16 * lane, imo = SC + (w < 3 ? w * REGSZ + CV_O + 4 : 47360) + 1008 - 16 * lane, im3 = w < 3 ? ime + 3072 : imo;
        int tca = L_TAB + (c >> 1) * 136, tcb = tca + 8 + 32 * w + 8 * q, tcl = L_TAB + (lane < 40 ? lane : 39) * 136, coa = L_CO + w * REGSZ + q * 64, rsa = l_rs(0, 0) + w * 256 + lane * 4;
        unsigned sgn = (c & 1) ? 0u : 0x80000000u;
        unsigned fko1 = w ? kNone : lane < 32 ? OFF_FK + (unsigned)((2 * kp + (i32 >> 4)) * 128) : OFF_FW + (unsigned)(b * 128);
        pin(fwo); pin(w2o); pin(fko1); pin(ime); pin(imo); pin(im3); pin(tca); pin(tcb); pin(tcl); pin(coa); pin(rsa); pin(sgn);
        SCHED();
        auto cw_ = [&]() __attribute__((always_inline)) {
            gldn<0, 2>(cr[0], cvo, p.Cre); gldn<0, 2>(cr[1], cvo, p.Cim);
            static_for<4>(LAMBDA(i) { gldn<i.value * 1024, 1>(wr[i.value], wro, p.ssmk); });
        };
        STAMP(33)
        gld2s<0>(nonce, 0u, p.ws + OFF_NONCE);
        asm volatile("s_waitcnt vmcnt(16)\n\ts_cmp_gt_i32 %3, 0\n\ts_cbranch_scc1 Lrole3\n\ts_waitcnt vmcnt(1)\nLrole3:" : "+v"(uL[0]), "+v"(uL[1]), "+v"(uL[2]) : "s"(kwi) : "memory", "scc");
        {
            f32x2 lam, co, lp[16];
            disc(uL[0], uL[1], uL[2], lam, co);
            if (lane < 32) *(f32x2*)(smem + L_CO + w * REGSZ + lane * 8) = 2.0f * co;
            lp[0] = cmul(lam, lam); lp[0] = cmul(lp[0], lp[0]); lp[1] = cmul(lp[0], lp[0]);
            lp[0] = f32x2{1.f, 0.f};
            lp[2] = cmul(lp[1], lp[1]); lp[3] = cmul(lp[2], lp[1]); lp[4] = cmul(lp[2], lp[2]);
            const f32x2 z2 = {0.f, 0.f};
            f32x2 t4[4];
            cfma4(t4, lp[4], lp[1], lp[2], lp[3], lp[4], z2, z2, z2, z2);
            lp[5] = t4[0]; lp[6] = t4[1]; lp[7] = t4[2]; lp[8] = t4[3];
            cfma4(t4, lp[8], lp[1], lp[2], lp[3], lp[4], z2, z2, z2, z2);
            lp[9] = t4[0]; lp[10] = t4[1]; lp[11] = t4[2]; lp[12] = t4[3];
            cfma4(t4, lp[8], lp[5], lp[6], lp[7], lp[7], z2, z2, z2, z2);
            lp[13] = t4[0]; lp[14] = t4[1]; lp[15] = t4[2];
            if (lane >= 32 && lane < 42) {
                f32x2* tl = (f32x2*)(smem + L_TAB + (10 * w + lane - 32) * 136);
                tl[0] = lam;
#pragma unroll
                for (int i = 0; i < 16; ++i) tl[1 + i] = lp[i];
            }
        }
        SCHED();
        STAMP(35)
        static_for<5>(LAMBDA(i) { gld64<256 * i.value>(sa[i.value], ssrc); });
        cw_();
        addrs(std::integral_constant<int, 0>{}, lane, w, b);
        v4u kvt[4];
        unsigned one2 = 0x3F803F80u, kq[12];
        float kf_[8];
        pin(one2);
        auto kvf = LAMBDA(n_) {
            constexpr int n = n_.value, h = n / 5, u = n % 5;
            if constexpr (u == 0) {
#pragma unroll
                for (int r = 0; r < 6; ++r) { kq[r] = __builtin_amdgcn_perm(ax[r][h], ax[r + 1][h], 0x05040100u); kq[6 + r] = __builtin_amdgcn_perm(ax[r][h], ax[r + 1][h], 0x07060302u); }
                if constexpr (h > 0) kvt[h - 1] = *(const v4u*)(smem + kbr);
            } else if constexpr (u == 1) {
                unsigned t[8];
                d2x4(t, kq[0], kq[1], kq[2], kq[3], ax[11 + h][1], ax[11 + h][1], ax[11 + h][1], ax[11 + h][1]);
                d2x4(t + 4, kq[6], kq[7], kq[8], kq[9], ax[11 + h][3], ax[11 + h][3], ax[11 + h][3], ax[11 + h][3]);
#pragma unroll
                for (int r = 0; r < 8; ++r) kf_[r] = __uint_as_float(t[r]);
            } else if constexpr (u == 2) {
#pragma unroll
                for (int r = 0; r < 8; ++r) kf_[r] = dot2a(kq[r + 2 * (r >> 2) + 2], ax[11 + h][2 * (r >> 2)], kf_[r]);
            } else if constexpr (u == 3) {
#pragma unroll
                for (int r = 0; r < 4; ++r) { kq[r] = ax[7 + r][h] << 16; kq[4 + r] = ax[7 + r][h] & 0xffff0000u; pin(kq[r]); pin(kq[4 + r]); }
            } else {
#pragma unroll
                for (int r = 0; r < 8; ++r) kf_[r] *= __uint_as_float(kq[r]);
                *(v2u*)(smem + kbw) = v2u{cvtpk(kf_[0], kf_[1]), cvtpk(kf_[2], kf_[3])};
                *(v2u*)(smem + (kbw ^ 64)) = v2u{cvtpk(kf_[4], kf_[5]), cvtpk(kf_[6], kf_[7])};
                if constexpr (h == 3) kvt[3] = *(const v4u*)(smem + kbr);
            }
            SCHED();
        };
        asm volatile("s_waitcnt vmcnt(%15)" : "+v"(ax[0]), "+v"(ax[1]), "+v"(ax[2]), "+v"(ax[3]), "+v"(ax[4]), "+v"(ax[5]), "+v"(ax[6]), "+v"(ax[7]), "+v"(ax[8]), "+v"(ax[9]),
                     "+v"(ax[10]), "+v"(ax[11]), "+v"(ax[12]), "+v"(ax[13]), "+v"(ax[14]) : "i"(11) : "memory");
        pin(nonce);
        STAMP(30)
        unsigned tch = 0u;
        constexpr int NF = KNF;
        static_assert(NF < 27, "");
        if (kw) {
            {
                static_for<20>(LAMBDA(n_) {
                    constexpr int n = n_.value, DN = 20 - KFD0;
                    site(); SCHED();
                    kvf(n_);
                    if constexpr (n >= KFD0) static_for<(n - KFD0 + 1) * NF / (DN ? DN : 1) - (n - KFD0) * NF / (DN ? DN : 1)>(LAMBDA(i_) { pre(std::integral_constant<int, (n - KFD0) * NF / (DN ? DN : 1) + i_.value>{}); });
                });
                if constexpr (KFD0 == 20) static_for<NF>(pre);
            }
        } else {
            if (w == 3) {
            const int l = lane, l3 = l & 3;
            unsigned a = OFF_SG + (unsigned)((((((l >> 3) & 1) * 32 + mt2) * 4 + ((l >> 1) & 3)) * 8 + nt2) * 256 + (l & 1) * 128);
            a = l >= 16 ? OFF_EG + (unsigned)((l >= 21 ? 163840 - 640 : 0) + (mt1 * 16 + nt1) * 640 + (l < 26 ? l - 16 : 5) * 128) : a;
            a = l >= 28 ? OFF_PS + (unsigned)(((mt2 * 4 + l3) * 8 + nt2) * 128) : a;
            a = l >= 32 ? OFF_FH + (unsigned)((((((l >> 2) & 1) * 32 + 2 * mt1 + (l3 >> 1)) * 4 + (nt1 >> 2)) * 8 + (nt1 & 3) * 2 + (l3 & 1)) * 128) : a;
            a = l >= 40 ? OFF_FX + (unsigned)(((mt1 * 4 + 2 * (mt2 & 1) + (l3 >> 1)) * 16 + nt2 * 2 + (l3 & 1)) * 128) : a;
            a = l >= 48 ? OFF_FG + (unsigned)((nt2 * 96 + i32) * 128) : a;
            const unsigned big = (l & 7) < 2 ? OFF_HF + (unsigned)((l & 1) * 5242880 + (8 * mt1 * 160 + 10 * nt1) * 256) : (l & 7) == 2 ? OFF_X2F + (unsigned)(2 * mt2 * 40960 + 2560 * nt2)
                               : (l & 7) < 5 ? OFF_KVT + (unsigned)((l & 1) * 2621440 + (80 * nt2 + i32) * 4096) : OFF_W2F + (unsigned)(nt2 * 204800 + ((l & 7) - 5) * 98304);
            a = l >= 56 ? big : a;
            tch = a;
        }
            vm_wait<KNW>();
            static_for<NF>(pre);
            undef4(kvt);
        }
        tag0 = __builtin_amdgcn_readfirstlane(nonce.x) + 1u; tag1 = __builtin_amdgcn_readfirstlane(nonce.y) + (tag0 == 0u ? 1u : 0u);
        SCHED();
        STAMP(34)
        v4u orr, oi;
        v2u gr;
        unsigned gro;
        {
            asm volatile("s_waitcnt vmcnt(%7)" : "+v"(cr[0]), "+v"(cr[1]), "+v"(sa[0]), "+v"(sa[1]), "+v"(sa[2]), "+v"(sa[3]), "+v"(sa[4]) : "i"(4 + NF) : "memory");
#pragma unroll
            for (int e = 0; e < 4; ++e) {
                const f32x4 co = *(const f32x4*)(smem + coa + e * 16);
                const f32x2 o0 = lo16f(cr[0][e]) * f32x2{co.x, -co.y} - lo16f(cr[1][e]) * f32x2{co.y, co.x};
                const f32x2 o1 = hi16f(cr[0][e]) * f32x2{co.z, -co.w} - hi16f(cr[1][e]) * f32x2{co.w, co.z};
                orr[e] = cvtpk(o0.x, o1.x); oi[e] = cvtpk(o0.y, o1.y);
            }
            pin(orr); pin(oi);
            SCHED();
        }
        static_for<(27 - NF) / 3>(LAMBDA(i_) { pre(std::integral_constant<int, NF + i_.value>{}); });
        {
            float s1[2] = {0.f, 0.f}, s2[2] = {0.f, 0.f};
#pragma unroll
            for (int i = 0; i < 5; ++i)
#pragma unroll
                for (int e = 0; e < 4; ++e) { s1[e & 1] = dot2a(sa[i][e], one2, s1[e & 1]); s2[e & 1] = dot2a(sa[i][e], sa[i][e], s2[e & 1]); }
            const float a1 = rowsum16(s1[0] + s1[1]), a2 = rowsum16(s2[0] + s2[1]);
            const float mean = a1 * (1.0f / D_);
            const float var = fmaxf(a2 * (1.0f / D_) - mean * mean, 0.f);
            const float ri = __builtin_amdgcn_rcpf(rbf(__builtin_amdgcn_sqrtf(rbf(rbf(var) + 1e-5f))));
            gr = v2u{__float_as_uint(q >= 2 ? a1 : c ? -ri * rbf(mean) : ri), tag0};
            gro = c < 2 ? OFF_ST + (unsigned)((mt1 * 16 + nt1) * 256 + ((q < 2 ? 8 * c : 14) + 2 * w + q) * 8) : kNone;
            pin(gr); pin(gro);
        }
        SCHED();
        STAMP(59)
        {
            static_for<27 - NF - (27 - NF) / 3>(LAMBDA(i_) { pre(std::integral_constant<int, NF + (27 - NF) / 3 + i_.value>{}); });
#pragma unroll
            for (int i = 0; i < 4; ++i) st128(kvt[i], rs, kvo + i * 8192);
            st128(orr, rs, for0); st128(oi, rs, for1);
        }
        st64(gr, rs, gro);
        SCHED();
        STAMP(23)
        asm volatile("s_waitcnt vmcnt(%4) lgkmcnt(0)" : "+v"(wr[0]), "+v"(wr[1]), "+v"(wr[2]), "+v"(wr[3]) : "i"(P1_PRE - 9) : "memory");
        __builtin_amdgcn_s_barrier();
        asm volatile("" ::: "memory");
        STAMP(24)
        v4u wf[2][5], af[2][2][2];
#pragma unroll
        for (int nb = 0; nb < 5; ++nb) wf[0][nb] = *(const v4u*)(smem + w1lane0 + nb * 20480 + (0) * 2048);
#pragma unroll
        for (int i = 0; i < 4; ++i) af[0][i >> 1][i & 1] = *(const v4u*)(smem + ((i >> 1) ? xlane1 : xlane0) + (i & 1) * 2048);
        SCHED();
        const char *kvtb = p.ws + OFF_KVT, *w2b0 = p.ws + 2048;
        pins(kvtb); pins(w2b0);
        v2u sg, tc[10], fw, tg2 = {tag0, tag1};
        pin(tg2);
        v4u sw0;
        unsigned ie[4][4], io[4][4], iw, zq[4];
        float rr_[8];
        auto kstep = LAMBDA(s_) {
            constexpr int s = s_.value, ph = s >> 2, s4 = s & 3, cu = s & 1, nx = cu ^ 1, jj = s >> 1, pp = jj & 1;
            if constexpr (s4 == 3 && ph < 4) {
                constexpr int pw = p1_pos(104 + 5 * (ph + 1)), px = p1_pos(4 * (jj + 1) + 3), pm = pw > px ? pw : px;
                constexpr int vm = p1_lim(p1_cnt(s, 0) - 1 - pm);
                asm volatile("" ::: "memory");
                __builtin_amdgcn_s_waitcnt((vm & 15) | ((vm >> 4) << 14) | 0x70);
                __builtin_amdgcn_s_barrier();
                asm volatile("" ::: "memory");
                STAMP(25 + ph)
                SCHED();
            }
            if constexpr (s == 15) {
                wait_st<p1_cnt(s, 0) - 1 - p1_pos(203)>(sg, sw0);
                WAVE_RETRY((((sg.y ^ tag0) | (sw0.y ^ tag0) | (sw0.w ^ tag0)) == 0u), sg = ld64(rs, sto); sw0 = ld128(rs, swo))
                STAMP(7)
            }
            if constexpr (s == kW2S) { vm_wait<p1_cnt(s, 0) - 1 - p1_pos(208)>(); SCHED(); }
            if constexpr (s == 18) {
                vm_wait<p1_cnt(s, 0) - 1 - (p1_pos(207) > p1_pos(208) ? p1_pos(207) : p1_pos(208))>();
                pin(fw); pin(sdq);
                kearly = __builtin_amdgcn_ballot_w64(!ISTAG(fw)) == 0ull;
                ORDER();
                if (kearly && cv) ld_ur(wro, kvtb);
                SCHED();
            }
            if constexpr (s4 != 3 || ph == 4) lgkm<(cu == 0 && s > 0 ? 3 : s == 17 ? 4 : s == 19 ? 2 : 0)>();
            SCHED();
            static_for<10>(LAMBDA(g_) {
                constexpr int g = g_.value, mb = g / 5, k = g % 5, it = p1_item(s, g), nt_ = g < 9 ? p1_item(s, g + 1) : -1;
                constexpr bool d0 = it >= 0 && it < 200, d1 = nt_ >= 0 && nt_ < 200;
                if constexpr (s == 0) mf0(acc[mb][k], af[pp][cu][mb], wf[cu][k]); else mf(acc[mb][k], af[pp][cu][mb], wf[cu][k]);
                SCHED();
                if constexpr (s < 19) {
                    if constexpr (g < 5) wf[nx][g] = *(const v4u*)(smem + (g < 3 ? (nx ? w1lane1 : w1lane0) : (nx ? w1hi1 : w1hi0)) + (g < 3 ? g : g - 3) * 20480 + ((s + 1) >> 1) * 1024);
                    if constexpr (cu == 1 && g == 5) vm_wait<p1_lim(p1_cnt(s, 5) - 1 - p1_pos(4 * (jj + 1) + 3))>();
                    if constexpr (cu == 1 && g >= 5 && g < 9)
                        af[pp ^ 1][(g - 5) >> 1][(g - 5) & 1] = *(const v4u*)(smem + (((g - 5) >> 1) ? xlane1 : xlane0) + ((jj + 1) % 3) * 4096 + ((g - 5) & 1) * 2048);
                }
                if constexpr (cu == 0 && g == 4 && s > 0) lgkm<(s == 8 || s == 10 || s == 14 ? 8 : 5)>();
                static_for<p1_cap(s, g)>(LAMBDA(e_) {
                    constexpr int na = p1_slot(0, s, g, 10) + e_.value, i = (na / 10) & 3, u = na % 10;
                    static_assert(p1_slot(0, 8, 0, 10) >= 40, "");
                    if constexpr (na < 40) {
                        if constexpr (u == 0) { if constexpr (i > 0) ror1(iw, wr[i - 1].w); else { iw = 0u; pin(iw); } }
                        else if constexpr (u < 5) rot1(ie[i][u - 1], wr[i][4 - u]);
                        else if constexpr (u == 5) shr1b(iw, wr[i].w);
                        else if constexpr (u < 9) mix1(io[i][u - 6], wr[i][9 - u], wr[i][8 - u]);
                        else mix1(io[i][3], wr[i].x, iw);
                    }
                    constexpr int nb = p1_slot(8, s, g, 3) + e_.value;
                    if constexpr (s >= 8 && s < 12 && g < 3) {
                        if constexpr (nb < 10) tc[nb] = *(const v2u*)(smem + (nb < 5 ? tca : tcb) + (nb % 5) * 1088);
                        else if constexpr (nb == 10) { const f32x2* tp = (const f32x2*)(smem + tcl); l8 = tp[2]; l32 = tp[5]; }
                        else if constexpr (nb == 11) l128 = *(const f32x2*)(smem + tcl + 72);
                        static_assert(p1_slot(8, 12, 0, 3) == 12, "");
                    }
                    constexpr int nc = p1_slot(12, s, g, 10) + e_.value;
                    if constexpr (s >= 12 && s < 14 && nc < 10) {
                        if constexpr (nc < 5) { lr[nc] = __uint_as_float(tc[nc].x); xor1(ls[nc], tc[nc].y, sgn); }
                        else { lqr[nc - 5] = __uint_as_float(tc[nc].x); xor1(lqs[nc - 5], tc[nc].y, sgn); }
                        static_assert(p1_slot(12, 14, 0, 10) >= 10, "");
                    }
                    if constexpr (s == 15 && g < 5) SW[g] = __int_as_float(__builtin_amdgcn_ds_bpermute(16 * c + 4 * (g >> 1), (g & 1) ? sw0.z : sw0.x));
                    constexpr bool img = (s == 16 && g >= 5 && g < 9 && e_.value == 0) || (s == 18 && (g == 5 || g == 6) && e_.value == 0);
                    constexpr int nd = p1_slot(15, s, g, 10) + e_.value - 5 - (s > 16 ? 4 : s == 16 && g >= 5 ? (g < 9 ? g - 4 : 4) : 0) - (s > 18 ? 2 : s == 18 && g >= 5 ? (g == 5 ? 1 : 2) : 0);
                    if constexpr (s >= 15 && nd >= 0 && !img) {
                        if constexpr (nd < 4) bc1<nd>(rr_[nd], sg.x);
                        else if constexpr (nd < 44) mulb<8 + (nd - 4) % 8>(tt[(nd - 4) % 8 / 4][(nd - 4) / 8][(nd - 4) % 4], sg.x, SW[(nd - 4) / 8]);
                        else if constexpr (nd < 48) bc1<nd - 40>(rr_[nd - 40], sg.x);
                        static_assert(p1_slot(15, 16, 0, 10) <= 9 && p1_slot(15, 20, 0, 10) >= 59, "");
                    }
                });
                if constexpr (s == 14 && g < 3) *(v4u*)(smem + ime + 1024 * g) = v4u{ie[3 - g][0], ie[3 - g][1], ie[3 - g][2], ie[3 - g][3]};
                if constexpr (s == 16 && g >= 5 && g < 9) *(v4u*)(smem + imo + 1024 * (8 - g)) = v4u{io[g - 5][0], io[g - 5][1], io[g - 5][2], io[g - 5][3]};
                if constexpr (s == 18 && g == 5) *(v4u*)(smem + im3) = v4u{ie[0][0], ie[0][1], ie[0][2], ie[0][3]};
                if constexpr (s == 18 && g == 6) *(unsigned*)(smem + rsa) = sg.x;
                if constexpr (s == 14 && (g == 6 || g == 8)) { zq[g - 6] = 0u; zq[g - 5] = 0u; pin(zq[g - 6]); pin(zq[g - 5]); }
                if constexpr (s == 19 && g == 0) *(v4u*)(smem + zpa) = v4u{zq[0], zq[1], zq[2], zq[3]};
                if constexpr (s == 18 && g == 1) touch_(tch, p.ws);
                if constexpr (s == 19 && g == 2) { if (__builtin_expect(!kearly, 0)) poll1(fwo, p.ws); }
                if constexpr (it >= 0) {
                    if constexpr (d0 && d1) DMA_LD1(it, d1 ? nt_ : 0);
                    else if constexpr (d0) DMA_LD(it);
                    else if constexpr (it == 202) gld2s<0>(sg, sto, p.ws);
                    else if constexpr (it == 203) gld4s<0>(sw0, swo, p.ws);
                    else if constexpr (it == 207) gldu16(sdq, (unsigned)chn * 2u, p.ssmD);
                    else if constexpr (it == 208) gld2s<0>(fw, fwo, p.ws);
                    else if constexpr (it >= 500) gld_w2<it % 50>(w2o, w2b0 + ((it % 50) / 5) * 5120);
                    else { static_assert(p1_pos(19) >= P1_PRE, ""); st64(tg2, rs, fko1); }
                }
                if constexpr (d1 && !d0) DMA_M0(d1 ? nt_ : 0);
                site();
                SCHED();
            });
        };
        static_for<20>(kstep);
        SCHED();
        rr[0] = f32x4{rr_[0], rr_[1], rr_[2], rr_[3]}; rr[1] = f32x4{rr_[4], rr_[5], rr_[6], rr_[7]};
        ACC_FENCE10(acc[0], acc[1]);
        STAMP(1)
    }

    auto layer_body = LAMBDA(layer_) {
        constexpr int layer = layer_.value;
        const int sga = sgn_;
        const bool cv = layer == 0 && cv0;
        v2u fh;
        unsigned ks[10];
        f32x4 ca[8], a2[4][5];
        float rsr, rsm;
        int otw, otr, ots;
        v4u cA[2][8], cB[2][8];
        constexpr int R2 = kCR1, LP = (7 - R2) & 1;
        static_assert(kFHP >= 7 - KG0 / 4 + 1, "");
        constexpr bool TB = (SBT >> layer) & 1;
        float bv[8][5];
        f32x2 pr[5][2], ps[5][2];
        auto powers = [&]() __attribute__((always_inline)) {
            if constexpr (TB) {
#pragma unroll
                for (int nb = 0; nb < 5; ++nb) {
                    const f32x2 m = {lr[nb] * lr[nb] - ls[nb] * ls[nb], 2.0f * lr[nb] * ls[nb]};
                    pr[nb][0] = f32x2{lr[nb], m.x}; ps[nb][0] = f32x2{ls[nb], m.y};
#pragma unroll
                    for (int j = 1; j < 2; ++j) pstep(pr[nb][j], ps[nb][j], pr[nb][j - 1], ps[nb][j - 1], m);
#pragma unroll
                    for (int j = 0; j < 2; ++j) { pin(pr[nb][j]); pin(ps[nb][j]); }
                }
                SCHED();
            }
        };
        {
            float a0[5], a1[5];
            static_for<2>(LAMBDA(m_) {
                constexpr int mb = m_.value;
#pragma unroll
                for (int nb = 0; nb < 5; ++nb) {
                    const f32x4 v = __builtin_elementwise_fma(acc[mb][nb], rr[mb], f32x4{tt[mb][nb][0], tt[mb][nb][1], tt[mb][nb][2], tt[mb][nb][3]});
#pragma unroll
                    for (int i = 0; i < 4; ++i) bv[4 * mb + i][nb] = v[i];
                }
                SCHED();
                if constexpr (TB) {
#pragma unroll
                    for (int i = mb ? 4 : 1; i < 4 + 4 * mb; ++i) cstepi(bv[i][0], bv[i][1], bv[i][2], bv[i][3], bv[i][4], bv[i - 1][0], bv[i - 1][1], bv[i - 1][2], bv[i - 1][3], bv[i - 1][4], lr, ls);
#pragma unroll
                    for (int nb = 0; nb < 5; ++nb) a0[nb] = bv[7][nb];
                }
                if constexpr (mb == 0 && !TB) cstep(a0, bv[0], lr, ls, bv[1][0], bv[1][1], bv[1][2], bv[1][3], bv[1][4]);
#pragma unroll
                for (int i8 = mb ? 4 : 2; i8 < (TB ? 0 : 4 + 4 * mb); i8 += 2) {
                    cstep(a1, a0, lr, ls, bv[i8][0], bv[i8][1], bv[i8][2], bv[i8][3], bv[i8][4]);
                    cstep(a0, a1, lr, ls, bv[i8 + 1][0], bv[i8 + 1][1], bv[i8 + 1][2], bv[i8 + 1][3], bv[i8 + 1][4]);
                }
                SCHED();
            });
#pragma unroll
            for (int nb = 0; nb < 5; ++nb) *(float*)(smem + sga + nb * 64) = a0[nb];
        }
        SCHED();
        const int w_ = w0;
        {
        const int w = w_, mt1 = mt1_;
        if (layer == 0 && __builtin_expect(!kearly, 0)) {
            unsigned f0_, f1_;
            asm volatile("s_waitcnt vmcnt(0)\n\t" AR("%0", 36, 0) AR("%1", 36, 1) : "=v"(f0_), "=v"(f1_)::"memory");
            kearly = __builtin_amdgcn_ballot_w64(((f0_ ^ tag0) | (f1_ ^ tag1)) != 0u) == 0ull;
            ORDER();
            if (kearly) KW_REQ()
        }
        asm volatile("s_waitcnt lgkmcnt(0)" ::: "memory");
        __builtin_amdgcn_s_barrier();
        asm volatile("" ::: "memory");
        STAMP(layer ? 14 : 2)
#pragma unroll
        for (int s = 0; s < 10; ++s) ks[s] = (unsigned)s * 1024u;
        constexpr int NX = 58;
        constexpr int NW2A = NW2P + 7 * kCR1 + kXA, NW3 = NW2P + kN3E + kN3;
        auto w2ld = LAMBDA(i_) {
            constexpr int i = i_.value;
            gld_w2<i>(w2o, p.ws + 2048 + ks[i / 5] * 5u);
        };
        auto w2a = LAMBDA(i_) {
            constexpr int k = i_.value;
            if constexpr (k < kXA) { w2ld(std::integral_constant<int, NW2P + 2 * k>{}); w2ld(std::integral_constant<int, NW2P + 2 * k + 1>{}); }
            else w2ld(std::integral_constant<int, NW2P + kXA + k>{});
        };
        auto w2x = LAMBDA(i_) {
            constexpr int i = i_.value < NX - 50 ? 50 + i_.value : NW2A + i_.value - (NX - 50) < 50 ? NW2A + i_.value - (NX - 50) : 99;
            if constexpr (i < 50) w2ld(std::integral_constant<int, i>{});
            else if constexpr (i < 52) lda4<40 + 4 * (i - 50), (i - 50) * 64>(xso, p.x0);
            else if constexpr (i == 52) lda4<48, 128>(xso - 2u * (unsigned)(bb & 16), p.x0);
            else if constexpr (i < NX) lda2<20 + 2 * (i - 53), 32 * (i - 53)>(dso, p.Dskip);
        };
        constexpr int NB = NX - NW2A, NB1 = KB1 < NB ? KB1 : NB, KPL = 7 * (8 - R2) - kFHP, NFH = NB - NB1 - (KPL - kFHK) > 0 ? NB - NB1 - (KPL - kFHK) : 0;
        auto w2b = LAMBDA(i_) {
            constexpr int k = i_.value - 7 * R2;
            if constexpr (k < NB1) w2x(std::integral_constant<int, k>{});
            if constexpr (k > kFHK) w2x(std::integral_constant<int, NB1 + k - kFHK - 1>{});
            if constexpr (k == kFHK) { vm_wait<NB1>(); st64(v2u{tag0, tag1}, rs, fho1); STAMP(39) }
            if constexpr (k == kFHK - 1) { STAMP(38) }
            if constexpr (k == KPL) poll1(fho, p.ws);
            static_assert(NB1 <= kFHK + 1 && NB1 + 7 * (7 - R2) + KG0 / 4 - kFHK - 1 >= NB && NW2A <= 50 && NW3 <= NW2A && NFH <= NB - NB1, "");
        };
        auto partb = [&]() __attribute__((always_inline)) {
            const float* sp = (const float*)(smem + spa);
            rsr = sp[0]; rsm = sp[8];
            if (layer == 1) { pin(rsr); pin(rsm); }
            if (layer == 1) {
                ACC_TIE5(dsk);
#pragma unroll
                for (int nb = 0; nb < 5; ++nb) { a2[3][nb] = xin[nb] + (xin[nb] * rsr + rsm) * unpk4(dsk[nb]); pin(a2[3][nb]); }
            }
        };
        constexpr int LBW = layer ? 2 : 3;
        if (w == 3) {
            site(); SCHED();
            f32x2 sv[16];
#pragma unroll
            for (int i = 0; i < 16; ++i) sv[i] = *(const f32x2*)(smem + spl + (l_sg(layer, i >> 2) - SC + (i & 3) * 320));
            f32x2 u[4], v[4], x[4];
            cfma4(u, l8, sv[0], sv[4], sv[8], sv[12], sv[1], sv[5], sv[9], sv[13]);
            cfma4(v, l8, u[0], u[1], u[2], u[3], sv[2], sv[6], sv[10], sv[14]);
            cfma4(x, l8, v[0], v[1], v[2], v[3], sv[3], sv[7], sv[11], sv[15]);
            f32x2 P[4][4];
            P[0][0] = f32x2{0.f, 0.f}; P[0][1] = x[0]; P[0][2] = cfma(l32, x[0], x[1]); P[0][3] = cfma(l32, P[0][2], x[2]);
            const f32x2 E = cfma(l32, P[0][3], x[3]);
            st128(v4u{__float_as_uint(E.x), tag0, __float_as_uint(E.y), tag0}, rs, es);
            SCHED();
            if (layer == 0) { FW_OK() static_for<kN3E>(LAMBDA(i_) { w2ld(std::integral_constant<int, NW2P + i_.value>{}); }); }
#pragma unroll
            for (int kk = 1; kk < 4; ++kk) cfma4(P[kk], l8, P[kk - 1][0], P[kk - 1][1], P[kk - 1][2], P[kk - 1][3], sv[kk - 1], sv[kk + 3], sv[kk + 7], sv[kk + 11]);
#pragma unroll
            for (int i = 0; i < 16; ++i) *(f32x2*)(smem + spl + (l_sg(layer, i >> 2) - SC + (i & 3) * 320)) = P[i & 3][i >> 2];
            if (layer == 0) partb();
            if (layer == 1) powers();
        }
        if (w == LBW) {
            site(); SCHED();
            f32x2 wg[17], t4[4];
            unsigned ea[15], tg[15];
            const f32x2 z2 = {0.f, 0.f};
            wg[1] = cmul(l128, l128); wg[2] = cmul(wg[1], wg[1]); wg[3] = cmul(wg[2], wg[1]); wg[4] = cmul(wg[2], wg[2]);
#pragma unroll
            for (int i = 4; i < 16; i += 4) {
                cfma4(t4, wg[i], wg[1], wg[2], wg[3], wg[4], z2, z2, z2, z2);
#pragma unroll
                for (int e = 0; e < 4; ++e) wg[i + 1 + e] = t4[e];
            }
#pragma unroll
            for (int i = 0; i < 15; ++i) {
                const int ch = mt1 - 1 - i;
                if (i) pin(wg[i]);
                ea[i] = ch < 0 ? kNone : (unsigned)ch * 10240u; tg[i] = ch < 0 ? 0u : tag0;
                pins(ea[i]); pins(tg[i]);
            }
            powers();
            f32x2 H0, H1;
            __builtin_amdgcn_s_sleep(layer ? KLB1 : KLB0);
            unsigned spins = dead ? kSpinCap : 0u;
            bool miss;
#pragma nounroll
            do {
                asm volatile("" ::: "memory");
                v4u eg[15];
#pragma unroll
                for (int i = 0; i < 15; ++i) eg[i] = ld128s(rs, eo, ea[i]);
                SCHED();
                unsigned x = __builtin_amdgcn_bitop3_b32(eg[0].y, eg[0].w, tg[0], 0x7E);
                H0 = f32x2{__uint_as_float(eg[0].x), __uint_as_float(eg[0].z)}; H1 = f32x2{0.f, 0.f};
#pragma unroll
                for (int i = 1; i < 15; i += 2) {
                    x |= __builtin_amdgcn_bitop3_b32(eg[i].y, eg[i].w, tg[i], 0x7E) | __builtin_amdgcn_bitop3_b32(eg[i + 1].y, eg[i + 1].w, tg[i + 1], 0x7E);
                    pin(x);
                    gacc2(H0, H1, wg[i], wg[i + 1], eg[i], eg[i + 1]);
                    SCHED();
                }
                miss = __builtin_amdgcn_ballot_w64(x != 0u) != 0ull;
            } while (miss && ++spins <= kSpinCap);
            dead = dead || miss;
            ORDER();
            *(f32x2*)(smem + spl + (L_CW - SC)) = H0 + H1;
            STAMP(layer ? 36 : 37)
            if (layer == 0) {
                static_for<NW3 - NW2P - kN3E>(LAMBDA(i_) { w2ld(std::integral_constant<int, NW2P + kN3E + i_.value>{}); });
            }
        }
        int w2_ = w;
        pins(w2_);
        if (layer == 0) UNDEF8(ca);
        if (layer == 0 && w2_ != LBW) {
            if (cv) {
                site(); SCHED();
                FW_OK()
                st_ur((unsigned)(size_t)(LAS char*)smem + (unsigned)uwa);
                STAMP(32)
                conv_part<0, kCR1>(ca, smem, ab, bb, w2a, [&]() __attribute__((always_inline)) {
                    v4u ad;
                    const unsigned dv = sdq << (adm & 16u);
#pragma unroll
                    for (int i = 0; i < 4; ++i) ad[i] = dv & (unsigned)__builtin_amdgcn_sbfe((int)adm, 8 + i, 1);
                    asm volatile("s_nop 1" : "+v"(ad));
                    return ad;
                }, cA, cB);
                partb();
                powers();
            } else {
                FW_OK()
                static_for<NW2A - NW2P>(LAMBDA(i_) { w2ld(std::integral_constant<int, NW2P + i_.value>{}); __builtin_amdgcn_s_sleep(1); });
                partb();
                powers();
            }
        }
        if (layer == 1 && w < LBW) { site(); SCHED(); powers(); }
        asm volatile("s_waitcnt lgkmcnt(0)" ::: "memory");
        __builtin_amdgcn_s_barrier();
        asm volatile("" ::: "memory");
        STAMP(layer ? 15 : 4)
        {
            float cw[5], ex[5];
#pragma unroll
            for (int nb = 0; nb < 5; ++nb) { cw[nb] = *(const float*)(smem + cwa + nb * 64); ex[nb] = *(const float*)(smem + sga + nb * 64); }
            v2u ta[5], td[5];
            if constexpr (TB) {
                cstepi(ex[0], ex[1], ex[2], ex[3], ex[4], cw[0], cw[1], cw[2], cw[3], cw[4], lqr, lqs);
                f32x2 cs[5];
                PIN5(ex);
#pragma unroll
                for (int nb = 0; nb < 5; ++nb) { cs[nb] = f32x2{ex[nb], dpp<0xB1>(ex[nb])}; pin(cs[nb]); }
                SCHED();
                static_for<5>(LAMBDA(n_) {
                    constexpr int nb = n_.value;
                    f32x2 h[4] = {{bv[0][nb], bv[1][nb]}, {bv[2][nb], bv[3][nb]}, {bv[4][nb], bv[5][nb]}, {bv[6][nb], bv[7][nb]}};
                    padd(h, pr[nb], ps[nb], cs[nb]);
                    *(v4u*)(smem + tbw + nb * 1024) = v4u{cvtpk(h[0].x, h[0].y), cvtpk(h[1].x, h[1].y), cvtpk(h[2].x, h[2].y), cvtpk(h[3].x, h[3].y)};
                    ta[nb] = __builtin_bit_cast(v2u, trread(smem, tbr + nb * 1024)); td[nb] = __builtin_bit_cast(v2u, trread(smem, tbr + nb * 1024 + 512));
                    SCHED();
                    if constexpr (nb >= LAG) { st128(v4u{ta[nb - LAG].x, ta[nb - LAG].y, td[nb - LAG].x, td[nb - LAG].y}, rs, hso + (nb - LAG) * 256); SCHED(); }
                });
            } else
            static_for<2>(LAMBDA(h_) {
                constexpr int n0 = 2 * h_.value;
                float hv[9][3];
                cstep3(hv[0], cw + n0, lqr + n0, lqs + n0, ex + n0);
                if constexpr (n0 == 0) {
                    chain24<0>(hv, bv, lr, ls);
                    chain24<0>(hv + 4, bv + 4, lr, ls);
                } else {
                    chain33<2>(hv, bv, lr, ls);
                    SCHED();
#pragma unroll
                    for (int rd = 0; rd < 2; ++rd) st128(v4u{ta[rd].x, ta[rd].y, td[rd].x, td[rd].y}, rs, hso + rd * 256);
                    SCHED();
                    chain33<2>(hv + 3, bv + 3, lr, ls);
                    chain32<2>(hv + 6, bv + 6, lr, ls);
                }
#pragma unroll
                for (int e = 0; e < (n0 ? 3 : 2); ++e) {
                    v4u o;
                    o.x = cvtpk(hv[1][e], hv[2][e]); o.y = cvtpk(hv[3][e], hv[4][e]);
                    o.z = cvtpk(hv[5][e], hv[6][e]); o.w = cvtpk(hv[7][e], hv[8][e]);
                    *(v4u*)(smem + tbw + (n0 + e) * 1024) = o;
                }
#pragma unroll
                for (int rd = n0; rd < (n0 ? 5 : 2); ++rd) { ta[rd] = __builtin_bit_cast(v2u, trread(smem, tbr + rd * 1024)); td[rd] = __builtin_bit_cast(v2u, trread(smem, tbr + rd * 1024 + 512)); }
                SCHED();
            });
#pragma unroll
            for (int rd = TB ? 5 - LAG : 2; rd < 5; ++rd) st128(v4u{ta[rd].x, ta[rd].y, td[rd].x, td[rd].y}, rs, hso + rd * 256);
            SCHED();
        }
#pragma unroll
        for (int s = 0; s < 10; ++s) pins(ks[s]);
        if constexpr (layer == 1) {
            partb();
            SCHED();
            if constexpr (OTR) {
                const int ln = (srca >> 4) & 63, od = (b0 >> 3) & 1, r0 = srca - 16 * ln;
                otw = (ln & 15) * 336 + (ln >> 4) * 16; otr = (ln & 12) * 336 + ((ln >> 4) + od) * 64 + (ln & 3) * 16; ots = otw + (od ? 0 : 256);
                ACC_TIE5(gt);
#pragma unroll
                for (int nb = 0; nb < 5; ++nb) *(v2u*)(smem + r0 + 10240 + otw / 2 + nb * 32) = gt[nb];
#pragma unroll
                for (int nb = 0; nb < 5; ++nb) gt[nb] = *(const v2u*)(smem + r0 + 10240 + (nb < 4 ? otr / 2 + nb * 168 : ots / 2));
                otw += r0; otr += r0; ots += r0;
                pin(otw); pin(otr); pin(ots);
                ACC_TIE5(gt);
                static_assert(MAP == 0 && (SC & 1023) == 0 && (INBOX & 1023) == 0 && L_CW + 320 <= SC + 2 * INBOX + 10240, "");
                SCHED();
            }
            DRAIN();
            st64(v2u{tag0, tag1}, rs, fho1);
        }
        STAMP(layer ? 16 : 5)
        if (cv) {
            if constexpr (layer == 0) site();
            SCHED();
            conv_part<kCR1, 8>(ca, smem, ab, bb, w2b, []() {}, cA, cB);
        } else if (layer == 0) {
            static_for<NB1>(LAMBDA(i_) { w2x(i_); __builtin_amdgcn_s_sleep(1); });
            vm_wait<NB1>();
            st64(v2u{tag0, tag1}, rs, fho1);
            static_for<NB - NB1 - NFH>(LAMBDA(i_) { w2x(std::integral_constant<int, NB1 + i_.value>{}); __builtin_amdgcn_s_sleep(1); });
            if (w == 3) static_for<NW2A - NW3>(LAMBDA(i_) { w2ld(std::integral_constant<int, NW3 + i_.value>{}); __builtin_amdgcn_s_sleep(1); });
            poll1(fho, p.ws);
            static_for<NFH>(LAMBDA(i_) { w2x(std::integral_constant<int, NB - NFH + i_.value>{}); __builtin_amdgcn_s_sleep(1); });
            static_assert(NFH <= NB - (NX - 50), "");
            UNDEF8(cA[LP]); UNDEF8(cB[LP]);
        }
        if (layer == 0) { unsigned f0_, f1_; asm volatile("s_waitcnt vmcnt(%2)\n\t" AR("%0", 36, 0) AR("%1", 36, 1) : "=v"(f0_), "=v"(f1_) : "i"(NFH) : "memory"); fh = v2u{f0_, f1_}; }
        STAMP(layer ? 29 : 6)
        }
        unsigned g[8][2];

            v2u pt[4];
            if (layer == 0) WAVE_RETRY(ISTAG(fh), fh = ld64(rs, fho))
            else { pipe_flag<KPF0>(pt, fho, p.ws, (u64)tag0 | ((u64)tag1 << 32), dead); ORDER(); }
            STAMP(layer ? 17 : 31)
            site(); SCHED();
            constexpr int RING2 = layer ? KR2 : KRT;
            v4u ar[RING2];
            constexpr int NRT = layer ? RING2 : (36 - KG0) / KRS + 2 < KRT ? (36 - KG0) / KRS + 2 : KRT;
            unsigned xs_[3][4];
            auto gate = LAMBDA(n_) {
                constexpr int n = n_.value;
                if constexpr (n >= 100) swp2n(g[2 * (n - 100)][0], g[2 * (n - 100) + 1][0], g[2 * (n - 100)][1], g[2 * (n - 100) + 1][1]);
                else if constexpr (n >= 0) cvt2(g[n][0], g[n][1], ca[n]);
            };
            if constexpr (layer == 0) {
#pragma unroll
                for (int i = 0; i < 2; ++i) { ar[i] = ld128s(rs, ho[0], ks[i]); SCHED(); }
                static_for<36 - KG0>(LAMBDA(n_) {
                    constexpr int n = n_.value, g = KG0 + n;
                    constexpr int a = g < 8 ? 0 : g < 15 ? 1 : g < 21 ? 2 : g < 26 ? 3 : g < 30 ? 4 : g < 33 ? 5 : g < 35 ? 6 : 7;
                    constexpr int e = g - (a * 8 - a * (a - 1) / 2), i = 2 + n / KRS;
                    mf(ca[a + e], cA[LP][a], cB[LP][e]);
                    SCHED();
                    if constexpr (n % KRS == 0 && i < NRT) ar[i] = ld128s(rs, ho[i / 10], ks[i % 10]);
                    if constexpr (n < 12) rda<40 + n>(xs_[n >> 2][n & 3]);
                    constexpr int gt_[12] = {0, 1, 2, -1, 100, -1, 3, -1, -1, 101, 4, -1};
                    if constexpr (g >= 24) gate(IC(gt_[g < 24 ? 0 : g - 24]));
                    site();
                    SCHED();
                });
                static_assert(KG0 <= 15 && NRT >= 12, "");
            } else {
#pragma unroll
                for (int i = 0; i < RING2; ++i) { ar[i] = ld128s(rs, ho[i / 10], ks[i % 10]); SCHED(); }
            }
        v4u gl[3];
        v2u f0;
        bool gearly = false;
        {
            const char* src = smem + srca;
            const char *glb1 = p.ws + 32 * 4096, *glb2 = p.ws + 48 * 4096;
            float zx[4], zd[4], zt[4], zz[5][4];
            if constexpr (STAMPS == 1) { STAMP(layer ? 50 : 40) }
            static_for<40>(LAMBDA(i_) {
                constexpr int i = i_.value, m = i / 10, s = i % 10;
                static_for<5>(LAMBDA(k_) {
                    constexpr int kk = k_.value, n = 2 * (i - 3) + kk / 2 - 3, nb = n / 8, u = n % 8, pv = i - 1 + RING2;
                    if constexpr (s == 0 && m < 3) mfa0<kk>(a2[m][kk], ar[i % RING2]); else mfa<5 * s + kk>(a2[m][kk], ar[i % RING2]);
                    SCHED();
                    constexpr int gu_[12] = {-1, 5, -1, -1, 102, 6, -1, -1, 7, -1, 103, -1};
                    if constexpr (layer == 0 && 5 * i + kk < 12) gate(IC(gu_[(5 * i + kk) % 12]));
                    if constexpr (layer == 0 && i == 25 && kk == 1) { pins(glb1); pins(glb2); }
                    if constexpr (layer == 0 && i >= 3 && i < 26 && (kk & 1)) {
                        if constexpr (n < 0) swp2n(xs_[n + 3][0], xs_[n + 3][2], xs_[n + 3][1], xs_[n + 3][3]);
                        else if constexpr (nb < 5) {
                            if constexpr (u < 2) unp2(zx[2 * u], zx[2 * u + 1], xs_[nb >> 1][2 * (nb & 1) + u]);
                            else if constexpr (u < 4) unp2(zd[2 * u - 4], zd[2 * u - 3], dsk[nb][u - 2]);
                            else if constexpr (u < 6) fma2(zt[2 * u - 8], zt[2 * u - 7], zx[2 * u - 8], zx[2 * u - 7], rsr, rsr, rsm, rsm);
                            else fma2(zz[nb][2 * u - 12], zz[nb][2 * u - 11], zt[2 * u - 12], zt[2 * u - 11], zd[2 * u - 12], zd[2 * u - 11], zx[2 * u - 12], zx[2 * u - 11]);
                        }
                    }
                    if constexpr (i == 0 && kk == 0 && layer == 1) { pin(pt[0]); pin(pt[1]); pin(pt[2]); pin(pt[3]); }
                    if constexpr (layer == 0 && i < 2 && kk > 0 && 12 + 4 * i + kk - 1 >= NRT && 12 + 4 * i + kk - 1 < RING2) ar[12 + 4 * i + kk - 1] = ld128s(rs, ho[1], ks[2 + 4 * i + kk - 1]);
                    if constexpr (layer == 0 && i == kGSI && kk > 0) st128(v4u{g[2 * kk - 2][0], g[2 * kk - 2][1], g[2 * kk - 1][0], g[2 * kk - 1][1]}, rs, gso + (kk - 1) * 1024);
                    if constexpr (layer == 0 && i > 2 && i <= 7 && kk == (i == kGSI ? 0 : 2)) {
                        { unsigned d0_, d1_; rda<20 + 2 * (i - 3)>(d0_); rda<21 + 2 * (i - 3)>(d1_); dsk[i - 3] = v2u{d0_, d1_}; }
                    }
                    if constexpr (kk == 0 && i > 0 && pv < 40) ar[pv % RING2] = ld128s(rs, ho[pv / 10], ks[pv % 10]);
                    constexpr int s1 = s - 1 - (KMB && layer == 0 && m == 1 ? kFGI - 10 : 0);
                    if constexpr (kk == 2 && m >= 1 && s1 >= 0 && s1 < 5) *(f32x4*)(smem + dst[m - 1] + s1 * 1024) = a2[m - 1][s1];
                    site();
                    if constexpr (i >= 37) a2[i - 37][kk] = *(const f32x4*)(src + (i - 37) * 5120 + kk * 1024);
                    SCHED();
                });
                if constexpr (i == 10 && (layer || !KMB)) { asm volatile("s_waitcnt lgkmcnt(0)" ::: "memory"); __builtin_amdgcn_s_barrier(); asm volatile("" ::: "memory"); }
                if constexpr (i == kFGI && layer == 0) {
                    static_assert(kFGI >= 10 && kFGI <= 14, "");
                    asm volatile("s_waitcnt vmcnt(%0) lgkmcnt(0)" ::"i"(kFGI - kGSI) : "memory");
                    __builtin_amdgcn_s_barrier(); asm volatile("" ::: "memory");
                    st64(v2u{tag0, tag1}, rs, fgo1);
                }
                if constexpr (i == 26 && layer == 0) {
#pragma unroll
                    for (int nb = 0; nb < 5; ++nb) { a2[3][nb] = f32x4{zz[nb][0], zz[nb][1], zz[nb][2], zz[nb][3]}; pin(a2[3][nb]); }
                }
                if constexpr (i == kGPI && layer == 0) gld2s<0>(f0, fgo, p.ws);
                if constexpr (i == kGCI && layer == 0) {
                    wait1(f0);
                    static_for<39 - i>(LAMBDA(r_) { pin(ar[(i + 1 + r_.value) % RING2]); });
                    gearly = __builtin_amdgcn_ballot_w64(!ISTAG(f0)) == 0ull;
                    ORDER();
                    undef3(gl);
                    if (__builtin_expect(gearly, 1)) { gld4t<0>(gl[0], glo, p.ws); gld4t<0>(gl[1], glo, glb1); gld4t<0>(gl[2], glo, glb2); }
                    else if (G2ND) poll2(fgo, fgo, p.ws);
                }
                if constexpr (i == 36) {
                    asm volatile("s_waitcnt lgkmcnt(0)" ::: "memory");
                    __builtin_amdgcn_s_barrier(); asm volatile("" ::: "memory");
                }
                if constexpr (STAMPS == 1 && (i == 0 || i == 9 || i == 10 || i == 15 || i == 25 || i == 35 || i == 36)) { STAMP((layer ? 51 : 41) + (i == 0 ? 0 : i == 9 ? 1 : i == 10 ? 2 : i == 15 ? 3 : i == 25 ? 4 : i == 35 ? 5 : 6)) }
                SCHED();
            });
            STAMP(layer ? 18 : 8)
        }
#pragma unroll
        for (int nb = 0; nb < 5; ++nb) a2[0][nb] = (a2[0][nb] + a2[1][nb]) + a2[2][nb];
        asm volatile("s_nop 1" : "+v"(a2[3][0]), "+v"(a2[3][1]), "+v"(a2[3][2]), "+v"(a2[3][3]), "+v"(a2[3][4]),
                     "+v"(a2[0][0]), "+v"(a2[0][1]), "+v"(a2[0][2]), "+v"(a2[0][3]), "+v"(a2[0][4]));
        float ty;
        {
            f32x4 s1, s2;
#pragma unroll
            for (int nb = 0; nb < 5; ++nb) {
                const f32x4 y = a2[3][nb] + a2[0][nb];
                xin[nb] = y;
                if (nb == 0) { s1 = y; s2 = y * y; } else { s1 += y; s2 += y * y; }
            }
            ty = xsum2h(hsum4(s1), hsum4(s2));
            st64(v2u{__float_as_uint(ty), tag0}, rs, sgs);
        }
        SCHED();
        STAMP(layer ? 19 : 9)
        if constexpr (layer == 1 && OTR) {
#pragma unroll
            for (int nb = 0; nb < 5; ++nb) *(f32x4*)(smem + otw + nb * 64) = xin[nb];
#pragma unroll
            for (int nb = 0; nb < 5; ++nb) xin[nb] = *(const f32x4*)(smem + (nb < 4 ? otr + nb * 336 : ots));
            SCHED();
        }
        int lane_o, b_o = b0, w_o = w0;
        asm volatile("v_mbcnt_lo_u32_b32 %0, -1, 0\n\tv_mbcnt_hi_u32_b32 %0, -1, %0" : "=v"(lane_o));
        asm volatile("" : "+s"(b_o), "+s"(w_o));
        const int lane = lane_o, b = b_o, w = w_o;
        const int c = lane & 15, q = lane >> 4;
        TILES()
        if (layer == 0 && __builtin_expect(!gearly, 0)) {
            if (G2ND) {
                unsigned f0_, f1_;
                asm volatile("s_waitcnt vmcnt(1)\n\t" AR("%0", 36, 0) AR("%1", 36, 1) : "=v"(f0_), "=v"(f1_)::"memory");
                f0 = v2u{f0_, f1_};
            }
            STAMP(63)
            WAVE_RETRY(ISTAG(f0), f0 = ld64(rs, fgo))
            gld4t<0>(gl[0], glo, p.ws); gld4t<0>(gl[1], glo, p.ws + 32 * 4096); gld4t<0>(gl[2], glo, p.ws + 48 * 4096);
            asm volatile("s_waitcnt vmcnt(0)" : "+v"(gl[0]), "+v"(gl[1]), "+v"(gl[2])::"memory");
        }
        if (layer == 0) {
            asm volatile("s_waitcnt vmcnt(1)" : "+v"(gl[0]), "+v"(gl[1]), "+v"(gl[2])::"memory");
            STAMP(60)
            char* tb = smem + SC + w * INBOX;
            *(v4u*)(tb + lane * 16) = gl[0]; *(v4u*)(tb + 1024 + lane * 16) = gl[1]; *(v4u*)(tb + 1536 + lane * 16) = gl[2];
#pragma unroll
            for (int nb = 0; nb < 5; ++nb) gt[nb] = __builtin_bit_cast(v2u, trread(tb, (16 * nb + 4 * q + (c >> 2)) * 32 + (c & 3) * 8));
        } else ACC_TIE5(gt);
        SCHED();
        f32x4 gf[5], yg[5];
#pragma unroll
        for (int nb = 0; nb < 5; ++nb) { gf[nb] = unpk4(gt[nb]); yg[nb] = xin[nb] * gf[nb]; pin(gf[nb]); pin(yg[nb]); }
        unsigned xs = OFF_X2F + (unsigned)((2 * mt2 + (w >> 1)) * 40960 + ((c >> 2) & 1) * 20480 + (10 * nt2 + 2 * (q & 1) + (q >> 1)) * 256 + (8 * (w & 1) + 4 * (c >> 3) + (c & 3)) * 16);
        unsigned pso = q < 2 ? OFF_PS + (unsigned)(((mt2 * 4 + w) * 8 + nt2) * 128 + c * 8 + q * 4) : kNone;
        unsigned fxo1 = lane ? kNone : OFF_FX + (unsigned)(((mt1 * 4 + 2 * (mt2 & 1) + (w >> 1)) * 16 + nt2 * 2 + (w & 1)) * 128);
        unsigned oo = (unsigned)(((64 * mt2 + 16 * w + c) * D_ + 80 * nt2 + 4 * q) * 4);
        unsigned o4 = (unsigned)(((64 * mt2 + 16 * w + (c & 12)) * D_ + 80 * nt2 + 16 * (q + (nt2 & 1)) + 4 * (c & 3)) * 4);
        if (OTR) oo += (nt2 & 1) ? 0u : 256u;
        unsigned xs2 = (q & 1) ? kNone : xs + 2048u;
        if (layer == 0) { pin(xs); pin(xs2); pin(pso); pin(fxo1); } else { pin(oo); if (OTR) pin(o4); }
        SCHED();
        {
            float a, d;
            STAMP(layer ? 62 : 61)
            if constexpr (layer == 0) site();
            pipe_gran<layer ? PDS : PDS0>(a, d, sgo, p.ws, tag0, dead);
            STAMP(layer ? 20 : 10)
            xsum2(a, d);
            const float me = a * (1.0f / D_);
            const float ri = __builtin_amdgcn_rsqf(fmaxf(d * (1.0f / D_) - me * me, 0.f) + 1e-5f), mr = -me * ri;
            if constexpr (layer == 1 && OTR) {
                static_for<5>(LAMBDA(j_) {
                    constexpr int jj = (j_.value + 4) % 5;
                    const float rj = jj < 4 ? dpp<0x55 * (jj & 3)>(ri) : ri, mj = jj < 4 ? dpp<0x55 * (jj & 3)>(mr) : mr;
                    const f32x4 o = yg[jj] * rj + (gf[jj] * mj + xin[jj]);
                    __builtin_amdgcn_raw_buffer_store_b128(__builtin_bit_cast(v4u, o), ro, jj < 4 ? o4 : oo, jj < 4 ? jj * 2560 : 0, OUTP);
                });
            } else if constexpr (layer == 0 && XI) {
#pragma unroll
                for (int i = 0; i < 3; ++i) {
                    v2u pk[2];
#pragma unroll
                    for (int e = 0; e < (i < 2 ? 2 : 1); ++e) {
                        const f32x4 x = yg[2 * i + e] * ri + (gf[2 * i + e] * mr + xin[2 * i + e]);
                        xin[2 * i + e] = x;
                        pk[e] = v2u{cvtpk(x.x, x.y), cvtpk(x.z, x.w)};
                    }
                    const v2u pa = pk[0], pb = pk[i < 2];
                    const auto r0 = __builtin_amdgcn_permlane16_swap(pa.x, pb.x, false, false);
                    const auto r1 = __builtin_amdgcn_permlane16_swap(pa.y, pb.y, false, false);
                    st128(v4u{r0[0], r1[0], r0[1], r1[1]}, rs, i < 2 ? xs + i * 1024 : xs2);
                    SCHED();
                }
            } else {
#pragma unroll
                for (int nb = 0; nb < 5; ++nb) xin[nb] = yg[nb] * ri + (gf[nb] * mr + xin[nb]);
            }
        }
        if constexpr (layer == 0) {
            v2u pk[5];
#pragma unroll
            for (int nb = 0; nb < 5; ++nb) { const f32x4 x = xin[nb]; pk[nb] = v2u{cvtpk(x.x, x.y), cvtpk(x.z, x.w)}; }
#pragma unroll
            for (int i = 0; i < (XI ? 0 : 3); ++i) {
                const v2u pa = pk[2 * i], pb = pk[i < 2 ? 2 * i + 1 : 4];
                const auto r0 = __builtin_amdgcn_permlane16_swap(pa.x, pb.x, false, false);
                const auto r1 = __builtin_amdgcn_permlane16_swap(pa.y, pb.y, false, false);
                if (i < 2 || (q & 1) == 0) st128(v4u{r0[0], r1[0], r0[1], r1[1]}, rs, xs + i * 1024);
            }
            {
                f32x4 s1, s2;
#pragma unroll
                for (int nb = 0; nb < 5; ++nb) { const f32x4 x = xin[nb]; if (nb == 0) { s1 = x; s2 = x * x; } else { s1 += x; s2 += x * x; } }
                __builtin_amdgcn_raw_buffer_store_b32(__float_as_uint(xsum2h(hsum4(s1), hsum4(s2))), rs, pso, 0, SC1);
            }
            SCHED();
            int w1lane0 = (c & 1) * 10240 + (c >> 1) * 128 + ((q ^ ((c & 1) | ((c >> 2) << 1))) * 16);
            int w1lane1 = (c & 1) * 10240 + (c >> 1) * 128 + (((4 + q) ^ ((c & 1) | ((c >> 2) << 1))) * 16);
            unsigned fxo = OFF_FX + (unsigned)((mt1 * 4 + w) * 2048 + (lane >> 2) * 128);
            unsigned xo = OFF_X2F + (unsigned)((4 * mt1 + w) * 40960 + lane * 16), xo1 = xo + 20480u;
            const int row = 8 * q + (c & 7);
            unsigned po = OFF_PS + (unsigned)((((2 * mt1 + (w >> 1)) * 4 + 2 * (w & 1) + (row >> 4)) * 8 + 4 * (c >> 3)) * 128 + (row & 15) * 8);
            int w1hi0 = w1lane0 + 61440, w1hi1 = w1lane1 + 61440;
            int rsw = l_rs(1, 0) + w * INBOX + lane * 4;
            if (GW) pin(rsw);
            pin(w1lane0); pin(w1lane1); pin(w1hi0); pin(w1hi1); pin(fxo); pin(xo); pin(xo1); pin(po);
            v4u wf[2][5];
#pragma unroll
            for (int nb = 0; nb < 5; ++nb) wf[0][nb] = *(const v4u*)(smem + w1lane0 + nb * 20480);
            v2u tg2 = {tag0, tag1};
            pin(tg2);
            SCHED();
            DRAIN();
            st64(tg2, rs, fxo1);
            if (b == 255 && w == 0) FLAG(OFF_NONCE)
            STAMP(11)
            addrs(std::integral_constant<int, 1>{}, lane, w, b);
            v2u pt[4];
            pipe_flag<KPFX>(pt, fxo, p.ws, (u64)tag0 | ((u64)tag1 << 32), dead);
            ORDER();
            STAMP(12)
            v4u ar[RING1][2];
#pragma unroll
            for (int s = 0; s < RING1; ++s)
#pragma unroll
                for (int mb = 0; mb < 2; ++mb) { ar[s][mb] = ld128s(rs, mb ? xo1 : xo, s * 1024); SCHED(); }
            v2u ps[4];
#pragma unroll
            for (int i = 0; i < 4; ++i) ps[i] = ld64(rs, po + i * 128);
            SCHED();
            float t1, t2, rinv, mri, rsel, rr_[8], rm_[8];
            static_for<20>(LAMBDA(s_) {
                constexpr int s = s_.value, cu = s & 1, nx = cu ^ 1;
                LGKM0() SCHED();
                static_for<10>(LAMBDA(g_) {
                    constexpr int g = g_.value, mb = g / 5, k = g % 5;
                    if constexpr (s == 0) mf0(acc[mb][k], ar[0][mb], wf[0][k]); else mf(acc[mb][k], ar[s % RING1][mb], wf[cu][k]);
                    SCHED();
                    if constexpr (s == 0 && g == 0) { pin(pt[0]); pin(pt[1]); pin(pt[2]); pin(pt[3]); }
                    if constexpr (s < 19 && g < 3) wf[nx][g] = *(const v4u*)(smem + (nx ? w1lane1 : w1lane0) + g * 20480 + ((s + 1) >> 1) * 1024);
                    if constexpr (s < 19 && (g == 3 || g == 4)) wf[nx][g] = *(const v4u*)(smem + (nx ? w1hi1 : w1hi0) + (g - 3) * 20480 + ((s + 1) >> 1) * 1024);
                    if constexpr (g == 5 && s + RING1 < 20) ar[s % RING1][0] = ld128s(rs, xo, (s + RING1) * 1024);
                    if constexpr (g == 2 && s > 0 && s - 1 + RING1 < 20) ar[(s - 1) % RING1][1] = ld128s(rs, xo1, (s - 1 + RING1) * 1024);
                    constexpr int z = GW ? s + 3 : s;
                    if constexpr (z == 8 && g == 8) { t1 = __uint_as_float(ps[0].x) + __uint_as_float(ps[1].x); pin(t1); t2 = __uint_as_float(ps[0].y) + __uint_as_float(ps[1].y); pin(t2); }
                    if constexpr (z == 9 && g == 6) { t1 += __uint_as_float(ps[2].x); pin(t1); t2 += __uint_as_float(ps[2].y); pin(t2); }
                    if constexpr (z == 9 && g == 7) { t1 += __uint_as_float(ps[3].x); pin(t1); t2 += __uint_as_float(ps[3].y); pin(t2); }
                    if constexpr (z == 10 && g == 6) { t1 += dpp<0x128>(t1); t2 += dpp<0x128>(t2); pin(t1); pin(t2); }
                    if constexpr (z == 10 && g == 7) { t1 *= (1.0f / D_); pin(t1); t2 *= (1.0f / D_); pin(t2); }
                    if constexpr (z == 10 && g == 8) { t2 = fmaxf(t2 - t1 * t1, 0.f); pin(t2); }
                    if constexpr (z == 11 && g == 6) { rinv = __builtin_amdgcn_rsqf(t2 + 1e-5f); pin(rinv); }
                    if constexpr (z == 11 && g == 8) { mri = -t1 * rinv; pin(mri); }
                    if constexpr (!GW && s == 12 && g == 4) *(float*)(smem + l_rs(1, 0) + w * INBOX + lane * 4) = c < 8 ? rinv : mri;
                    if constexpr (GW && s == 19 && g == 1) selhi(rsel, mri);
                    if constexpr (GW && s == 19 && g == 0) { rsel = rinv; pin(rsel); }
                    if constexpr (GW && s == 19 && g == 2) *(float*)(smem + rsw) = rsel;
                    if constexpr (z >= 12 && z < 15 && g >= 6 && g < 9 && 3 * (z - 12) + g - 6 < 8) bc2b<3 * (z - 12) + g - 6>(rr_[3 * (z - 12) + g - 6], rm_[3 * (z - 12) + g - 6], rinv, mri);
                    if constexpr (!GW && s >= 15 && s < 19 && g >= 5) TT_GAP(5 * (s - 15) + g - 5)
                    if constexpr (GW && s >= 12 && s < 19 && g >= 6 && g < 9 && 3 * (s - 12) + g - 6 < 20) TT_GAP(3 * (s - 12) + g - 6)
                    site();
                    SCHED();
                });
            });
            rr[0] = f32x4{rr_[0], rr_[1], rr_[2], rr_[3]}; rr[1] = f32x4{rr_[4], rr_[5], rr_[6], rr_[7]};
            asm volatile("kpf_end2:");
            ACC_FENCE10(acc[0], acc[1]);
            STAMP(13)
        } else {
#pragma unroll
            for (int nb = 0; nb < (OTR ? 0 : 5); ++nb) __builtin_amdgcn_raw_buffer_store_b128(__builtin_bit_cast(v4u, xin[nb]), ro, oo + 64 * nb, 0, OUTP);
            STAMP(21)
            STAMP_DUMP()
            asm volatile("kpf_end:");
        }
    };
    layer_body(std::integral_constant<int, 0>{});
    layer_body(std::integral_constant<int, 1>{});
}

extern "C" void kernel_launch(void* const* d_in, const int* in_sizes, int n_in,
                              void* d_out, int out_size, void* d_ws, size_t ws_size,
                              hipStream_t stream) {
    Params p;
    p.x0 = (const u16*)d_in[0];
    p.Lre = (const u16*)d_in[1];
    p.Lim = (const u16*)d_in[2];
    p.Bre = (const u16*)d_in[3];
    p.Bim = (const u16*)d_in[4];
    p.Cre = (const u16*)d_in[5];
    p.Cim = (const u16*)d_in[6];
    p.logdt = (const u16*)d_in[7];
    p.Dskip = (const u16*)d_in[8];
    p.k = (const u16*)d_in[10];
    p.v = (const u16*)d_in[11];
    p.shk = (const u16*)d_in[12];
    p.ssmk = (const u16*)d_in[13];
    p.ssmD = (const u16*)d_in[14];
    p.ws = (char*)d_ws;
    p.out = (float*)d_out;
    k_main<<<256, 256, 0, stream>>>(p);
}
```

```cpp
#include <hip/hip_runtime.h>
#include <stdint.h>
#include <utility>

#define STAMPS 0

typedef unsigned short u16;
typedef unsigned long long u64;
typedef unsigned v4u __attribute__((ext_vector_type(4)));
typedef unsigned v2u __attribute__((ext_vector_type(2)));
typedef float f32x4 __attribute__((ext_vector_type(4)));
typedef float f32x2 __attribute__((ext_vector_type(2)));
typedef short s16x4 __attribute__((ext_vector_type(4)));
typedef __bf16 bf16x2v __attribute__((ext_vector_type(2)));
#define LAS __attribute__((address_space(3)))
#define DI __device__ __forceinline__

constexpr int S_ = 2048, D_ = 640;
#define KG0 12
#define KRT 12
#define KRS 2
#define KB1 4
#define KMB 1
#define SBT 2
#define LAG 1
#define OTR 1
#define XI 1
#define GW 0
#define POL 0
#define KR1 5
#define KR2 10
constexpr int RING1 = KR1;
#define KCR1 3
constexpr int kCR1 = KCR1;
#define KXA 10
#define KN3E 17
#define KN3 6
constexpr int kXA = KXA;
constexpr int kN3E = KN3E, kN3 = KN3;
#define KFHK 4
#define KFHP 12
constexpr int kFHK = KFHK, kFHP = KFHP;
#define KGPI 27
#define KGCI 33
#define KFGI 11
#define G2ND 1
#define KGSI 2
constexpr int kGSI = KGSI, kFGI = KFGI, kGPI = KGPI, kGCI = KGCI;

constexpr unsigned OFF_NONCE = 0;
constexpr unsigned OFF_CV = 1024;
constexpr unsigned OFF_ST = 4096;
constexpr unsigned OFF_FW = OFF_ST + 65536;
constexpr unsigned OFF_FK = OFF_FW + 131072;
constexpr unsigned OFF_KVT = OFF_FK + 81920;
constexpr unsigned RUN = 16384, PG = 4194304, MID = 1536;
constexpr unsigned OFF_SG = 8 * PG;
constexpr unsigned OFF_FH = OFF_SG + 32 * RUN;
constexpr unsigned OFF_FX = OFF_FH + 16 * RUN;
constexpr unsigned OFF_PS = OFF_FX + 8 * RUN;
constexpr unsigned OFF_EG = OFF_PS + 8 * RUN + MID;
constexpr unsigned OFF_X2F = 9 * PG + MID;
constexpr unsigned OFF_HF = 11 * PG + MID;
constexpr unsigned OFF_W2F = 19 * PG + MID;
constexpr unsigned OFF_GT = 23 * PG;
constexpr unsigned WS_BYTES = 25 * PG;
constexpr unsigned kso(int s) { return (unsigned)(s / 5) * RUN + (unsigned)(s % 5) * 1024u; }
constexpr unsigned kNone = 0x40000000u;
constexpr unsigned OFF_DBG = 0x1800000;
#define MAP 0
#define TILES() const int xq = b & 7, j = b >> 3, mt2 = MAP ? 8 * (xq >> 1) + (j >> 2) : 4 * xq + (j >> 3), nt2 = MAP ? 4 * (xq & 1) + (j & 3) : j & 7, mt1 = mt2 >> 1, \
    nt1 = MAP ? 8 * (xq & 1) + (j & 7) : j & 15, i32 = 16 * (mt2 & 1) + mt1;

constexpr int SC = 102400;
constexpr int REGSZ = 13056;
constexpr int L_XR = SC;
constexpr int L_CO = SC + 12288;
constexpr int L_KB = SC + 51712;
constexpr int L_TAB = SC + 55808;
constexpr int L_CW = SC + 39168;
constexpr int INBOX = 15360;
constexpr int l_rs(int l, int w) { return l ? SC + w * INBOX : L_KB + w * 256; }
constexpr int l_sg(int l, int w) { return l ? SC + w * INBOX + 256 : SC + (w == 0 ? 43264 : w == 1 ? 46080 : w == 2 ? 52736 : 56320); }
constexpr int l_tb(int l, int w) { return l ? SC + w * INBOX + 2048 : SC + 40960 + w * 5120; }
constexpr int LDS_BYTES = 163840;
constexpr int CV_E = 4608;
constexpr int CV_O = CV_E + (1056 + 15) * 4;

struct Params {
    const u16 *x0, *Lre, *Lim, *Bre, *Bim, *Cre, *Cim, *logdt, *Dskip, *k, *v, *shk, *ssmk, *ssmD;
    char* ws;
    float* out;
};

#define MF "v_mfma_f32_16x16x32_bf16 "
DI void mf(f32x4& c, v4u a, v4u b) { asm(MF "%0, %1, %2, %0" : "+v"(c) : "v"(a), "v"(b)); }
DI void mf0(f32x4& c, v4u a, v4u b) { asm(MF "%0, %1, %2, 0" : "=&v"(c) : "v"(a), "v"(b)); }
constexpr int W2R = 56;
#define WA(n) "a[%c" #n ":%c" #n "+3]"
template <int R> DI void mfa(f32x4& c, v4u a) { asm(MF "%0, " WA(2) ", %1, %0" : "+v"(c) : "v"(a), "i"(W2R + 4 * R)); }
template <int R> DI void mfa0(f32x4& c, v4u a) { asm(MF "%0, " WA(2) ", %1, 0" : "=&v"(c) : "v"(a), "i"(W2R + 4 * R)); }
#define ACC_FENCE10(c, d) asm volatile("" : "+v"(c[0]), "+v"(c[1]), "+v"(c[2]), "+v"(c[3]), "+v"(c[4]), "+v"(d[0]), "+v"(d[1]), "+v"(d[2]), "+v"(d[3]), "+v"(d[4]))
#define ACC_TIE5(c) asm volatile("" : "+v"(c[0]), "+v"(c[1]), "+v"(c[2]), "+v"(c[3]), "+v"(c[4]))
#define QP " quad_perm:[1,0,3,2] row_mask:0xf bank_mask:0xf"
DI void cstep(float (&t)[5], const float (&h)[5], const float (&lr)[5], const float (&ls)[5], float b0, float b1, float b2, float b3, float b4) {
    asm("v_fma_f32 %0, %10, %5, %20\n\tv_fma_f32 %1, %11, %6, %21\n\tv_fma_f32 %2, %12, %7, %22\n\tv_fma_f32 %3, %13, %8, %23\n\tv_fma_f32 %4, %14, %9, %24\n\t"
        "v_fmac_f32_dpp %0, %5, %15" QP "\n\tv_fmac_f32_dpp %1, %6, %16" QP "\n\tv_fmac_f32_dpp %2, %7, %17" QP "\n\tv_fmac_f32_dpp %3, %8, %18" QP "\n\tv_fmac_f32_dpp %4, %9, %19" QP
        : "=&v"(t[0]), "=&v"(t[1]), "=&v"(t[2]), "=&v"(t[3]), "=&v"(t[4])
        : "v"(h[0]), "v"(h[1]), "v"(h[2]), "v"(h[3]), "v"(h[4]), "v"(lr[0]), "v"(lr[1]), "v"(lr[2]), "v"(lr[3]), "v"(lr[4]),
          "v"(ls[0]), "v"(ls[1]), "v"(ls[2]), "v"(ls[3]), "v"(ls[4]), "v"(b0), "v"(b1), "v"(b2), "v"(b3), "v"(b4));
}
DI void cstep3(float* t, const float* h, const float* lr, const float* ls, const float* b) {
    asm("v_fma_f32 %0, %6, %3, %12\n\tv_fma_f32 %1, %7, %4, %13\n\tv_fma_f32 %2, %8, %5, %14\n\t"
        "v_fmac_f32_dpp %0, %3, %9" QP "\n\tv_fmac_f32_dpp %1, %4, %10" QP "\n\tv_fmac_f32_dpp %2, %5, %11" QP
        : "=&v"(t[0]), "=&v"(t[1]), "=&v"(t[2])
        : "v"(h[0]), "v"(h[1]), "v"(h[2]), "v"(lr[0]), "v"(lr[1]), "v"(lr[2]), "v"(ls[0]), "v"(ls[1]), "v"(ls[2]), "v"(b[0]), "v"(b[1]), "v"(b[2]));
}
template <int o> DI void chain24(float (*t)[3], const float (*b)[5], const float* lr, const float* ls) {
    asm("v_fma_f32 %0, %18, %16, %8\n\t" "v_fma_f32 %1, %19, %17, %9\n\t" "v_fmac_f32_dpp %0, %16, %20" QP "\n\t" "v_fmac_f32_dpp %1, %17, %21" QP "\n\t"
        "v_fma_f32 %2, %18, %0, %10\n\t" "v_fma_f32 %3, %19, %1, %11\n\t" "v_fmac_f32_dpp %2, %0, %20" QP "\n\t" "v_fmac_f32_dpp %3, %1, %21" QP "\n\t"
        "v_fma_f32 %4, %18, %2, %12\n\t" "v_fma_f32 %5, %19, %3, %13\n\t" "v_fmac_f32_dpp %4, %2, %20" QP "\n\t" "v_fmac_f32_dpp %5, %3, %21" QP "\n\t"
        "v_fma_f32 %6, %18, %4, %14\n\t" "v_fma_f32 %7, %19, %5, %15\n\t" "v_fmac_f32_dpp %6, %4, %20" QP "\n\t" "v_fmac_f32_dpp %7, %5, %21" QP "\n\t"
        : "=&v"(t[1][0]), "=&v"(t[1][1]), "=&v"(t[2][0]), "=&v"(t[2][1]), "=&v"(t[3][0]), "=&v"(t[3][1]), "=&v"(t[4][0]), "=&v"(t[4][1])
        : "v"(b[0][o + 0]), "v"(b[0][o + 1]), "v"(b[1][o + 0]), "v"(b[1][o + 1]), "v"(b[2][o + 0]), "v"(b[2][o + 1]), "v"(b[3][o + 0]), "v"(b[3][o + 1]), "v"(t[0][0]), "v"(t[0][1]), "v"(lr[o + 0]), "v"(lr[o + 1]), "v"(ls[o + 0]), "v"(ls[o + 1]));
}
template <int o> DI void chain33(float (*t)[3], const float (*b)[5], const float* lr, const float* ls) {
    asm("v_fma_f32 %0, %21, %18, %9\n\t" "v_fma_f32 %1, %22, %19, %10\n\t" "v_fma_f32 %2, %23, %20, %11\n\t" "v_fmac_f32_dpp %0, %18, %24" QP "\n\t" "v_fmac_f32_dpp %1, %19, %25" QP "\n\t" "v_fmac_f32_dpp %2, %20, %26" QP "\n\t"
        "v_fma_f32 %3, %21, %0, %12\n\t" "v_fma_f32 %4, %22, %1, %13\n\t" "v_fma_f32 %5, %23, %2, %14\n\t" "v_fmac_f32_dpp %3, %0, %24" QP "\n\t" "v_fmac_f32_dpp %4, %1, %25" QP "\n\t" "v_fmac_f32_dpp %5, %2, %26" QP "\n\t"
        "v_fma_f32 %6, %21, %3, %15\n\t" "v_fma_f32 %7, %22, %4, %16\n\t" "v_fma_f32 %8, %23, %5, %17\n\t" "v_fmac_f32_dpp %6, %3, %24" QP "\n\t" "v_fmac_f32_dpp %7, %4, %25" QP "\n\t" "v_fmac_f32_dpp %8, %5, %26" QP "\n\t"
        : "=&v"(t[1][0]), "=&v"(t[1][1]), "=&v"(t[1][2]), "=&v"(t[2][0]), "=&v"(t[2][1]), "=&v"(t[2][2]), "=&v"(t[3][0]), "=&v"(t[3][1]), "=&v"(t[3][2])
        : "v"(b[0][o + 0]), "v"(b[0][o + 1]), "v"(b[0][o + 2]), "v"(b[1][o + 0]), "v"(b[1][o + 1]), "v"(b[1][o + 2]), "v"(b[2][o + 0]), "v"(b[2][o + 1]), "v"(b[2][o + 2]), "v"(t[0][0]), "v"(t[0][1]), "v"(t[0][2]), "v"(lr[o + 0]), "v"(lr[o + 1]), "v"(lr[o + 2]), "v"(ls[o + 0]), "v"(ls[o + 1]), "v"(ls[o + 2]));
}
template <int o> DI void chain32(float (*t)[3], const float (*b)[5], const float* lr, const float* ls) {
    asm("v_fma_f32 %0, %15, %12, %6\n\t" "v_fma_f32 %1, %16, %13, %7\n\t" "v_fma_f32 %2, %17, %14, %8\n\t" "v_fmac_f32_dpp %0, %12, %18" QP "\n\t" "v_fmac_f32_dpp %1, %13, %19" QP "\n\t" "v_fmac_f32_dpp %2, %14, %20" QP "\n\t"
        "v_fma_f32 %3, %15, %0, %9\n\t" "v_fma_f32 %4, %16, %1, %10\n\t" "v_fma_f32 %5, %17, %2, %11\n\t" "v_fmac_f32_dpp %3, %0, %18" QP "\n\t" "v_fmac_f32_dpp %4, %1, %19" QP "\n\t" "v_fmac_f32_dpp %5, %2, %20" QP "\n\t"
        : "=&v"(t[1][0]), "=&v"(t[1][1]), "=&v"(t[1][2]), "=&v"(t[2][0]), "=&v"(t[2][1]), "=&v"(t[2][2])
        : "v"(b[0][o + 0]), "v"(b[0][o + 1]), "v"(b[0][o + 2]), "v"(b[1][o + 0]), "v"(b[1][o + 1]), "v"(b[1][o + 2]), "v"(t[0][0]), "v"(t[0][1]), "v"(t[0][2]), "v"(lr[o + 0]), "v"(lr[o + 1]), "v"(lr[o + 2]), "v"(ls[o + 0]), "v"(ls[o + 1]), "v"(ls[o + 2]));
}
DI void pstep(f32x2& xr, f32x2& xs, f32x2 pr, f32x2 ps, f32x2 m) {
    asm("v_pk_mul_f32 %0, %2, %4 op_sel_hi:[1,0]\n\tv_pk_mul_f32 %1, %3, %4 op_sel_hi:[1,0]\n\t"
        "v_pk_fma_f32 %0, %3, %4, %0 op_sel:[0,1,0] neg_lo:[1,0,0] neg_hi:[1,0,0]\n\tv_pk_fma_f32 %1, %2, %4, %1 op_sel:[0,1,0]"
        : "=&v"(xr), "=&v"(xs) : "v"(pr), "v"(ps), "v"(m));
}
DI void padd(f32x2 (&h)[4], const f32x2 (&pr)[2], const f32x2 (&ps)[2], f32x2 c) {
    f32x2 d;
    asm("v_pk_mul_f32 %4, %6, %9 op_sel:[1,0]\n\tv_pk_fma_f32 %0, %5, %9, %0 op_sel_hi:[1,0,1]\n\tv_pk_fma_f32 %1, %6, %9, %1 op_sel_hi:[1,0,1]\n\t"
        "v_pk_fma_f32 %4, %8, %9, %4 op_sel:[1,1,0] op_sel_hi:[1,0,1] neg_hi:[1,0,0]\n\tv_pk_fma_f32 %0, %7, %9, %0 op_sel:[0,1,0]\n\tv_pk_fma_f32 %1, %8, %9, %1 op_sel:[0,1,0]\n\t"
        "v_pk_fma_f32 %2, %5, %4, %2 op_sel_hi:[1,0,1]\n\tv_pk_fma_f32 %3, %6, %4, %3 op_sel_hi:[1,0,1]\n\tv_pk_fma_f32 %2, %7, %4, %2 op_sel:[0,1,0]\n\tv_pk_fma_f32 %3, %8, %4, %3 op_sel:[0,1,0]"
        : "+v"(h[0]), "+v"(h[1]), "+v"(h[2]), "+v"(h[3]), "=&v"(d) : "v"(pr[0]), "v"(pr[1]), "v"(ps[0]), "v"(ps[1]), "v"(c));
}
DI void cstepi(float& b0, float& b1, float& b2, float& b3, float& b4, float h0, float h1, float h2, float h3, float h4, const float (&lr)[5], const float (&ls)[5]) {
    asm("v_fmac_f32 %0, %10, %5\n\tv_fmac_f32 %1, %11, %6\n\tv_fmac_f32 %2, %12, %7\n\tv_fmac_f32 %3, %13, %8\n\tv_fmac_f32 %4, %14, %9\n\t"
        "v_fmac_f32_dpp %0, %5, %15" QP "\n\tv_fmac_f32_dpp %1, %6, %16" QP "\n\tv_fmac_f32_dpp %2, %7, %17" QP "\n\tv_fmac_f32_dpp %3, %8, %18" QP "\n\tv_fmac_f32_dpp %4, %9, %19" QP
        : "+v"(b0), "+v"(b1), "+v"(b2), "+v"(b3), "+v"(b4)
        : "v"(h0), "v"(h1), "v"(h2), "v"(h3), "v"(h4), "v"(lr[0]), "v"(lr[1]), "v"(lr[2]), "v"(lr[3]), "v"(lr[4]), "v"(ls[0]), "v"(ls[1]), "v"(ls[2]), "v"(ls[3]), "v"(ls[4]));
}
#define UNDEF8(c) asm volatile("" : "=v"(c[0]), "=v"(c[1]), "=v"(c[2]), "=v"(c[3]), "=v"(c[4]), "=v"(c[5]), "=v"(c[6]), "=v"(c[7]))
#define PIN5(t) asm volatile("s_nop 1" : "+v"(t[0]), "+v"(t[1]), "+v"(t[2]), "+v"(t[3]), "+v"(t[4]))
DI unsigned cvtpk(float lo, float hi) {
    f32x2 v = {lo, hi};
    return __builtin_bit_cast(unsigned, __builtin_convertvector(v, bf16x2v));
}
DI float lo16f(unsigned u) { return __uint_as_float(u << 16); }
DI float hi16f(unsigned u) { return __uint_as_float(u & 0xffff0000u); }
DI float rbf(float v) { return lo16f(cvtpk(v, 0.f)); }
template <int CTRL> DI float dpp(float x) {
    return __int_as_float(__builtin_amdgcn_update_dpp(0, __float_as_int(x), CTRL, 0xF, 0xF, true));
}
DI float rowsum16(float x) {
    x += dpp<0x128>(x); x += dpp<0x124>(x); x += dpp<0x122>(x); x += dpp<0x121>(x);
    return x;
}
DI float xsum32(float x) { auto r = __builtin_amdgcn_permlane32_swap(__float_as_uint(x), __float_as_uint(x), false, false); return __uint_as_float(r[0]) + __uint_as_float(r[1]); }
DI void xsum2(float& a, float& d) {
    auto r = __builtin_amdgcn_permlane16_swap(__float_as_uint(a), __float_as_uint(d), false, false);
    const float s = __uint_as_float(r[0]) + __uint_as_float(r[1]);
    const float t = xsum32(s);
    auto u = __builtin_amdgcn_permlane16_swap(__float_as_uint(t), __float_as_uint(t), false, false);
    a = __uint_as_float(u[0]); d = __uint_as_float(u[1]);
}
DI float xsum2h(float a, float d) {
    auto r = __builtin_amdgcn_permlane16_swap(__float_as_uint(a), __float_as_uint(d), false, false);
    return xsum32(__uint_as_float(r[0]) + __uint_as_float(r[1]));
}
DI float hsum4(f32x4 v) { return (v.x + v.y) + (v.z + v.w); }
typedef __amdgpu_buffer_rsrc_t rsrc_t;
constexpr int SC1 = 16;
DI v4u ld128(rsrc_t rs, unsigned off) { return __builtin_amdgcn_raw_buffer_load_b128(rs, off, 0, SC1); }
DI v4u ld128s(rsrc_t rs, unsigned off, unsigned soff) { return __builtin_amdgcn_raw_buffer_load_b128(rs, off, soff, SC1); }
DI v2u ld64(rsrc_t rs, unsigned off) { return __builtin_amdgcn_raw_buffer_load_b64(rs, off, 0, SC1); }
DI void st128(v4u v, rsrc_t rs, unsigned off) { __builtin_amdgcn_raw_buffer_store_b128(v, rs, off, 0, SC1); }
DI void st64(v2u v, rsrc_t rs, unsigned off) { __builtin_amdgcn_raw_buffer_store_b64(v, rs, off, 0, SC1); }
#define LGKM0() { __builtin_amdgcn_s_waitcnt(0xC07F); asm volatile("" ::: "memory"); }
#define DRAIN() asm volatile("s_waitcnt vmcnt(0)" ::: "memory")
#define ORDER() __builtin_amdgcn_fence(__ATOMIC_ACQUIRE, "wavefront")
#define SCHED() __builtin_amdgcn_sched_barrier(0)
constexpr unsigned kSpinCap = 1u << 17;
#define WAVE_WAIT(...)                                                      \
    {                                                                       \
        unsigned spins_ = dead ? kSpinCap : 0u;                             \
        _Pragma("nounroll") for (;;) {                                      \
            asm volatile("" ::: "memory");                                  \
            bool ok;                                                        \
            __VA_ARGS__;                                                    \
            if (__builtin_amdgcn_ballot_w64(!ok) == 0ull) break;            \
            if (++spins_ > kSpinCap) { dead = true; break; }                \
            __builtin_amdgcn_s_sleep(1);                                    \
        }                                                                   \
        ORDER();                                                            \
    }
#define KPD1 1
#define KPDS 0
#define KPDS0 0
#define KPF0 11
#define KPFX 3
#define KPF1 3
constexpr int PD1 = KPD1, PDS = KPDS, PDS0 = KPDS0;
#define PL(r) "global_load_dwordx2 " r ", %5, %6 sc1\n\t"
#define PS_(r) "s_waitcnt vmcnt(1)\n\tv_cmp_ne_u64 vcc, %7, " r "\n\ts_cbranch_vccz 2f\n\t" PL(r)
template <int D0> DI void pipe_flag(v2u (&t)[4], unsigned off, const void* base, u64 tag, bool& dead) {
    unsigned n = dead ? 0u : kSpinCap;
    asm volatile("s_sleep %8\n\t" PL("%0") "s_sleep %9\n\t" PL("%1")
                 "1:\n\t" PS_("%0") PS_("%1") "s_sub_u32 %4, %4, 1\n\ts_cbranch_scc0 1b\n2:"
                 : "=&v"(t[0]), "=&v"(t[1]), "=&v"(t[2]), "=&v"(t[3]), "+s"(n) : "v"(off), "s"(base), "s"(tag), "i"(D0), "i"(KPF1) : "memory", "vcc", "scc");
    dead = __builtin_amdgcn_readfirstlane(n) == ~0u;
}
#define WAVE_RETRY(OK, ...) { if (__builtin_expect(__builtin_amdgcn_ballot_w64(!(OK)) != 0ull, 0)) WAVE_WAIT({ __VA_ARGS__; ok = OK; }) ORDER(); }
#define STAMP(i)
#define STAMP_DUMP()

#define KLB0 0
#define KLB1 12
DI void site() { asm volatile(""); }
template <class F, int... I> DI void static_for_impl(std::integer_sequence<int, I...>, F&& f) { (f(std::integral_constant<int, I>{}), ...); }
template <int N, class F> DI void static_for(F&& f) { static_for_impl(std::make_integer_sequence<int, N>{}, f); }
#define IC(n) std::integral_constant<int, (n)>{}
#define LAMBDA(v) [&](auto v) __attribute__((always_inline))
#define PK2 " op_sel:[1,1,0] op_sel_hi:[1,0,1] neg_lo:[1,0,0]"
DI f32x2 cmul(f32x2 a, f32x2 b) {
    f32x2 t;
    asm("v_pk_mul_f32 %0, %1, %2 op_sel_hi:[0,1]\n\ts_nop 0\n\tv_pk_fma_f32 %0, %1, %2, %0" PK2 "\n\ts_nop 0" : "=&v"(t) : "v"(a), "v"(b));
    return t;
}
DI f32x2 cfma(f32x2 a, f32x2 b, f32x2 c) {
    f32x2 t;
    asm("v_pk_fma_f32 %0, %1, %2, %3 op_sel_hi:[0,1,1]\n\ts_nop 0\n\tv_pk_fma_f32 %0, %1, %2, %0" PK2 "\n\ts_nop 0" : "=&v"(t) : "v"(a), "v"(b), "v"(c));
    return t;
}
DI void cfma4(f32x2 (&t)[4], f32x2 a, f32x2 b0, f32x2 b1, f32x2 b2, f32x2 b3, f32x2 c0, f32x2 c1, f32x2 c2, f32x2 c3) {
    asm("v_pk_fma_f32 %0, %4, %5, %9 op_sel_hi:[0,1,1]\n\tv_pk_fma_f32 %1, %4, %6, %10 op_sel_hi:[0,1,1]\n\tv_pk_fma_f32 %2, %4, %7, %11 op_sel_hi:[0,1,1]\n\tv_pk_fma_f32 %3, %4, %8, %12 op_sel_hi:[0,1,1]\n\t"
        "v_pk_fma_f32 %0, %4, %5, %0" PK2 "\n\tv_pk_fma_f32 %1, %4, %6, %1" PK2 "\n\tv_pk_fma_f32 %2, %4, %7, %2" PK2 "\n\tv_pk_fma_f32 %3, %4, %8, %3" PK2 "\n\ts_nop 0"
        : "=&v"(t[0]), "=&v"(t[1]), "=&v"(t[2]), "=&v"(t[3]) : "v"(a), "v"(b0), "v"(b1), "v"(b2), "v"(b3), "v"(c0), "v"(c1), "v"(c2), "v"(c3));
}
DI void gacc2(f32x2& h0, f32x2& h1, f32x2 w0, f32x2 w1, v4u e0, v4u e1) {
    const v2u a0 = {e0.x, e0.y}, b0 = {e0.z, e0.w}, a1 = {e1.x, e1.y}, b1 = {e1.z, e1.w};
    asm("v_pk_fma_f32 %0, %4, %2, %0 op_sel_hi:[0,1,1]\n\tv_pk_fma_f32 %1, %6, %3, %1 op_sel_hi:[0,1,1]\n\t"
        "v_pk_fma_f32 %0, %5, %2, %0 op_sel:[0,1,0] op_sel_hi:[0,0,1] neg_lo:[1,0,0]\n\tv_pk_fma_f32 %1, %7, %3, %1 op_sel:[0,1,0] op_sel_hi:[0,0,1] neg_lo:[1,0,0]"
        : "+v"(h0), "+v"(h1) : "v"(w0), "v"(w1), "v"(a0), "v"(b0), "v"(a1), "v"(b1));
}
DI s16x4 trread(char* smem, int byteoff) {
    return __builtin_amdgcn_ds_read_tr16_b64_v4i16((LAS s16x4*)(smem + byteoff));
}

#define CV_LOAD_A(A, a, rr_, hf) { const unsigned* ap_ = (const unsigned*)(smem + ab[(a) >> 1] + (((a) & 1) ? 0 : 512) + 64 * (7 - (rr_)) + 8 * (hf)); A[a][2 * (hf)] = ap_[0]; A[a][2 * (hf) + 1] = ap_[1]; }
#define CV_LOAD_B(B, e, rr_) { B[e] = *(const v4u*)(smem + bb + 64 * (7 - (rr_)) + 512 * (e)); }
constexpr int cv_pair(int lo, int hi, int n) {
    for (int a = 0; a < 8; ++a)
        for (int e = 0; e < 8; ++e)
            if (a + e >= lo && a + e < hi && n-- == 0) return 8 * a + e;
    return -1;
}
template <int R, int PF, bool OWN, class F>
DI void conv_round(f32x4 (&ca)[8], v4u (&A)[8], const v4u (&B)[8], v4u (&An)[8], v4u (&Bn)[8], const char* smem, const int (&ab)[4], int bb, F&& fill) {
    static_for<(R == 7 ? KG0 : 36)>(LAMBDA(g_) {
        constexpr int g = g_.value, a = cv_pair(0, 8, g) / 8, e = cv_pair(0, 8, g) % 8;
        mf(ca[a + e], A[a], B[e]);
        SCHED();
        if constexpr (OWN && g < 12) { CV_LOAD_A(A, 2 + g / 2, R, g & 1) SCHED(); }
        if constexpr (g < 4 * PF && (g & 3) != 3) { if constexpr ((g & 3) == 2) CV_LOAD_B(Bn, g >> 2, R + 1) else CV_LOAD_A(An, g >> 2, R + 1, g & 1) SCHED(); }
        site();
        if constexpr (R == 7) SCHED();
        if constexpr ((g & 3) == 3 && g < 28) { fill(std::integral_constant<int, 7 * R + g / 4>{}); SCHED(); }
    });
}
template <int R0, int R1, class F, class M>
DI void conv_part(f32x4 (&ca)[8], const char* smem, const int (&ab)[4], int bb, F&& fill, M&& mid, v4u (&A)[2][8], v4u (&B)[2][8]) {
    if constexpr (R0 > 0) { CV_LOAD_A(A[0], 0, R0, 0) CV_LOAD_A(A[0], 0, R0, 1) SCHED(); }
    static_for<8>(LAMBDA(a_) { constexpr int a = a_.value; CV_LOAD_B(B[0], a, R0) SCHED(); });
    if constexpr (R0 > 0) { CV_LOAD_A(A[0], 1, R0, 0) CV_LOAD_A(A[0], 1, R0, 1) SCHED(); }
    else {
        CV_LOAD_A(A[0], 0, 0, 0) CV_LOAD_A(A[0], 0, 0, 1) SCHED();
        const v4u ad = mid();
        SCHED();
        static_for<8>(LAMBDA(e_) {
            constexpr int e = e_.value;
            mf0(ca[e], ad, B[0][e]); SCHED();
            if constexpr (e < 7) { CV_LOAD_A(A[0], e + 1, 0, 0) CV_LOAD_A(A[0], e + 1, 0, 1) SCHED(); }
        });
    }
    static_for<R1 - R0>(LAMBDA(r_) {
        constexpr int r = r_.value;
        conv_round<R0 + r, (R0 + r + 1 < R1 ? 8 : 0), (r == 0 && R0 > 0)>(ca, A[r & 1], B[r & 1], A[(r & 1) ^ 1], B[(r & 1) ^ 1], smem, ab, bb, fill);
    });
}
template <int IMM> DI void gld(v4u& dst, unsigned voff, const void* sbase) {
    asm volatile("global_load_dwordx4 %0, %1, %2 offset:%3" : "=v"(dst) : "v"(voff), "s"(sbase), "i"(IMM));
}
#define OUTP 16
#define NTM 31
template <int IMM, int BIT> DI void gldn(v4u& dst, unsigned voff, const void* sbase) {
    if constexpr ((NTM >> BIT) & 1) asm volatile("global_load_dwordx4 %0, %1, %2 offset:%3 nt" : "=v"(dst) : "v"(voff), "s"(sbase), "i"(IMM));
    else gld<IMM>(dst, voff, sbase);
}
DI void gldu16(unsigned& dst, unsigned voff, const void* sbase) { asm volatile("global_load_ushort %0, %1, %2" : "=v"(dst) : "v"(voff), "s"(sbase)); }
template <int IMM> DI void gld64(v4u& dst, const void* a) {
    if constexpr ((NTM >> 5) & 1) asm volatile("global_load_dwordx4 %0, %1, off offset:%2 nt" : "=v"(dst) : "v"(a), "i"(IMM));
    else asm volatile("global_load_dwordx4 %0, %1, off offset:%2" : "=v"(dst) : "v"(a), "i"(IMM));
}
template <int V> DI void SETM0(unsigned ldsbase) { asm volatile("s_add_u32 m0, %0, %1" ::"s"(ldsbase), "i"(V)); }
template <int GOFF> DI void GLD0(unsigned voff, const char* sbase) { asm volatile("global_load_lds_dwordx4 %0, %1 offset:%2" ::"v"(voff), "s"(sbase), "i"(GOFF) : "memory"); }
template <int GOFF, int V> DI void GLD1(unsigned voff, const char* sbase, unsigned ldsbase) {
    asm volatile("global_load_lds_dwordx4 %0, %1 offset:%3\n\ts_add_u32 m0, %2, %4" ::"v"(voff), "s"(sbase), "s"(ldsbase), "i"(GOFF), "i"(V) : "memory");
}
constexpr int dma_goff(int it) { return it < 40 ? (it >> 2) * 128 : ((it - 100) / 5) * 256; }
constexpr int dma_loff(int it) { return it < 40 ? L_XR + ((it >> 2) % 3) * 4096 + (it & 3) * 1024 : ((it - 100) % 5) * 20480 + ((it - 100) / 5) * 2048; }
#define KFD0 10
#define KNF 13
#define KNW 11
#define KW2S 17
#define KW2M 0x4A
#define KFWP 14
constexpr int kW2S = KW2S, kW2M = KW2M;
constexpr int NW2P = (20 - kW2S) * __builtin_popcount(kW2M);
constexpr int P1_PRE = 36;
constexpr int p1_pre(int i) { return i < 5 ? 100 + i : i < 13 ? i - 5 : i < 18 ? 105 + (i - 13) : i < 22 ? 8 + (i - 18) : i < 27 ? 110 + (i - 22) : 400; }
constexpr int p1_item(int s, int g) {
    if (s == 7 && g == 0) return 324;
    if (g == 9) return s < 10 ? 115 + s : s == 11 ? 202 : s == 12 ? 203 : -1;
    if (s == KFWP && g == 1) return 208;
    if (s >= kW2S && ((kW2M >> g) & 1)) return 500 + (s - kW2S) * __builtin_popcount(kW2M) + __builtin_popcount(kW2M & ((1 << g) - 1));
    if ((s & 1) == 0 && s <= 12) return g >= 5 ? 4 * (s / 2 + 3) + (g - 5) : -1;
    if (s == 14 && g == 5) return 207;
    return -1;
}
constexpr int p1_cnt(int s, int g) {
    int n = P1_PRE;
    for (int t = 0; t <= s; ++t)
        for (int u = 0; u < 10; ++u) {
            if (t == s && u >= g) break;
            if (p1_item(t, u) >= 0 && p1_item(t, u) != 319) ++n;
        }
    return n;
}
constexpr int p1_pos(int code) {
    for (int i = 0; i < P1_PRE; ++i) if (p1_pre(i) == code) return i;
    for (int t = 0; t < 20; ++t)
        for (int u = 0; u < 10; ++u) if (p1_item(t, u) == code) return p1_cnt(t, u);
    return -1000;
}
constexpr int p1_cap(int s, int g) {
    int u = (s < 19 && g < 5) + (g == 9 && s < 19);
    if ((s & 1) && s < 19 && g >= 5 && g < 9) u += 1 + (g == 5);
    if ((s & 1) == 0 && g == 4) u += 1;
    const int it = p1_item(s, g), nt = g < 9 ? p1_item(s, g + 1) : -1;
    u += (it >= 0 && it < 500 && it != 208) + (nt >= 0 && nt < 200);
    if ((s == 18 && g == 1) || (s == 19 && g == 2) || (g == 9 && ((s & 3) == 2 || s == 17))) u += 2;
    return u < 2 ? 2 - u : 0;
}
constexpr int p1_slot(int s0, int s, int g, int gmax) {
    int n = 0;
    for (int t = s0; t <= s; ++t)
        for (int u = 0; u < gmax && !(t == s && u >= g); ++u) n += p1_cap(t, u);
    return n;
}
constexpr int p1_lim(int n) { return n > 63 ? 63 : n; }
template <int IMM> DI void gld4s(v4u& dst, unsigned voff, const void* sbase) { asm volatile("global_load_dwordx4 %0, %1, %2 offset:%3 sc1" : "=v"(dst) : "v"(voff), "s"(sbase), "i"(IMM)); }
template <int IMM> DI void gld2s(v2u& dst, unsigned voff, const void* sbase) { asm volatile("global_load_dwordx2 %0, %1, %2 offset:%3 sc1" : "=v"(dst) : "v"(voff), "s"(sbase), "i"(IMM)); }
template <class T> DI void pin(T& v) { asm volatile("" : "+v"(v)); }
DI void mul2(float& a, float& b, float x, float y, float s) { asm volatile("v_mul_f32 %0, %2, %4\n\tv_mul_f32 %1, %3, %4" : "=&v"(a), "=&v"(b) : "v"(x), "v"(y), "v"(s)); }
DI void rot1(unsigned& a, unsigned x) { asm volatile("v_alignbit_b32 %0, %1, %1, 16" : "=v"(a) : "v"(x)); }
DI void mix1(unsigned& a, unsigned x, unsigned y) { asm volatile("v_bfi_b32 %0, %3, %1, %2" : "=v"(a) : "v"(x), "v"(y), "s"(0xffffu)); }
DI void ror1(unsigned& o, unsigned prev) { asm volatile("v_mov_b32_dpp %0, %1 wave_ror:1 row_mask:0xf bank_mask:0xf" : "=v"(o) : "v"(prev)); }
DI void shr1b(unsigned& o, unsigned x) { asm volatile("v_mov_b32_dpp %0, %1 wave_shr:1 row_mask:0xf bank_mask:0xf" : "+v"(o) : "v"(x)); }
DI void xor1(float& a, unsigned x, unsigned s) { asm volatile("v_xor_b32 %0, %1, %2" : "=v"(a) : "v"(x), "v"(s)); }
template <int G> DI void bc1(float& a, unsigned x) { asm volatile("v_mov_b32_dpp %0, %1 row_newbcast:%2 row_mask:0xf bank_mask:0xf" : "=v"(a) : "v"(x), "i"(G)); }
template <int G> DI void mulb(float& o, unsigned x, float y) { asm volatile("v_mul_f32_dpp %0, %1, %2 row_newbcast:%3 row_mask:0xf bank_mask:0xf" : "=v"(o) : "v"(x), "v"(y), "i"(G)); }
template <int N> DI void lgkm() { __builtin_amdgcn_s_waitcnt(0xC07F | (N << 8)); asm volatile("" ::: "memory"); }
DI void unp2(float& a, float& b, unsigned x) { asm volatile("v_lshlrev_b32 %0, 16, %2\n\tv_and_b32 %1, 0xffff0000, %2" : "=&v"(a), "=&v"(b) : "v"(x)); }
#define GL(a) "global_load_dwordx4 a[" #a ":" #a "+3], %7, %8 sc1\n\tglobal_load_dwordx4 a[" #a "+4:" #a "+7], %7, %8 offset:256 sc1\n\t"
#define AR(d, a, o) "v_accvgpr_read_b32 " d ", a[" #a "+" #o "]\n\t"
#define GS_(a) "s_waitcnt vmcnt(4)\n\t" AR("%2", a, 1) AR("%3", a, 3) AR("%4", a, 5) AR("%5", a, 7) AR("%0", a, 0) AR("%1", a, 2)                              \
    "v_xor_b32 %2, %9, %2\n\tv_bitop3_b32 %4, %4, %9, %2 bitop3:0xbe\n\tv_bitop3_b32 %4, %3, %9, %4 bitop3:0xbe\n\tv_bitop3_b32 %4, %5, %9, %4 bitop3:0xbe\n\t" \
    AR("%2", a, 4) AR("%3", a, 6) "v_cmp_ne_u32 vcc, 0, %4\n\ts_cbranch_vccz 2f\n\t" GL(a)
template <int D0> DI void pipe_gran(float& a, float& d, unsigned off, const void* base, unsigned tag, bool& dead) {
    unsigned n = dead ? 0u : kSpinCap, y, z;
    float a1, d1;
    asm volatile("s_sleep %10\n\t" GL(32) "s_sleep %11\n\t" GL(40) "s_sleep %11\n\t" GL(48)
                 "1:\n\t" GS_(32) GS_(40) GS_(48) "s_sub_u32 %6, %6, 1\n\ts_cbranch_scc0 1b\n2:"
                 : "=&v"(a), "=&v"(d), "=&v"(a1), "=&v"(d1), "=&v"(y), "=&v"(z), "+s"(n) : "v"(off), "s"(base), "s"(tag), "i"(D0), "i"(PD1) : "memory", "vcc", "scc");
    dead = __builtin_amdgcn_readfirstlane(n) == ~0u;
    a += a1; d += d1;
}
DI void fma2(float& a, float& b, float x, float y, float s, float t, float u, float v) {
    asm volatile("v_fma_f32 %0, %2, %4, %6\n\tv_fma_f32 %1, %3, %5, %7" : "=&v"(a), "=&v"(b) : "v"(x), "v"(y), "v"(s), "v"(t), "v"(u), "v"(v));
}
DI void selhi(float& a, float x) { asm volatile("v_mov_b32_dpp %0, %1 quad_perm:[0,1,2,3] row_mask:0xf bank_mask:0xc" : "+v"(a) : "v"(x)); }
DI void cvt2(unsigned& a, unsigned& b, f32x4 v) { asm volatile("v_cvt_pk_bf16_f32 %0, %2, %3\n\tv_cvt_pk_bf16_f32 %1, %4, %5" : "=&v"(a), "=&v"(b) : "v"(v.x), "v"(v.y), "v"(v.z), "v"(v.w)); }
DI void swp2n(unsigned& a, unsigned& b, unsigned& c, unsigned& d) { asm volatile("v_permlane16_swap_b32 %0, %1\n\tv_permlane16_swap_b32 %2, %3" : "+v"(a), "+v"(b), "+v"(c), "+v"(d)); }
template <int G> DI void bc2b(float& a, float& b, float x, float y) {
    asm volatile("v_mov_b32_dpp %0, %2 row_newbcast:%4 row_mask:0xf bank_mask:0xf\n\tv_mov_b32_dpp %1, %3 row_newbcast:%4 row_mask:0xf bank_mask:0xf" : "=&v"(a), "=&v"(b) : "v"(x), "v"(y), "i"(G));
}
template <class T> DI void pins(T& v) { asm volatile("" : "+s"(v)); }
template <int I> DI void gld_w2(unsigned voff, const void* sbase) {
    asm volatile("global_load_dwordx4 " WA(2) ", %0, %1 offset:%3 sc1" ::"v"(voff), "s"(sbase), "i"(W2R + 4 * I), "i"((I % 5) * 1024 - 2048));
}
DI void undef4(v4u (&x)[4]) { asm volatile("" : "=v"(x[0]), "=v"(x[1]), "=v"(x[2]), "=v"(x[3])); }
DI void wait1(v2u& v) { asm volatile("s_waitcnt vmcnt(0)" : "+v"(v)::"memory"); }
DI void ld_ur(unsigned o, const void* base) {
    asm volatile("global_load_dwordx4 a[40:40+3], %0, %1 sc1\n\tglobal_load_dwordx4 a[44:44+3], %0, %1 offset:1024 sc1\n\t"
                 "global_load_dwordx4 a[48:48+3], %0, %1 offset:2048 sc1\n\tglobal_load_dwordx4 a[52:52+3], %0, %1 offset:3072 sc1" ::"v"(o), "s"(base));
}
DI void st_ur(unsigned a) {
    asm volatile("s_waitcnt vmcnt(%1)\n\tds_write_b128 %0, a[40:40+3] offset:480\n\tds_write_b128 %0, a[44:44+3] offset:1504\n\t"
                 "ds_write_b128 %0, a[48:48+3] offset:2528\n\tds_write_b128 %0, a[52:52+3] offset:3552" ::"v"(a), "i"(2 * __builtin_popcount(kW2M)) : "memory");
}
template <int A, int IMM> DI void lda4(unsigned o, const void* base) { asm volatile("global_load_dwordx4 a[%c2:%c2+3], %0, %1 offset:%3" ::"v"(o), "s"(base), "i"(A), "i"(IMM)); }
template <int A, int IMM> DI void lda2(unsigned o, const void* base) { asm volatile("global_load_dwordx2 a[%c2:%c2+1], %0, %1 offset:%3" ::"v"(o), "s"(base), "i"(A), "i"(IMM)); }
template <int A> DI void rda(unsigned& v) { asm volatile("v_accvgpr_read_b32 %0, a[%c1+0]" : "=v"(v) : "i"(A)); }
DI void poll1(unsigned a, const void* base) { asm volatile("global_load_dwordx2 a[36:36+1], %0, %1 sc1" ::"v"(a), "s"(base)); }
DI void poll2(unsigned a, unsigned b, const void* base) { asm volatile("global_load_dwordx2 a[36:36+1], %0, %2 sc1\n\tglobal_load_dwordx2 a[36+2:36+3], %1, %2 sc1" ::"v"(a), "v"(b), "s"(base)); }
DI void touch_(unsigned a, const void* base) { asm volatile("global_load_dword a[32+1], %0, %1" ::"v"(a), "s"(base)); }
template <int N> DI void wait_st(v2u& a, v4u& b) { asm volatile("s_waitcnt vmcnt(%2)" : "+v"(a), "+v"(b) : "i"(N) : "memory"); }
DI void undef3(v4u (&x)[3]) { asm volatile("" : "=v"(x[0]), "=v"(x[1]), "=v"(x[2])); }
template <int IMM> DI void gld4t(v4u& dst, unsigned voff, const void* sbase) { asm volatile("global_load_dwordx4 %0, %1, %2 offset:%3 sc1" : "+v"(dst) : "v"(voff), "s"(sbase), "i"(IMM)); }
template <int N> DI void vm_wait() { asm volatile("s_waitcnt vmcnt(%0)" ::"i"(N) : "memory"); }
DI float dot2a(unsigned a, unsigned b, float c) {
    return __builtin_amdgcn_fdot2_f32_bf16(__builtin_bit_cast(bf16x2v, a), __builtin_bit_cast(bf16x2v, b), c, false);
}
DI void disc(unsigned l0, unsigned l1, unsigned l2, f32x2& lam, f32x2& co) {
    const float dt = rbf(__builtin_amdgcn_exp2f(lo16f(l2) * 1.44269504088896f));
    const float lre = lo16f(l0), lim = lo16f(l1);
    const float e_ = __builtin_amdgcn_exp2f(dt * lre * 1.44269504088896f);
    const float rv = __builtin_amdgcn_fractf(dt * lim * 0.159154943091895f);
    lam = f32x2{e_ * __builtin_amdgcn_cosf(rv), e_ * __builtin_amdgcn_sinf(rv)};
    const float nr = lam.x - 1.0f, ni = lam.y, id = __builtin_amdgcn_rcpf(lre * lre + lim * lim);
    co = f32x2{(nr * lre + ni * lim) * id, (ni * lre - nr * lim) * id};
}
DI f32x4 unpk4(v2u u) { return f32x4{lo16f(u.x), hi16f(u.x), lo16f(u.y), hi16f(u.y)}; }
#define D2 "v_dot2_f32_bf16 "
#define CP "v_cvt_pk_bf16_f32 "
DI void d2x4(unsigned* o, unsigned a0, unsigned a1, unsigned a2, unsigned a3, unsigned b0, unsigned b1, unsigned b2, unsigned b3) {
    asm volatile(D2 "%0, %4, %8, 0\n\t" D2 "%1, %5, %9, 0\n\t" D2 "%2, %6, %10, 0\n\t" D2 "%3, %7, %11, 0"
                 : "=&v"(o[0]), "=&v"(o[1]), "=&v"(o[2]), "=&v"(o[3]) : "v"(a0), "v"(a1), "v"(a2), "v"(a3), "v"(b0), "v"(b1), "v"(b2), "v"(b3));
}

__global__ __launch_bounds__(256, 1) __attribute__((aligned(4096))) void k_main(Params p) {
    __shared__ __attribute__((aligned(16))) char smem[LDS_BYTES];
    p.ws += (0u - (unsigned)(size_t)p.ws) & 0x1FFFFFu;
    const rsrc_t rs = __builtin_amdgcn_make_buffer_rsrc((void*)p.ws, 0, WS_BYTES, 0x00020000);
    const int w0 = __builtin_amdgcn_readfirstlane(threadIdx.x >> 6), b0 = blockIdx.x;
    unsigned pol, sdo;
    const char* wl;
    rsrc_t rl;
    const rsrc_t ro = __builtin_amdgcn_make_buffer_rsrc((void*)p.out, 0, S_ * D_ * 4, 0x00020000);
    bool dead = false;
    asm volatile("" ::: "a255");
    float SW[5];
    f32x4 xin[5];
    v2u gt[5], dsk[5];
    f32x4 acc[2][5];
    f32x4 rr[2];
    float tt[2][5][4];
    float lr[5], ls[5], lqr[5], lqs[5];
    f32x2 l8, l32, l128;
    bool kearly;
    unsigned sdq;
    int sgn_, ab[4], bb, zpa, uwa, spa, tbw, tbr, cwa, spl, dst[3];
    unsigned sgo, sgs, glo, gso, fgo, fgo1;
    int srca;
    unsigned adm, dso, w2o, wro, fwo, xso, fho1, fho, hso, eo, es, ho[4];
    bool cv0;
    int mt1_;
    auto addrs = [&](auto layer_, int lane, int w, int b) __attribute__((always_inline)) {
        constexpr int layer = layer_.value;
        const int c = lane & 15, q = lane >> 4;
        TILES()
        static_assert(l_sg(0, 0) == SC + 0xA900 && l_sg(0, 1) == SC + 0xB400 && l_sg(0, 2) == SC + 0xCE00 && l_sg(0, 3) == SC + 0xDC00, "");
        sgn_ = (layer ? l_sg(1, 0) + w * INBOX : SC + (int)((0xDC00CE00B400A900ull >> (16 * w)) & 0xffff)) + q * 320 + c * 4;
        const int pl = lane < 40 ? lane : 39, tbb = l_tb(layer, 0) + w * (layer ? INBOX : 5120);
        spl = SC + pl * 8;
        eo = OFF_EG + (unsigned)((layer * 2 + (nt1 >> 3)) * 16 * RUN + (nt1 & 7) * 640 + pl * 16);
        es = eo + (unsigned)mt1 * RUN;
        spa = l_rs(layer, 0) + (2 * (mt2 & 1) + (w >> 1)) * (layer ? INBOX : 256) + (2 * (w & 1) + (c >> 3)) * 64 + (c & 7) * 4;
        tbw = tbb + c * 64 + ((q ^ ((c >> 1) & 3)) * 16);
        tbr = tbb + (2 * (c >> 2) + (q & 1)) * 64 + (((2 * (q >> 1) + ((c >> 1) & 1)) ^ (c >> 2)) * 16) + (c & 1) * 8;
        hso = OFF_HF + (unsigned)((layer * 4 + (xq & 3)) * PG + ((8 * (mt1 & 1) + 2 * w + (q >> 1)) * 8 + (nt1 >> 1)) * RUN + (10 * (nt1 & 1) + 5 * (q & 1)) * 256 + c * 16);
        cwa = L_CW + c * 4;
        fho1 = lane ? kNone : OFF_FH + (unsigned)((2 * (mt1 & 7) + (w >> 1)) * RUN + (layer * 32 + nt1 * 2 + (w & 1)) * 128);
        fho = OFF_FH + (unsigned)((mt2 & 15) * RUN + (layer * 4 + w) * 1024 + (lane >> 3) * 128);
        pin(sgn_); pin(spl); pin(eo); pin(es); pin(spa); pin(tbw); pin(tbr); pin(hso); pin(cwa); pin(fho1); pin(fho);
        const unsigned hb_ = OFF_HF + (unsigned)((layer * 4 + (xq & 3)) * PG + (32 * (mt2 & 3) + 2 * w) * RUN);
#pragma unroll
        for (int m = 0; m < 4; ++m) { ho[m] = hb_ + (unsigned)(((m + 1 + w) & 3) * 8 * RUN + lane * 16); pin(ho[m]); }
#pragma unroll
        for (int m = 0; m < 3; ++m) { dst[m] = SC + ((w + 1 + m) & 3) * INBOX + m * 5120 + lane * 16; pin(dst[m]); }
        mt1_ = mt1;
        sgo = OFF_SG + (unsigned)((layer * 16 + (mt2 & 15)) * RUN + w * 2048 + c * 16);
        sgs = q < 2 ? sgo + (unsigned)(nt2 * 256 + q * 8) : kNone;
        sgo += (unsigned)(q * 512);
        pin(sgo); pin(sgs);
        if constexpr (layer == 0) {
            srca = SC + w * INBOX + lane * 16;
            const unsigned gb_ = OFF_GT + (unsigned)((nt2 >> 2) * PG + (nt2 & 3) * 32 * RUN);
            const int cl = i32 + 32 * w;
            glo = gb_ + (unsigned)((lane >> 3) * RUN + ((lane >> 1) & 3) * 2048 + (64 * (mt2 & 15) + 16 * w) * 2 + (lane & 1) * 16);
            gso = cv0 ? gb_ + (unsigned)((cl >> 2) * RUN + (cl & 3) * 2048 + (q & 1) * 512 + c * 32 + (q >> 1) * 16) : kNone;
            fgo = gb_ + (unsigned)(20 * RUN + (lane >> 1) * 128);
            fgo1 = w == 0 ? gb_ + (unsigned)(20 * RUN + i32 * 128 + (lane >> 5) * 8192) : kNone;
            pin(srca); pin(glo); pin(gso); pin(fgo); pin(fgo1);
            const int r0 = SC + (w < 3 ? w : 0) * REGSZ, y0 = 2031 + 8 * q - c, l34 = lane < 33 ? lane : 33;
#pragma unroll
            for (int i = 0; i < 4; ++i) { ab[i] = r0 + ((y0 & 1) ? CV_O : CV_E) + ((y0 + 1) >> 1) * 4 - 448 - 512 - 1024 * i; pin(ab[i]); }
            bb = r0 + 32 * c + 16 * q;
            zpa = w < 3 ? r0 + l34 * 16 + (l34 >= 30 ? CV_E + 4096 - 480 : 0) + (l34 >= 32 ? CV_O + 4100 - 32 - CV_E - 4096 : 0) : SC + 47360 + lane * 16;
            uwa = r0 + lane * 16;
            xso = (unsigned)((64 * mt2 + 16 * w + c) * D_ + 80 * nt2 + 16 * (q & 1) + 8 * (q >> 1)) * 2u;
            dso = (unsigned)(160 * nt2 + 8 * q);
            adm = (unsigned)((c & 1) * 16 + (q == 2 + (c >> 3) ? 256 << ((c & 7) >> 1) : 0));
            pin(adm);
            pin(bb); pin(zpa); pin(uwa); pin(xso); pin(dso);
        }
        SCHED();
    };
    unsigned tag0, tag1;
#define FLAG(off) { if (lane == 0) st64(v2u{tag0, tag1}, rs, off); }
#define ISTAG(f) (((f.x ^ tag0) | (f.y ^ tag1)) == 0u)
#define KW_REQ() { if (cv) ld_ur(wro, p.ws + OFF_KVT); static_for<NW2P>(LAMBDA(i_) { gld_w2<i_.value>(w2o, wl + 2048 + (i_.value / 5) * RUN); }); }
#define FW_OK() { if (__builtin_expect(!kearly, 0)) { WAVE_WAIT({ const v2u fw_ = ld64(rs, fwo); ok = ISTAG(fw_); }) KW_REQ() } }
#define TT_GAP(n) { constexpr int mb_ = (n) / 10, nb_ = ((n) % 10) / 2, e_ = ((n) & 1) * 2; \
        mul2(tt[mb_][nb_][e_], tt[mb_][nb_][e_ + 1], rm_[4 * mb_ + e_], rm_[4 * mb_ + e_ + 1], SW[nb_]); }
    {
        const int tid = threadIdx.x, lane = tid & 63, w = w0, b = b0;
        const int c = lane & 15, q = lane >> 4;
        TILES()
        STAMP(0)
        const unsigned lds0 = (unsigned)(size_t)(LAS char*)smem;
        v2u nonce;
        {
            u64 pc;
            int len, len2;
            asm volatile("s_getpc_b64 %0\nkpf_begin:\n\ts_mov_b32 %1, kpf_end-kpf_begin\n\ts_mov_b32 %2, kpf_end2-kpf_begin" : "=s"(pc), "=s"(len), "=s"(len2));
            len = len > len2 ? len : len2;
            const int o0 = (((j & 15) * 4 + w) + 64 * (lane >> 3)) * 128;
            touch_((unsigned)(o0 < len ? o0 : 0), (const void*)pc);
        }
        const bool cv = w + (i32 >> 4) < 3;
        const int chn = 80 * nt2 + i32 + 32 * (cv ? w : 0);
        const bool kw = w - (i32 >> 4) < 2;
        const int kp = 16 * nt2 + mt1, ku = 5 * kp + (i32 < 16 ? w & 1 : 2 + (w < 3 ? w : 0)), kc = ku >> 6, kr = ku & 63;
        const int lc = cv ? lane : 8;
        int kwi = 2 + (i32 >> 4) - w;
        v4u ax[15], sa[5], cr[2], wr[4];
        unsigned uL[3];
        const int cu_ = (b < 200 ? b : 0) * 4 + w, cdb = cu_ / 20, ccg = cu_ % 20;
        {
            int ca_ = 32 * ccg, cb_ = 40 * nt1 + 10 * w - 32;
            asm volatile("" : "+s"(ca_), "+s"(cb_), "+s"(kwi));
            unsigned cpo = (unsigned)((lane < 32 ? ca_ : cb_) + (lane < 41 ? lane : 41)) * 2u;
            unsigned ksh = (unsigned)(64 * kc + 8 * (lane & 7)) * 2u, ko = (unsigned)(32 * kr + 4 * (lane >> 3)) * 1280u + ksh, kh = ko - 3840u;
            ksh *= 4u;
            pin(cpo); pin(ksh); pin(ko); pin(kh);
            SCHED();
            gldu16(uL[0], cpo, p.Lre); gldu16(uL[1], cpo, p.Lim); gldu16(uL[2], cpo, p.logdt);
            const v4u rk = {(unsigned)(u64)p.k, (unsigned)((u64)p.k >> 32) & 0xffffu, S_ * D_ * 2u, 0x00020000u};
#define L4(d, v, s_, o, f) "global_load_dwordx4 %" #d ", %" #v ", %" #s_ " offset:" #o f "\n\t"
#define B4(d, o) "buffer_load_dwordx4 %" #d ", %17, %21, 0 offen offset:" #o " nt\n\t"
            asm volatile("s_cmp_lt_i32 %22, 1\n\ts_cbranch_scc1 Lrole2\n\t"
                         L4(3, 15, 18, 0, " nt") L4(4, 15, 18, 1280, " nt") L4(5, 15, 18, 2560, " nt") L4(6, 15, 18, 3840, " nt")
                         L4(7, 15, 20, 0, " nt") L4(8, 15, 20, 1280, " nt") L4(9, 15, 20, 2560, " nt") L4(10, 15, 20, 3840, " nt")
                         B4(0, 0) B4(1, 1280) B4(2, 2560) L4(11, 16, 19, 0, "") L4(12, 16, 19, 16, "") L4(13, 16, 19, 32, "") L4(14, 16, 19, 48, "")
                         "\nLrole2:"
                         : "=&v"(ax[0]), "=&v"(ax[1]), "=&v"(ax[2]), "=&v"(ax[3]), "=&v"(ax[4]), "=&v"(ax[5]), "=&v"(ax[6]), "=&v"(ax[7]), "=&v"(ax[8]), "=&v"(ax[9]),
                           "=&v"(ax[10]), "=&v"(ax[11]), "=&v"(ax[12]), "=&v"(ax[13]), "=&v"(ax[14])
                         : "v"(ko), "v"(ksh), "v"(kh), "s"(p.k), "s"(p.shk), "s"(p.v), "s"(rk), "s"(kwi) : "scc");
            SCHED();
        }
        const char* ssrc;
        {
            u64 pa = (u64)(p.x0 + (128 * mt1 + 8 * nt1 + 2 * w) * D_), pb = (u64)(((mt1 & 1) ? p.Bim : p.Bre) + (40 * nt1 + (mt1 >> 1)) * D_);
            asm volatile("" : "+s"(pa), "+s"(pb));
            const int nbx = 2 * w + q - 2, r = q < 2 ? q : 8 * (nbx < 5 ? nbx : 4);
            ssrc = (const char*)(q < 2 ? pa : pb) + (unsigned)(r * 1280 + c * 16);
            pin(ssrc);
        }
        SCHED();
        STAMP(22)
        const int row8 = lane >> 3;
        const unsigned wvo = (unsigned)(40 * nt1 + row8) * 1280u + (unsigned)(((lane & 7) ^ ((w & 1) | ((row8 >> 1) << 1))) * 16 + (w >> 1) * 128);
        const char* const wbase = (const char*)((w & 1) ? p.Bim : p.Bre);
        const unsigned w1l = lds0 + (unsigned)(((w & 1) * 10 + (w >> 1)) * 1024);
        const unsigned xvo0 = (unsigned)(8 * (row8 >> 1) + 2 * (row8 & 1)) * 1280u + (unsigned)(((lane & 7) ^ ((row8 >> 1) << 1)) * 16);
        const unsigned xvo1 = (xvo0 ^ 16u) + 1280u;
        const char* const xbase = (const char*)p.x0 + (size_t)(128 * mt1 + 32 * w) * 1280;
        const unsigned xl = lds0 + (unsigned)(w * REGSZ);
        unsigned xv[4] = {xvo0, xvo1, xvo0 + 5120u, xvo1 + 5120u}, wv[5];
#pragma unroll
        for (int i = 0; i < 5; ++i) { wv[i] = wvo + i * 10240u; pin(wv[i]); if (i < 4) pin(xv[i]); }
#define DMA_M0(it) SETM0<dma_loff(it) - dma_goff(it)>((it) < 40 ? xl : w1l)
#define DMA_LD1(it, nx_) GLD1<dma_goff(it), dma_loff(nx_) - dma_goff(nx_)>((it) < 40 ? xv[(it) & 3] : wv[(it) < 40 ? 0 : ((it) - 100) % 5], (it) < 40 ? xbase : wbase, (nx_) < 40 ? xl : w1l)
#define DMA_LD(it) GLD0<dma_goff(it)>((it) < 40 ? xv[(it) & 3] : wv[(it) < 40 ? 0 : ((it) - 100) % 5], (it) < 40 ? xbase : wbase)
        const int swz = (c & 1) | ((c >> 2) << 1);
        int w1lane0 = (c & 1) * 10240 + (c >> 1) * 128 + ((q ^ swz) * 16);
        int w1lane1 = (c & 1) * 10240 + (c >> 1) * 128 + (((4 + q) ^ swz) * 16);
        int xlane0 = L_XR + w * REGSZ + (c & 1) * 1024 + (c >> 1) * 128 + ((q ^ swz) * 16);
        int xlane1 = L_XR + w * REGSZ + (c & 1) * 1024 + (c >> 1) * 128 + (((4 + q) ^ swz) * 16);
        unsigned cvo = (unsigned)((16 * cdb + c) * D_ + 32 * ccg + 8 * q) * 2u, for0, for1;
        {
            const int G = 4 * ccg + q, ch = 10 * (G / 5) + G % 5, ci = ch + 5;
            const unsigned fo = OFF_W2F + (unsigned)((cdb / 10) * PG + ((cdb / 5) & 1) * 64 * RUN + (cdb % 5) * 1024 + c * 16);
            for0 = fo + (unsigned)((ch >> 2) * RUN + (ch & 3) * 256); for1 = fo + (unsigned)((ci >> 2) * RUN + (ci & 3) * 256);
        }
        unsigned sto = OFF_ST + (unsigned)((mt1 * 16 + 4 * w + q) * 256 + c * 8), swo = (lane & 3) == 3 ? OFF_CV + (unsigned)((lane >> 2) * 128) : OFF_ST + (unsigned)(((lane >> 2) * 16 + nt1) * 256 + 128 + (lane & 3) * 16);
        unsigned kvo = OFF_KVT + (unsigned)((64 * kc + 8 * (lane >> 3) + ((lane >> 2) & 1)) * 4096 + (32 * kr + 8 * (lane & 3)) * 2);
        int kbw = L_KB + w * 1024 + (lane & 7) * 128 + (((lane >> 3) * 8) ^ ((lane & 7) * 16));
        int kbr = L_KB + w * 1024 + (lane >> 3) * 128 + (((lane & 7) * 16) ^ ((lane >> 3) * 16));
        const unsigned dum = kNone;
        if (!kw) kvo = dum;
        if (b >= 200) { for0 = dum; for1 = dum; cvo = (unsigned)((b * 4 + w) * 128); }
        wro = (unsigned)(chn * 4096 + lc * 16);
        cv0 = cv;
        pin(kvo); pin(kbw); pin(kbr);
        int w1hi0 = w1lane0 + 61440, w1hi1 = w1lane1 + 61440;
        pin(w1hi0); pin(w1hi1);
        pin(w1lane0); pin(w1lane1); pin(xlane0); pin(xlane1); pin(cvo); pin(for0); pin(for1); pin(sto); pin(swo); pin(wro);
        SCHED();
        auto pre = LAMBDA(i_) {
            constexpr int it = p1_pre(i_.value);
            SCHED();
            DMA_M0(it);
            asm volatile("s_nop 0");
            DMA_LD(it);
            SCHED();
        };
        {
            int f0 = 2 * ((chn & ~63) / 5), f1 = 2 * ((chn | 63) / 5) + 1, nk = cv ? 28 : 0;
            asm volatile("" : "+s"(f0), "+s"(f1), "+s"(nk));
            const int l10 = lane - 32 < 0 || lane - 32 > 9 ? 0 : lane - 32;
            fwo = lane < nk ? OFF_FK + (unsigned)(f0 + lane < f1 ? f0 + lane : f1) * 128u : OFF_FW + (unsigned)(25 * nt2 + w + 5 * (l10 >> 1) + (l10 & 1)) * 128u;
        }
        w2o = OFF_W2F + (unsigned)((nt2 >> 1) * PG + ((nt2 & 1) * 64 + 10 * w) * RUN + lane * 16);
        int ime = SC + (w < 3 ? w * REGSZ + CV_E : 43264) + 1008 - 16 * lane, imo = SC + (w < 3 ? w * REGSZ + CV_O + 4 : 47360) + 1008 - 16 * lane, im3 = w < 3 ? ime + 3072 : imo;
        int tca = L_TAB + (c >> 1) * 136, tcb = tca + 8 + 32 * w + 8 * q, tcl = L_TAB + (lane < 40 ? lane : 39) * 136, coa = L_CO + w * REGSZ + q * 64, rsa = l_rs(0, 0) + w * 256 + lane * 4;
        unsigned sgn = (c & 1) ? 0u : 0x80000000u;
        unsigned fko1 = w ? kNone : lane < 32 ? OFF_FK + (unsigned)((2 * kp + (i32 >> 4)) * 128) : OFF_FW + (unsigned)(b * 128);
        pin(fwo); pin(w2o); pin(fko1); pin(ime); pin(imo); pin(im3); pin(tca); pin(tcb); pin(tcl); pin(coa); pin(rsa); pin(sgn);
        SCHED();
        auto cw_ = [&]() __attribute__((always_inline)) {
            gldn<0, 2>(cr[0], cvo, p.Cre); gldn<0, 2>(cr[1], cvo, p.Cim);
            static_for<4>(LAMBDA(i) { gldn<i.value * 1024, 1>(wr[i.value], wro, p.ssmk); });
        };
        STAMP(33)
        gld2s<0>(nonce, 0u, p.ws + OFF_NONCE);
        asm volatile("s_waitcnt vmcnt(16)\n\ts_cmp_gt_i32 %3, 0\n\ts_cbranch_scc1 Lrole3\n\ts_waitcnt vmcnt(1)\nLrole3:" : "+v"(uL[0]), "+v"(uL[1]), "+v"(uL[2]) : "s"(kwi) : "memory", "scc");
        {
            f32x2 lam, co, lp[16];
            disc(uL[0], uL[1], uL[2], lam, co);
            if (lane < 32) *(f32x2*)(smem + L_CO + w * REGSZ + lane * 8) = 2.0f * co;
            lp[0] = cmul(lam, lam); lp[0] = cmul(lp[0], lp[0]); lp[1] = cmul(lp[0], lp[0]);
            lp[0] = f32x2{1.f, 0.f};
            lp[2] = cmul(lp[1], lp[1]); lp[3] = cmul(lp[2], lp[1]); lp[4] = cmul(lp[2], lp[2]);
            const f32x2 z2 = {0.f, 0.f};
            f32x2 t4[4];
            cfma4(t4, lp[4], lp[1], lp[2], lp[3], lp[4], z2, z2, z2, z2);
            lp[5] = t4[0]; lp[6] = t4[1]; lp[7] = t4[2]; lp[8] = t4[3];
            cfma4(t4, lp[8], lp[1], lp[2], lp[3], lp[4], z2, z2, z2, z2);
            lp[9] = t4[0]; lp[10] = t4[1]; lp[11] = t4[2]; lp[12] = t4[3];
            cfma4(t4, lp[8], lp[5], lp[6], lp[7], lp[7], z2, z2, z2, z2);
            lp[13] = t4[0]; lp[14] = t4[1]; lp[15] = t4[2];
            if (lane >= 32 && lane < 42) {
                f32x2* tl = (f32x2*)(smem + L_TAB + (10 * w + lane - 32) * 136);
                tl[0] = lam;
#pragma unroll
                for (int i = 0; i < 16; ++i) tl[1 + i] = lp[i];
            }
        }
        SCHED();
        STAMP(35)
        static_for<5>(LAMBDA(i) { gld64<256 * i.value>(sa[i.value], ssrc); });
        cw_();
        addrs(std::integral_constant<int, 0>{}, lane, w, b);
        v4u kvt[4];
        unsigned one2 = 0x3F803F80u, kq[12];
        float kf_[8];
        pin(one2);
        auto kvf = LAMBDA(n_) {
            constexpr int n = n_.value, h = n / 5, u = n % 5;
            if constexpr (u == 0) {
#pragma unroll
                for (int r = 0; r < 6; ++r) { kq[r] = __builtin_amdgcn_perm(ax[r][h], ax[r + 1][h], 0x05040100u); kq[6 + r] = __builtin_amdgcn_perm(ax[r][h], ax[r + 1][h], 0x07060302u); }
                if constexpr (h > 0) kvt[h - 1] = *(const v4u*)(smem + kbr);
            } else if constexpr (u == 1) {
                unsigned t[8];
                d2x4(t, kq[0], kq[1], kq[2], kq[3], ax[11 + h][1], ax[11 + h][1], ax[11 + h][1], ax[11 + h][1]);
                d2x4(t + 4, kq[6], kq[7], kq[8], kq[9], ax[11 + h][3], ax[11 + h][3], ax[11 + h][3], ax[11 + h][3]);
#pragma unroll
                for (int r = 0; r < 8; ++r) kf_[r] = __uint_as_float(t[r]);
            } else if constexpr (u == 2) {
#pragma unroll
                for (int r = 0; r < 8; ++r) kf_[r] = dot2a(kq[r + 2 * (r >> 2) + 2], ax[11 + h][2 * (r >> 2)], kf_[r]);
            } else if constexpr (u == 3) {
#pragma unroll
                for (int r = 0; r < 4; ++r) { kq[r] = ax[7 + r][h] << 16; kq[4 + r] = ax[7 + r][h] & 0xffff0000u; pin(kq[r]); pin(kq[4 + r]); }
            } else {
#pragma unroll
                for (int r = 0; r < 8; ++r) kf_[r] *= __uint_as_float(kq[r]);
                *(v2u*)(smem + kbw) = v2u{cvtpk(kf_[0], kf_[1]), cvtpk(kf_[2], kf_[3])};
                *(v2u*)(smem + (kbw ^ 64)) = v2u{cvtpk(kf_[4], kf_[5]), cvtpk(kf_[6], kf_[7])};
                if constexpr (h == 3) kvt[3] = *(const v4u*)(smem + kbr);
            }
            SCHED();
        };
        asm volatile("s_waitcnt vmcnt(%15)" : "+v"(ax[0]), "+v"(ax[1]), "+v"(ax[2]), "+v"(ax[3]), "+v"(ax[4]), "+v"(ax[5]), "+v"(ax[6]), "+v"(ax[7]), "+v"(ax[8]), "+v"(ax[9]),
                     "+v"(ax[10]), "+v"(ax[11]), "+v"(ax[12]), "+v"(ax[13]), "+v"(ax[14]) : "i"(11) : "memory");
        pin(nonce);
        STAMP(30)
        unsigned tch = 0u;
        constexpr int NF = KNF;
        static_assert(NF < 27, "");
        if (kw) {
            {
                static_for<20>(LAMBDA(n_) {
                    constexpr int n = n_.value, DN = 20 - KFD0;
                    site(); SCHED();
                    kvf(n_);
                    if constexpr (n >= KFD0) static_for<(n - KFD0 + 1) * NF / (DN ? DN : 1) - (n - KFD0) * NF / (DN ? DN : 1)>(LAMBDA(i_) { pre(std::integral_constant<int, (n - KFD0) * NF / (DN ? DN : 1) + i_.value>{}); });
                });
                if constexpr (KFD0 == 20) static_for<NF>(pre);
            }
        } else {
            if (w == 3) {
            const int l = lane, l3 = l & 3;
            unsigned a = OFF_SG + (unsigned)((((l >> 3) & 1) * 16 + (mt2 & 15)) * RUN + ((l >> 1) & 3) * 2048 + nt2 * 256 + (l & 1) * 128);
            a = l >= 16 ? OFF_PS + (unsigned)((mt1 & 7) * RUN + (((mt2 & 1) * 4 + l3) * 8 + nt2) * 128) : a;
            a = l >= 20 ? OFF_FX + (unsigned)((mt1 & 7) * RUN + ((2 * (mt2 & 1) + (l3 >> 1)) * 16 + nt2 * 2 + (l3 & 1)) * 128) : a;
            a = l >= 24 ? OFF_FH + (unsigned)((2 * (mt1 & 7) + (l3 >> 1)) * RUN + (((l >> 2) & 1) * 32 + nt1 * 2 + (l3 & 1)) * 128) : a;
            const unsigned eg_ = OFF_EG + (unsigned)(((((l >> 3) & 1) * 2 + (nt1 >> 3)) * 16 + mt1) * RUN + (nt1 & 7) * 640 + (l & 7) * 128);
            const unsigned fg_ = OFF_GT + (unsigned)((nt2 >> 2) * PG + ((nt2 & 3) * 32 + 20) * RUN + i32 * 128);
            a = l >= 32 ? ((l & 7) < 5 ? eg_ : fg_) + (unsigned)((l & 16) * 512) : a;
            tch = a;
        }
            vm_wait<KNW>();
            static_for<NF>(pre);
            if (w == 2 && b < 16) {
                unsigned v0, v1, a0, a1, a2, a3, t0, t1;
                asm volatile("s_waitcnt lgkmcnt(0)\n\ts_load_dword %2, %8, 0x0 glc\n\ts_load_dword %3, %8, 0x2000 glc\n\ts_load_dword %4, %8, 0x1000 glc\n\ts_load_dword %5, %8, 0x3000 glc\n\ts_waitcnt lgkmcnt(0)\n\t"
                             "s_not_b32 %6, %2\n\ts_not_b32 %7, %3\n\ts_load_dword %6, %8, 0x0 glc\n\ts_load_dword %7, %8, 0x2000 glc\n\ts_waitcnt lgkmcnt(1)\n\t"
                             "s_cmp_eq_u32 %7, %3\n\ts_cselect_b32 %0, 1, 0\n\ts_waitcnt lgkmcnt(0)\n\t"
                             "s_not_b32 %6, %4\n\ts_not_b32 %7, %5\n\ts_load_dword %7, %8, 0x3000 glc\n\ts_load_dword %6, %8, 0x1000 glc\n\ts_waitcnt lgkmcnt(1)\n\t"
                             "s_cmp_eq_u32 %6, %4\n\ts_cselect_b32 %1, 0, 1\n\ts_waitcnt lgkmcnt(0)"
                             : "=&s"(v0), "=&s"(v1), "=&s"(a0), "=&s"(a1), "=&s"(a2), "=&s"(a3), "=&s"(t0), "=&s"(t1) : "s"(p.ws + (OFF_W2F - MID + (unsigned)(40 + b) * RUN)) : "memory", "scc");
                const unsigned sd = (unsigned)(b >> 2) & 1u;
                st128(v4u{v0 ^ sd, nonce.x + 1u, v1 ^ sd, nonce.x + 1u}, rs, lane ? kNone : OFF_CV + (unsigned)(b * 128));
            }
            undef4(kvt);
        }
        tag0 = __builtin_amdgcn_readfirstlane(nonce.x) + 1u; tag1 = __builtin_amdgcn_readfirstlane(nonce.y) + (tag0 == 0u ? 1u : 0u);
        SCHED();
        STAMP(34)
        v4u orr, oi;
        v2u gr;
        unsigned gro;
        {
            asm volatile("s_waitcnt vmcnt(%7)" : "+v"(cr[0]), "+v"(cr[1]), "+v"(sa[0]), "+v"(sa[1]), "+v"(sa[2]), "+v"(sa[3]), "+v"(sa[4]) : "i"(4 + NF) : "memory");
#pragma unroll
            for (int e = 0; e < 4; ++e) {
                const f32x4 co = *(const f32x4*)(smem + coa + e * 16);
                const f32x2 o0 = lo16f(cr[0][e]) * f32x2{co.x, -co.y} - lo16f(cr[1][e]) * f32x2{co.y, co.x};
                const f32x2 o1 = hi16f(cr[0][e]) * f32x2{co.z, -co.w} - hi16f(cr[1][e]) * f32x2{co.w, co.z};
                orr[e] = cvtpk(o0.x, o1.x); oi[e] = cvtpk(o0.y, o1.y);
            }
            pin(orr); pin(oi);
            SCHED();
        }
        static_for<(27 - NF) / 3>(LAMBDA(i_) { pre(std::integral_constant<int, NF + i_.value>{}); });
        {
            float s1[2] = {0.f, 0.f}, s2[2] = {0.f, 0.f};
#pragma unroll
            for (int i = 0; i < 5; ++i)
#pragma unroll
                for (int e = 0; e < 4; ++e) { s1[e & 1] = dot2a(sa[i][e], one2, s1[e & 1]); s2[e & 1] = dot2a(sa[i][e], sa[i][e], s2[e & 1]); }
            const float a1 = rowsum16(s1[0] + s1[1]), a2 = rowsum16(s2[0] + s2[1]);
            const float mean = a1 * (1.0f / D_);
            const float var = fmaxf(a2 * (1.0f / D_) - mean * mean, 0.f);
            const float ri = __builtin_amdgcn_rcpf(rbf(__builtin_amdgcn_sqrtf(rbf(rbf(var) + 1e-5f))));
            gr = v2u{__float_as_uint(q >= 2 ? a1 : c ? -ri * rbf(mean) : ri), tag0};
            gro = c < 2 ? OFF_ST + (unsigned)((mt1 * 16 + nt1) * 256 + ((q < 2 ? 8 * c : 14) + 2 * w + q) * 8) : kNone;
            pin(gr); pin(gro);
        }
        SCHED();
        STAMP(59)
        {
            static_for<27 - NF - (27 - NF) / 3>(LAMBDA(i_) { pre(std::integral_constant<int, NF + (27 - NF) / 3 + i_.value>{}); });
#pragma unroll
            for (int i = 0; i < 4; ++i) st128(kvt[i], rs, kvo + i * 8192);
            st128(orr, rs, for0); st128(oi, rs, for1);
            __builtin_amdgcn_raw_buffer_store_b128(orr, rs, for0, 8192, SC1); __builtin_amdgcn_raw_buffer_store_b128(oi, rs, for1, 8192, SC1);
        }
        st64(gr, rs, gro);
        SCHED();
        STAMP(23)
        asm volatile("s_waitcnt vmcnt(%4) lgkmcnt(0)" : "+v"(wr[0]), "+v"(wr[1]), "+v"(wr[2]), "+v"(wr[3]) : "i"(P1_PRE - 9) : "memory");
        __builtin_amdgcn_s_barrier();
        asm volatile("" ::: "memory");
        STAMP(24)
        v4u wf[2][5], af[2][2][2];
#pragma unroll
        for (int nb = 0; nb < 5; ++nb) wf[0][nb] = *(const v4u*)(smem + w1lane0 + nb * 20480 + (0) * 2048);
#pragma unroll
        for (int i = 0; i < 4; ++i) af[0][i >> 1][i & 1] = *(const v4u*)(smem + ((i >> 1) ? xlane1 : xlane0) + (i & 1) * 2048);
        SCHED();
        const char *kvtb = p.ws + OFF_KVT, *w2b0;
        u64 vb0, vb1;
        pins(kvtb);
        v2u sg, tc[10], fw, tg2 = {tag0, tag1};
        pin(tg2);
        v4u sw0;
        unsigned ie[4][4], io[4][4], iw, zq[4];
        float rr_[8];
        auto kstep = LAMBDA(s_) {
            constexpr int s = s_.value, ph = s >> 2, s4 = s & 3, cu = s & 1, nx = cu ^ 1, jj = s >> 1, pp = jj & 1;
            if constexpr (s4 == 3 && ph < 4) {
                constexpr int pw = p1_pos(104 + 5 * (ph + 1)), px = p1_pos(4 * (jj + 1) + 3), pm = pw > px ? pw : px;
                constexpr int vm = p1_lim(p1_cnt(s, 0) - 1 - pm);
                asm volatile("" ::: "memory");
                __builtin_amdgcn_s_waitcnt((vm & 15) | ((vm >> 4) << 14) | 0x70);
                __builtin_amdgcn_s_barrier();
                asm volatile("" ::: "memory");
                STAMP(25 + ph)
                SCHED();
            }
            if constexpr (s == 15) {
                wait_st<p1_cnt(s, 0) - 1 - p1_pos(203)>(sg, sw0);
                WAVE_RETRY((((sg.y ^ tag0) | (sw0.y ^ tag0) | (sw0.w ^ tag0)) == 0u), sg = ld64(rs, sto); sw0 = ld128(rs, swo))
                STAMP(7)
            }
            if constexpr (s == 16) {
                const u64 m = 0x8888888888888888ull;
                pol = __builtin_popcountll(vb0 & m) + __builtin_popcountll(vb1 & m) > 16 ? 8192u : 0u;
                sdo = pol ^ (unsigned)((b0 & 4) * 2048);
                wl = p.ws + sdo; w2b0 = wl + 2048;
                pins(pol); pins(sdo); pins(wl); pins(w2b0);
                rl = __builtin_amdgcn_make_buffer_rsrc((void*)wl, 0, WS_BYTES, 0x00020000);
                static_assert(kW2S > 16, "");
            }
            if constexpr (s == kW2S) { vm_wait<p1_cnt(s, 0) - 1 - p1_pos(208)>(); SCHED(); }
            if constexpr (s == 18) {
                vm_wait<p1_cnt(s, 0) - 1 - (p1_pos(207) > p1_pos(208) ? p1_pos(207) : p1_pos(208))>();
                pin(fw); pin(sdq);
                kearly = __builtin_amdgcn_ballot_w64(!ISTAG(fw)) == 0ull;
                ORDER();
                if (kearly && cv) ld_ur(wro, kvtb);
                SCHED();
            }
            if constexpr (s4 != 3 || ph == 4) lgkm<(cu == 0 && s > 0 ? 3 : s == 17 ? 4 : s == 19 ? 2 : 0)>();
            SCHED();
            static_for<10>(LAMBDA(g_) {
                constexpr int g = g_.value, mb = g / 5, k = g % 5, it = p1_item(s, g), nt_ = g < 9 ? p1_item(s, g + 1) : -1;
                constexpr bool d0 = it >= 0 && it < 200, d1 = nt_ >= 0 && nt_ < 200;
                if constexpr (s == 0) mf0(acc[mb][k], af[pp][cu][mb], wf[cu][k]); else mf(acc[mb][k], af[pp][cu][mb], wf[cu][k]);
                SCHED();
                if constexpr (s < 19) {
                    if constexpr (g < 5) wf[nx][g] = *(const v4u*)(smem + (g < 3 ? (nx ? w1lane1 : w1lane0) : (nx ? w1hi1 : w1hi0)) + (g < 3 ? g : g - 3) * 20480 + ((s + 1) >> 1) * 1024);
                    if constexpr (cu == 1 && g == 5) vm_wait<p1_lim(p1_cnt(s, 5) - 1 - p1_pos(4 * (jj + 1) + 3))>();
                    if constexpr (cu == 1 && g >= 5 && g < 9)
                        af[pp ^ 1][(g - 5) >> 1][(g - 5) & 1] = *(const v4u*)(smem + (((g - 5) >> 1) ? xlane1 : xlane0) + ((jj + 1) % 3) * 4096 + ((g - 5) & 1) * 2048);
                }
                if constexpr (cu == 0 && g == 4 && s > 0) lgkm<(s == 8 || s == 10 || s == 14 ? 8 : 5)>();
                static_for<p1_cap(s, g)>(LAMBDA(e_) {
                    constexpr int na = p1_slot(0, s, g, 10) + e_.value, i = (na / 10) & 3, u = na % 10;
                    static_assert(p1_slot(0, 8, 0, 10) >= 40, "");
                    if constexpr (na < 40) {
                        if constexpr (u == 0) { if constexpr (i > 0) ror1(iw, wr[i - 1].w); else { iw = 0u; pin(iw); } }
                        else if constexpr (u < 5) rot1(ie[i][u - 1], wr[i][4 - u]);
                        else if constexpr (u == 5) shr1b(iw, wr[i].w);
                        else if constexpr (u < 9) mix1(io[i][u - 6], wr[i][9 - u], wr[i][8 - u]);
                        else mix1(io[i][3], wr[i].x, iw);
                    }
                    constexpr int nb = p1_slot(8, s, g, 3) + e_.value;
                    if constexpr (s >= 8 && s < 12 && g < 3) {
                        if constexpr (nb < 10) tc[nb] = *(const v2u*)(smem + (nb < 5 ? tca : tcb) + (nb % 5) * 1088);
                        else if constexpr (nb == 10) { const f32x2* tp = (const f32x2*)(smem + tcl); l8 = tp[2]; l32 = tp[5]; }
                        else if constexpr (nb == 11) l128 = *(const f32x2*)(smem + tcl + 72);
                        static_assert(p1_slot(8, 12, 0, 3) == 12, "");
                    }
                    constexpr int nc = p1_slot(12, s, g, 10) + e_.value;
                    if constexpr (s >= 12 && s < 14 && nc < 10) {
                        if constexpr (nc < 5) { lr[nc] = __uint_as_float(tc[nc].x); xor1(ls[nc], tc[nc].y, sgn); }
                        else { lqr[nc - 5] = __uint_as_float(tc[nc].x); xor1(lqs[nc - 5], tc[nc].y, sgn); }
                        static_assert(p1_slot(12, 14, 0, 10) >= 10, "");
                    }
                    if constexpr (s == 15 && g < 5) SW[g] = __int_as_float(__builtin_amdgcn_ds_bpermute(16 * c + 4 * (g >> 1), (g & 1) ? sw0.z : sw0.x));
                    constexpr bool img = (s == 16 && g >= 5 && g < 9 && e_.value == 0) || (s == 18 && (g == 5 || g == 6) && e_.value == 0);
                    constexpr int nd = p1_slot(15, s, g, 10) + e_.value - 5 - (s > 16 ? 4 : s == 16 && g >= 5 ? (g < 9 ? g - 4 : 4) : 0) - (s > 18 ? 2 : s == 18 && g >= 5 ? (g == 5 ? 1 : 2) : 0);
                    if constexpr (s >= 15 && nd >= 0 && !img) {
                        if constexpr (nd < 4) bc1<nd>(rr_[nd], sg.x);
                        else if constexpr (nd < 44) mulb<8 + (nd - 4) % 8>(tt[(nd - 4) % 8 / 4][(nd - 4) / 8][(nd - 4) % 4], sg.x, SW[(nd - 4) / 8]);
                        else if constexpr (nd < 48) bc1<nd - 40>(rr_[nd - 40], sg.x);
                        static_assert(p1_slot(15, 16, 0, 10) <= 9 && p1_slot(15, 20, 0, 10) >= 59, "");
                    }
                });
                if constexpr (s == 15 && g == 6) { vb0 = __builtin_amdgcn_ballot_w64(sw0.x == 1u); pins(vb0); }
                if constexpr (s == 15 && g == 7) { vb1 = __builtin_amdgcn_ballot_w64(sw0.z == 1u); pins(vb1); }
                if constexpr (s == 14 && g < 3) *(v4u*)(smem + ime + 1024 * g) = v4u{ie[3 - g][0], ie[3 - g][1], ie[3 - g][2], ie[3 - g][3]};
                if constexpr (s == 16 && g >= 5 && g < 9) *(v4u*)(smem + imo + 1024 * (8 - g)) = v4u{io[g - 5][0], io[g - 5][1], io[g - 5][2], io[g - 5][3]};
                if constexpr (s == 18 && g == 5) *(v4u*)(smem + im3) = v4u{ie[0][0], ie[0][1], ie[0][2], ie[0][3]};
                if constexpr (s == 18 && g == 6) *(unsigned*)(smem + rsa) = sg.x;
                if constexpr (s == 14 && (g == 6 || g == 8)) { zq[g - 6] = 0u; zq[g - 5] = 0u; pin(zq[g - 6]); pin(zq[g - 5]); }
                if constexpr (s == 19 && g == 0) *(v4u*)(smem + zpa) = v4u{zq[0], zq[1], zq[2], zq[3]};
                if constexpr (s == 18 && g == 1) touch_(tch ^ sdo, p.ws);
                if constexpr (s == 19 && g == 2) { if (__builtin_expect(!kearly, 0)) poll1(fwo, p.ws); }
                if constexpr (it >= 0) {
                    if constexpr (d0 && d1) DMA_LD1(it, d1 ? nt_ : 0);
                    else if constexpr (d0) DMA_LD(it);
                    else if constexpr (it == 202) gld2s<0>(sg, sto, p.ws);
                    else if constexpr (it == 203) gld4s<0>(sw0, swo, p.ws);
                    else if constexpr (it == 207) gldu16(sdq, (unsigned)chn * 2u, p.ssmD);
                    else if constexpr (it == 208) gld2s<0>(fw, fwo, p.ws);
                    else if constexpr (it >= 500) gld_w2<it % 50>(w2o, w2b0 + ((it % 50) / 5) * RUN);
                    else { static_assert(p1_pos(19) >= P1_PRE, ""); st64(tg2, rs, fko1); }
                }
                if constexpr (d1 && !d0) DMA_M0(d1 ? nt_ : 0);
                site();
                SCHED();
            });
        };
        static_for<20>(kstep);
        SCHED();
        rr[0] = f32x4{rr_[0], rr_[1], rr_[2], rr_[3]}; rr[1] = f32x4{rr_[4], rr_[5], rr_[6], rr_[7]};
        ACC_FENCE10(acc[0], acc[1]);
        STAMP(1)
    }

    auto layer_body = LAMBDA(layer_) {
        constexpr int layer = layer_.value;
        const int sga = sgn_;
        const bool cv = layer == 0 && cv0;
        v2u fh;
        unsigned ks[10];
        f32x4 ca[8], a2[4][5];
        float rsr, rsm;
        int otw, otr, ots;
        v4u cA[2][8], cB[2][8];
        constexpr int R2 = kCR1, LP = (7 - R2) & 1;
        static_assert(kFHP >= 7 - KG0 / 4 + 1, "");
        constexpr bool TB = (SBT >> layer) & 1;
        float bv[8][5];
        f32x2 pr[5][2], ps[5][2];
        auto powers = [&]() __attribute__((always_inline)) {
            if constexpr (TB) {
#pragma unroll
                for (int nb = 0; nb < 5; ++nb) {
                    const f32x2 m = {lr[nb] * lr[nb] - ls[nb] * ls[nb], 2.0f * lr[nb] * ls[nb]};
                    pr[nb][0] = f32x2{lr[nb], m.x}; ps[nb][0] = f32x2{ls[nb], m.y};
#pragma unroll
                    for (int j = 1; j < 2; ++j) pstep(pr[nb][j], ps[nb][j], pr[nb][j - 1], ps[nb][j - 1], m);
#pragma unroll
                    for (int j = 0; j < 2; ++j) { pin(pr[nb][j]); pin(ps[nb][j]); }
                }
                SCHED();
            }
        };
        {
            float a0[5], a1[5];
            static_for<2>(LAMBDA(m_) {
                constexpr int mb = m_.value;
#pragma unroll
                for (int nb = 0; nb < 5; ++nb) {
                    const f32x4 v = __builtin_elementwise_fma(acc[mb][nb], rr[mb], f32x4{tt[mb][nb][0], tt[mb][nb][1], tt[mb][nb][2], tt[mb][nb][3]});
#pragma unroll
                    for (int i = 0; i < 4; ++i) bv[4 * mb + i][nb] = v[i];
                }
                SCHED();
                if constexpr (TB) {
#pragma unroll
                    for (int i = mb ? 4 : 1; i < 4 + 4 * mb; ++i) cstepi(bv[i][0], bv[i][1], bv[i][2], bv[i][3], bv[i][4], bv[i - 1][0], bv[i - 1][1], bv[i - 1][2], bv[i - 1][3], bv[i - 1][4], lr, ls);
#pragma unroll
                    for (int nb = 0; nb < 5; ++nb) a0[nb] = bv[7][nb];
                }
                if constexpr (mb == 0 && !TB) cstep(a0, bv[0], lr, ls, bv[1][0], bv[1][1], bv[1][2], bv[1][3], bv[1][4]);
#pragma unroll
                for (int i8 = mb ? 4 : 2; i8 < (TB ? 0 : 4 + 4 * mb); i8 += 2) {
                    cstep(a1, a0, lr, ls, bv[i8][0], bv[i8][1], bv[i8][2], bv[i8][3], bv[i8][4]);
                    cstep(a0, a1, lr, ls, bv[i8 + 1][0], bv[i8 + 1][1], bv[i8 + 1][2], bv[i8 + 1][3], bv[i8 + 1][4]);
                }
                SCHED();
            });
#pragma unroll
            for (int nb = 0; nb < 5; ++nb) *(float*)(smem + sga + nb * 64) = a0[nb];
        }
        SCHED();
        const int w_ = w0;
        {
        const int w = w_, mt1 = mt1_;
        if (layer == 0 && __builtin_expect(!kearly, 0)) {
            unsigned f0_, f1_;
            asm volatile("s_waitcnt vmcnt(0)\n\t" AR("%0", 36, 0) AR("%1", 36, 1) : "=v"(f0_), "=v"(f1_)::"memory");
            kearly = __builtin_amdgcn_ballot_w64(((f0_ ^ tag0) | (f1_ ^ tag1)) != 0u) == 0ull;
            ORDER();
            if (kearly) KW_REQ()
        }
        asm volatile("s_waitcnt lgkmcnt(0)" ::: "memory");
        __builtin_amdgcn_s_barrier();
        asm volatile("" ::: "memory");
        STAMP(layer ? 14 : 2)
#pragma unroll
        for (int s = 0; s < 10; ++s) ks[s] = kso(s);
        constexpr int NX = 58;
        constexpr int NW2A = NW2P + 7 * kCR1 + kXA, NW3 = NW2P + kN3E + kN3;
        auto w2ld = LAMBDA(i_) {
            constexpr int i = i_.value;
            gld_w2<i>(w2o, wl + 2048 + (i / 5) * RUN);
        };
        auto w2a = LAMBDA(i_) {
            constexpr int k = i_.value;
            if constexpr (k < kXA) { w2ld(std::integral_constant<int, NW2P + 2 * k>{}); w2ld(std::integral_constant<int, NW2P + 2 * k + 1>{}); }
            else w2ld(std::integral_constant<int, NW2P + kXA + k>{});
        };
        auto w2x = LAMBDA(i_) {
            constexpr int i = i_.value < NX - 50 ? 50 + i_.value : NW2A + i_.value - (NX - 50) < 50 ? NW2A + i_.value - (NX - 50) : 99;
            if constexpr (i < 50) w2ld(std::integral_constant<int, i>{});
            else if constexpr (i < 52) lda4<40 + 4 * (i - 50), (i - 50) * 64>(xso, p.x0);
            else if constexpr (i == 52) lda4<48, 128>(xso - 2u * (unsigned)(bb & 16), p.x0);
            else if constexpr (i < NX) lda2<20 + 2 * (i - 53), 32 * (i - 53)>(dso, p.Dskip);
        };
        constexpr int NB = NX - NW2A, NB1 = KB1 < NB ? KB1 : NB, KPL = 7 * (8 - R2) - kFHP, NFH = NB - NB1 - (KPL - kFHK) > 0 ? NB - NB1 - (KPL - kFHK) : 0;
        auto w2b = LAMBDA(i_) {
            constexpr int k = i_.value - 7 * R2;
            if constexpr (k < NB1) w2x(std::integral_constant<int, k>{});
            if constexpr (k > kFHK) w2x(std::integral_constant<int, NB1 + k - kFHK - 1>{});
            if constexpr (k == kFHK) { vm_wait<NB1>(); st64(v2u{tag0, tag1}, rl, fho1); STAMP(39) }
            if constexpr (k == kFHK - 1) { STAMP(38) }
            if constexpr (k == KPL) poll1(fho, wl);
            static_assert(NB1 <= kFHK + 1 && NB1 + 7 * (7 - R2) + KG0 / 4 - kFHK - 1 >= NB && NW2A <= 50 && NW3 <= NW2A && NFH <= NB - NB1, "");
        };
        auto partb = [&]() __attribute__((always_inline)) {
            const float* sp = (const float*)(smem + spa);
            rsr = sp[0]; rsm = sp[8];
            if (layer == 1) { pin(rsr); pin(rsm); }
            if (layer == 1) {
                ACC_TIE5(dsk);
#pragma unroll
                for (int nb = 0; nb < 5; ++nb) { a2[3][nb] = xin[nb] + (xin[nb] * rsr + rsm) * unpk4(dsk[nb]); pin(a2[3][nb]); }
            }
        };
        constexpr int LBW = layer ? 2 : 3;
        if (w == 3) {
            site(); SCHED();
            f32x2 sv[16];
#pragma unroll
            for (int i = 0; i < 16; ++i) sv[i] = *(const f32x2*)(smem + spl + (l_sg(layer, i >> 2) - SC + (i & 3) * 320));
            f32x2 u[4], v[4], x[4];
            cfma4(u, l8, sv[0], sv[4], sv[8], sv[12], sv[1], sv[5], sv[9], sv[13]);
            cfma4(v, l8, u[0], u[1], u[2], u[3], sv[2], sv[6], sv[10], sv[14]);
            cfma4(x, l8, v[0], v[1], v[2], v[3], sv[3], sv[7], sv[11], sv[15]);
            f32x2 P[4][4];
            P[0][0] = f32x2{0.f, 0.f}; P[0][1] = x[0]; P[0][2] = cfma(l32, x[0], x[1]); P[0][3] = cfma(l32, P[0][2], x[2]);
            const f32x2 E = cfma(l32, P[0][3], x[3]);
            __builtin_amdgcn_raw_buffer_store_b128(v4u{__float_as_uint(E.x), tag0, __float_as_uint(E.y), tag0}, rs, es, sdo ^ 8192u, SC1);
            st128(v4u{__float_as_uint(E.x), tag0, __float_as_uint(E.y), tag0}, rl, es);
            SCHED();
            if (layer == 0) { FW_OK() static_for<kN3E>(LAMBDA(i_) { w2ld(std::integral_constant<int, NW2P + i_.value>{}); }); }
#pragma unroll
            for (int kk = 1; kk < 4; ++kk) cfma4(P[kk], l8, P[kk - 1][0], P[kk - 1][1], P[kk - 1][2], P[kk - 1][3], sv[kk - 1], sv[kk + 3], sv[kk + 7], sv[kk + 11]);
#pragma unroll
            for (int i = 0; i < 16; ++i) *(f32x2*)(smem + spl + (l_sg(layer, i >> 2) - SC + (i & 3) * 320)) = P[i & 3][i >> 2];
            if (layer == 0) partb();
            if (layer == 1) powers();
        }
        if (w == LBW) {
            site(); SCHED();
            f32x2 wg[17], t4[4];
            unsigned ea[15], tg[15];
            unsigned m14 = (unsigned)mt1;
            pins(m14);
            m14 *= RUN;
            const f32x2 z2 = {0.f, 0.f};
            wg[1] = cmul(l128, l128); wg[2] = cmul(wg[1], wg[1]); wg[3] = cmul(wg[2], wg[1]); wg[4] = cmul(wg[2], wg[2]);
#pragma unroll
            for (int i = 4; i < 16; i += 4) {
                cfma4(t4, wg[i], wg[1], wg[2], wg[3], wg[4], z2, z2, z2, z2);
#pragma unroll
                for (int e = 0; e < 4; ++e) wg[i + 1 + e] = t4[e];
            }
#pragma unroll
            for (int i = 0; i < 15; ++i) {
                const int ch = mt1 - 1 - i;
                if (i) pin(wg[i]);
                ea[i] = ch < 0 ? kNone : m14 - (unsigned)(i + 1) * RUN; tg[i] = ch < 0 ? 0u : tag0;
                pins(ea[i]); pins(tg[i]);
            }
            powers();
            f32x2 H0, H1;
            __builtin_amdgcn_s_sleep(layer ? KLB1 : KLB0);
            unsigned spins = dead ? kSpinCap : 0u;
            bool miss;
#pragma nounroll
            do {
                asm volatile("" ::: "memory");
                v4u eg[15];
#pragma unroll
                for (int i = 0; i < 15; ++i) eg[i] = ld128s(rl, eo, ea[i]);
                SCHED();
                unsigned x = __builtin_amdgcn_bitop3_b32(eg[0].y, eg[0].w, tg[0], 0x7E);
                H0 = f32x2{__uint_as_float(eg[0].x), __uint_as_float(eg[0].z)}; H1 = f32x2{0.f, 0.f};
#pragma unroll
                for (int i = 1; i < 15; i += 2) {
                    x |= __builtin_amdgcn_bitop3_b32(eg[i].y, eg[i].w, tg[i], 0x7E) | __builtin_amdgcn_bitop3_b32(eg[i + 1].y, eg[i + 1].w, tg[i + 1], 0x7E);
                    pin(x);
                    gacc2(H0, H1, wg[i], wg[i + 1], eg[i], eg[i + 1]);
                    SCHED();
                }
                miss = __builtin_amdgcn_ballot_w64(x != 0u) != 0ull;
            } while (miss && ++spins <= kSpinCap);
            dead = dead || miss;
            ORDER();
            *(f32x2*)(smem + spl + (L_CW - SC)) = H0 + H1;
            STAMP(layer ? 36 : 37)
            if (layer == 0) {
                static_for<NW3 - NW2P - kN3E>(LAMBDA(i_) { w2ld(std::integral_constant<int, NW2P + kN3E + i_.value>{}); });
            }
        }
        int w2_ = w;
        pins(w2_);
        if (layer == 0) UNDEF8(ca);
        if (layer == 0 && w2_ != LBW) {
            if (cv) {
                site(); SCHED();
                FW_OK()
                st_ur((unsigned)(size_t)(LAS char*)smem + (unsigned)uwa);
                STAMP(32)
                conv_part<0, kCR1>(ca, smem, ab, bb, w2a, [&]() __attribute__((always_inline)) {
                    v4u ad;
                    const unsigned dv = sdq << (adm & 16u);
#pragma unroll
                    for (int i = 0; i < 4; ++i) ad[i] = dv & (unsigned)__builtin_amdgcn_sbfe((int)adm, 8 + i, 1);
                    asm volatile("s_nop 1" : "+v"(ad));
                    return ad;
                }, cA, cB);
                partb();
                powers();
            } else {
                FW_OK()
                static_for<NW2A - NW2P>(LAMBDA(i_) { w2ld(std::integral_constant<int, NW2P + i_.value>{}); __builtin_amdgcn_s_sleep(1); });
                partb();
                powers();
            }
        }
        if (layer == 1 && w < LBW) { site(); SCHED(); powers(); }
        asm volatile("s_waitcnt lgkmcnt(0)" ::: "memory");
        __builtin_amdgcn_s_barrier();
        asm volatile("" ::: "memory");
        STAMP(layer ? 15 : 4)
        {
            float cw[5], ex[5];
#pragma unroll
            for (int nb = 0; nb < 5; ++nb) { cw[nb] = *(const float*)(smem + cwa + nb * 64); ex[nb] = *(const float*)(smem + sga + nb * 64); }
            v2u ta[5], td[5];
            if constexpr (TB) {
                cstepi(ex[0], ex[1], ex[2], ex[3], ex[4], cw[0], cw[1], cw[2], cw[3], cw[4], lqr, lqs);
                f32x2 cs[5];
                PIN5(ex);
#pragma unroll
                for (int nb = 0; nb < 5; ++nb) { cs[nb] = f32x2{ex[nb], dpp<0xB1>(ex[nb])}; pin(cs[nb]); }
                SCHED();
                static_for<5>(LAMBDA(n_) {
                    constexpr int nb = n_.value;
                    f32x2 h[4] = {{bv[0][nb], bv[1][nb]}, {bv[2][nb], bv[3][nb]}, {bv[4][nb], bv[5][nb]}, {bv[6][nb], bv[7][nb]}};
                    padd(h, pr[nb], ps[nb], cs[nb]);
                    *(v4u*)(smem + tbw + nb * 1024) = v4u{cvtpk(h[0].x, h[0].y), cvtpk(h[1].x, h[1].y), cvtpk(h[2].x, h[2].y), cvtpk(h[3].x, h[3].y)};
                    ta[nb] = __builtin_bit_cast(v2u, trread(smem, tbr + nb * 1024)); td[nb] = __builtin_bit_cast(v2u, trread(smem, tbr + nb * 1024 + 512));
                    SCHED();
                    if constexpr (nb >= LAG) { st128(v4u{ta[nb - LAG].x, ta[nb - LAG].y, td[nb - LAG].x, td[nb - LAG].y}, rl, hso + (nb - LAG) * 256); SCHED(); }
                });
            } else
            static_for<2>(LAMBDA(h_) {
                constexpr int n0 = 2 * h_.value;
                float hv[9][3];
                cstep3(hv[0], cw + n0, lqr + n0, lqs + n0, ex + n0);
                if constexpr (n0 == 0) {
                    chain24<0>(hv, bv, lr, ls);
                    chain24<0>(hv + 4, bv + 4, lr, ls);
                } else {
                    chain33<2>(hv, bv, lr, ls);
                    SCHED();
#pragma unroll
                    for (int rd = 0; rd < 2; ++rd) st128(v4u{ta[rd].x, ta[rd].y, td[rd].x, td[rd].y}, rl, hso + rd * 256);
                    SCHED();
                    chain33<2>(hv + 3, bv + 3, lr, ls);
                    chain32<2>(hv + 6, bv + 6, lr, ls);
                }
#pragma unroll
                for (int e = 0; e < (n0 ? 3 : 2); ++e) {
                    v4u o;
                    o.x = cvtpk(hv[1][e], hv[2][e]); o.y = cvtpk(hv[3][e], hv[4][e]);
                    o.z = cvtpk(hv[5][e], hv[6][e]); o.w = cvtpk(hv[7][e], hv[8][e]);
                    *(v4u*)(smem + tbw + (n0 + e) * 1024) = o;
                }
#pragma unroll
                for (int rd = n0; rd < (n0 ? 5 : 2); ++rd) { ta[rd] = __builtin_bit_cast(v2u, trread(smem, tbr + rd * 1024)); td[rd] = __builtin_bit_cast(v2u, trread(smem, tbr + rd * 1024 + 512)); }
                SCHED();
            });
#pragma unroll
            for (int rd = TB ? 5 - LAG : 2; rd < 5; ++rd) st128(v4u{ta[rd].x, ta[rd].y, td[rd].x, td[rd].y}, rl, hso + rd * 256);
            SCHED();
        }
#pragma unroll
        for (int s = 0; s < 10; ++s) pins(ks[s]);
        if constexpr (layer == 1) {
            partb();
            SCHED();
            if constexpr (OTR) {
                const int ln = (srca >> 4) & 63, od = (b0 >> 3) & 1, r0 = srca - 16 * ln;
                otw = (ln & 15) * 336 + (ln >> 4) * 16; otr = (ln & 12) * 336 + ((ln >> 4) + od) * 64 + (ln & 3) * 16; ots = otw + (od ? 0 : 256);
                ACC_TIE5(gt);
#pragma unroll
                for (int nb = 0; nb < 5; ++nb) *(v2u*)(smem + r0 + 10240 + otw / 2 + nb * 32) = gt[nb];
#pragma unroll
                for (int nb = 0; nb < 5; ++nb) gt[nb] = *(const v2u*)(smem + r0 + 10240 + (nb < 4 ? otr / 2 + nb * 168 : ots / 2));
                otw += r0; otr += r0; ots += r0;
                pin(otw); pin(otr); pin(ots);
                ACC_TIE5(gt);
                static_assert(MAP == 0 && (SC & 1023) == 0 && (INBOX & 1023) == 0 && L_CW + 320 <= SC + 2 * INBOX + 10240, "");
                SCHED();
            }
            DRAIN();
            st64(v2u{tag0, tag1}, rl, fho1);
        }
        STAMP(layer ? 16 : 5)
        if (cv) {
            if constexpr (layer == 0) site();
            SCHED();
            conv_part<kCR1, 8>(ca, smem, ab, bb, w2b, []() {}, cA, cB);
        } else if (layer == 0) {
            static_for<NB1>(LAMBDA(i_) { w2x(i_); __builtin_amdgcn_s_sleep(1); });
            vm_wait<NB1>();
            st64(v2u{tag0, tag1}, rl, fho1);
            static_for<NB - NB1 - NFH>(LAMBDA(i_) { w2x(std::integral_constant<int, NB1 + i_.value>{}); __builtin_amdgcn_s_sleep(1); });
            if (w == 3) static_for<NW2A - NW3>(LAMBDA(i_) { w2ld(std::integral_constant<int, NW3 + i_.value>{}); __builtin_amdgcn_s_sleep(1); });
            poll1(fho, wl);
            static_for<NFH>(LAMBDA(i_) { w2x(std::integral_constant<int, NB - NFH + i_.value>{}); __builtin_amdgcn_s_sleep(1); });
            static_assert(NFH <= NB - (NX - 50), "");
            UNDEF8(cA[LP]); UNDEF8(cB[LP]); UNDEF8(ca);
        }
        if (layer == 0) { unsigned f0_, f1_; asm volatile("s_waitcnt vmcnt(%2)\n\t" AR("%0", 36, 0) AR("%1", 36, 1) : "=v"(f0_), "=v"(f1_) : "i"(NFH) : "memory"); fh = v2u{f0_, f1_}; }
        STAMP(layer ? 29 : 6)
        }
        unsigned g[8][2];

            v2u pt[4];
            if (layer == 0) WAVE_RETRY(ISTAG(fh), fh = ld64(rl, fho))
            else { pipe_flag<KPF0>(pt, fho, wl, (u64)tag0 | ((u64)tag1 << 32), dead); ORDER(); }
            STAMP(layer ? 17 : 31)
            site(); SCHED();
            constexpr int RING2 = layer ? KR2 : KRT;
            v4u ar[RING2];
            constexpr int NRT = layer ? RING2 : (36 - KG0) / KRS + 2 < KRT ? (36 - KG0) / KRS + 2 : KRT;
            unsigned xs_[3][4];
            auto gate = LAMBDA(n_) {
                constexpr int n = n_.value;
                if constexpr (n >= 100) swp2n(g[2 * (n - 100)][0], g[2 * (n - 100) + 1][0], g[2 * (n - 100)][1], g[2 * (n - 100) + 1][1]);
                else if constexpr (n >= 0) cvt2(g[n][0], g[n][1], ca[n]);
            };
            if constexpr (layer == 0) {
#pragma unroll
                for (int i = 0; i < 2; ++i) { ar[i] = ld128s(rl, ho[0], ks[i]); SCHED(); }
                static_for<36 - KG0>(LAMBDA(n_) {
                    constexpr int n = n_.value, g = KG0 + n;
                    constexpr int a = g < 8 ? 0 : g < 15 ? 1 : g < 21 ? 2 : g < 26 ? 3 : g < 30 ? 4 : g < 33 ? 5 : g < 35 ? 6 : 7;
                    constexpr int e = g - (a * 8 - a * (a - 1) / 2), i = 2 + n / KRS;
                    mf(ca[a + e], cA[LP][a], cB[LP][e]);
                    SCHED();
                    if constexpr (n % KRS == 0 && i < NRT) ar[i] = ld128s(rl, ho[i / 10], ks[i % 10]);
                    if constexpr (n < 12) rda<40 + n>(xs_[n >> 2][n & 3]);
                    constexpr int gt_[12] = {0, 1, 2, -1, 100, -1, 3, -1, -1, 101, 4, -1};
                    if constexpr (g >= 24) gate(IC(gt_[g < 24 ? 0 : g - 24]));
                    site();
                    SCHED();
                });
                static_assert(KG0 <= 15 && NRT >= 12, "");
            } else {
#pragma unroll
                for (int i = 0; i < RING2; ++i) { ar[i] = ld128s(rl, ho[i / 10], ks[i % 10]); SCHED(); }
            }
        v4u gl[3];
        v2u f0;
        bool gearly = false;
        {
            const char* src = smem + srca;
            const char *glb1 = wl + 8 * RUN, *glb2 = wl + 12 * RUN;
            float zx[4], zd[4], zt[4], zz[5][4];
            if constexpr (STAMPS == 1) { STAMP(layer ? 50 : 40) }
            static_for<40>(LAMBDA(i_) {
                constexpr int i = i_.value, m = i / 10, s = i % 10;
                static_for<5>(LAMBDA(k_) {
                    constexpr int kk = k_.value, n = 2 * (i - 3) + kk / 2 - 3, nb = n / 8, u = n % 8, pv = i - 1 + RING2;
                    if constexpr (s == 0 && m < 3) mfa0<kk>(a2[m][kk], ar[i % RING2]); else mfa<5 * s + kk>(a2[m][kk], ar[i % RING2]);
                    SCHED();
                    constexpr int gu_[12] = {-1, 5, -1, -1, 102, 6, -1, -1, 7, -1, 103, -1};
                    if constexpr (layer == 0 && 5 * i + kk < 12) gate(IC(gu_[(5 * i + kk) % 12]));
                    if constexpr (layer == 0 && i == 25 && kk == 1) { pins(glb1); pins(glb2); }
                    if constexpr (layer == 0 && i >= 3 && i < 26 && (kk & 1)) {
                        if constexpr (n < 0) swp2n(xs_[n + 3][0], xs_[n + 3][2], xs_[n + 3][1], xs_[n + 3][3]);
                        else if constexpr (nb < 5) {
                            if constexpr (u < 2) unp2(zx[2 * u], zx[2 * u + 1], xs_[nb >> 1][2 * (nb & 1) + u]);
                            else if constexpr (u < 4) unp2(zd[2 * u - 4], zd[2 * u - 3], dsk[nb][u - 2]);
                            else if constexpr (u < 6) fma2(zt[2 * u - 8], zt[2 * u - 7], zx[2 * u - 8], zx[2 * u - 7], rsr, rsr, rsm, rsm);
                            else fma2(zz[nb][2 * u - 12], zz[nb][2 * u - 11], zt[2 * u - 12], zt[2 * u - 11], zd[2 * u - 12], zd[2 * u - 11], zx[2 * u - 12], zx[2 * u - 11]);
                        }
                    }
                    if constexpr (i == 0 && kk == 0 && layer == 1) { pin(pt[0]); pin(pt[1]); pin(pt[2]); pin(pt[3]); }
                    if constexpr (layer == 0 && i < 2 && kk > 0 && 12 + 4 * i + kk - 1 >= NRT && 12 + 4 * i + kk - 1 < RING2) ar[12 + 4 * i + kk - 1] = ld128s(rl, ho[1], ks[2 + 4 * i + kk - 1]);
                    if constexpr (layer == 0 && i == kGSI && kk > 0) __builtin_amdgcn_raw_buffer_store_b128(v4u{g[2 * kk - 2][0], g[2 * kk - 2][1], g[2 * kk - 1][0], g[2 * kk - 1][1]}, rs, gso + ((kk - 1) & 1) * 1024, kk < 3 ? pol : pol ^ 8192u, SC1);
                    if constexpr (layer == 0 && i > 2 && i <= 7 && kk == (i == kGSI ? 0 : 2)) {
                        { unsigned d0_, d1_; rda<20 + 2 * (i - 3)>(d0_); rda<21 + 2 * (i - 3)>(d1_); dsk[i - 3] = v2u{d0_, d1_}; }
                    }
                    if constexpr (kk == 0 && i > 0 && pv < 40) ar[pv % RING2] = ld128s(rl, ho[pv / 10], ks[pv % 10]);
                    constexpr int s1 = s - 1 - (KMB && layer == 0 && m == 1 ? kFGI - 10 : 0);
                    if constexpr (kk == 2 && m >= 1 && s1 >= 0 && s1 < 5) *(f32x4*)(smem + dst[m - 1] + s1 * 1024) = a2[m - 1][s1];
                    site();
                    if constexpr (i >= 37) a2[i - 37][kk] = *(const f32x4*)(src + (i - 37) * 5120 + kk * 1024);
                    SCHED();
                });
                if constexpr (i == 10 && (layer || !KMB)) { asm volatile("s_waitcnt lgkmcnt(0)" ::: "memory"); __builtin_amdgcn_s_barrier(); asm volatile("" ::: "memory"); }
                if constexpr (i == kFGI && layer == 0) {
                    static_assert(kFGI >= 10 && kFGI <= 14, "");
                    asm volatile("s_waitcnt vmcnt(%0) lgkmcnt(0)" ::"i"(kFGI - kGSI) : "memory");
                    __builtin_amdgcn_s_barrier(); asm volatile("" ::: "memory");
                    st64(v2u{tag0, tag1}, rs, fgo1);
                }
                if constexpr (i == 26 && layer == 0) {
#pragma unroll
                    for (int nb = 0; nb < 5; ++nb) { a2[3][nb] = f32x4{zz[nb][0], zz[nb][1], zz[nb][2], zz[nb][3]}; pin(a2[3][nb]); }
                }
                if constexpr (i == kGPI && layer == 0) gld2s<0>(f0, fgo, wl);
                if constexpr (i == kGCI && layer == 0) {
                    wait1(f0);
                    static_for<39 - i>(LAMBDA(r_) { pin(ar[(i + 1 + r_.value) % RING2]); });
                    gearly = __builtin_amdgcn_ballot_w64(!ISTAG(f0)) == 0ull;
                    ORDER();
                    undef3(gl);
                    if (__builtin_expect(gearly, 1)) { gld4t<0>(gl[0], glo, wl); gld4t<0>(gl[1], glo, glb1); gld4t<0>(gl[2], glo, glb2); }
                    else if (G2ND) poll2(fgo, fgo, wl);
                }
                if constexpr (i == 36) {
                    asm volatile("s_waitcnt lgkmcnt(0)" ::: "memory");
                    __builtin_amdgcn_s_barrier(); asm volatile("" ::: "memory");
                }
                if constexpr (STAMPS == 1 && (i == 0 || i == 9 || i == 10 || i == 15 || i == 25 || i == 35 || i == 36)) { STAMP((layer ? 51 : 41) + (i == 0 ? 0 : i == 9 ? 1 : i == 10 ? 2 : i == 15 ? 3 : i == 25 ? 4 : i == 35 ? 5 : 6)) }
                SCHED();
            });
            STAMP(layer ? 18 : 8)
        }
#pragma unroll
        for (int nb = 0; nb < 5; ++nb) a2[0][nb] = (a2[0][nb] + a2[1][nb]) + a2[2][nb];
        asm volatile("s_nop 1" : "+v"(a2[3][0]), "+v"(a2[3][1]), "+v"(a2[3][2]), "+v"(a2[3][3]), "+v"(a2[3][4]),
                     "+v"(a2[0][0]), "+v"(a2[0][1]), "+v"(a2[0][2]), "+v"(a2[0][3]), "+v"(a2[0][4]));
        float ty;
        {
            f32x4 s1, s2;
#pragma unroll
            for (int nb = 0; nb < 5; ++nb) {
                const f32x4 y = a2[3][nb] + a2[0][nb];
                xin[nb] = y;
                if (nb == 0) { s1 = y; s2 = y * y; } else { s1 += y; s2 += y * y; }
            }
            ty = xsum2h(hsum4(s1), hsum4(s2));
            st64(v2u{__float_as_uint(ty), tag0}, rl, sgs);
        }
        SCHED();
        STAMP(layer ? 19 : 9)
        if constexpr (layer == 1 && OTR) {
#pragma unroll
            for (int nb = 0; nb < 5; ++nb) *(f32x4*)(smem + otw + nb * 64) = xin[nb];
#pragma unroll
            for (int nb = 0; nb < 5; ++nb) xin[nb] = *(const f32x4*)(smem + (nb < 4 ? otr + nb * 336 : ots));
            SCHED();
        }
        int lane_o, b_o = b0, w_o = w0;
        asm volatile("v_mbcnt_lo_u32_b32 %0, -1, 0\n\tv_mbcnt_hi_u32_b32 %0, -1, %0" : "=v"(lane_o));
        asm volatile("" : "+s"(b_o), "+s"(w_o));
        const int lane = lane_o, b = b_o, w = w_o;
        const int c = lane & 15, q = lane >> 4;
        TILES()
        if (layer == 0 && __builtin_expect(!gearly, 0)) {
            if (G2ND) {
                unsigned f0_, f1_;
                asm volatile("s_waitcnt vmcnt(1)\n\t" AR("%0", 36, 0) AR("%1", 36, 1) : "=v"(f0_), "=v"(f1_)::"memory");
                f0 = v2u{f0_, f1_};
            }
            STAMP(63)
            WAVE_RETRY(ISTAG(f0), f0 = ld64(rl, fgo))
            gld4t<0>(gl[0], glo, wl); gld4t<0>(gl[1], glo, wl + 8 * RUN); gld4t<0>(gl[2], glo, wl + 12 * RUN);
            asm volatile("s_waitcnt vmcnt(0)" : "+v"(gl[0]), "+v"(gl[1]), "+v"(gl[2])::"memory");
        }
        if (layer == 0) {
            asm volatile("s_waitcnt vmcnt(1)" : "+v"(gl[0]), "+v"(gl[1]), "+v"(gl[2])::"memory");
            STAMP(60)
            char* tb = smem + SC + w * INBOX;
            *(v4u*)(tb + lane * 16) = gl[0]; *(v4u*)(tb + 1024 + lane * 16) = gl[1]; *(v4u*)(tb + 1536 + lane * 16) = gl[2];
#pragma unroll
            for (int nb = 0; nb < 5; ++nb) gt[nb] = __builtin_bit_cast(v2u, trread(tb, (16 * nb + 4 * q + (c >> 2)) * 32 + (c & 3) * 8));
        } else ACC_TIE5(gt);
        SCHED();
        f32x4 gf[5], yg[5];
#pragma unroll
        for (int nb = 0; nb < 5; ++nb) { gf[nb] = unpk4(gt[nb]); yg[nb] = xin[nb] * gf[nb]; pin(gf[nb]); pin(yg[nb]); }
        unsigned xs = OFF_X2F + (unsigned)(((mt2 >> 3) & 1) * PG + ((2 * (mt2 & 7) + (w >> 1)) * 8 + ((c >> 2) & 1) * 4 + (nt2 >> 1)) * RUN + (10 * (nt2 & 1) + 2 * (q & 1) + (q >> 1)) * 256 + (8 * (w & 1) + 4 * (c >> 3) + (c & 3)) * 16);
        unsigned pso = q < 2 ? OFF_PS + (unsigned)((mt1 & 7) * RUN + (((mt2 & 1) * 4 + w) * 8 + nt2) * 128 + c * 8 + q * 4) : kNone;
        unsigned fxo1 = lane ? kNone : OFF_FX + (unsigned)((mt1 & 7) * RUN + ((2 * (mt2 & 1) + (w >> 1)) * 16 + nt2 * 2 + (w & 1)) * 128);
        unsigned oo = (unsigned)(((64 * mt2 + 16 * w + c) * D_ + 80 * nt2 + 4 * q) * 4);
        unsigned o4 = (unsigned)(((64 * mt2 + 16 * w + (c & 12)) * D_ + 80 * nt2 + 16 * (q + (nt2 & 1)) + 4 * (c & 3)) * 4);
        if (OTR) oo += (nt2 & 1) ? 0u : 256u;
        unsigned xs2 = (q & 1) ? kNone : xs + 2048u;
        if (layer == 0) { pin(xs); pin(xs2); pin(pso); pin(fxo1); } else { pin(oo); if (OTR) pin(o4); }
        SCHED();
        {
            float a, d;
            STAMP(layer ? 62 : 61)
            if constexpr (layer == 0) site();
            pipe_gran<layer ? PDS : PDS0>(a, d, sgo, wl, tag0, dead);
            STAMP(layer ? 20 : 10)
            xsum2(a, d);
            const float me = a * (1.0f / D_);
            const float ri = __builtin_amdgcn_rsqf(fmaxf(d * (1.0f / D_) - me * me, 0.f) + 1e-5f), mr = -me * ri;
            if constexpr (layer == 1 && OTR) {
                static_for<5>(LAMBDA(j_) {
                    constexpr int jj = (j_.value + 4) % 5;
                    const float rj = jj < 4 ? dpp<0x55 * (jj & 3)>(ri) : ri, mj = jj < 4 ? dpp<0x55 * (jj & 3)>(mr) : mr;
                    const f32x4 o = yg[jj] * rj + (gf[jj] * mj + xin[jj]);
                    __builtin_amdgcn_raw_buffer_store_b128(__builtin_bit_cast(v4u, o), ro, jj < 4 ? o4 : oo, jj < 4 ? jj * 2560 : 0, OUTP);
                });
            } else if constexpr (layer == 0 && XI) {
#pragma unroll
                for (int i = 0; i < 3; ++i) {
                    v2u pk[2];
#pragma unroll
                    for (int e = 0; e < (i < 2 ? 2 : 1); ++e) {
                        const f32x4 x = yg[2 * i + e] * ri + (gf[2 * i + e] * mr + xin[2 * i + e]);
                        xin[2 * i + e] = x;
                        pk[e] = v2u{cvtpk(x.x, x.y), cvtpk(x.z, x.w)};
                    }
                    const v2u pa = pk[0], pb = pk[i < 2];
                    const auto r0 = __builtin_amdgcn_permlane16_swap(pa.x, pb.x, false, false);
                    const auto r1 = __builtin_amdgcn_permlane16_swap(pa.y, pb.y, false, false);
                    st128(v4u{r0[0], r1[0], r0[1], r1[1]}, rl, i < 2 ? xs + i * 1024 : xs2);
                    SCHED();
                }
            } else {
#pragma unroll
                for (int nb = 0; nb < 5; ++nb) xin[nb] = yg[nb] * ri + (gf[nb] * mr + xin[nb]);
            }
        }
        if constexpr (layer == 0) {
            v2u pk[5];
#pragma unroll
            for (int nb = 0; nb < 5; ++nb) { const f32x4 x = xin[nb]; pk[nb] = v2u{cvtpk(x.x, x.y), cvtpk(x.z, x.w)}; }
#pragma unroll
            for (int i = 0; i < (XI ? 0 : 3); ++i) {
                const v2u pa = pk[2 * i], pb = pk[i < 2 ? 2 * i + 1 : 4];
                const auto r0 = __builtin_amdgcn_permlane16_swap(pa.x, pb.x, false, false);
                const auto r1 = __builtin_amdgcn_permlane16_swap(pa.y, pb.y, false, false);
                if (i < 2 || (q & 1) == 0) st128(v4u{r0[0], r1[0], r0[1], r1[1]}, rl, xs + i * 1024);
            }
            {
                f32x4 s1, s2;
#pragma unroll
                for (int nb = 0; nb < 5; ++nb) { const f32x4 x = xin[nb]; if (nb == 0) { s1 = x; s2 = x * x; } else { s1 += x; s2 += x * x; } }
                __builtin_amdgcn_raw_buffer_store_b32(__float_as_uint(xsum2h(hsum4(s1), hsum4(s2))), rl, pso, 0, SC1);
            }
            SCHED();
            int w1lane0 = (c & 1) * 10240 + (c >> 1) * 128 + ((q ^ ((c & 1) | ((c >> 2) << 1))) * 16);
            int w1lane1 = (c & 1) * 10240 + (c >> 1) * 128 + (((4 + q) ^ ((c & 1) | ((c >> 2) << 1))) * 16);
            unsigned fxo = OFF_FX + (unsigned)((mt1 & 7) * RUN + w * 2048 + (lane >> 2) * 128);
            unsigned xo = OFF_X2F + (unsigned)(((mt1 >> 2) & 1) * PG + (4 * (mt1 & 3) + w) * 8 * RUN + lane * 16), xo1 = xo + 4 * RUN;
            const int row = 8 * q + (c & 7);
            unsigned po = OFF_PS + (unsigned)((mt1 & 7) * RUN + ((2 * w + (row >> 4)) * 8 + 4 * (c >> 3)) * 128 + (row & 15) * 8);
            int w1hi0 = w1lane0 + 61440, w1hi1 = w1lane1 + 61440;
            int rsw = l_rs(1, 0) + w * INBOX + lane * 4;
            if (GW) pin(rsw);
            pin(w1lane0); pin(w1lane1); pin(w1hi0); pin(w1hi1); pin(fxo); pin(xo); pin(xo1); pin(po);
            v4u wf[2][5];
#pragma unroll
            for (int nb = 0; nb < 5; ++nb) wf[0][nb] = *(const v4u*)(smem + w1lane0 + nb * 20480);
            v2u tg2 = {tag0, tag1};
            pin(tg2);
            SCHED();
            DRAIN();
            st64(tg2, rl, fxo1);
            if (b == 255 && w == 0) FLAG(OFF_NONCE)
            STAMP(11)
            addrs(std::integral_constant<int, 1>{}, lane, w, b);
            v2u pt[4];
            pipe_flag<KPFX>(pt, fxo, wl, (u64)tag0 | ((u64)tag1 << 32), dead);
            ORDER();
            STAMP(12)
            v4u ar[RING1][2];
#pragma unroll
            for (int s = 0; s < RING1; ++s)
#pragma unroll
                for (int mb = 0; mb < 2; ++mb) { ar[s][mb] = ld128s(rl, mb ? xo1 : xo, kso(s)); SCHED(); }
            v2u ps[4];
#pragma unroll
            for (int i = 0; i < 4; ++i) ps[i] = ld64(rl, po + i * 128);
            SCHED();
            float t1, t2, rinv, mri, rsel, rr_[8], rm_[8];
            static_for<20>(LAMBDA(s_) {
                constexpr int s = s_.value, cu = s & 1, nx = cu ^ 1;
                LGKM0() SCHED();
                static_for<10>(LAMBDA(g_) {
                    constexpr int g = g_.value, mb = g / 5, k = g % 5;
                    if constexpr (s == 0) mf0(acc[mb][k], ar[0][mb], wf[0][k]); else mf(acc[mb][k], ar[s % RING1][mb], wf[cu][k]);
                    SCHED();
                    if constexpr (s == 0 && g == 0) { pin(pt[0]); pin(pt[1]); pin(pt[2]); pin(pt[3]); }
                    if constexpr (s < 19 && g < 3) wf[nx][g] = *(const v4u*)(smem + (nx ? w1lane1 : w1lane0) + g * 20480 + ((s + 1) >> 1) * 1024);
                    if constexpr (s < 19 && (g == 3 || g == 4)) wf[nx][g] = *(const v4u*)(smem + (nx ? w1hi1 : w1hi0) + (g - 3) * 20480 + ((s + 1) >> 1) * 1024);
                    if constexpr (g == 5 && s + RING1 < 20) ar[s % RING1][0] = ld128s(rl, xo, kso(s + RING1));
                    if constexpr (g == 2 && s > 0 && s - 1 + RING1 < 20) ar[(s - 1) % RING1][1] = ld128s(rl, xo1, kso(s - 1 + RING1));
                    constexpr int z = GW ? s + 3 : s;
                    if constexpr (z == 8 && g == 8) { t1 = __uint_as_float(ps[0].x) + __uint_as_float(ps[1].x); pin(t1); t2 = __uint_as_float(ps[0].y) + __uint_as_float(ps[1].y); pin(t2); }
                    if constexpr (z == 9 && g == 6) { t1 += __uint_as_float(ps[2].x); pin(t1); t2 += __uint_as_float(ps[2].y); pin(t2); }
                    if constexpr (z == 9 && g == 7) { t1 += __uint_as_float(ps[3].x); pin(t1); t2 += __uint_as_float(ps[3].y); pin(t2); }
                    if constexpr (z == 10 && g == 6) { t1 += dpp<0x128>(t1); t2 += dpp<0x128>(t2); pin(t1); pin(t2); }
                    if constexpr (z == 10 && g == 7) { t1 *= (1.0f / D_); pin(t1); t2 *= (1.0f / D_); pin(t2); }
                    if constexpr (z == 10 && g == 8) { t2 = fmaxf(t2 - t1 * t1, 0.f); pin(t2); }
                    if constexpr (z == 11 && g == 6) { rinv = __builtin_amdgcn_rsqf(t2 + 1e-5f); pin(rinv); }
                    if constexpr (z == 11 && g == 8) { mri = -t1 * rinv; pin(mri); }
                    if constexpr (!GW && s == 12 && g == 4) *(float*)(smem + l_rs(1, 0) + w * INBOX + lane * 4) = c < 8 ? rinv : mri;
                    if constexpr (GW && s == 19 && g == 1) selhi(rsel, mri);
                    if constexpr (GW && s == 19 && g == 0) { rsel = rinv; pin(rsel); }
                    if constexpr (GW && s == 19 && g == 2) *(float*)(smem + rsw) = rsel;
                    if constexpr (z >= 12 && z < 15 && g >= 6 && g < 9 && 3 * (z - 12) + g - 6 < 8) bc2b<3 * (z - 12) + g - 6>(rr_[3 * (z - 12) + g - 6], rm_[3 * (z - 12) + g - 6], rinv, mri);
                    if constexpr (!GW && s >= 15 && s < 19 && g >= 5) TT_GAP(5 * (s - 15) + g - 5)
                    if constexpr (GW && s >= 12 && s < 19 && g >= 6 && g < 9 && 3 * (s - 12) + g - 6 < 20) TT_GAP(3 * (s - 12) + g - 6)
                    site();
                    SCHED();
                });
            });
            rr[0] = f32x4{rr_[0], rr_[1], rr_[2], rr_[3]}; rr[1] = f32x4{rr_[4], rr_[5], rr_[6], rr_[7]};
            asm volatile("kpf_end2:");
            ACC_FENCE10(acc[0], acc[1]);
            STAMP(13)
        } else {
#pragma unroll
            for (int nb = 0; nb < (OTR ? 0 : 5); ++nb) __builtin_amdgcn_raw_buffer_store_b128(__builtin_bit_cast(v4u, xin[nb]), ro, oo + 64 * nb, 0, OUTP);
            STAMP(21)
            STAMP_DUMP()
            asm volatile("kpf_end:");
        }
    };
    layer_body(std::integral_constant<int, 0>{});
    layer_body(std::integral_constant<int, 1>{});
}

extern "C" void kernel_launch(void* const* d_in, const int* in_sizes, int n_in,
                              void* d_out, int out_size, void* d_ws, size_t ws_size,
                              hipStream_t stream) {
    Params p;
    p.x0 = (const u16*)d_in[0];
    p.Lre = (const u16*)d_in[1];
    p.Lim = (const u16*)d_in[2];
    p.Bre = (const u16*)d_in[3];
    p.Bim = (const u16*)d_in[4];
    p.Cre = (const u16*)d_in[5];
    p.Cim = (const u16*)d_in[6];
    p.logdt = (const u16*)d_in[7];
    p.Dskip = (const u16*)d_in[8];
    p.k = (const u16*)d_in[10];
    p.v = (const u16*)d_in[11];
    p.shk = (const u16*)d_in[12];
    p.ssmk = (const u16*)d_in[13];
    p.ssmD = (const u16*)d_in[14];
    p.ws = (char*)d_ws;
    p.out = (float*)d_out;
    k_main<<<256, 256, 0, stream>>>(p);
}
```

```cpp
#include <hip/hip_runtime.h>
#include <stdint.h>
#include <utility>

#define STAMPS 0

typedef unsigned short u16;
typedef unsigned long long u64;
typedef unsigned v4u __attribute__((ext_vector_type(4)));
typedef unsigned v2u __attribute__((ext_vector_type(2)));
typedef float f32x4 __attribute__((ext_vector_type(4)));
typedef float f32x2 __attribute__((ext_vector_type(2)));
typedef short s16x4 __attribute__((ext_vector_type(4)));
typedef __bf16 bf16x2v __attribute__((ext_vector_type(2)));
#define LAS __attribute__((address_space(3)))
#define DI __device__ __forceinline__

constexpr int S_ = 2048, D_ = 640;
#define KG0 12
#define KRT 12
#define KRS 2
#define KB1 4
#define KMB 1
#define SBT 2
#define LAG 1
#define OTR 1
#define XI 1
#define GW 0
#define POL 0
#define SKP 0
#define SK0 0
#define SK5 850
#define SKX 0
#define SK1 0
#define SK2 60
#define PRM0 2
#define PRM1 1
#define KR1 5
#define KR2 10
constexpr int RING1 = KR1;
#define KCR1 3
constexpr int kCR1 = KCR1;
#define KXA 10
#define KN3E 17
#define KN3 6
constexpr int kXA = KXA;
constexpr int kN3E = KN3E, kN3 = KN3;
#define KFHK 4
#define KFHP 12
constexpr int kFHK = KFHK, kFHP = KFHP;
#define KGPI 27
#define KGCI 33
#define KFGI 11
#define G2ND 1
#define KGSI 2
constexpr int kGSI = KGSI, kFGI = KFGI, kGPI = KGPI, kGCI = KGCI;

constexpr unsigned OFF_NONCE = 0;
constexpr unsigned OFF_CV = 1024;
constexpr unsigned OFF_ST = 4096;
constexpr unsigned OFF_FW = OFF_ST + 65536;
constexpr unsigned OFF_FK = OFF_FW + 131072;
constexpr unsigned OFF_KVT = OFF_FK + 81920;
constexpr unsigned RUN = 16384, PG = 4194304, MID = 1536;
constexpr unsigned OFF_SG = 8 * PG;
constexpr unsigned OFF_FH = OFF_SG + 32 * RUN;
constexpr unsigned OFF_FX = OFF_FH + 16 * RUN;
constexpr unsigned OFF_PS = OFF_FX + 8 * RUN;
constexpr unsigned OFF_EG = OFF_PS + 8 * RUN + MID;
constexpr unsigned OFF_X2F = 9 * PG + MID;
constexpr unsigned OFF_HF = 11 * PG + MID;
constexpr unsigned OFF_W2F = 19 * PG + MID;
constexpr unsigned OFF_GT = 23 * PG;
constexpr unsigned WS_BYTES = 25 * PG;
constexpr unsigned kso(int s) { return (unsigned)(s / 5) * RUN + (unsigned)(s % 5) * 1024u; }
constexpr unsigned kNone = 0x40000000u;
constexpr unsigned OFF_DBG = 0x1800000;
#define MAP 0
#define TILES() const int xq = b & 7, j = b >> 3, mt2 = MAP ? 8 * (xq >> 1) + (j >> 2) : 4 * xq + (j >> 3), nt2 = MAP ? 4 * (xq & 1) + (j & 3) : j & 7, mt1 = mt2 >> 1, \
    nt1 = MAP ? 8 * (xq & 1) + (j & 7) : j & 15, i32 = 16 * (mt2 & 1) + mt1;

constexpr int SC = 102400;
constexpr int REGSZ = 13056;
constexpr int L_XR = SC;
constexpr int L_CO = SC + 12288;
constexpr int L_KB = SC + 51712;
constexpr int L_TAB = SC + 55808;
constexpr int L_CW = SC + 39168;
constexpr int INBOX = 15360;
constexpr int l_rs(int l, int w) { return l ? SC + w * INBOX : L_KB + w * 256; }
constexpr int l_sg(int l, int w) { return l ? SC + w * INBOX + 256 : SC + (w == 0 ? 43264 : w == 1 ? 46080 : w == 2 ? 52736 : 56320); }
constexpr int l_tb(int l, int w) { return l ? SC + w * INBOX + 2048 : SC + 40960 + w * 5120; }
constexpr int LDS_BYTES = 163840;
constexpr int CV_E = 4608;
constexpr int CV_O = CV_E + (1056 + 15) * 4;

struct Params {
    const u16 *x0, *Lre, *Lim, *Bre, *Bim, *Cre, *Cim, *logdt, *Dskip, *k, *v, *shk, *ssmk, *ssmD;
    char* ws;
    float* out;
};

#define MF "v_mfma_f32_16x16x32_bf16 "
DI void mf(f32x4& c, v4u a, v4u b) { asm(MF "%0, %1, %2, %0" : "+v"(c) : "v"(a), "v"(b)); }
DI void mf0(f32x4& c, v4u a, v4u b) { asm(MF "%0, %1, %2, 0" : "=&v"(c) : "v"(a), "v"(b)); }
constexpr int W2R = 56;
#define WA(n) "a[%c" #n ":%c" #n "+3]"
template <int R> DI void mfa(f32x4& c, v4u a) { asm(MF "%0, " WA(2) ", %1, %0" : "+v"(c) : "v"(a), "i"(W2R + 4 * R)); }
template <int R> DI void mfa0(f32x4& c, v4u a) { asm(MF "%0, " WA(2) ", %1, 0" : "=&v"(c) : "v"(a), "i"(W2R + 4 * R)); }
#define ACC_FENCE10(c, d) asm volatile("" : "+v"(c[0]), "+v"(c[1]), "+v"(c[2]), "+v"(c[3]), "+v"(c[4]), "+v"(d[0]), "+v"(d[1]), "+v"(d[2]), "+v"(d[3]), "+v"(d[4]))
#define ACC_TIE5(c) asm volatile("" : "+v"(c[0]), "+v"(c[1]), "+v"(c[2]), "+v"(c[3]), "+v"(c[4]))
#define QP " quad_perm:[1,0,3,2] row_mask:0xf bank_mask:0xf"
DI void cstep(float (&t)[5], const float (&h)[5], const float (&lr)[5], const float (&ls)[5], float b0, float b1, float b2, float b3, float b4) {
    asm("v_fma_f32 %0, %10, %5, %20\n\tv_fma_f32 %1, %11, %6, %21\n\tv_fma_f32 %2, %12, %7, %22\n\tv_fma_f32 %3, %13, %8, %23\n\tv_fma_f32 %4, %14, %9, %24\n\t"
        "v_fmac_f32_dpp %0, %5, %15" QP "\n\tv_fmac_f32_dpp %1, %6, %16" QP "\n\tv_fmac_f32_dpp %2, %7, %17" QP "\n\tv_fmac_f32_dpp %3, %8, %18" QP "\n\tv_fmac_f32_dpp %4, %9, %19" QP
        : "=&v"(t[0]), "=&v"(t[1]), "=&v"(t[2]), "=&v"(t[3]), "=&v"(t[4])
        : "v"(h[0]), "v"(h[1]), "v"(h[2]), "v"(h[3]), "v"(h[4]), "v"(lr[0]), "v"(lr[1]), "v"(lr[2]), "v"(lr[3]), "v"(lr[4]),
          "v"(ls[0]), "v"(ls[1]), "v"(ls[2]), "v"(ls[3]), "v"(ls[4]), "v"(b0), "v"(b1), "v"(b2), "v"(b3), "v"(b4));
}
DI void cstep3(float* t, const float* h, const float* lr, const float* ls, const float* b) {
    asm("v_fma_f32 %0, %6, %3, %12\n\tv_fma_f32 %1, %7, %4, %13\n\tv_fma_f32 %2, %8, %5, %14\n\t"
        "v_fmac_f32_dpp %0, %3, %9" QP "\n\tv_fmac_f32_dpp %1, %4, %10" QP "\n\tv_fmac_f32_dpp %2, %5, %11" QP
        : "=&v"(t[0]), "=&v"(t[1]), "=&v"(t[2])
        : "v"(h[0]), "v"(h[1]), "v"(h[2]), "v"(lr[0]), "v"(lr[1]), "v"(lr[2]), "v"(ls[0]), "v"(ls[1]), "v"(ls[2]), "v"(b[0]), "v"(b[1]), "v"(b[2]));
}
template <int o> DI void chain24(float (*t)[3], const float (*b)[5], const float* lr, const float* ls) {
    asm("v_fma_f32 %0, %18, %16, %8\n\t" "v_fma_f32 %1, %19, %17, %9\n\t" "v_fmac_f32_dpp %0, %16, %20" QP "\n\t" "v_fmac_f32_dpp %1, %17, %21" QP "\n\t"
        "v_fma_f32 %2, %18, %0, %10\n\t" "v_fma_f32 %3, %19, %1, %11\n\t" "v_fmac_f32_dpp %2, %0, %20" QP "\n\t" "v_fmac_f32_dpp %3, %1, %21" QP "\n\t"
        "v_fma_f32 %4, %18, %2, %12\n\t" "v_fma_f32 %5, %19, %3, %13\n\t" "v_fmac_f32_dpp %4, %2, %20" QP "\n\t" "v_fmac_f32_dpp %5, %3, %21" QP "\n\t"
        "v_fma_f32 %6, %18, %4, %14\n\t" "v_fma_f32 %7, %19, %5, %15\n\t" "v_fmac_f32_dpp %6, %4, %20" QP "\n\t" "v_fmac_f32_dpp %7, %5, %21" QP "\n\t"
        : "=&v"(t[1][0]), "=&v"(t[1][1]), "=&v"(t[2][0]), "=&v"(t[2][1]), "=&v"(t[3][0]), "=&v"(t[3][1]), "=&v"(t[4][0]), "=&v"(t[4][1])
        : "v"(b[0][o + 0]), "v"(b[0][o + 1]), "v"(b[1][o + 0]), "v"(b[1][o + 1]), "v"(b[2][o + 0]), "v"(b[2][o + 1]), "v"(b[3][o + 0]), "v"(b[3][o + 1]), "v"(t[0][0]), "v"(t[0][1]), "v"(lr[o + 0]), "v"(lr[o + 1]), "v"(ls[o + 0]), "v"(ls[o + 1]));
}
template <int o> DI void chain33(float (*t)[3], const float (*b)[5], const float* lr, const float* ls) {
    asm("v_fma_f32 %0, %21, %18, %9\n\t" "v_fma_f32 %1, %22, %19, %10\n\t" "v_fma_f32 %2, %23, %20, %11\n\t" "v_fmac_f32_dpp %0, %18, %24" QP "\n\t" "v_fmac_f32_dpp %1, %19, %25" QP "\n\t" "v_fmac_f32_dpp %2, %20, %26" QP "\n\t"
        "v_fma_f32 %3, %21, %0, %12\n\t" "v_fma_f32 %4, %22, %1, %13\n\t" "v_fma_f32 %5, %23, %2, %14\n\t" "v_fmac_f32_dpp %3, %0, %24" QP "\n\t" "v_fmac_f32_dpp %4, %1, %25" QP "\n\t" "v_fmac_f32_dpp %5, %2, %26" QP "\n\t"
        "v_fma_f32 %6, %21, %3, %15\n\t" "v_fma_f32 %7, %22, %4, %16\n\t" "v_fma_f32 %8, %23, %5, %17\n\t" "v_fmac_f32_dpp %6, %3, %24" QP "\n\t" "v_fmac_f32_dpp %7, %4, %25" QP "\n\t" "v_fmac_f32_dpp %8, %5, %26" QP "\n\t"
        : "=&v"(t[1][0]), "=&v"(t[1][1]), "=&v"(t[1][2]), "=&v"(t[2][0]), "=&v"(t[2][1]), "=&v"(t[2][2]), "=&v"(t[3][0]), "=&v"(t[3][1]), "=&v"(t[3][2])
        : "v"(b[0][o + 0]), "v"(b[0][o + 1]), "v"(b[0][o + 2]), "v"(b[1][o + 0]), "v"(b[1][o + 1]), "v"(b[1][o + 2]), "v"(b[2][o + 0]), "v"(b[2][o + 1]), "v"(b[2][o + 2]), "v"(t[0][0]), "v"(t[0][1]), "v"(t[0][2]), "v"(lr[o + 0]), "v"(lr[o + 1]), "v"(lr[o + 2]), "v"(ls[o + 0]), "v"(ls[o + 1]), "v"(ls[o + 2]));
}
template <int o> DI void chain32(float (*t)[3], const float (*b)[5], const float* lr, const float* ls) {
    asm("v_fma_f32 %0, %15, %12, %6\n\t" "v_fma_f32 %1, %16, %13, %7\n\t" "v_fma_f32 %2, %17, %14, %8\n\t" "v_fmac_f32_dpp %0, %12, %18" QP "\n\t" "v_fmac_f32_dpp %1, %13, %19" QP "\n\t" "v_fmac_f32_dpp %2, %14, %20" QP "\n\t"
        "v_fma_f32 %3, %15, %0, %9\n\t" "v_fma_f32 %4, %16, %1, %10\n\t" "v_fma_f32 %5, %17, %2, %11\n\t" "v_fmac_f32_dpp %3, %0, %18" QP "\n\t" "v_fmac_f32_dpp %4, %1, %19" QP "\n\t" "v_fmac_f32_dpp %5, %2, %20" QP "\n\t"
        : "=&v"(t[1][0]), "=&v"(t[1][1]), "=&v"(t[1][2]), "=&v"(t[2][0]), "=&v"(t[2][1]), "=&v"(t[2][2])
        : "v"(b[0][o + 0]), "v"(b[0][o + 1]), "v"(b[0][o + 2]), "v"(b[1][o + 0]), "v"(b[1][o + 1]), "v"(b[1][o + 2]), "v"(t[0][0]), "v"(t[0][1]), "v"(t[0][2]), "v"(lr[o + 0]), "v"(lr[o + 1]), "v"(lr[o + 2]), "v"(ls[o + 0]), "v"(ls[o + 1]), "v"(ls[o + 2]));
}
DI void pstep(f32x2& xr, f32x2& xs, f32x2 pr, f32x2 ps, f32x2 m) {
    asm("v_pk_mul_f32 %0, %2, %4 op_sel_hi:[1,0]\n\tv_pk_mul_f32 %1, %3, %4 op_sel_hi:[1,0]\n\t"
        "v_pk_fma_f32 %0, %3, %4, %0 op_sel:[0,1,0] neg_lo:[1,0,0] neg_hi:[1,0,0]\n\tv_pk_fma_f32 %1, %2, %4, %1 op_sel:[0,1,0]"
        : "=&v"(xr), "=&v"(xs) : "v"(pr), "v"(ps), "v"(m));
}
DI void padd(f32x2 (&h)[4], const f32x2 (&pr)[2], const f32x2 (&ps)[2], f32x2 c) {
    f32x2 d;
    asm("v_pk_mul_f32 %4, %6, %9 op_sel:[1,0]\n\tv_pk_fma_f32 %0, %5, %9, %0 op_sel_hi:[1,0,1]\n\tv_pk_fma_f32 %1, %6, %9, %1 op_sel_hi:[1,0,1]\n\t"
        "v_pk_fma_f32 %4, %8, %9, %4 op_sel:[1,1,0] op_sel_hi:[1,0,1] neg_hi:[1,0,0]\n\tv_pk_fma_f32 %0, %7, %9, %0 op_sel:[0,1,0]\n\tv_pk_fma_f32 %1, %8, %9, %1 op_sel:[0,1,0]\n\t"
        "v_pk_fma_f32 %2, %5, %4, %2 op_sel_hi:[1,0,1]\n\tv_pk_fma_f32 %3, %6, %4, %3 op_sel_hi:[1,0,1]\n\tv_pk_fma_f32 %2, %7, %4, %2 op_sel:[0,1,0]\n\tv_pk_fma_f32 %3, %8, %4, %3 op_sel:[0,1,0]"
        : "+v"(h[0]), "+v"(h[1]), "+v"(h[2]), "+v"(h[3]), "=&v"(d) : "v"(pr[0]), "v"(pr[1]), "v"(ps[0]), "v"(ps[1]), "v"(c));
}
DI void cstepi(float& b0, float& b1, float& b2, float& b3, float& b4, float h0, float h1, float h2, float h3, float h4, const float (&lr)[5], const float (&ls)[5]) {
    asm("v_fmac_f32 %0, %10, %5\n\tv_fmac_f32 %1, %11, %6\n\tv_fmac_f32 %2, %12, %7\n\tv_fmac_f32 %3, %13, %8\n\tv_fmac_f32 %4, %14, %9\n\t"
        "v_fmac_f32_dpp %0, %5, %15" QP "\n\tv_fmac_f32_dpp %1, %6, %16" QP "\n\tv_fmac_f32_dpp %2, %7, %17" QP "\n\tv_fmac_f32_dpp %3, %8, %18" QP "\n\tv_fmac_f32_dpp %4, %9, %19" QP
        : "+v"(b0), "+v"(b1), "+v"(b2), "+v"(b3), "+v"(b4)
        : "v"(h0), "v"(h1), "v"(h2), "v"(h3), "v"(h4), "v"(lr[0]), "v"(lr[1]), "v"(lr[2]), "v"(lr[3]), "v"(lr[4]), "v"(ls[0]), "v"(ls[1]), "v"(ls[2]), "v"(ls[3]), "v"(ls[4]));
}
#define UNDEF8(c) asm volatile("" : "=v"(c[0]), "=v"(c[1]), "=v"(c[2]), "=v"(c[3]), "=v"(c[4]), "=v"(c[5]), "=v"(c[6]), "=v"(c[7]))
#define PIN5(t) asm volatile("s_nop 1" : "+v"(t[0]), "+v"(t[1]), "+v"(t[2]), "+v"(t[3]), "+v"(t[4]))
DI unsigned cvtpk(float lo, float hi) {
    f32x2 v = {lo, hi};
    return __builtin_bit_cast(unsigned, __builtin_convertvector(v, bf16x2v));
}
DI float lo16f(unsigned u) { return __uint_as_float(u << 16); }
DI float hi16f(unsigned u) { return __uint_as_float(u & 0xffff0000u); }
DI float rbf(float v) { return lo16f(cvtpk(v, 0.f)); }
template <int CTRL> DI float dpp(float x) {
    return __int_as_float(__builtin_amdgcn_update_dpp(0, __float_as_int(x), CTRL, 0xF, 0xF, true));
}
DI float rowsum16(float x) {
    x += dpp<0x128>(x); x += dpp<0x124>(x); x += dpp<0x122>(x); x += dpp<0x121>(x);
    return x;
}
DI float xsum32(float x) { auto r = __builtin_amdgcn_permlane32_swap(__float_as_uint(x), __float_as_uint(x), false, false); return __uint_as_float(r[0]) + __uint_as_float(r[1]); }
DI void xsum2(float& a, float& d) {
    auto r = __builtin_amdgcn_permlane16_swap(__float_as_uint(a), __float_as_uint(d), false, false);
    const float s = __uint_as_float(r[0]) + __uint_as_float(r[1]);
    const float t = xsum32(s);
    auto u = __builtin_amdgcn_permlane16_swap(__float_as_uint(t), __float_as_uint(t), false, false);
    a = __uint_as_float(u[0]); d = __uint_as_float(u[1]);
}
DI float xsum2h(float a, float d) {
    auto r = __builtin_amdgcn_permlane16_swap(__float_as_uint(a), __float_as_uint(d), false, false);
    return xsum32(__uint_as_float(r[0]) + __uint_as_float(r[1]));
}
DI float hsum4(f32x4 v) { return (v.x + v.y) + (v.z + v.w); }
typedef __amdgpu_buffer_rsrc_t rsrc_t;
constexpr int SC1 = 16;
DI v4u ld128(rsrc_t rs, unsigned off) { return __builtin_amdgcn_raw_buffer_load_b128(rs, off, 0, SC1); }
DI v4u ld128s(rsrc_t rs, unsigned off, unsigned soff) { return __builtin_amdgcn_raw_buffer_load_b128(rs, off, soff, SC1); }
DI v2u ld64(rsrc_t rs, unsigned off) { return __builtin_amdgcn_raw_buffer_load_b64(rs, off, 0, SC1); }
DI void st128(v4u v, rsrc_t rs, unsigned off) { __builtin_amdgcn_raw_buffer_store_b128(v, rs, off, 0, SC1); }
DI void st64(v2u v, rsrc_t rs, unsigned off) { __builtin_amdgcn_raw_buffer_store_b64(v, rs, off, 0, SC1); }
#define LGKM0() { __builtin_amdgcn_s_waitcnt(0xC07F); asm volatile("" ::: "memory"); }
#define DRAIN() asm volatile("s_waitcnt vmcnt(0)" ::: "memory")
#define ORDER() __builtin_amdgcn_fence(__ATOMIC_ACQUIRE, "wavefront")
#define SCHED() __builtin_amdgcn_sched_barrier(0)
constexpr unsigned kSpinCap = 1u << 17;
#define WAVE_WAIT(...)                                                      \
    {                                                                       \
        unsigned spins_ = dead ? kSpinCap : 0u;                             \
        _Pragma("nounroll") for (;;) {                                      \
            asm volatile("" ::: "memory");                                  \
            bool ok;                                                        \
            __VA_ARGS__;                                                    \
            if (__builtin_amdgcn_ballot_w64(!ok) == 0ull) break;            \
            if (++spins_ > kSpinCap) { dead = true; break; }                \
            __builtin_amdgcn_s_sleep(1);                                    \
        }                                                                   \
        ORDER();                                                            \
    }
#define KPD1 1
#define KPDS 0
#define KPDS0 0
#define KPF0 11
#define KPFX 3
#define KPF1 3
constexpr int PD1 = KPD1, PDS = KPDS, PDS0 = KPDS0;
#define PL(r) "global_load_dwordx2 " r ", %5, %6 sc1\n\t"
#define PS_(r) "s_waitcnt vmcnt(1)\n\tv_cmp_ne_u64 vcc, %7, " r "\n\ts_cbranch_vccz 2f\n\t" PL(r)
template <int D0> DI void pipe_flag(v2u (&t)[4], unsigned off, const void* base, u64 tag, bool& dead) {
    unsigned n = dead ? 0u : kSpinCap;
    asm volatile("s_sleep %8\n\t" PL("%0") "s_sleep %9\n\t" PL("%1")
                 "1:\n\t" PS_("%0") PS_("%1") "s_sub_u32 %4, %4, 1\n\ts_cbranch_scc0 1b\n2:"
                 : "=&v"(t[0]), "=&v"(t[1]), "=&v"(t[2]), "=&v"(t[3]), "+s"(n) : "v"(off), "s"(base), "s"(tag), "i"(D0), "i"(KPF1) : "memory", "vcc", "scc");
    dead = __builtin_amdgcn_readfirstlane(n) == ~0u;
}
#define WAVE_RETRY(OK, ...) { if (__builtin_expect(__builtin_amdgcn_ballot_w64(!(OK)) != 0ull, 0)) WAVE_WAIT({ __VA_ARGS__; ok = OK; }) ORDER(); }
#define STAMP(i)
#define STAMP_DUMP()

#define KLB0 0
#define KLB1 12
DI void site() { asm volatile(""); }
template <int N> DI void skip() { if constexpr (N > 0) asm volatile("s_branch 1f\n\t.rept %c0\n\ts_nop 0\n\t.endr\n1:" ::"i"(N)); }
template <class F, int... I> DI void static_for_impl(std::integer_sequence<int, I...>, F&& f) { (f(std::integral_constant<int, I>{}), ...); }
template <int N, class F> DI void static_for(F&& f) { static_for_impl(std::make_integer_sequence<int, N>{}, f); }
#define IC(n) std::integral_constant<int, (n)>{}
#define LAMBDA(v) [&](auto v) __attribute__((always_inline))
#define PK2 " op_sel:[1,1,0] op_sel_hi:[1,0,1] neg_lo:[1,0,0]"
DI f32x2 cmul(f32x2 a, f32x2 b) {
    f32x2 t;
    asm("v_pk_mul_f32 %0, %1, %2 op_sel_hi:[0,1]\n\ts_nop 0\n\tv_pk_fma_f32 %0, %1, %2, %0" PK2 "\n\ts_nop 0" : "=&v"(t) : "v"(a), "v"(b));
    return t;
}
DI f32x2 cfma(f32x2 a, f32x2 b, f32x2 c) {
    f32x2 t;
    asm("v_pk_fma_f32 %0, %1, %2, %3 op_sel_hi:[0,1,1]\n\ts_nop 0\n\tv_pk_fma_f32 %0, %1, %2, %0" PK2 "\n\ts_nop 0" : "=&v"(t) : "v"(a), "v"(b), "v"(c));
    return t;
}
DI void cfma4(f32x2 (&t)[4], f32x2 a, f32x2 b0, f32x2 b1, f32x2 b2, f32x2 b3, f32x2 c0, f32x2 c1, f32x2 c2, f32x2 c3) {
    asm("v_pk_fma_f32 %0, %4, %5, %9 op_sel_hi:[0,1,1]\n\tv_pk_fma_f32 %1, %4, %6, %10 op_sel_hi:[0,1,1]\n\tv_pk_fma_f32 %2, %4, %7, %11 op_sel_hi:[0,1,1]\n\tv_pk_fma_f32 %3, %4, %8, %12 op_sel_hi:[0,1,1]\n\t"
        "v_pk_fma_f32 %0, %4, %5, %0" PK2 "\n\tv_pk_fma_f32 %1, %4, %6, %1" PK2 "\n\tv_pk_fma_f32 %2, %4, %7, %2" PK2 "\n\tv_pk_fma_f32 %3, %4, %8, %3" PK2 "\n\ts_nop 0"
        : "=&v"(t[0]), "=&v"(t[1]), "=&v"(t[2]), "=&v"(t[3]) : "v"(a), "v"(b0), "v"(b1), "v"(b2), "v"(b3), "v"(c0), "v"(c1), "v"(c2), "v"(c3));
}
DI void gacc2(f32x2& h0, f32x2& h1, f32x2 w0, f32x2 w1, v4u e0, v4u e1) {
    const v2u a0 = {e0.x, e0.y}, b0 = {e0.z, e0.w}, a1 = {e1.x, e1.y}, b1 = {e1.z, e1.w};
    asm("v_pk_fma_f32 %0, %4, %2, %0 op_sel_hi:[0,1,1]\n\tv_pk_fma_f32 %1, %6, %3, %1 op_sel_hi:[0,1,1]\n\t"
        "v_pk_fma_f32 %0, %5, %2, %0 op_sel:[0,1,0] op_sel_hi:[0,0,1] neg_lo:[1,0,0]\n\tv_pk_fma_f32 %1, %7, %3, %1 op_sel:[0,1,0] op_sel_hi:[0,0,1] neg_lo:[1,0,0]"
        : "+v"(h0), "+v"(h1) : "v"(w0), "v"(w1), "v"(a0), "v"(b0), "v"(a1), "v"(b1));
}
DI s16x4 trread(char* smem, int byteoff) {
    return __builtin_amdgcn_ds_read_tr16_b64_v4i16((LAS s16x4*)(smem + byteoff));
}

#define CV_LOAD_A(A, a, rr_, hf) { const unsigned* ap_ = (const unsigned*)(smem + ab[(a) >> 1] + (((a) & 1) ? 0 : 512) + 64 * (7 - (rr_)) + 8 * (hf)); A[a][2 * (hf)] = ap_[0]; A[a][2 * (hf) + 1] = ap_[1]; }
#define CV_LOAD_B(B, e, rr_) { B[e] = *(const v4u*)(smem + bb + 64 * (7 - (rr_)) + 512 * (e)); }
constexpr int cv_pair(int lo, int hi, int n) {
    for (int a = 0; a < 8; ++a)
        for (int e = 0; e < 8; ++e)
            if (a + e >= lo && a + e < hi && n-- == 0) return 8 * a + e;
    return -1;
}
template <int R, int PF, bool OWN, class F>
DI void conv_round(f32x4 (&ca)[8], v4u (&A)[8], const v4u (&B)[8], v4u (&An)[8], v4u (&Bn)[8], const char* smem, const int (&ab)[4], int bb, F&& fill) {
    static_for<(R == 7 ? KG0 : 36)>(LAMBDA(g_) {
        constexpr int g = g_.value, a = cv_pair(0, 8, g) / 8, e = cv_pair(0, 8, g) % 8;
        mf(ca[a + e], A[a], B[e]);
        SCHED();
        if constexpr (OWN && g < 12) { CV_LOAD_A(A, 2 + g / 2, R, g & 1) SCHED(); }
        if constexpr (g < 4 * PF && (g & 3) != 3) { if constexpr ((g & 3) == 2) CV_LOAD_B(Bn, g >> 2, R + 1) else CV_LOAD_A(An, g >> 2, R + 1, g & 1) SCHED(); }
        site();
        if constexpr (R == 7) SCHED();
        if constexpr ((g & 3) == 3 && g < 28) { fill(std::integral_constant<int, 7 * R + g / 4>{}); SCHED(); }
    });
}
template <int R0, int R1, class F, class M>
DI void conv_part(f32x4 (&ca)[8], const char* smem, const int (&ab)[4], int bb, F&& fill, M&& mid, v4u (&A)[2][8], v4u (&B)[2][8]) {
    if constexpr (R0 > 0) { CV_LOAD_A(A[0], 0, R0, 0) CV_LOAD_A(A[0], 0, R0, 1) SCHED(); }
    static_for<8>(LAMBDA(a_) { constexpr int a = a_.value; CV_LOAD_B(B[0], a, R0) SCHED(); });
    if constexpr (R0 > 0) { CV_LOAD_A(A[0], 1, R0, 0) CV_LOAD_A(A[0], 1, R0, 1) SCHED(); }
    else {
        CV_LOAD_A(A[0], 0, 0, 0) CV_LOAD_A(A[0], 0, 0, 1) SCHED();
        const v4u ad = mid();
        SCHED();
        static_for<8>(LAMBDA(e_) {
            constexpr int e = e_.value;
            mf0(ca[e], ad, B[0][e]); SCHED();
            if constexpr (e < 7) { CV_LOAD_A(A[0], e + 1, 0, 0) CV_LOAD_A(A[0], e + 1, 0, 1) SCHED(); }
        });
    }
    static_for<R1 - R0>(LAMBDA(r_) {
        constexpr int r = r_.value;
        conv_round<R0 + r, (R0 + r + 1 < R1 ? 8 : 0), (r == 0 && R0 > 0)>(ca, A[r & 1], B[r & 1], A[(r & 1) ^ 1], B[(r & 1) ^ 1], smem, ab, bb, fill);
    });
}
template <int IMM> DI void gld(v4u& dst, unsigned voff, const void* sbase) {
    asm volatile("global_load_dwordx4 %0, %1, %2 offset:%3" : "=v"(dst) : "v"(voff), "s"(sbase), "i"(IMM));
}
#define OUTP 16
#define NTM 31
template <int IMM, int BIT> DI void gldn(v4u& dst, unsigned voff, const void* sbase) {
    if constexpr ((NTM >> BIT) & 1) asm volatile("global_load_dwordx4 %0, %1, %2 offset:%3 nt" : "=v"(dst) : "v"(voff), "s"(sbase), "i"(IMM));
    else gld<IMM>(dst, voff, sbase);
}
DI void gldu16(unsigned& dst, unsigned voff, const void* sbase) { asm volatile("global_load_ushort %0, %1, %2" : "=v"(dst) : "v"(voff), "s"(sbase)); }
template <int IMM> DI void gld64(v4u& dst, const void* a) {
    if constexpr ((NTM >> 5) & 1) asm volatile("global_load_dwordx4 %0, %1, off offset:%2 nt" : "=v"(dst) : "v"(a), "i"(IMM));
    else asm volatile("global_load_dwordx4 %0, %1, off offset:%2" : "=v"(dst) : "v"(a), "i"(IMM));
}
template <int V> DI void SETM0(unsigned ldsbase) { asm volatile("s_add_u32 m0, %0, %1" ::"s"(ldsbase), "i"(V)); }
template <int GOFF> DI void GLD0(unsigned voff, const char* sbase) { asm volatile("global_load_lds_dwordx4 %0, %1 offset:%2" ::"v"(voff), "s"(sbase), "i"(GOFF) : "memory"); }
template <int GOFF, int V> DI void GLD1(unsigned voff, const char* sbase, unsigned ldsbase) {
    asm volatile("global_load_lds_dwordx4 %0, %1 offset:%3\n\ts_add_u32 m0, %2, %4" ::"v"(voff), "s"(sbase), "s"(ldsbase), "i"(GOFF), "i"(V) : "memory");
}
constexpr int dma_goff(int it) { return it < 40 ? (it >> 2) * 128 : ((it - 100) / 5) * 256; }
constexpr int dma_loff(int it) { return it < 40 ? L_XR + ((it >> 2) % 3) * 4096 + (it & 3) * 1024 : ((it - 100) % 5) * 20480 + ((it - 100) / 5) * 2048; }
#define KFD0 10
#define KNF 13
#define KNW 11
#define KW2S 17
#define KW2M 0x4A
#define KFWP 14
constexpr int kW2S = KW2S, kW2M = KW2M;
constexpr int NW2P = (20 - kW2S) * __builtin_popcount(kW2M);
constexpr int P1_PRE = 36;
constexpr int p1_pre(int i) { return i < 5 ? 100 + i : i < 13 ? i - 5 : i < 18 ? 105 + (i - 13) : i < 22 ? 8 + (i - 18) : i < 27 ? 110 + (i - 22) : 400; }
constexpr int p1_item(int s, int g) {
    if (s == 7 && g == 0) return 324;
    if (g == 9) return s < 10 ? 115 + s : s == 11 ? 202 : s == 12 ? 203 : -1;
    if (s == KFWP && g == 1) return 208;
    if (s >= kW2S && ((kW2M >> g) & 1)) return 500 + (s - kW2S) * __builtin_popcount(kW2M) + __builtin_popcount(kW2M & ((1 << g) - 1));
    if ((s & 1) == 0 && s <= 12) return g >= 5 ? 4 * (s / 2 + 3) + (g - 5) : -1;
    if (s == 14 && g == 5) return 207;
    return -1;
}
constexpr int p1_cnt(int s, int g) {
    int n = P1_PRE;
    for (int t = 0; t <= s; ++t)
        for (int u = 0; u < 10; ++u) {
            if (t == s && u >= g) break;
            if (p1_item(t, u) >= 0 && p1_item(t, u) != 319) ++n;
        }
    return n;
}
constexpr int p1_pos(int code) {
    for (int i = 0; i < P1_PRE; ++i) if (p1_pre(i) == code) return i;
    for (int t = 0; t < 20; ++t)
        for (int u = 0; u < 10; ++u) if (p1_item(t, u) == code) return p1_cnt(t, u);
    return -1000;
}
constexpr int p1_cap(int s, int g) {
    int u = (s < 19 && g < 5) + (g == 9 && s < 19);
    if ((s & 1) && s < 19 && g >= 5 && g < 9) u += 1 + (g == 5);
    if ((s & 1) == 0 && g == 4) u += 1;
    const int it = p1_item(s, g), nt = g < 9 ? p1_item(s, g + 1) : -1;
    u += (it >= 0 && it < 500 && it != 208) + (nt >= 0 && nt < 200);
    if ((s == 18 && g == 1) || (s == 19 && g == 2) || (g == 9 && ((s & 3) == 2 || s == 17))) u += 2;
    return u < 2 ? 2 - u : 0;
}
constexpr int p1_slot(int s0, int s, int g, int gmax) {
    int n = 0;
    for (int t = s0; t <= s; ++t)
        for (int u = 0; u < gmax && !(t == s && u >= g); ++u) n += p1_cap(t, u);
    return n;
}
constexpr int p1_lim(int n) { return n > 63 ? 63 : n; }
template <int IMM> DI void gld4s(v4u& dst, unsigned voff, const void* sbase) { asm volatile("global_load_dwordx4 %0, %1, %2 offset:%3 sc1" : "=v"(dst) : "v"(voff), "s"(sbase), "i"(IMM)); }
template <int IMM> DI void gld2s(v2u& dst, unsigned voff, const void* sbase) { asm volatile("global_load_dwordx2 %0, %1, %2 offset:%3 sc1" : "=v"(dst) : "v"(voff), "s"(sbase), "i"(IMM)); }
template <class T> DI void pin(T& v) { asm volatile("" : "+v"(v)); }
DI void mul2(float& a, float& b, float x, float y, float s) { asm volatile("v_mul_f32 %0, %2, %4\n\tv_mul_f32 %1, %3, %4" : "=&v"(a), "=&v"(b) : "v"(x), "v"(y), "v"(s)); }
DI void rot1(unsigned& a, unsigned x) { asm volatile("v_alignbit_b32 %0, %1, %1, 16" : "=v"(a) : "v"(x)); }
DI void mix1(unsigned& a, unsigned x, unsigned y) { asm volatile("v_bfi_b32 %0, %3, %1, %2" : "=v"(a) : "v"(x), "v"(y), "s"(0xffffu)); }
DI void ror1(unsigned& o, unsigned prev) { asm volatile("v_mov_b32_dpp %0, %1 wave_ror:1 row_mask:0xf bank_mask:0xf" : "=v"(o) : "v"(prev)); }
DI void shr1b(unsigned& o, unsigned x) { asm volatile("v_mov_b32_dpp %0, %1 wave_shr:1 row_mask:0xf bank_mask:0xf" : "+v"(o) : "v"(x)); }
DI void xor1(float& a, unsigned x, unsigned s) { asm volatile("v_xor_b32 %0, %1, %2" : "=v"(a) : "v"(x), "v"(s)); }
template <int G> DI void bc1(float& a, unsigned x) { asm volatile("v_mov_b32_dpp %0, %1 row_newbcast:%2 row_mask:0xf bank_mask:0xf" : "=v"(a) : "v"(x), "i"(G)); }
template <int G> DI void mulb(float& o, unsigned x, float y) { asm volatile("v_mul_f32_dpp %0, %1, %2 row_newbcast:%3 row_mask:0xf bank_mask:0xf" : "=v"(o) : "v"(x), "v"(y), "i"(G)); }
template <int N> DI void lgkm() { __builtin_amdgcn_s_waitcnt(0xC07F | (N << 8)); asm volatile("" ::: "memory"); }
DI void unp2(float& a, float& b, unsigned x) { asm volatile("v_lshlrev_b32 %0, 16, %2\n\tv_and_b32 %1, 0xffff0000, %2" : "=&v"(a), "=&v"(b) : "v"(x)); }
#define GL(a) "global_load_dwordx4 a[" #a ":" #a "+3], %7, %8 sc1\n\tglobal_load_dwordx4 a[" #a "+4:" #a "+7], %7, %8 offset:256 sc1\n\t"
#define AR(d, a, o) "v_accvgpr_read_b32 " d ", a[" #a "+" #o "]\n\t"
#define GS_(a) "s_waitcnt vmcnt(4)\n\t" AR("%2", a, 1) AR("%3", a, 3) AR("%4", a, 5) AR("%5", a, 7) AR("%0", a, 0) AR("%1", a, 2)                              \
    "v_xor_b32 %2, %9, %2\n\tv_bitop3_b32 %4, %4, %9, %2 bitop3:0xbe\n\tv_bitop3_b32 %4, %3, %9, %4 bitop3:0xbe\n\tv_bitop3_b32 %4, %5, %9, %4 bitop3:0xbe\n\t" \
    AR("%2", a, 4) AR("%3", a, 6) "v_cmp_ne_u32 vcc, 0, %4\n\ts_cbranch_vccz 2f\n\t" GL(a)
template <int A> DI void gran_req(unsigned off, const void* base) {
    asm volatile("global_load_dwordx4 a[%c2:%c2+3], %0, %1 sc1\n\tglobal_load_dwordx4 a[%c2+4:%c2+7], %0, %1 offset:256 sc1" ::"v"(off), "s"(base), "i"(A));
}
template <int D0, int NP> DI void pipe_gran(float& a, float& d, unsigned off, const void* base, unsigned tag, bool& dead) {
    unsigned n = dead ? 0u : kSpinCap, y, z;
    float a1, d1;
    if constexpr (NP < 1) { __builtin_amdgcn_s_sleep(D0); gran_req<32>(off, base); __builtin_amdgcn_s_sleep(PD1); }
    if constexpr (NP < 2) { gran_req<40>(off, base); __builtin_amdgcn_s_sleep(PD1); }
    asm volatile(GL(48)
                 "1:\n\t" GS_(32) GS_(40) GS_(48) "s_sub_u32 %6, %6, 1\n\ts_cbranch_scc0 1b\n2:"
                 : "=&v"(a), "=&v"(d), "=&v"(a1), "=&v"(d1), "=&v"(y), "=&v"(z), "+s"(n) : "v"(off), "s"(base), "s"(tag) : "memory", "vcc", "scc");
    dead = __builtin_amdgcn_readfirstlane(n) == ~0u;
    a += a1; d += d1;
}
DI void fma2(float& a, float& b, float x, float y, float s, float t, float u, float v) {
    asm volatile("v_fma_f32 %0, %2, %4, %6\n\tv_fma_f32 %1, %3, %5, %7" : "=&v"(a), "=&v"(b) : "v"(x), "v"(y), "v"(s), "v"(t), "v"(u), "v"(v));
}
DI void selhi(float& a, float x) { asm volatile("v_mov_b32_dpp %0, %1 quad_perm:[0,1,2,3] row_mask:0xf bank_mask:0xc" : "+v"(a) : "v"(x)); }
DI void cvt2(unsigned& a, unsigned& b, f32x4 v) { asm volatile("v_cvt_pk_bf16_f32 %0, %2, %3\n\tv_cvt_pk_bf16_f32 %1, %4, %5" : "=&v"(a), "=&v"(b) : "v"(v.x), "v"(v.y), "v"(v.z), "v"(v.w)); }
DI void swp2n(unsigned& a, unsigned& b, unsigned& c, unsigned& d) { asm volatile("v_permlane16_swap_b32 %0, %1\n\tv_permlane16_swap_b32 %2, %3" : "+v"(a), "+v"(b), "+v"(c), "+v"(d)); }
template <int G> DI void bc2b(float& a, float& b, float x, float y) {
    asm volatile("v_mov_b32_dpp %0, %2 row_newbcast:%4 row_mask:0xf bank_mask:0xf\n\tv_mov_b32_dpp %1, %3 row_newbcast:%4 row_mask:0xf bank_mask:0xf" : "=&v"(a), "=&v"(b) : "v"(x), "v"(y), "i"(G));
}
template <class T> DI void pins(T& v) { asm volatile("" : "+s"(v)); }
template <int I> DI void gld_w2(unsigned voff, const void* sbase) {
    asm volatile("global_load_dwordx4 " WA(2) ", %0, %1 offset:%3 sc1" ::"v"(voff), "s"(sbase), "i"(W2R + 4 * I), "i"((I % 5) * 1024 - 2048));
}
DI void undef4(v4u (&x)[4]) { asm volatile("" : "=v"(x[0]), "=v"(x[1]), "=v"(x[2]), "=v"(x[3])); }
DI void wait1(v2u& v) { asm volatile("s_waitcnt vmcnt(0)" : "+v"(v)::"memory"); }
DI void ld_ur(unsigned o, const void* base) {
    asm volatile("global_load_dwordx4 a[40:40+3], %0, %1 sc1\n\tglobal_load_dwordx4 a[44:44+3], %0, %1 offset:1024 sc1\n\t"
                 "global_load_dwordx4 a[48:48+3], %0, %1 offset:2048 sc1\n\tglobal_load_dwordx4 a[52:52+3], %0, %1 offset:3072 sc1" ::"v"(o), "s"(base));
}
DI void st_ur(unsigned a) {
    asm volatile("s_waitcnt vmcnt(%1)\n\tds_write_b128 %0, a[40:40+3] offset:480\n\tds_write_b128 %0, a[44:44+3] offset:1504\n\t"
                 "ds_write_b128 %0, a[48:48+3] offset:2528\n\tds_write_b128 %0, a[52:52+3] offset:3552" ::"v"(a), "i"(2 * __builtin_popcount(kW2M)) : "memory");
}
template <int A, int IMM> DI void lda4(unsigned o, const void* base) { asm volatile("global_load_dwordx4 a[%c2:%c2+3], %0, %1 offset:%3" ::"v"(o), "s"(base), "i"(A), "i"(IMM)); }
template <int A, int IMM> DI void lda2(unsigned o, const void* base) { asm volatile("global_load_dwordx2 a[%c2:%c2+1], %0, %1 offset:%3" ::"v"(o), "s"(base), "i"(A), "i"(IMM)); }
template <int A> DI void rda(unsigned& v) { asm volatile("v_accvgpr_read_b32 %0, a[%c1+0]" : "=v"(v) : "i"(A)); }
DI void poll1(unsigned a, const void* base) { asm volatile("global_load_dwordx2 a[36:36+1], %0, %1 sc1" ::"v"(a), "s"(base)); }
DI void poll2(unsigned a, unsigned b, const void* base) { asm volatile("global_load_dwordx2 a[36:36+1], %0, %2 sc1\n\tglobal_load_dwordx2 a[36+2:36+3], %1, %2 sc1" ::"v"(a), "v"(b), "s"(base)); }
DI void touch_(unsigned a, const void* base) { asm volatile("global_load_dword a[32+1], %0, %1" ::"v"(a), "s"(base)); }
template <int N> DI void wait_st(v2u& a, v4u& b) { asm volatile("s_waitcnt vmcnt(%2)" : "+v"(a), "+v"(b) : "i"(N) : "memory"); }
DI void undef3(v4u (&x)[3]) { asm volatile("" : "=v"(x[0]), "=v"(x[1]), "=v"(x[2])); }
template <int IMM> DI void gld4t(v4u& dst, unsigned voff, const void* sbase) { asm volatile("global_load_dwordx4 %0, %1, %2 offset:%3 sc1" : "+v"(dst) : "v"(voff), "s"(sbase), "i"(IMM)); }
template <int N> DI void vm_wait() { asm volatile("s_waitcnt vmcnt(%0)" ::"i"(N) : "memory"); }
DI float dot2a(unsigned a, unsigned b, float c) {
    return __builtin_amdgcn_fdot2_f32_bf16(__builtin_bit_cast(bf16x2v, a), __builtin_bit_cast(bf16x2v, b), c, false);
}
DI void disc(unsigned l0, unsigned l1, unsigned l2, f32x2& lam, f32x2& co) {
    const float dt = rbf(__builtin_amdgcn_exp2f(lo16f(l2) * 1.44269504088896f));
    const float lre = lo16f(l0), lim = lo16f(l1);
    const float e_ = __builtin_amdgcn_exp2f(dt * lre * 1.44269504088896f);
    const float rv = __builtin_amdgcn_fractf(dt * lim * 0.159154943091895f);
    lam = f32x2{e_ * __builtin_amdgcn_cosf(rv), e_ * __builtin_amdgcn_sinf(rv)};
    const float nr = lam.x - 1.0f, ni = lam.y, id = __builtin_amdgcn_rcpf(lre * lre + lim * lim);
    co = f32x2{(nr * lre + ni * lim) * id, (ni * lre - nr * lim) * id};
}
DI f32x4 unpk4(v2u u) { return f32x4{lo16f(u.x), hi16f(u.x), lo16f(u.y), hi16f(u.y)}; }
#define D2 "v_dot2_f32_bf16 "
#define CP "v_cvt_pk_bf16_f32 "
DI void d2x4(unsigned* o, unsigned a0, unsigned a1, unsigned a2, unsigned a3, unsigned b0, unsigned b1, unsigned b2, unsigned b3) {
    asm volatile(D2 "%0, %4, %8, 0\n\t" D2 "%1, %5, %9, 0\n\t" D2 "%2, %6, %10, 0\n\t" D2 "%3, %7, %11, 0"
                 : "=&v"(o[0]), "=&v"(o[1]), "=&v"(o[2]), "=&v"(o[3]) : "v"(a0), "v"(a1), "v"(a2), "v"(a3), "v"(b0), "v"(b1), "v"(b2), "v"(b3));
}

__global__ __launch_bounds__(256, 1) __attribute__((aligned(4096))) void k_main(Params p) {
    __shared__ __attribute__((aligned(16))) char smem[LDS_BYTES];
    p.ws += (0u - (unsigned)(size_t)p.ws) & 0x1FFFFFu;
    const rsrc_t rs = __builtin_amdgcn_make_buffer_rsrc((void*)p.ws, 0, WS_BYTES, 0x00020000);
    const int w0 = __builtin_amdgcn_readfirstlane(threadIdx.x >> 6), b0 = blockIdx.x;
    unsigned pol, sdo;
    const char* wl;
    rsrc_t rl;
    const rsrc_t ro = __builtin_amdgcn_make_buffer_rsrc((void*)p.out, 0, S_ * D_ * 4, 0x00020000);
    bool dead = false;
    asm volatile("" ::: "a255");
    float SW[5];
    f32x4 xin[5];
    v2u gt[5], dsk[5];
    f32x4 acc[2][5];
    f32x4 rr[2];
    float tt[2][5][4];
    float lr[5], ls[5], lqr[5], lqs[5];
    f32x2 l8, l32, l128;
    bool kearly;
    unsigned sdq;
    int sgn_, ab[4], bb, zpa, uwa, spa, tbw, tbr, cwa, spl, dst[3];
    unsigned sgo, sgs, glo, gso, fgo, fgo1;
    int srca;
    unsigned adm, dso, w2o, wro, fwo, xso, fho1, fho, hso, eo, es, ho[4];
    bool cv0;
    int mt1_;
    auto addrs = [&](auto layer_, int lane, int w, int b) __attribute__((always_inline)) {
        constexpr int layer = layer_.value;
        const int c = lane & 15, q = lane >> 4;
        TILES()
        static_assert(l_sg(0, 0) == SC + 0xA900 && l_sg(0, 1) == SC + 0xB400 && l_sg(0, 2) == SC + 0xCE00 && l_sg(0, 3) == SC + 0xDC00, "");
        sgn_ = (layer ? l_sg(1, 0) + w * INBOX : SC + (int)((0xDC00CE00B400A900ull >> (16 * w)) & 0xffff)) + q * 320 + c * 4;
        const int pl = lane < 40 ? lane : 39, tbb = l_tb(layer, 0) + w * (layer ? INBOX : 5120);
        spl = SC + pl * 8;
        eo = OFF_EG + (unsigned)((layer * 2 + (nt1 >> 3)) * 16 * RUN + (nt1 & 7) * 640 + pl * 16);
        es = eo + (unsigned)mt1 * RUN;
        spa = l_rs(layer, 0) + (2 * (mt2 & 1) + (w >> 1)) * (layer ? INBOX : 256) + (2 * (w & 1) + (c >> 3)) * 64 + (c & 7) * 4;
        tbw = tbb + c * 64 + ((q ^ ((c >> 1) & 3)) * 16);
        tbr = tbb + (2 * (c >> 2) + (q & 1)) * 64 + (((2 * (q >> 1) + ((c >> 1) & 1)) ^ (c >> 2)) * 16) + (c & 1) * 8;
        hso = OFF_HF + (unsigned)((layer * 4 + ((mt1 >> 1) & 3)) * PG + ((8 * (mt1 & 1) + 2 * w + (q >> 1)) * 8 + (nt1 >> 1)) * RUN + (10 * (nt1 & 1) + 5 * (q & 1)) * 256 + c * 16);
        cwa = L_CW + c * 4;
        fho1 = lane ? kNone : OFF_FH + (unsigned)((2 * (mt1 & 7) + (w >> 1)) * RUN + (layer * 32 + nt1 * 2 + (w & 1)) * 128);
        fho = OFF_FH + (unsigned)((mt2 & 15) * RUN + (layer * 4 + w) * 1024 + (lane >> 3) * 128);
        pin(sgn_); pin(spl); pin(eo); pin(es); pin(spa); pin(tbw); pin(tbr); pin(hso); pin(cwa); pin(fho1); pin(fho);
        const unsigned hb_ = OFF_HF + (unsigned)((layer * 4 + ((mt1 >> 1) & 3)) * PG + (32 * (mt2 & 3) + 2 * w) * RUN);
#pragma unroll
        for (int m = 0; m < 4; ++m) { ho[m] = hb_ + (unsigned)(((m + 1 + w) & 3) * 8 * RUN + lane * 16); pin(ho[m]); }
#pragma unroll
        for (int m = 0; m < 3; ++m) { dst[m] = SC + ((w + 1 + m) & 3) * INBOX + m * 5120 + lane * 16; pin(dst[m]); }
        mt1_ = mt1;
        sgo = OFF_SG + (unsigned)((layer * 16 + (mt2 & 15)) * RUN + w * 2048 + c * 16);
        sgs = q < 2 ? sgo + (unsigned)(nt2 * 256 + q * 8) : kNone;
        sgo += (unsigned)(q * 512);
        pin(sgo); pin(sgs);
        if constexpr (layer == 0) {
            srca = SC + w * INBOX + lane * 16;
            const unsigned gb_ = OFF_GT + (unsigned)((nt2 >> 2) * PG + (nt2 & 3) * 32 * RUN);
            const int cl = i32 + 32 * w;
            glo = gb_ + (unsigned)((lane >> 3) * RUN + ((lane >> 1) & 3) * 2048 + (64 * (mt2 & 15) + 16 * w) * 2 + (lane & 1) * 16);
            gso = cv0 ? gb_ + (unsigned)((cl >> 2) * RUN + (cl & 3) * 2048 + (q & 1) * 512 + c * 32 + (q >> 1) * 16) : kNone;
            fgo = gb_ + (unsigned)(20 * RUN + (lane >> 1) * 128);
            fgo1 = w == 0 ? gb_ + (unsigned)(20 * RUN + i32 * 128 + (lane >> 5) * 8192) : kNone;
            pin(srca); pin(glo); pin(gso); pin(fgo); pin(fgo1);
            const int r0 = SC + (w < 3 ? w : 0) * REGSZ, y0 = 2031 + 8 * q - c, l34 = lane < 33 ? lane : 33;
#pragma unroll
            for (int i = 0; i < 4; ++i) { ab[i] = r0 + ((y0 & 1) ? CV_O : CV_E) + ((y0 + 1) >> 1) * 4 - 448 - 512 - 1024 * i; pin(ab[i]); }
            bb = r0 + 32 * c + 16 * q;
            zpa = w < 3 ? r0 + l34 * 16 + (l34 >= 30 ? CV_E + 4096 - 480 : 0) + (l34 >= 32 ? CV_O + 4100 - 32 - CV_E - 4096 : 0) : SC + 47360 + lane * 16;
            uwa = r0 + lane * 16;
            xso = (unsigned)((64 * mt2 + 16 * w + c) * D_ + 80 * nt2 + 16 * (q & 1) + 8 * (q >> 1)) * 2u;
            dso = (unsigned)(160 * nt2 + 8 * q);
            adm = (unsigned)((c & 1) * 16 + (q == 2 + (c >> 3) ? 256 << ((c & 7) >> 1) : 0));
            pin(adm);
            pin(bb); pin(zpa); pin(uwa); pin(xso); pin(dso);
        }
        SCHED();
    };
    unsigned tag0, tag1;
#define FLAG(off) { if (lane == 0) st64(v2u{tag0, tag1}, rs, off); }
#define ISTAG(f) (((f.x ^ tag0) | (f.y ^ tag1)) == 0u)
#define KW_REQ() { if (cv) ld_ur(wro, p.ws + OFF_KVT); static_for<NW2P>(LAMBDA(i_) { gld_w2<i_.value>(w2o, wl + 2048 + (i_.value / 5) * RUN); }); }
#define FW_OK() { if (__builtin_expect(!kearly, 0)) { WAVE_WAIT({ const v2u fw_ = ld64(rs, fwo); ok = ISTAG(fw_); }) KW_REQ() } }
#define TT_GAP(n) { constexpr int mb_ = (n) / 10, nb_ = ((n) % 10) / 2, e_ = ((n) & 1) * 2; \
        mul2(tt[mb_][nb_][e_], tt[mb_][nb_][e_ + 1], rm_[4 * mb_ + e_], rm_[4 * mb_ + e_ + 1], SW[nb_]); }
    {
        const int tid = threadIdx.x, lane = tid & 63, w = w0, b = b0;
        const int c = lane & 15, q = lane >> 4;
        TILES()
        STAMP(0)
        const unsigned lds0 = (unsigned)(size_t)(LAS char*)smem;
        v2u nonce;
        {
            u64 pc;
            int len, len2;
            asm volatile("s_getpc_b64 %0\nkpf_begin:\n\ts_mov_b32 %1, kpf_end-kpf_begin\n\ts_mov_b32 %2, kpf_end2-kpf_begin" : "=s"(pc), "=s"(len), "=s"(len2));
            len = len > len2 ? len : len2;
            const int o0 = (((j & 15) * 4 + w) + 64 * (lane >> 3)) * 128;
            touch_((unsigned)(o0 < len ? o0 : 0), (const void*)pc);
        }
        const bool cv = w + (i32 >> 4) < 3;
        const int chn = 80 * nt2 + i32 + 32 * (cv ? w : 0);
        const bool kw = w - (i32 >> 4) < 2;
        const int kp = 16 * nt2 + mt1, ku = 5 * kp + (i32 < 16 ? w & 1 : 2 + (w < 3 ? w : 0)), kc = ku >> 6, kr = ku & 63;
        const int lc = cv ? lane : 8;
        int kwi = 2 + (i32 >> 4) - w;
        v4u ax[15], sa[5], cr[2], wr[4];
        unsigned uL[3];
        const int cu_ = (b < 200 ? b : 0) * 4 + w, cdb = cu_ / 20, ccg = cu_ % 20;
        {
            int ca_ = 32 * ccg, cb_ = 40 * nt1 + 10 * w - 32;
            asm volatile("" : "+s"(ca_), "+s"(cb_), "+s"(kwi));
            unsigned cpo = (unsigned)((lane < 32 ? ca_ : cb_) + (lane < 41 ? lane : 41)) * 2u;
            unsigned ksh = (unsigned)(64 * kc + 8 * (lane & 7)) * 2u, ko = (unsigned)(32 * kr + 4 * (lane >> 3)) * 1280u + ksh, kh = ko - 3840u;
            ksh *= 4u;
            pin(cpo); pin(ksh); pin(ko); pin(kh);
            SCHED();
            gldu16(uL[0], cpo, p.Lre); gldu16(uL[1], cpo, p.Lim); gldu16(uL[2], cpo, p.logdt);
            const v4u rk = {(unsigned)(u64)p.k, (unsigned)((u64)p.k >> 32) & 0xffffu, S_ * D_ * 2u, 0x00020000u};
#define L4(d, v, s_, o, f) "global_load_dwordx4 %" #d ", %" #v ", %" #s_ " offset:" #o f "\n\t"
#define B4(d, o) "buffer_load_dwordx4 %" #d ", %17, %21, 0 offen offset:" #o " nt\n\t"
            asm volatile("s_cmp_lt_i32 %22, 1\n\ts_cbranch_scc1 Lrole2\n\t"
                         L4(3, 15, 18, 0, " nt") L4(4, 15, 18, 1280, " nt") L4(5, 15, 18, 2560, " nt") L4(6, 15, 18, 3840, " nt")
                         L4(7, 15, 20, 0, " nt") L4(8, 15, 20, 1280, " nt") L4(9, 15, 20, 2560, " nt") L4(10, 15, 20, 3840, " nt")
                         B4(0, 0) B4(1, 1280) B4(2, 2560) L4(11, 16, 19, 0, "") L4(12, 16, 19, 16, "") L4(13, 16, 19, 32, "") L4(14, 16, 19, 48, "")
                         "\nLrole2:"
                         : "=&v"(ax[0]), "=&v"(ax[1]), "=&v"(ax[2]), "=&v"(ax[3]), "=&v"(ax[4]), "=&v"(ax[5]), "=&v"(ax[6]), "=&v"(ax[7]), "=&v"(ax[8]), "=&v"(ax[9]),
                           "=&v"(ax[10]), "=&v"(ax[11]), "=&v"(ax[12]), "=&v"(ax[13]), "=&v"(ax[14])
                         : "v"(ko), "v"(ksh), "v"(kh), "s"(p.k), "s"(p.shk), "s"(p.v), "s"(rk), "s"(kwi) : "scc");
            SCHED();
        }
        const char* ssrc;
        {
            u64 pa = (u64)(p.x0 + (128 * mt1 + 8 * nt1 + 2 * w) * D_), pb = (u64)(((mt1 & 1) ? p.Bim : p.Bre) + (40 * nt1 + (mt1 >> 1)) * D_);
            asm volatile("" : "+s"(pa), "+s"(pb));
            const int nbx = 2 * w + q - 2, r = q < 2 ? q : 8 * (nbx < 5 ? nbx : 4);
            ssrc = (const char*)(q < 2 ? pa : pb) + (unsigned)(r * 1280 + c * 16);
            pin(ssrc);
        }
        SCHED();
        STAMP(22)
        const int row8 = lane >> 3;
        const unsigned wvo = (unsigned)(40 * nt1 + row8) * 1280u + (unsigned)(((lane & 7) ^ ((w & 1) | ((row8 >> 1) << 1))) * 16 + (w >> 1) * 128);
        const char* const wbase = (const char*)((w & 1) ? p.Bim : p.Bre);
        const unsigned w1l = lds0 + (unsigned)(((w & 1) * 10 + (w >> 1)) * 1024);
        const unsigned xvo0 = (unsigned)(8 * (row8 >> 1) + 2 * (row8 & 1)) * 1280u + (unsigned)(((lane & 7) ^ ((row8 >> 1) << 1)) * 16);
        const unsigned xvo1 = (xvo0 ^ 16u) + 1280u;
        const char* const xbase = (const char*)p.x0 + (size_t)(128 * mt1 + 32 * w) * 1280;
        const unsigned xl = lds0 + (unsigned)(w * REGSZ);
        unsigned xv[4] = {xvo0, xvo1, xvo0 + 5120u, xvo1 + 5120u}, wv[5];
#pragma unroll
        for (int i = 0; i < 5; ++i) { wv[i] = wvo + i * 10240u; pin(wv[i]); if (i < 4) pin(xv[i]); }
#define DMA_M0(it) SETM0<dma_loff(it) - dma_goff(it)>((it) < 40 ? xl : w1l)
#define DMA_LD1(it, nx_) GLD1<dma_goff(it), dma_loff(nx_) - dma_goff(nx_)>((it) < 40 ? xv[(it) & 3] : wv[(it) < 40 ? 0 : ((it) - 100) % 5], (it) < 40 ? xbase : wbase, (nx_) < 40 ? xl : w1l)
#define DMA_LD(it) GLD0<dma_goff(it)>((it) < 40 ? xv[(it) & 3] : wv[(it) < 40 ? 0 : ((it) - 100) % 5], (it) < 40 ? xbase : wbase)
        const int swz = (c & 1) | ((c >> 2) << 1);
        int w1lane0 = (c & 1) * 10240 + (c >> 1) * 128 + ((q ^ swz) * 16);
        int w1lane1 = (c & 1) * 10240 + (c >> 1) * 128 + (((4 + q) ^ swz) * 16);
        int xlane0 = L_XR + w * REGSZ + (c & 1) * 1024 + (c >> 1) * 128 + ((q ^ swz) * 16);
        int xlane1 = L_XR + w * REGSZ + (c & 1) * 1024 + (c >> 1) * 128 + (((4 + q) ^ swz) * 16);
        unsigned cvo = (unsigned)((16 * cdb + c) * D_ + 32 * ccg + 8 * q) * 2u, for0, for1;
        {
            const int G = 4 * ccg + q, ch = 10 * (G / 5) + G % 5, ci = ch + 5;
            const unsigned fo = OFF_W2F + (unsigned)((cdb / 10) * PG + ((cdb / 5) & 1) * 64 * RUN + (cdb % 5) * 1024 + c * 16);
            for0 = fo + (unsigned)((ch >> 2) * RUN + (ch & 3) * 256); for1 = fo + (unsigned)((ci >> 2) * RUN + (ci & 3) * 256);
        }
        unsigned sto = OFF_ST + (unsigned)((mt1 * 16 + 4 * w + q) * 256 + c * 8), swo = (lane & 3) == 3 ? OFF_CV + (unsigned)((lane >> 2) * 128) : OFF_ST + (unsigned)(((lane >> 2) * 16 + nt1) * 256 + 128 + (lane & 3) * 16);
        unsigned kvo = OFF_KVT + (unsigned)((64 * kc + 8 * (lane >> 3) + ((lane >> 2) & 1)) * 4096 + (32 * kr + 8 * (lane & 3)) * 2);
        int kbw = L_KB + w * 1024 + (lane & 7) * 128 + (((lane >> 3) * 8) ^ ((lane & 7) * 16));
        int kbr = L_KB + w * 1024 + (lane >> 3) * 128 + (((lane & 7) * 16) ^ ((lane >> 3) * 16));
        const unsigned dum = kNone;
        if (!kw) kvo = dum;
        if (b >= 200) { for0 = dum; for1 = dum; cvo = (unsigned)((b * 4 + w) * 128); }
        wro = (unsigned)(chn * 4096 + lc * 16);
        cv0 = cv;
        pin(kvo); pin(kbw); pin(kbr);
        int w1hi0 = w1lane0 + 61440, w1hi1 = w1lane1 + 61440;
        pin(w1hi0); pin(w1hi1);
        pin(w1lane0); pin(w1lane1); pin(xlane0); pin(xlane1); pin(cvo); pin(for0); pin(for1); pin(sto); pin(swo); pin(wro);
        SCHED();
        auto pre = LAMBDA(i_) {
            constexpr int it = p1_pre(i_.value);
            SCHED();
            DMA_M0(it);
            asm volatile("s_nop 0");
            DMA_LD(it);
            SCHED();
        };
        {
            int f0 = 2 * ((chn & ~63) / 5), f1 = 2 * ((chn | 63) / 5) + 1, nk = cv ? 28 : 0;
            asm volatile("" : "+s"(f0), "+s"(f1), "+s"(nk));
            const int l10 = lane - 32 < 0 || lane - 32 > 9 ? 0 : lane - 32;
            fwo = lane < nk ? OFF_FK + (unsigned)(f0 + lane < f1 ? f0 + lane : f1) * 128u : OFF_FW + (unsigned)(25 * nt2 + w + 5 * (l10 >> 1) + (l10 & 1)) * 128u;
        }
        w2o = OFF_W2F + (unsigned)((nt2 >> 1) * PG + ((nt2 & 1) * 64 + 10 * w) * RUN + lane * 16);
        int ime = SC + (w < 3 ? w * REGSZ + CV_E : 43264) + 1008 - 16 * lane, imo = SC + (w < 3 ? w * REGSZ + CV_O + 4 : 47360) + 1008 - 16 * lane, im3 = w < 3 ? ime + 3072 : imo;
        int tca = L_TAB + (c >> 1) * 136, tcb = tca + 8 + 32 * w + 8 * q, tcl = L_TAB + (lane < 40 ? lane : 39) * 136, coa = L_CO + w * REGSZ + q * 64, rsa = l_rs(0, 0) + w * 256 + lane * 4;
        unsigned sgn = (c & 1) ? 0u : 0x80000000u;
        unsigned fko1 = w ? kNone : lane < 32 ? OFF_FK + (unsigned)((2 * kp + (i32 >> 4)) * 128) : OFF_FW + (unsigned)(b * 128);
        pin(fwo); pin(w2o); pin(fko1); pin(ime); pin(imo); pin(im3); pin(tca); pin(tcb); pin(tcl); pin(coa); pin(rsa); pin(sgn);
        SCHED();
        auto cw_ = [&]() __attribute__((always_inline)) {
            gldn<0, 2>(cr[0], cvo, p.Cre); gldn<0, 2>(cr[1], cvo, p.Cim);
            static_for<4>(LAMBDA(i) { gldn<i.value * 1024, 1>(wr[i.value], wro, p.ssmk); });
        };
        STAMP(33)
        gld2s<0>(nonce, 0u, p.ws + OFF_NONCE);
        asm volatile("s_waitcnt vmcnt(16)\n\ts_cmp_gt_i32 %3, 0\n\ts_cbranch_scc1 Lrole3\n\ts_waitcnt vmcnt(1)\nLrole3:" : "+v"(uL[0]), "+v"(uL[1]), "+v"(uL[2]) : "s"(kwi) : "memory", "scc");
        {
            f32x2 lam, co, lp[16];
            disc(uL[0], uL[1], uL[2], lam, co);
            if (lane < 32) *(f32x2*)(smem + L_CO + w * REGSZ + lane * 8) = 2.0f * co;
            lp[0] = cmul(lam, lam); lp[0] = cmul(lp[0], lp[0]); lp[1] = cmul(lp[0], lp[0]);
            lp[0] = f32x2{1.f, 0.f};
            lp[2] = cmul(lp[1], lp[1]); lp[3] = cmul(lp[2], lp[1]); lp[4] = cmul(lp[2], lp[2]);
            const f32x2 z2 = {0.f, 0.f};
            f32x2 t4[4];
            cfma4(t4, lp[4], lp[1], lp[2], lp[3], lp[4], z2, z2, z2, z2);
            lp[5] = t4[0]; lp[6] = t4[1]; lp[7] = t4[2]; lp[8] = t4[3];
            cfma4(t4, lp[8], lp[1], lp[2], lp[3], lp[4], z2, z2, z2, z2);
            lp[9] = t4[0]; lp[10] = t4[1]; lp[11] = t4[2]; lp[12] = t4[3];
            cfma4(t4, lp[8], lp[5], lp[6], lp[7], lp[7], z2, z2, z2, z2);
            lp[13] = t4[0]; lp[14] = t4[1]; lp[15] = t4[2];
            if (lane >= 32 && lane < 42) {
                f32x2* tl = (f32x2*)(smem + L_TAB + (10 * w + lane - 32) * 136);
                tl[0] = lam;
#pragma unroll
                for (int i = 0; i < 16; ++i) tl[1 + i] = lp[i];
            }
        }
        SCHED();
        STAMP(35)
        static_for<5>(LAMBDA(i) { gld64<256 * i.value>(sa[i.value], ssrc); });
        cw_();
        addrs(std::integral_constant<int, 0>{}, lane, w, b);
        v4u kvt[4];
        unsigned one2 = 0x3F803F80u, kq[12];
        float kf_[8];
        pin(one2);
        auto kvf = LAMBDA(n_) {
            constexpr int n = n_.value, h = n / 5, u = n % 5;
            if constexpr (u == 0) {
#pragma unroll
                for (int r = 0; r < 6; ++r) { kq[r] = __builtin_amdgcn_perm(ax[r][h], ax[r + 1][h], 0x05040100u); kq[6 + r] = __builtin_amdgcn_perm(ax[r][h], ax[r + 1][h], 0x07060302u); }
                if constexpr (h > 0) kvt[h - 1] = *(const v4u*)(smem + kbr);
            } else if constexpr (u == 1) {
                unsigned t[8];
                d2x4(t, kq[0], kq[1], kq[2], kq[3], ax[11 + h][1], ax[11 + h][1], ax[11 + h][1], ax[11 + h][1]);
                d2x4(t + 4, kq[6], kq[7], kq[8], kq[9], ax[11 + h][3], ax[11 + h][3], ax[11 + h][3], ax[11 + h][3]);
#pragma unroll
                for (int r = 0; r < 8; ++r) kf_[r] = __uint_as_float(t[r]);
            } else if constexpr (u == 2) {
#pragma unroll
                for (int r = 0; r < 8; ++r) kf_[r] = dot2a(kq[r + 2 * (r >> 2) + 2], ax[11 + h][2 * (r >> 2)], kf_[r]);
            } else if constexpr (u == 3) {
#pragma unroll
                for (int r = 0; r < 4; ++r) { kq[r] = ax[7 + r][h] << 16; kq[4 + r] = ax[7 + r][h] & 0xffff0000u; pin(kq[r]); pin(kq[4 + r]); }
            } else {
#pragma unroll
                for (int r = 0; r < 8; ++r) kf_[r] *= __uint_as_float(kq[r]);
                *(v2u*)(smem + kbw) = v2u{cvtpk(kf_[0], kf_[1]), cvtpk(kf_[2], kf_[3])};
                *(v2u*)(smem + (kbw ^ 64)) = v2u{cvtpk(kf_[4], kf_[5]), cvtpk(kf_[6], kf_[7])};
                if constexpr (h == 3) kvt[3] = *(const v4u*)(smem + kbr);
            }
            SCHED();
        };
        asm volatile("s_waitcnt vmcnt(%15)" : "+v"(ax[0]), "+v"(ax[1]), "+v"(ax[2]), "+v"(ax[3]), "+v"(ax[4]), "+v"(ax[5]), "+v"(ax[6]), "+v"(ax[7]), "+v"(ax[8]), "+v"(ax[9]),
                     "+v"(ax[10]), "+v"(ax[11]), "+v"(ax[12]), "+v"(ax[13]), "+v"(ax[14]) : "i"(11) : "memory");
        pin(nonce);
        STAMP(30)
        unsigned tch = 0u;
        constexpr int NF = KNF;
        static_assert(NF < 27, "");
        if (kw) {
            {
                static_for<20>(LAMBDA(n_) {
                    constexpr int n = n_.value, DN = 20 - KFD0;
                    site(); SCHED();
                    kvf(n_);
                    if constexpr (n >= KFD0) static_for<(n - KFD0 + 1) * NF / (DN ? DN : 1) - (n - KFD0) * NF / (DN ? DN : 1)>(LAMBDA(i_) { pre(std::integral_constant<int, (n - KFD0) * NF / (DN ? DN : 1) + i_.value>{}); });
                });
                if constexpr (KFD0 == 20) static_for<NF>(pre);
            }
        } else {
            if (w == 3) {
            const int l = lane, l3 = l & 3;
            unsigned a = OFF_SG + (unsigned)((((l >> 3) & 1) * 16 + (mt2 & 15)) * RUN + ((l >> 1) & 3) * 2048 + nt2 * 256 + (l & 1) * 128);
            unsigned a1 = OFF_PS + (unsigned)((mt1 & 7) * RUN + (((mt2 & 1) * 4 + l3) * 8 + nt2) * 128);
            unsigned a2 = OFF_FX + (unsigned)((mt1 & 7) * RUN + ((2 * (mt2 & 1) + (l3 >> 1)) * 16 + nt2 * 2 + (l3 & 1)) * 128);
            unsigned a3 = OFF_FH + (unsigned)((2 * (mt1 & 7) + (l3 >> 1)) * RUN + (((l >> 2) & 1) * 32 + nt1 * 2 + (l3 & 1)) * 128);
            unsigned a4 = OFF_EG + (unsigned)(((((l >> 3) & 1) * 2 + (nt1 >> 3)) * 16 + mt1) * RUN + (nt1 & 7) * 640 + (l & 7) * 128 + (l & 16) * 512);
            unsigned a5 = OFF_GT + (unsigned)((nt2 >> 2) * PG + ((nt2 & 3) * 32 + 20) * RUN + i32 * 128 + (l & 16) * 512);
            unsigned a6 = OFF_HF - MID + (unsigned)((((l >> 3) & 1) * 4 + ((mt1 >> 1) & 3)) * PG + j * 4 * RUN);
            unsigned a7 = OFF_X2F - MID + (unsigned)(((mt1 >> 2) & 1) * PG + ((xq & 1) * 64 + j * 2) * RUN);
            pin(a); pin(a1); pin(a2); pin(a3); pin(a4); pin(a5); pin(a6); pin(a7);
            a4 = (l & 7) == 5 ? a5 : a4; a4 = (l & 7) == 6 ? a6 : a4; a4 = (l & 7) == 7 ? a7 : a4;
            a = l >= 16 ? a1 : a; a = l >= 20 ? a2 : a; a = l >= 24 ? a3 : a; a = l >= 32 ? a4 : a;
            tch = a;
        }
            vm_wait<KNW>();
            static_for<NF>(pre);
            if (w == 2 && b < 16) {
                unsigned v0, v1, a0, a1, a2, a3, t0, t1;
                asm volatile("s_waitcnt lgkmcnt(0)\n\ts_load_dword %2, %8, 0x0 glc\n\ts_load_dword %3, %8, 0x2000 glc\n\ts_load_dword %4, %8, 0x1000 glc\n\ts_load_dword %5, %8, 0x3000 glc\n\ts_waitcnt lgkmcnt(0)\n\t"
                             "s_not_b32 %6, %2\n\ts_not_b32 %7, %3\n\ts_load_dword %6, %8, 0x0 glc\n\ts_load_dword %7, %8, 0x2000 glc\n\ts_waitcnt lgkmcnt(1)\n\t"
                             "s_cmp_eq_u32 %7, %3\n\ts_cselect_b32 %0, 1, 0\n\ts_waitcnt lgkmcnt(0)\n\t"
                             "s_not_b32 %6, %4\n\ts_not_b32 %7, %5\n\ts_load_dword %7, %8, 0x3000 glc\n\ts_load_dword %6, %8, 0x1000 glc\n\ts_waitcnt lgkmcnt(1)\n\t"
                             "s_cmp_eq_u32 %6, %4\n\ts_cselect_b32 %1, 0, 1\n\ts_waitcnt lgkmcnt(0)"
                             : "=&s"(v0), "=&s"(v1), "=&s"(a0), "=&s"(a1), "=&s"(a2), "=&s"(a3), "=&s"(t0), "=&s"(t1) : "s"(p.ws + (OFF_W2F - MID + (unsigned)(40 + b) * RUN)) : "memory", "scc");
                const unsigned sd = (unsigned)(b >> 2) & 1u;
                st128(v4u{v0 ^ sd, nonce.x + 1u, v1 ^ sd, nonce.x + 1u}, rs, lane ? kNone : OFF_CV + (unsigned)(b * 128));
            }
            skip<SKP>();
            undef4(kvt);
        }
        tag0 = __builtin_amdgcn_readfirstlane(nonce.x) + 1u; tag1 = __builtin_amdgcn_readfirstlane(nonce.y) + (tag0 == 0u ? 1u : 0u);
        SCHED();
        STAMP(34)
        v4u orr, oi;
        v2u gr;
        unsigned gro;
        {
            asm volatile("s_waitcnt vmcnt(%7)" : "+v"(cr[0]), "+v"(cr[1]), "+v"(sa[0]), "+v"(sa[1]), "+v"(sa[2]), "+v"(sa[3]), "+v"(sa[4]) : "i"(4 + NF) : "memory");
#pragma unroll
            for (int e = 0; e < 4; ++e) {
                const f32x4 co = *(const f32x4*)(smem + coa + e * 16);
                const f32x2 o0 = lo16f(cr[0][e]) * f32x2{co.x, -co.y} - lo16f(cr[1][e]) * f32x2{co.y, co.x};
                const f32x2 o1 = hi16f(cr[0][e]) * f32x2{co.z, -co.w} - hi16f(cr[1][e]) * f32x2{co.w, co.z};
                orr[e] = cvtpk(o0.x, o1.x); oi[e] = cvtpk(o0.y, o1.y);
            }
            pin(orr); pin(oi);
            SCHED();
        }
        static_for<(27 - NF) / 3>(LAMBDA(i_) { pre(std::integral_constant<int, NF + i_.value>{}); });
        {
            float s1[2] = {0.f, 0.f}, s2[2] = {0.f, 0.f};
#pragma unroll
            for (int i = 0; i < 5; ++i)
#pragma unroll
                for (int e = 0; e < 4; ++e) { s1[e & 1] = dot2a(sa[i][e], one2, s1[e & 1]); s2[e & 1] = dot2a(sa[i][e], sa[i][e], s2[e & 1]); }
            const float a1 = rowsum16(s1[0] + s1[1]), a2 = rowsum16(s2[0] + s2[1]);
            const float mean = a1 * (1.0f / D_);
            const float var = fmaxf(a2 * (1.0f / D_) - mean * mean, 0.f);
            const float ri = __builtin_amdgcn_rcpf(rbf(__builtin_amdgcn_sqrtf(rbf(rbf(var) + 1e-5f))));
            gr = v2u{__float_as_uint(q >= 2 ? a1 : c ? -ri * rbf(mean) : ri), tag0};
            gro = c < 2 ? OFF_ST + (unsigned)((mt1 * 16 + nt1) * 256 + ((q < 2 ? 8 * c : 14) + 2 * w + q) * 8) : kNone;
            pin(gr); pin(gro);
        }
        SCHED();
        STAMP(59)
        {
            static_for<27 - NF - (27 - NF) / 3>(LAMBDA(i_) { pre(std::integral_constant<int, NF + (27 - NF) / 3 + i_.value>{}); });
#pragma unroll
            for (int i = 0; i < 4; ++i) st128(kvt[i], rs, kvo + i * 8192);
            st128(orr, rs, for0); st128(oi, rs, for1);
            __builtin_amdgcn_raw_buffer_store_b128(orr, rs, for0, 8192, SC1); __builtin_amdgcn_raw_buffer_store_b128(oi, rs, for1, 8192, SC1);
        }
        st64(gr, rs, gro);
        SCHED();
        STAMP(23)
        asm volatile("s_waitcnt vmcnt(%4) lgkmcnt(0)" : "+v"(wr[0]), "+v"(wr[1]), "+v"(wr[2]), "+v"(wr[3]) : "i"(P1_PRE - 9) : "memory");
        __builtin_amdgcn_s_barrier();
        asm volatile("" ::: "memory");
        STAMP(24)
        v4u wf[2][5], af[2][2][2];
#pragma unroll
        for (int nb = 0; nb < 5; ++nb) wf[0][nb] = *(const v4u*)(smem + w1lane0 + nb * 20480 + (0) * 2048);
#pragma unroll
        for (int i = 0; i < 4; ++i) af[0][i >> 1][i & 1] = *(const v4u*)(smem + ((i >> 1) ? xlane1 : xlane0) + (i & 1) * 2048);
        SCHED();
        const char *kvtb = p.ws + OFF_KVT, *w2b0;
        u64 vb0, vb1;
        pins(kvtb);
        v2u sg, tc[10], fw, tg2 = {tag0, tag1};
        pin(tg2);
        v4u sw0;
        unsigned ie[4][4], io[4][4], iw, zq[4];
        float rr_[8];
        auto kstep = LAMBDA(s_) {
            constexpr int s = s_.value, ph = s >> 2, s4 = s & 3, cu = s & 1, nx = cu ^ 1, jj = s >> 1, pp = jj & 1;
            if constexpr (s4 == 3 && ph < 4) {
                constexpr int pw = p1_pos(104 + 5 * (ph + 1)), px = p1_pos(4 * (jj + 1) + 3), pm = pw > px ? pw : px;
                constexpr int vm = p1_lim(p1_cnt(s, 0) - 1 - pm);
                asm volatile("" ::: "memory");
                __builtin_amdgcn_s_waitcnt((vm & 15) | ((vm >> 4) << 14) | 0x70);
                __builtin_amdgcn_s_barrier();
                asm volatile("" ::: "memory");
                STAMP(25 + ph)
                SCHED();
            }
            if constexpr (s == 15) {
                wait_st<p1_cnt(s, 0) - 1 - p1_pos(203)>(sg, sw0);
                WAVE_RETRY((((sg.y ^ tag0) | (sw0.y ^ tag0) | (sw0.w ^ tag0)) == 0u), sg = ld64(rs, sto); sw0 = ld128(rs, swo))
                STAMP(7)
            }
            if constexpr (s == 16) {
                const u64 m = 0x8888888888888888ull;
                pol = __builtin_popcountll(vb0 & m) + __builtin_popcountll(vb1 & m) > 16 ? 8192u : 0u;
                sdo = pol ^ (unsigned)((b0 & 4) * 2048);
                wl = p.ws + sdo; w2b0 = wl + 2048;
                pins(pol); pins(sdo); pins(wl); pins(w2b0);
                rl = __builtin_amdgcn_make_buffer_rsrc((void*)wl, 0, WS_BYTES, 0x00020000);
                static_assert(kW2S > 16, "");
            }
            if constexpr (s == kW2S) { vm_wait<p1_cnt(s, 0) - 1 - p1_pos(208)>(); SCHED(); }
            if constexpr (s == 18) {
                vm_wait<p1_cnt(s, 0) - 1 - (p1_pos(207) > p1_pos(208) ? p1_pos(207) : p1_pos(208))>();
                pin(fw); pin(sdq);
                kearly = __builtin_amdgcn_ballot_w64(!ISTAG(fw)) == 0ull;
                ORDER();
                if (kearly && cv) ld_ur(wro, kvtb);
                SCHED();
            }
            if constexpr (s4 != 3 || ph == 4) lgkm<(cu == 0 && s > 0 ? 3 : s == 17 ? 4 : s == 19 ? 2 : 0)>();
            SCHED();
            static_for<10>(LAMBDA(g_) {
                constexpr int g = g_.value, mb = g / 5, k = g % 5, it = p1_item(s, g), nt_ = g < 9 ? p1_item(s, g + 1) : -1;
                constexpr bool d0 = it >= 0 && it < 200, d1 = nt_ >= 0 && nt_ < 200;
                if constexpr (s == 0) mf0(acc[mb][k], af[pp][cu][mb], wf[cu][k]); else mf(acc[mb][k], af[pp][cu][mb], wf[cu][k]);
                SCHED();
                if constexpr (s < 19) {
                    if constexpr (g < 5) wf[nx][g] = *(const v4u*)(smem + (g < 3 ? (nx ? w1lane1 : w1lane0) : (nx ? w1hi1 : w1hi0)) + (g < 3 ? g : g - 3) * 20480 + ((s + 1) >> 1) * 1024);
                    if constexpr (cu == 1 && g == 5) vm_wait<p1_lim(p1_cnt(s, 5) - 1 - p1_pos(4 * (jj + 1) + 3))>();
                    if constexpr (cu == 1 && g >= 5 && g < 9)
                        af[pp ^ 1][(g - 5) >> 1][(g - 5) & 1] = *(const v4u*)(smem + (((g - 5) >> 1) ? xlane1 : xlane0) + ((jj + 1) % 3) * 4096 + ((g - 5) & 1) * 2048);
                }
                if constexpr (cu == 0 && g == 4 && s > 0) lgkm<(s == 8 || s == 10 || s == 14 ? 8 : 5)>();
                static_for<p1_cap(s, g)>(LAMBDA(e_) {
                    constexpr int na = p1_slot(0, s, g, 10) + e_.value, i = (na / 10) & 3, u = na % 10;
                    static_assert(p1_slot(0, 8, 0, 10) >= 40, "");
                    if constexpr (na < 40) {
                        if constexpr (u == 0) { if constexpr (i > 0) ror1(iw, wr[i - 1].w); else { iw = 0u; pin(iw); } }
                        else if constexpr (u < 5) rot1(ie[i][u - 1], wr[i][4 - u]);
                        else if constexpr (u == 5) shr1b(iw, wr[i].w);
                        else if constexpr (u < 9) mix1(io[i][u - 6], wr[i][9 - u], wr[i][8 - u]);
                        else mix1(io[i][3], wr[i].x, iw);
                    }
                    constexpr int nb = p1_slot(8, s, g, 3) + e_.value;
                    if constexpr (s >= 8 && s < 12 && g < 3) {
                        if constexpr (nb < 10) tc[nb] = *(const v2u*)(smem + (nb < 5 ? tca : tcb) + (nb % 5) * 1088);
                        else if constexpr (nb == 10) { const f32x2* tp = (const f32x2*)(smem + tcl); l8 = tp[2]; l32 = tp[5]; }
                        else if constexpr (nb == 11) l128 = *(const f32x2*)(smem + tcl + 72);
                        static_assert(p1_slot(8, 12, 0, 3) == 12, "");
                    }
                    constexpr int nc = p1_slot(12, s, g, 10) + e_.value;
                    if constexpr (s >= 12 && s < 14 && nc < 10) {
                        if constexpr (nc < 5) { lr[nc] = __uint_as_float(tc[nc].x); xor1(ls[nc], tc[nc].y, sgn); }
                        else { lqr[nc - 5] = __uint_as_float(tc[nc].x); xor1(lqs[nc - 5], tc[nc].y, sgn); }
                        static_assert(p1_slot(12, 14, 0, 10) >= 10, "");
                    }
                    if constexpr (s == 15 && g < 5) SW[g] = __int_as_float(__builtin_amdgcn_ds_bpermute(16 * c + 4 * (g >> 1), (g & 1) ? sw0.z : sw0.x));
                    constexpr bool img = (s == 16 && g >= 5 && g < 9 && e_.value == 0) || (s == 18 && (g == 5 || g == 6) && e_.value == 0);
                    constexpr int nd = p1_slot(15, s, g, 10) + e_.value - 5 - (s > 16 ? 4 : s == 16 && g >= 5 ? (g < 9 ? g - 4 : 4) : 0) - (s > 18 ? 2 : s == 18 && g >= 5 ? (g == 5 ? 1 : 2) : 0);
                    if constexpr (s >= 15 && nd >= 0 && !img) {
                        if constexpr (nd < 4) bc1<nd>(rr_[nd], sg.x);
                        else if constexpr (nd < 44) mulb<8 + (nd - 4) % 8>(tt[(nd - 4) % 8 / 4][(nd - 4) / 8][(nd - 4) % 4], sg.x, SW[(nd - 4) / 8]);
                        else if constexpr (nd < 48) bc1<nd - 40>(rr_[nd - 40], sg.x);
                        static_assert(p1_slot(15, 16, 0, 10) <= 9 && p1_slot(15, 20, 0, 10) >= 59, "");
                    }
                });
                if constexpr (s == 15 && g == 6) { vb0 = __builtin_amdgcn_ballot_w64(sw0.x == 1u); pins(vb0); }
                if constexpr (s == 15 && g == 7) { vb1 = __builtin_amdgcn_ballot_w64(sw0.z == 1u); pins(vb1); }
                if constexpr (s == 14 && g < 3) *(v4u*)(smem + ime + 1024 * g) = v4u{ie[3 - g][0], ie[3 - g][1], ie[3 - g][2], ie[3 - g][3]};
                if constexpr (s == 16 && g >= 5 && g < 9) *(v4u*)(smem + imo + 1024 * (8 - g)) = v4u{io[g - 5][0], io[g - 5][1], io[g - 5][2], io[g - 5][3]};
                if constexpr (s == 18 && g == 5) *(v4u*)(smem + im3) = v4u{ie[0][0], ie[0][1], ie[0][2], ie[0][3]};
                if constexpr (s == 18 && g == 6) *(unsigned*)(smem + rsa) = sg.x;
                if constexpr (s == 14 && (g == 6 || g == 8)) { zq[g - 6] = 0u; zq[g - 5] = 0u; pin(zq[g - 6]); pin(zq[g - 5]); }
                if constexpr (s == 19 && g == 0) *(v4u*)(smem + zpa) = v4u{zq[0], zq[1], zq[2], zq[3]};
                if constexpr (s == 18 && g == 1) touch_(tch ^ sdo, p.ws);
                if constexpr (s == 19 && g == 2) { if (__builtin_expect(!kearly, 0)) poll1(fwo, p.ws); }
                if constexpr (it >= 0) {
                    if constexpr (d0 && d1) DMA_LD1(it, d1 ? nt_ : 0);
                    else if constexpr (d0) DMA_LD(it);
                    else if constexpr (it == 202) gld2s<0>(sg, sto, p.ws);
                    else if constexpr (it == 203) gld4s<0>(sw0, swo, p.ws);
                    else if constexpr (it == 207) gldu16(sdq, (unsigned)chn * 2u, p.ssmD);
                    else if constexpr (it == 208) gld2s<0>(fw, fwo, p.ws);
                    else if constexpr (it >= 500) gld_w2<it % 50>(w2o, w2b0 + ((it % 50) / 5) * RUN);
                    else { static_assert(p1_pos(19) >= P1_PRE, ""); st64(tg2, rs, fko1); }
                }
                if constexpr (d1 && !d0) DMA_M0(d1 ? nt_ : 0);
                site();
                SCHED();
            });
        };
        static_for<20>(kstep);
        SCHED();
        rr[0] = f32x4{rr_[0], rr_[1], rr_[2], rr_[3]}; rr[1] = f32x4{rr_[4], rr_[5], rr_[6], rr_[7]};
        ACC_FENCE10(acc[0], acc[1]);
        STAMP(1)
    }

    auto layer_body = LAMBDA(layer_) {
        constexpr int layer = layer_.value;
        const int sga = sgn_;
        const bool cv = layer == 0 && cv0;
        v2u fh;
        unsigned ks[10];
        f32x4 ca[8], a2[4][5];
        float rsr, rsm;
        int otw, otr, ots;
        v4u cA[2][8], cB[2][8];
        constexpr int R2 = kCR1, LP = (7 - R2) & 1;
        static_assert(kFHP >= 7 - KG0 / 4 + 1, "");
        constexpr bool TB = (SBT >> layer) & 1;
        float bv[8][5];
        f32x2 pr[5][2], ps[5][2];
        auto powers = [&]() __attribute__((always_inline)) {
            if constexpr (TB) {
#pragma unroll
                for (int nb = 0; nb < 5; ++nb) {
                    const f32x2 m = {lr[nb] * lr[nb] - ls[nb] * ls[nb], 2.0f * lr[nb] * ls[nb]};
                    pr[nb][0] = f32x2{lr[nb], m.x}; ps[nb][0] = f32x2{ls[nb], m.y};
#pragma unroll
                    for (int j = 1; j < 2; ++j) pstep(pr[nb][j], ps[nb][j], pr[nb][j - 1], ps[nb][j - 1], m);
#pragma unroll
                    for (int j = 0; j < 2; ++j) { pin(pr[nb][j]); pin(ps[nb][j]); }
                }
                SCHED();
            }
        };
        {
            float a0[5], a1[5];
            static_for<2>(LAMBDA(m_) {
                constexpr int mb = m_.value;
#pragma unroll
                for (int nb = 0; nb < 5; ++nb) {
                    const f32x4 v = __builtin_elementwise_fma(acc[mb][nb], rr[mb], f32x4{tt[mb][nb][0], tt[mb][nb][1], tt[mb][nb][2], tt[mb][nb][3]});
#pragma unroll
                    for (int i = 0; i < 4; ++i) bv[4 * mb + i][nb] = v[i];
                }
                SCHED();
                if constexpr (TB) {
#pragma unroll
                    for (int i = mb ? 4 : 1; i < 4 + 4 * mb; ++i) cstepi(bv[i][0], bv[i][1], bv[i][2], bv[i][3], bv[i][4], bv[i - 1][0], bv[i - 1][1], bv[i - 1][2], bv[i - 1][3], bv[i - 1][4], lr, ls);
#pragma unroll
                    for (int nb = 0; nb < 5; ++nb) a0[nb] = bv[7][nb];
                }
                if constexpr (mb == 0 && !TB) cstep(a0, bv[0], lr, ls, bv[1][0], bv[1][1], bv[1][2], bv[1][3], bv[1][4]);
#pragma unroll
                for (int i8 = mb ? 4 : 2; i8 < (TB ? 0 : 4 + 4 * mb); i8 += 2) {
                    cstep(a1, a0, lr, ls, bv[i8][0], bv[i8][1], bv[i8][2], bv[i8][3], bv[i8][4]);
                    cstep(a0, a1, lr, ls, bv[i8 + 1][0], bv[i8 + 1][1], bv[i8 + 1][2], bv[i8 + 1][3], bv[i8 + 1][4]);
                }
                SCHED();
            });
#pragma unroll
            for (int nb = 0; nb < 5; ++nb) *(float*)(smem + sga + nb * 64) = a0[nb];
        }
        SCHED();
        const int w_ = w0;
        {
        const int w = w_, mt1 = mt1_;
        if (layer == 0 && __builtin_expect(!kearly, 0)) {
            unsigned f0_, f1_;
            asm volatile("s_waitcnt vmcnt(0)\n\t" AR("%0", 36, 0) AR("%1", 36, 1) : "=v"(f0_), "=v"(f1_)::"memory");
            kearly = __builtin_amdgcn_ballot_w64(((f0_ ^ tag0) | (f1_ ^ tag1)) != 0u) == 0ull;
            ORDER();
            if (kearly) KW_REQ()
        }
        asm volatile("s_waitcnt lgkmcnt(0)" ::: "memory");
        __builtin_amdgcn_s_barrier();
        asm volatile("" ::: "memory");
        STAMP(layer ? 14 : 2)
#pragma unroll
        for (int s = 0; s < 10; ++s) ks[s] = kso(s);
        constexpr int NX = 58;
        constexpr int NW2A = NW2P + 7 * kCR1 + kXA, NW3 = NW2P + kN3E + kN3;
        auto w2ld = LAMBDA(i_) {
            constexpr int i = i_.value;
            gld_w2<i>(w2o, wl + 2048 + (i / 5) * RUN);
        };
        auto w2a = LAMBDA(i_) {
            constexpr int k = i_.value;
            if constexpr (k < kXA) { w2ld(std::integral_constant<int, NW2P + 2 * k>{}); w2ld(std::integral_constant<int, NW2P + 2 * k + 1>{}); }
            else w2ld(std::integral_constant<int, NW2P + kXA + k>{});
        };
        auto w2x = LAMBDA(i_) {
            constexpr int i = i_.value < NX - 50 ? 50 + i_.value : NW2A + i_.value - (NX - 50) < 50 ? NW2A + i_.value - (NX - 50) : 99;
            if constexpr (i < 50) w2ld(std::integral_constant<int, i>{});
            else if constexpr (i < 52) lda4<40 + 4 * (i - 50), (i - 50) * 64>(xso, p.x0);
            else if constexpr (i == 52) lda4<48, 128>(xso - 2u * (unsigned)(bb & 16), p.x0);
            else if constexpr (i < NX) lda2<20 + 2 * (i - 53), 32 * (i - 53)>(dso, p.Dskip);
        };
        constexpr int NB = NX - NW2A, NB1 = KB1 < NB ? KB1 : NB, KPL = 7 * (8 - R2) - kFHP, NFH = NB - NB1 - (KPL - kFHK) > 0 ? NB - NB1 - (KPL - kFHK) : 0;
        auto w2b = LAMBDA(i_) {
            constexpr int k = i_.value - 7 * R2;
            if constexpr (k < NB1) w2x(std::integral_constant<int, k>{});
            if constexpr (k > kFHK) w2x(std::integral_constant<int, NB1 + k - kFHK - 1>{});
            if constexpr (k == kFHK) { vm_wait<NB1>(); st64(v2u{tag0, tag1}, rl, fho1); STAMP(39) }
            if constexpr (k == kFHK - 1) { STAMP(38) }
            if constexpr (k == KPL) poll1(fho, wl);
            static_assert(NB1 <= kFHK + 1 && NB1 + 7 * (7 - R2) + KG0 / 4 - kFHK - 1 >= NB && NW2A <= 50 && NW3 <= NW2A && NFH <= NB - NB1, "");
        };
        auto partb = [&]() __attribute__((always_inline)) {
            const float* sp = (const float*)(smem + spa);
            rsr = sp[0]; rsm = sp[8];
            if (layer == 1) { pin(rsr); pin(rsm); }
            if (layer == 1) {
                ACC_TIE5(dsk);
#pragma unroll
                for (int nb = 0; nb < 5; ++nb) { a2[3][nb] = xin[nb] + (xin[nb] * rsr + rsm) * unpk4(dsk[nb]); pin(a2[3][nb]); }
            }
        };
        constexpr int LBW = layer ? 2 : 3;
        if (w == 3) {
            site(); SCHED();
            f32x2 sv[16];
#pragma unroll
            for (int i = 0; i < 16; ++i) sv[i] = *(const f32x2*)(smem + spl + (l_sg(layer, i >> 2) - SC + (i & 3) * 320));
            f32x2 u[4], v[4], x[4];
            cfma4(u, l8, sv[0], sv[4], sv[8], sv[12], sv[1], sv[5], sv[9], sv[13]);
            cfma4(v, l8, u[0], u[1], u[2], u[3], sv[2], sv[6], sv[10], sv[14]);
            cfma4(x, l8, v[0], v[1], v[2], v[3], sv[3], sv[7], sv[11], sv[15]);
            f32x2 P[4][4];
            P[0][0] = f32x2{0.f, 0.f}; P[0][1] = x[0]; P[0][2] = cfma(l32, x[0], x[1]); P[0][3] = cfma(l32, P[0][2], x[2]);
            const f32x2 E = cfma(l32, P[0][3], x[3]);
            __builtin_amdgcn_raw_buffer_store_b128(v4u{__float_as_uint(E.x), tag0, __float_as_uint(E.y), tag0}, rs, es, sdo ^ 8192u, SC1);
            st128(v4u{__float_as_uint(E.x), tag0, __float_as_uint(E.y), tag0}, rl, es);
            SCHED();
            if (layer == 0) { FW_OK() static_for<kN3E>(LAMBDA(i_) { w2ld(std::integral_constant<int, NW2P + i_.value>{}); }); }
#pragma unroll
            for (int kk = 1; kk < 4; ++kk) cfma4(P[kk], l8, P[kk - 1][0], P[kk - 1][1], P[kk - 1][2], P[kk - 1][3], sv[kk - 1], sv[kk + 3], sv[kk + 7], sv[kk + 11]);
#pragma unroll
            for (int i = 0; i < 16; ++i) *(f32x2*)(smem + spl + (l_sg(layer, i >> 2) - SC + (i & 3) * 320)) = P[i & 3][i >> 2];
            if (layer == 0) partb();
            if (layer == 1) powers();
        }
        if (w == LBW) {
            site(); SCHED();
            f32x2 wg[17], t4[4];
            unsigned ea[15], tg[15];
            unsigned m14 = (unsigned)mt1;
            pins(m14);
            m14 *= RUN;
            const f32x2 z2 = {0.f, 0.f};
            wg[1] = cmul(l128, l128); wg[2] = cmul(wg[1], wg[1]); wg[3] = cmul(wg[2], wg[1]); wg[4] = cmul(wg[2], wg[2]);
#pragma unroll
            for (int i = 4; i < 16; i += 4) {
                cfma4(t4, wg[i], wg[1], wg[2], wg[3], wg[4], z2, z2, z2, z2);
#pragma unroll
                for (int e = 0; e < 4; ++e) wg[i + 1 + e] = t4[e];
            }
#pragma unroll
            for (int i = 0; i < 15; ++i) {
                const int ch = mt1 - 1 - i;
                if (i) pin(wg[i]);
                ea[i] = ch < 0 ? kNone : m14 - (unsigned)(i + 1) * RUN; tg[i] = ch < 0 ? 0u : tag0;
                pins(ea[i]); pins(tg[i]);
            }
            powers();
            f32x2 H0, H1;
            skip<(layer ? SK1 : SK0)>();
            __builtin_amdgcn_s_sleep(layer ? KLB1 : KLB0);
            unsigned spins = dead ? kSpinCap : 0u;
            bool miss;
#pragma nounroll
            do {
                asm volatile("" ::: "memory");
                v4u eg[15];
#pragma unroll
                for (int i = 0; i < 15; ++i) eg[i] = ld128s(rl, eo, ea[i]);
                SCHED();
                unsigned x = __builtin_amdgcn_bitop3_b32(eg[0].y, eg[0].w, tg[0], 0x7E);
                H0 = f32x2{__uint_as_float(eg[0].x), __uint_as_float(eg[0].z)}; H1 = f32x2{0.f, 0.f};
#pragma unroll
                for (int i = 1; i < 15; i += 2) {
                    x |= __builtin_amdgcn_bitop3_b32(eg[i].y, eg[i].w, tg[i], 0x7E) | __builtin_amdgcn_bitop3_b32(eg[i + 1].y, eg[i + 1].w, tg[i + 1], 0x7E);
                    pin(x);
                    gacc2(H0, H1, wg[i], wg[i + 1], eg[i], eg[i + 1]);
                    SCHED();
                }
                miss = __builtin_amdgcn_ballot_w64(x != 0u) != 0ull;
            } while (miss && ++spins <= kSpinCap);
            dead = dead || miss;
            ORDER();
            *(f32x2*)(smem + spl + (L_CW - SC)) = H0 + H1;
            STAMP(layer ? 36 : 37)
            if (layer == 0) {
                static_for<NW3 - NW2P - kN3E>(LAMBDA(i_) { w2ld(std::integral_constant<int, NW2P + kN3E + i_.value>{}); });
            }
        }
        int w2_ = w;
        pins(w2_);
        if (layer == 0) UNDEF8(ca);
        if (layer == 0 && w2_ != LBW) {
            if (cv) {
                site(); SCHED();
                FW_OK()
                st_ur((unsigned)(size_t)(LAS char*)smem + (unsigned)uwa);
                STAMP(32)
                conv_part<0, kCR1>(ca, smem, ab, bb, w2a, [&]() __attribute__((always_inline)) {
                    v4u ad;
                    const unsigned dv = sdq << (adm & 16u);
#pragma unroll
                    for (int i = 0; i < 4; ++i) ad[i] = dv & (unsigned)__builtin_amdgcn_sbfe((int)adm, 8 + i, 1);
                    asm volatile("s_nop 1" : "+v"(ad));
                    return ad;
                }, cA, cB);
                partb();
                powers();
            } else {
                FW_OK()
                static_for<NW2A - NW2P>(LAMBDA(i_) { w2ld(std::integral_constant<int, NW2P + i_.value>{}); __builtin_amdgcn_s_sleep(1); });
                partb();
                powers();
            }
        }
        if (layer == 1 && w < LBW) { site(); SCHED(); powers(); }
        asm volatile("s_waitcnt lgkmcnt(0)" ::: "memory");
        __builtin_amdgcn_s_barrier();
        asm volatile("" ::: "memory");
        STAMP(layer ? 15 : 4)
        {
            float cw[5], ex[5];
#pragma unroll
            for (int nb = 0; nb < 5; ++nb) { cw[nb] = *(const float*)(smem + cwa + nb * 64); ex[nb] = *(const float*)(smem + sga + nb * 64); }
            v2u ta[5], td[5];
            if constexpr (TB) {
                cstepi(ex[0], ex[1], ex[2], ex[3], ex[4], cw[0], cw[1], cw[2], cw[3], cw[4], lqr, lqs);
                f32x2 cs[5];
                PIN5(ex);
#pragma unroll
                for (int nb = 0; nb < 5; ++nb) { cs[nb] = f32x2{ex[nb], dpp<0xB1>(ex[nb])}; pin(cs[nb]); }
                SCHED();
                static_for<5>(LAMBDA(n_) {
                    constexpr int nb = n_.value;
                    f32x2 h[4] = {{bv[0][nb], bv[1][nb]}, {bv[2][nb], bv[3][nb]}, {bv[4][nb], bv[5][nb]}, {bv[6][nb], bv[7][nb]}};
                    padd(h, pr[nb], ps[nb], cs[nb]);
                    *(v4u*)(smem + tbw + nb * 1024) = v4u{cvtpk(h[0].x, h[0].y), cvtpk(h[1].x, h[1].y), cvtpk(h[2].x, h[2].y), cvtpk(h[3].x, h[3].y)};
                    ta[nb] = __builtin_bit_cast(v2u, trread(smem, tbr + nb * 1024)); td[nb] = __builtin_bit_cast(v2u, trread(smem, tbr + nb * 1024 + 512));
                    SCHED();
                    if constexpr (nb >= LAG) { st128(v4u{ta[nb - LAG].x, ta[nb - LAG].y, td[nb - LAG].x, td[nb - LAG].y}, rl, hso + (nb - LAG) * 256); SCHED(); }
                });
            } else
            static_for<2>(LAMBDA(h_) {
                constexpr int n0 = 2 * h_.value;
                float hv[9][3];
                cstep3(hv[0], cw + n0, lqr + n0, lqs + n0, ex + n0);
                if constexpr (n0 == 0) {
                    chain24<0>(hv, bv, lr, ls);
                    chain24<0>(hv + 4, bv + 4, lr, ls);
                } else {
                    chain33<2>(hv, bv, lr, ls);
                    SCHED();
#pragma unroll
                    for (int rd = 0; rd < 2; ++rd) st128(v4u{ta[rd].x, ta[rd].y, td[rd].x, td[rd].y}, rl, hso + rd * 256);
                    SCHED();
                    chain33<2>(hv + 3, bv + 3, lr, ls);
                    chain32<2>(hv + 6, bv + 6, lr, ls);
                }
#pragma unroll
                for (int e = 0; e < (n0 ? 3 : 2); ++e) {
                    v4u o;
                    o.x = cvtpk(hv[1][e], hv[2][e]); o.y = cvtpk(hv[3][e], hv[4][e]);
                    o.z = cvtpk(hv[5][e], hv[6][e]); o.w = cvtpk(hv[7][e], hv[8][e]);
                    *(v4u*)(smem + tbw + (n0 + e) * 1024) = o;
                }
#pragma unroll
                for (int rd = n0; rd < (n0 ? 5 : 2); ++rd) { ta[rd] = __builtin_bit_cast(v2u, trread(smem, tbr + rd * 1024)); td[rd] = __builtin_bit_cast(v2u, trread(smem, tbr + rd * 1024 + 512)); }
                SCHED();
            });
#pragma unroll
            for (int rd = TB ? 5 - LAG : 2; rd < 5; ++rd) st128(v4u{ta[rd].x, ta[rd].y, td[rd].x, td[rd].y}, rl, hso + rd * 256);
            SCHED();
        }
#pragma unroll
        for (int s = 0; s < 10; ++s) pins(ks[s]);
        if constexpr (layer == 1) {
            partb();
            SCHED();
            if constexpr (OTR) {
                const int ln = (srca >> 4) & 63, od = (b0 >> 3) & 1, r0 = srca - 16 * ln;
                otw = (ln & 15) * 336 + (ln >> 4) * 16; otr = (ln & 12) * 336 + ((ln >> 4) + od) * 64 + (ln & 3) * 16; ots = otw + (od ? 0 : 256);
                ACC_TIE5(gt);
#pragma unroll
                for (int nb = 0; nb < 5; ++nb) *(v2u*)(smem + r0 + 10240 + otw / 2 + nb * 32) = gt[nb];
#pragma unroll
                for (int nb = 0; nb < 5; ++nb) gt[nb] = *(const v2u*)(smem + r0 + 10240 + (nb < 4 ? otr / 2 + nb * 168 : ots / 2));
                otw += r0; otr += r0; ots += r0;
                pin(otw); pin(otr); pin(ots);
                ACC_TIE5(gt);
                static_assert((SC & 1023) == 0 && (INBOX & 1023) == 0 && L_CW + 320 <= SC + 2 * INBOX + 10240, "");
                SCHED();
            }
            DRAIN();
            st64(v2u{tag0, tag1}, rl, fho1);
        }
        STAMP(layer ? 16 : 5)
        if (cv) {
            if constexpr (layer == 0) site();
            SCHED();
            conv_part<kCR1, 8>(ca, smem, ab, bb, w2b, []() {}, cA, cB);
        } else if (layer == 0) {
            static_for<NB1>(LAMBDA(i_) { w2x(i_); __builtin_amdgcn_s_sleep(1); });
            vm_wait<NB1>();
            st64(v2u{tag0, tag1}, rl, fho1);
            static_for<NB - NB1 - NFH>(LAMBDA(i_) { w2x(std::integral_constant<int, NB1 + i_.value>{}); __builtin_amdgcn_s_sleep(1); });
            if (w == 3) static_for<NW2A - NW3>(LAMBDA(i_) { w2ld(std::integral_constant<int, NW3 + i_.value>{}); __builtin_amdgcn_s_sleep(1); });
            skip<SK5>();
            poll1(fho, wl);
            static_for<NFH>(LAMBDA(i_) { w2x(std::integral_constant<int, NB - NFH + i_.value>{}); __builtin_amdgcn_s_sleep(1); });
            static_assert(NFH <= NB - (NX - 50), "");
            UNDEF8(cA[LP]); UNDEF8(cB[LP]); UNDEF8(ca);
        }
        if (layer == 0) { unsigned f0_, f1_; asm volatile("s_waitcnt vmcnt(%2)\n\t" AR("%0", 36, 0) AR("%1", 36, 1) : "=v"(f0_), "=v"(f1_) : "i"(NFH) : "memory"); fh = v2u{f0_, f1_}; }
        STAMP(layer ? 29 : 6)
        }
        unsigned g[8][2];

            v2u pt[4];
            if (layer == 0) WAVE_RETRY(ISTAG(fh), fh = ld64(rl, fho))
            else { skip<SK2>(); pipe_flag<KPF0>(pt, fho, wl, (u64)tag0 | ((u64)tag1 << 32), dead); ORDER(); }
            STAMP(layer ? 17 : 31)
            site(); SCHED();
            constexpr int RING2 = layer ? KR2 : KRT;
            v4u ar[RING2];
            constexpr int NRT = layer ? RING2 : (36 - KG0) / KRS + 2 < KRT ? (36 - KG0) / KRS + 2 : KRT;
            unsigned xs_[3][4];
            auto gate = LAMBDA(n_) {
                constexpr int n = n_.value;
                if constexpr (n >= 100) swp2n(g[2 * (n - 100)][0], g[2 * (n - 100) + 1][0], g[2 * (n - 100)][1], g[2 * (n - 100) + 1][1]);
                else if constexpr (n >= 0) cvt2(g[n][0], g[n][1], ca[n]);
            };
            if constexpr (layer == 0) {
#pragma unroll
                for (int i = 0; i < 2; ++i) { ar[i] = ld128s(rl, ho[0], ks[i]); SCHED(); }
                static_for<36 - KG0>(LAMBDA(n_) {
                    constexpr int n = n_.value, g = KG0 + n;
                    constexpr int a = g < 8 ? 0 : g < 15 ? 1 : g < 21 ? 2 : g < 26 ? 3 : g < 30 ? 4 : g < 33 ? 5 : g < 35 ? 6 : 7;
                    constexpr int e = g - (a * 8 - a * (a - 1) / 2), i = 2 + n / KRS;
                    mf(ca[a + e], cA[LP][a], cB[LP][e]);
                    SCHED();
                    if constexpr (n % KRS == 0 && i < NRT) ar[i] = ld128s(rl, ho[i / 10], ks[i % 10]);
                    if constexpr (n < 12) rda<40 + n>(xs_[n >> 2][n & 3]);
                    constexpr int gt_[12] = {0, 1, 2, -1, 100, -1, 3, -1, -1, 101, 4, -1};
                    if constexpr (g >= 24) gate(IC(gt_[g < 24 ? 0 : g - 24]));
                    site();
                    SCHED();
                });
                static_assert(KG0 <= 15 && NRT >= 12, "");
            } else {
#pragma unroll
                for (int i = 0; i < RING2; ++i) { ar[i] = ld128s(rl, ho[i / 10], ks[i % 10]); SCHED(); }
            }
        v4u gl[3];
        v2u f0;
        bool gearly = false;
        {
            const char* src = smem + srca;
            const char *glb1 = wl + 8 * RUN, *glb2 = wl + 12 * RUN;
            float zx[4], zd[4], zt[4], zz[5][4];
            if constexpr (STAMPS == 1) { STAMP(layer ? 50 : 40) }
            static_for<40>(LAMBDA(i_) {
                constexpr int i = i_.value, m = i / 10, s = i % 10;
                static_for<5>(LAMBDA(k_) {
                    constexpr int kk = k_.value, n = 2 * (i - 3) + kk / 2 - 3, nb = n / 8, u = n % 8, pv = i - 1 + RING2;
                    if constexpr (s == 0 && m < 3) mfa0<kk>(a2[m][kk], ar[i % RING2]); else mfa<5 * s + kk>(a2[m][kk], ar[i % RING2]);
                    SCHED();
                    constexpr int gu_[12] = {-1, 5, -1, -1, 102, 6, -1, -1, 7, -1, 103, -1};
                    if constexpr (layer == 0 && 5 * i + kk < 12) gate(IC(gu_[(5 * i + kk) % 12]));
                    if constexpr (layer == 0 && i == 25 && kk == 1) { pins(glb1); pins(glb2); }
                    if constexpr (layer == 0 && i >= 3 && i < 26 && (kk & 1)) {
                        if constexpr (n < 0) swp2n(xs_[n + 3][0], xs_[n + 3][2], xs_[n + 3][1], xs_[n + 3][3]);
                        else if constexpr (nb < 5) {
                            if constexpr (u < 2) unp2(zx[2 * u], zx[2 * u + 1], xs_[nb >> 1][2 * (nb & 1) + u]);
                            else if constexpr (u < 4) unp2(zd[2 * u - 4], zd[2 * u - 3], dsk[nb][u - 2]);
                            else if constexpr (u < 6) fma2(zt[2 * u - 8], zt[2 * u - 7], zx[2 * u - 8], zx[2 * u - 7], rsr, rsr, rsm, rsm);
                            else fma2(zz[nb][2 * u - 12], zz[nb][2 * u - 11], zt[2 * u - 12], zt[2 * u - 11], zd[2 * u - 12], zd[2 * u - 11], zx[2 * u - 12], zx[2 * u - 11]);
                        }
                    }
                    if constexpr (i == 0 && kk == 0 && layer == 1) { pin(pt[0]); pin(pt[1]); pin(pt[2]); pin(pt[3]); }
                    if constexpr (layer == 0 && i < 2 && kk > 0 && 12 + 4 * i + kk - 1 >= NRT && 12 + 4 * i + kk - 1 < RING2) ar[12 + 4 * i + kk - 1] = ld128s(rl, ho[1], ks[2 + 4 * i + kk - 1]);
                    if constexpr (layer == 0 && i == kGSI && kk > 0) __builtin_amdgcn_raw_buffer_store_b128(v4u{g[2 * kk - 2][0], g[2 * kk - 2][1], g[2 * kk - 1][0], g[2 * kk - 1][1]}, rs, gso + ((kk - 1) & 1) * 1024, kk < 3 ? pol : pol ^ 8192u, SC1);
                    if constexpr (layer == 0 && i > 2 && i <= 7 && kk == (i == kGSI ? 0 : 2)) {
                        { unsigned d0_, d1_; rda<20 + 2 * (i - 3)>(d0_); rda<21 + 2 * (i - 3)>(d1_); dsk[i - 3] = v2u{d0_, d1_}; }
                    }
                    if constexpr (kk == 0 && i > 0 && pv < 40) ar[pv % RING2] = ld128s(rl, ho[pv / 10], ks[pv % 10]);
                    constexpr int s1 = s - 1 - (KMB && layer == 0 && m == 1 ? kFGI - 10 : 0);
                    if constexpr (kk == 2 && m >= 1 && s1 >= 0 && s1 < 5) *(f32x4*)(smem + dst[m - 1] + s1 * 1024) = a2[m - 1][s1];
                    site();
                    if constexpr (i >= 37) a2[i - 37][kk] = *(const f32x4*)(src + (i - 37) * 5120 + kk * 1024);
                    SCHED();
                });
                if constexpr (i == 10 && (layer || !KMB)) { asm volatile("s_waitcnt lgkmcnt(0)" ::: "memory"); __builtin_amdgcn_s_barrier(); asm volatile("" ::: "memory"); }
                if constexpr (i == kFGI && layer == 0) {
                    static_assert(kFGI >= 10 && kFGI <= 14, "");
                    asm volatile("s_waitcnt vmcnt(%0) lgkmcnt(0)" ::"i"(kFGI - kGSI) : "memory");
                    __builtin_amdgcn_s_barrier(); asm volatile("" ::: "memory");
                    st64(v2u{tag0, tag1}, rs, fgo1);
                }
                if constexpr (i == 26 && layer == 0) {
#pragma unroll
                    for (int nb = 0; nb < 5; ++nb) { a2[3][nb] = f32x4{zz[nb][0], zz[nb][1], zz[nb][2], zz[nb][3]}; pin(a2[3][nb]); }
                }
                if constexpr (i == kGPI && layer == 0) gld2s<0>(f0, fgo, wl);
                if constexpr (i == kGCI && layer == 0) {
                    wait1(f0);
                    static_for<39 - i>(LAMBDA(r_) { pin(ar[(i + 1 + r_.value) % RING2]); });
                    gearly = __builtin_amdgcn_ballot_w64(!ISTAG(f0)) == 0ull;
                    ORDER();
                    undef3(gl);
                    if (__builtin_expect(gearly, 1)) { gld4t<0>(gl[0], glo, wl); gld4t<0>(gl[1], glo, glb1); gld4t<0>(gl[2], glo, glb2); }
                    else if (G2ND) poll2(fgo, fgo, wl);
                }
                if constexpr (i == 36) {
                    asm volatile("s_waitcnt lgkmcnt(0)" ::: "memory");
                    __builtin_amdgcn_s_barrier(); asm volatile("" ::: "memory");
                }
                if constexpr (STAMPS == 1 && (i == 0 || i == 9 || i == 10 || i == 15 || i == 25 || i == 35 || i == 36)) { STAMP((layer ? 51 : 41) + (i == 0 ? 0 : i == 9 ? 1 : i == 10 ? 2 : i == 15 ? 3 : i == 25 ? 4 : i == 35 ? 5 : 6)) }
                SCHED();
            });
            STAMP(layer ? 18 : 8)
        }
#pragma unroll
        for (int nb = 0; nb < 5; ++nb) a2[0][nb] = (a2[0][nb] + a2[1][nb]) + a2[2][nb];
        asm volatile("s_nop 1" : "+v"(a2[3][0]), "+v"(a2[3][1]), "+v"(a2[3][2]), "+v"(a2[3][3]), "+v"(a2[3][4]),
                     "+v"(a2[0][0]), "+v"(a2[0][1]), "+v"(a2[0][2]), "+v"(a2[0][3]), "+v"(a2[0][4]));
        float ty;
        {
            f32x4 s1, s2;
#pragma unroll
            for (int nb = 0; nb < 5; ++nb) {
                const f32x4 y = a2[3][nb] + a2[0][nb];
                xin[nb] = y;
                if (nb == 0) { s1 = y; s2 = y * y; } else { s1 += y; s2 += y * y; }
            }
            ty = xsum2h(hsum4(s1), hsum4(s2));
            st64(v2u{__float_as_uint(ty), tag0}, rl, sgs);
        }
        SCHED();
        STAMP(layer ? 19 : 9)
        if constexpr (layer == 1 && OTR) {
#pragma unroll
            for (int nb = 0; nb < 5; ++nb) *(f32x4*)(smem + otw + nb * 64) = xin[nb];
#pragma unroll
            for (int nb = 0; nb < 5; ++nb) xin[nb] = *(const f32x4*)(smem + (nb < 4 ? otr + nb * 336 : ots));
            SCHED();
        }
        int lane_o, b_o = b0, w_o = w0;
        asm volatile("v_mbcnt_lo_u32_b32 %0, -1, 0\n\tv_mbcnt_hi_u32_b32 %0, -1, %0" : "=v"(lane_o));
        asm volatile("" : "+s"(b_o), "+s"(w_o));
        const int lane = lane_o, b = b_o, w = w_o;
        const int c = lane & 15, q = lane >> 4;
        TILES()
        if (layer == 0 && __builtin_expect(!gearly, 0)) {
            if (G2ND) {
                unsigned f0_, f1_;
                asm volatile("s_waitcnt vmcnt(1)\n\t" AR("%0", 36, 0) AR("%1", 36, 1) : "=v"(f0_), "=v"(f1_)::"memory");
                f0 = v2u{f0_, f1_};
            }
            STAMP(63)
            WAVE_RETRY(ISTAG(f0), f0 = ld64(rl, fgo))
            gld4t<0>(gl[0], glo, wl); gld4t<0>(gl[1], glo, wl + 8 * RUN); gld4t<0>(gl[2], glo, wl + 12 * RUN);
            asm volatile("s_waitcnt vmcnt(0)" : "+v"(gl[0]), "+v"(gl[1]), "+v"(gl[2])::"memory");
        }
        if (layer == 0) {
            asm volatile("s_waitcnt vmcnt(1)" : "+v"(gl[0]), "+v"(gl[1]), "+v"(gl[2])::"memory");
            STAMP(60)
            char* tb = smem + SC + w * INBOX;
            *(v4u*)(tb + lane * 16) = gl[0]; *(v4u*)(tb + 1024 + lane * 16) = gl[1]; *(v4u*)(tb + 1536 + lane * 16) = gl[2];
#pragma unroll
            for (int nb = 0; nb < 5; ++nb) gt[nb] = __builtin_bit_cast(v2u, trread(tb, (16 * nb + 4 * q + (c >> 2)) * 32 + (c & 3) * 8));
        } else ACC_TIE5(gt);
        SCHED();
        constexpr int PR = layer ? PRM1 : PRM0, NP = (PR & 1) + (PR >> 1);
        if constexpr (PR & 1) gran_req<32>(sgo, wl);
        SCHED();
        f32x4 gf[5], yg[5];
#pragma unroll
        for (int nb = 0; nb < 5; ++nb) { gf[nb] = unpk4(gt[nb]); yg[nb] = xin[nb] * gf[nb]; pin(gf[nb]); pin(yg[nb]); }
        SCHED();
        if constexpr (PR & 2) gran_req<(PR & 1 ? 40 : 32)>(sgo, wl);
        SCHED();
        unsigned xs = OFF_X2F + (unsigned)(((mt2 >> 3) & 1) * PG + ((2 * (mt2 & 7) + (w >> 1)) * 8 + ((c >> 2) & 1) * 4 + (nt2 >> 1)) * RUN + (10 * (nt2 & 1) + 2 * (q & 1) + (q >> 1)) * 256 + (8 * (w & 1) + 4 * (c >> 3) + (c & 3)) * 16);
        unsigned pso = q < 2 ? OFF_PS + (unsigned)((mt1 & 7) * RUN + (((mt2 & 1) * 4 + w) * 8 + nt2) * 128 + c * 8 + q * 4) : kNone;
        unsigned fxo1 = lane ? kNone : OFF_FX + (unsigned)((mt1 & 7) * RUN + ((2 * (mt2 & 1) + (w >> 1)) * 16 + nt2 * 2 + (w & 1)) * 128);
        unsigned oo = (unsigned)(((64 * mt2 + 16 * w + c) * D_ + 80 * nt2 + 4 * q) * 4);
        unsigned o4 = (unsigned)(((64 * mt2 + 16 * w + (c & 12)) * D_ + 80 * nt2 + 16 * (q + (nt2 & 1)) + 4 * (c & 3)) * 4);
        if (OTR) oo += (nt2 & 1) ? 0u : 256u;
        unsigned xs2 = (q & 1) ? kNone : xs + 2048u;
        if (layer == 0) { pin(xs); pin(xs2); pin(pso); pin(fxo1); } else { pin(oo); if (OTR) pin(o4); }
        SCHED();
        {
            float a, d;
            STAMP(layer ? 62 : 61)
            if constexpr (layer == 0) site();
            pipe_gran<(layer ? PDS : PDS0), NP>(a, d, sgo, wl, tag0, dead);
            STAMP(layer ? 20 : 10)
            xsum2(a, d);
            const float me = a * (1.0f / D_);
            const float ri = __builtin_amdgcn_rsqf(fmaxf(d * (1.0f / D_) - me * me, 0.f) + 1e-5f), mr = -me * ri;
            if constexpr (layer == 1 && OTR) {
                static_for<5>(LAMBDA(j_) {
                    constexpr int jj = (j_.value + 4) % 5;
                    const float rj = jj < 4 ? dpp<0x55 * (jj & 3)>(ri) : ri, mj = jj < 4 ? dpp<0x55 * (jj & 3)>(mr) : mr;
                    const f32x4 o = yg[jj] * rj + (gf[jj] * mj + xin[jj]);
                    __builtin_amdgcn_raw_buffer_store_b128(__builtin_bit_cast(v4u, o), ro, jj < 4 ? o4 : oo, jj < 4 ? jj * 2560 : 0, OUTP);
                });
            } else if constexpr (layer == 0 && XI) {
#pragma unroll
                for (int i = 0; i < 3; ++i) {
                    v2u pk[2];
#pragma unroll
                    for (int e = 0; e < (i < 2 ? 2 : 1); ++e) {
                        const f32x4 x = yg[2 * i + e] * ri + (gf[2 * i + e] * mr + xin[2 * i + e]);
                        xin[2 * i + e] = x;
                        pk[e] = v2u{cvtpk(x.x, x.y), cvtpk(x.z, x.w)};
                    }
                    const v2u pa = pk[0], pb = pk[i < 2];
                    const auto r0 = __builtin_amdgcn_permlane16_swap(pa.x, pb.x, false, false);
                    const auto r1 = __builtin_amdgcn_permlane16_swap(pa.y, pb.y, false, false);
                    st128(v4u{r0[0], r1[0], r0[1], r1[1]}, rl, i < 2 ? xs + i * 1024 : xs2);
                    SCHED();
                }
            } else {
#pragma unroll
                for (int nb = 0; nb < 5; ++nb) xin[nb] = yg[nb] * ri + (gf[nb] * mr + xin[nb]);
            }
        }
        if constexpr (layer == 0) {
            v2u pk[5];
#pragma unroll
            for (int nb = 0; nb < 5; ++nb) { const f32x4 x = xin[nb]; pk[nb] = v2u{cvtpk(x.x, x.y), cvtpk(x.z, x.w)}; }
#pragma unroll
            for (int i = 0; i < (XI ? 0 : 3); ++i) {
                const v2u pa = pk[2 * i], pb = pk[i < 2 ? 2 * i + 1 : 4];
                const auto r0 = __builtin_amdgcn_permlane16_swap(pa.x, pb.x, false, false);
                const auto r1 = __builtin_amdgcn_permlane16_swap(pa.y, pb.y, false, false);
                if (i < 2 || (q & 1) == 0) st128(v4u{r0[0], r1[0], r0[1], r1[1]}, rl, xs + i * 1024);
            }
            {
                f32x4 s1, s2;
#pragma unroll
                for (int nb = 0; nb < 5; ++nb) { const f32x4 x = xin[nb]; if (nb == 0) { s1 = x; s2 = x * x; } else { s1 += x; s2 += x * x; } }
                __builtin_amdgcn_raw_buffer_store_b32(__float_as_uint(xsum2h(hsum4(s1), hsum4(s2))), rl, pso, 0, SC1);
            }
            SCHED();
            int w1lane0 = (c & 1) * 10240 + (c >> 1) * 128 + ((q ^ ((c & 1) | ((c >> 2) << 1))) * 16);
            int w1lane1 = (c & 1) * 10240 + (c >> 1) * 128 + (((4 + q) ^ ((c & 1) | ((c >> 2) << 1))) * 16);
            unsigned fxo = OFF_FX + (unsigned)((mt1 & 7) * RUN + w * 2048 + (lane >> 2) * 128);
            unsigned xo = OFF_X2F + (unsigned)(((mt1 >> 2) & 1) * PG + (4 * (mt1 & 3) + w) * 8 * RUN + lane * 16), xo1 = xo + 4 * RUN;
            const int row = 8 * q + (c & 7);
            unsigned po = OFF_PS + (unsigned)((mt1 & 7) * RUN + ((2 * w + (row >> 4)) * 8 + 4 * (c >> 3)) * 128 + (row & 15) * 8);
            int w1hi0 = w1lane0 + 61440, w1hi1 = w1lane1 + 61440;
            int rsw = l_rs(1, 0) + w * INBOX + lane * 4;
            if (GW) pin(rsw);
            pin(w1lane0); pin(w1lane1); pin(w1hi0); pin(w1hi1); pin(fxo); pin(xo); pin(xo1); pin(po);
            v4u wf[2][5];
#pragma unroll
            for (int nb = 0; nb < 5; ++nb) wf[0][nb] = *(const v4u*)(smem + w1lane0 + nb * 20480);
            v2u tg2 = {tag0, tag1};
            pin(tg2);
            SCHED();
            DRAIN();
            st64(tg2, rl, fxo1);
            if (b == 255 && w == 0) FLAG(OFF_NONCE)
            STAMP(11)
            addrs(std::integral_constant<int, 1>{}, lane, w, b);
            v2u pt[4];
            skip<SKX>();
            pipe_flag<KPFX>(pt, fxo, wl, (u64)tag0 | ((u64)tag1 << 32), dead);
            ORDER();
            STAMP(12)
            v4u ar[RING1][2];
#pragma unroll
            for (int s = 0; s < RING1; ++s)
#pragma unroll
                for (int mb = 0; mb < 2; ++mb) { ar[s][mb] = ld128s(rl, mb ? xo1 : xo, kso(s)); SCHED(); }
            v2u ps[4];
#pragma unroll
            for (int i = 0; i < 4; ++i) ps[i] = ld64(rl, po + i * 128);
            SCHED();
            float t1, t2, rinv, mri, rsel, rr_[8], rm_[8];
            static_for<20>(LAMBDA(s_) {
                constexpr int s = s_.value, cu = s & 1, nx = cu ^ 1;
                LGKM0() SCHED();
                static_for<10>(LAMBDA(g_) {
                    constexpr int g = g_.value, mb = g / 5, k = g % 5;
                    if constexpr (s == 0) mf0(acc[mb][k], ar[0][mb], wf[0][k]); else mf(acc[mb][k], ar[s % RING1][mb], wf[cu][k]);
                    SCHED();
                    if constexpr (s == 0 && g == 0) { pin(pt[0]); pin(pt[1]); pin(pt[2]); pin(pt[3]); }
                    if constexpr (s < 19 && g < 3) wf[nx][g] = *(const v4u*)(smem + (nx ? w1lane1 : w1lane0) + g * 20480 + ((s + 1) >> 1) * 1024);
                    if constexpr (s < 19 && (g == 3 || g == 4)) wf[nx][g] = *(const v4u*)(smem + (nx ? w1hi1 : w1hi0) + (g - 3) * 20480 + ((s + 1) >> 1) * 1024);
                    if constexpr (g == 5 && s + RING1 < 20) ar[s % RING1][0] = ld128s(rl, xo, kso(s + RING1));
                    if constexpr (g == 2 && s > 0 && s - 1 + RING1 < 20) ar[(s - 1) % RING1][1] = ld128s(rl, xo1, kso(s - 1 + RING1));
                    constexpr int z = GW ? s + 3 : s;
                    if constexpr (z == 8 && g == 8) { t1 = __uint_as_float(ps[0].x) + __uint_as_float(ps[1].x); pin(t1); t2 = __uint_as_float(ps[0].y) + __uint_as_float(ps[1].y); pin(t2); }
                    if constexpr (z == 9 && g == 6) { t1 += __uint_as_float(ps[2].x); pin(t1); t2 += __uint_as_float(ps[2].y); pin(t2); }
                    if constexpr (z == 9 && g == 7) { t1 += __uint_as_float(ps[3].x); pin(t1); t2 += __uint_as_float(ps[3].y); pin(t2); }
                    if constexpr (z == 10 && g == 6) { t1 += dpp<0x128>(t1); t2 += dpp<0x128>(t2); pin(t1); pin(t2); }
                    if constexpr (z == 10 && g == 7) { t1 *= (1.0f / D_); pin(t1); t2 *= (1.0f / D_); pin(t2); }
                    if constexpr (z == 10 && g == 8) { t2 = fmaxf(t2 - t1 * t1, 0.f); pin(t2); }
                    if constexpr (z == 11 && g == 6) { rinv = __builtin_amdgcn_rsqf(t2 + 1e-5f); pin(rinv); }
                    if constexpr (z == 11 && g == 8) { mri = -t1 * rinv; pin(mri); }
                    if constexpr (!GW && s == 12 && g == 4) *(float*)(smem + l_rs(1, 0) + w * INBOX + lane * 4) = c < 8 ? rinv : mri;
                    if constexpr (GW && s == 19 && g == 1) selhi(rsel, mri);
                    if constexpr (GW && s == 19 && g == 0) { rsel = rinv; pin(rsel); }
                    if constexpr (GW && s == 19 && g == 2) *(float*)(smem + rsw) = rsel;
                    if constexpr (z >= 12 && z < 15 && g >= 6 && g < 9 && 3 * (z - 12) + g - 6 < 8) bc2b<3 * (z - 12) + g - 6>(rr_[3 * (z - 12) + g - 6], rm_[3 * (z - 12) + g - 6], rinv, mri);
                    if constexpr (!GW && s >= 15 && s < 19 && g >= 5) TT_GAP(5 * (s - 15) + g - 5)
                    if constexpr (GW && s >= 12 && s < 19 && g >= 6 && g < 9 && 3 * (s - 12) + g - 6 < 20) TT_GAP(3 * (s - 12) + g - 6)
                    site();
                    SCHED();
                });
            });
            rr[0] = f32x4{rr_[0], rr_[1], rr_[2], rr_[3]}; rr[1] = f32x4{rr_[4], rr_[5], rr_[6], rr_[7]};
            asm volatile("kpf_end2:");
            ACC_FENCE10(acc[0], acc[1]);
            STAMP(13)
        } else {
#pragma unroll
            for (int nb = 0; nb < (OTR ? 0 : 5); ++nb) __builtin_amdgcn_raw_buffer_store_b128(__builtin_bit_cast(v4u, xin[nb]), ro, oo + 64 * nb, 0, OUTP);
            STAMP(21)
            STAMP_DUMP()
            asm volatile("kpf_end:");
        }
    };
    layer_body(std::integral_constant<int, 0>{});
    layer_body(std::integral_constant<int, 1>{});
}

extern "C" void kernel_launch(void* const* d_in, const int* in_sizes, int n_in,
                              void* d_out, int out_size, void* d_ws, size_t ws_size,
                              hipStream_t stream) {
    Params p;
    p.x0 = (const u16*)d_in[0];
    p.Lre = (const u16*)d_in[1];
    p.Lim = (const u16*)d_in[2];
    p.Bre = (const u16*)d_in[3];
    p.Bim = (const u16*)d_in[4];
    p.Cre = (const u16*)d_in[5];
    p.Cim = (const u16*)d_in[6];
    p.logdt = (const u16*)d_in[7];
    p.Dskip = (const u16*)d_in[8];
    p.k = (const u16*)d_in[10];
    p.v = (const u16*)d_in[11];
    p.shk = (const u16*)d_in[12];
    p.ssmk = (const u16*)d_in[13];
    p.ssmD = (const u16*)d_in[14];
    p.ws = (char*)d_ws;
    p.out = (float*)d_out;
    k_main<<<256, 256, 0, stream>>>(p);
}
```

```cpp
#include <hip/hip_runtime.h>
#include <stdint.h>
#include <utility>

#define STAMPS 0

typedef unsigned short u16;
typedef unsigned long long u64;
typedef unsigned v4u __attribute__((ext_vector_type(4)));
typedef unsigned v2u __attribute__((ext_vector_type(2)));
typedef float f32x4 __attribute__((ext_vector_type(4)));
typedef float f32x2 __attribute__((ext_vector_type(2)));
typedef short s16x4 __attribute__((ext_vector_type(4)));
typedef __bf16 bf16x2v __attribute__((ext_vector_type(2)));
#define LAS __attribute__((address_space(3)))
#define DI __device__ __forceinline__

constexpr int S_ = 2048, D_ = 640;
#define KG0 12
#define KRT 12
#define KRS 2
#define KB1 4
#define KMB 1
#define SBT 2
#define LAG 1
#define OTR 1
#define XI 1
#define GW 0
#define POL 0
#define SKP 0
#define SK0 0
#define SK5 850
#define SKX 0
#define SK1 0
#define SK2 60
#define PRM0 2
#define PRM1 1
#define WD 0
#define KR1 5
#define KR2 10
constexpr int RING1 = KR1;
#define KCR1 3
constexpr int kCR1 = KCR1;
#define KXA 10
#define KN3E 17
#define KN3 6
constexpr int kXA = KXA;
constexpr int kN3E = KN3E, kN3 = KN3;
#define KFHK 4
#define KFHP 12
constexpr int kFHK = KFHK, kFHP = KFHP;
#define KGPI 27
#define KGCI 33
#define KFGI 11
#define G2ND 1
#define KGSI 2
constexpr int kGSI = KGSI, kFGI = KFGI, kGPI = KGPI, kGCI = KGCI;

constexpr unsigned OFF_NONCE = 0;
constexpr unsigned OFF_CV = 1024;
constexpr unsigned OFF_ST = 4096;
constexpr unsigned OFF_FW = OFF_ST + 65536;
constexpr unsigned OFF_FK = OFF_FW + 131072;
constexpr unsigned OFF_KVT = OFF_FK + 81920;
constexpr unsigned RUN = 16384, PG = 4194304, MID = 1536;
constexpr unsigned OFF_SG = 8 * PG;
constexpr unsigned OFF_FH = OFF_SG + 32 * RUN;
constexpr unsigned OFF_FX = OFF_FH + 16 * RUN;
constexpr unsigned OFF_PS = OFF_FX + 8 * RUN;
constexpr unsigned OFF_EG = OFF_PS + 8 * RUN + MID;
constexpr unsigned OFF_X2F = 9 * PG + MID;
constexpr unsigned OFF_HF = 11 * PG + MID;
constexpr unsigned OFF_W2F = 19 * PG + MID;
constexpr unsigned OFF_GT = 23 * PG;
constexpr unsigned WS_BYTES = 25 * PG;
constexpr unsigned kso(int s) { return (unsigned)(s / 5) * RUN + (unsigned)(s % 5) * 1024u; }
constexpr unsigned kNone = 0x40000000u;
constexpr unsigned OFF_DBG = 0x1800000;
#define MAP 0
#define TILES() const int xq = b & 7, j = b >> 3, mt2 = MAP ? 8 * (xq >> 1) + (j >> 2) : 4 * xq + (j >> 3), nt2 = MAP ? 4 * (xq & 1) + (j & 3) : j & 7, mt1 = mt2 >> 1, \
    nt1 = MAP ? 8 * (xq & 1) + (j & 7) : j & 15, i32 = 16 * (mt2 & 1) + mt1;

constexpr int SC = 102400;
constexpr int REGSZ = 13056;
constexpr int L_XR = SC;
constexpr int L_CO = SC + 12288;
constexpr int L_KB = SC + 51712;
constexpr int L_TAB = SC + 55808;
constexpr int L_CW = SC + 39168;
constexpr int INBOX = 15360;
constexpr int l_rs(int l, int w) { return l ? SC + w * INBOX : L_KB + w * 256; }
constexpr int l_sg(int l, int w) { return l ? SC + w * INBOX + 256 : SC + (w == 0 ? 43264 : w == 1 ? 46080 : w == 2 ? 52736 : 56320); }
constexpr int l_tb(int l, int w) { return l ? SC + w * INBOX + 2048 : SC + 40960 + w * 5120; }
constexpr int LDS_BYTES = 163840;
constexpr int CV_E = 4608;
constexpr int CV_O = CV_E + (1056 + 15) * 4;

struct Params {
    const u16 *x0, *Lre, *Lim, *Bre, *Bim, *Cre, *Cim, *logdt, *Dskip, *k, *v, *shk, *ssmk, *ssmD;
    char* ws;
    float* out;
};

#define MF "v_mfma_f32_16x16x32_bf16 "
DI void mf(f32x4& c, v4u a, v4u b) { asm(MF "%0, %1, %2, %0" : "+v"(c) : "v"(a), "v"(b)); }
DI void mf0(f32x4& c, v4u a, v4u b) { asm(MF "%0, %1, %2, 0" : "=&v"(c) : "v"(a), "v"(b)); }
constexpr int W2R = 56;
#define WA(n) "a[%c" #n ":%c" #n "+3]"
template <int R> DI void mfa(f32x4& c, v4u a) { asm(MF "%0, " WA(2) ", %1, %0" : "+v"(c) : "v"(a), "i"(W2R + 4 * R)); }
template <int R> DI void mfa0(f32x4& c, v4u a) { asm(MF "%0, " WA(2) ", %1, 0" : "=&v"(c) : "v"(a), "i"(W2R + 4 * R)); }
#define ACC_FENCE10(c, d) asm volatile("" : "+v"(c[0]), "+v"(c[1]), "+v"(c[2]), "+v"(c[3]), "+v"(c[4]), "+v"(d[0]), "+v"(d[1]), "+v"(d[2]), "+v"(d[3]), "+v"(d[4]))
#define ACC_TIE5(c) asm volatile("" : "+v"(c[0]), "+v"(c[1]), "+v"(c[2]), "+v"(c[3]), "+v"(c[4]))
#define QP " quad_perm:[1,0,3,2] row_mask:0xf bank_mask:0xf"
DI void cstep(float (&t)[5], const float (&h)[5], const float (&lr)[5], const float (&ls)[5], float b0, float b1, float b2, float b3, float b4) {
    asm("v_fma_f32 %0, %10, %5, %20\n\tv_fma_f32 %1, %11, %6, %21\n\tv_fma_f32 %2, %12, %7, %22\n\tv_fma_f32 %3, %13, %8, %23\n\tv_fma_f32 %4, %14, %9, %24\n\t"
        "v_fmac_f32_dpp %0, %5, %15" QP "\n\tv_fmac_f32_dpp %1, %6, %16" QP "\n\tv_fmac_f32_dpp %2, %7, %17" QP "\n\tv_fmac_f32_dpp %3, %8, %18" QP "\n\tv_fmac_f32_dpp %4, %9, %19" QP
        : "=&v"(t[0]), "=&v"(t[1]), "=&v"(t[2]), "=&v"(t[3]), "=&v"(t[4])
        : "v"(h[0]), "v"(h[1]), "v"(h[2]), "v"(h[3]), "v"(h[4]), "v"(lr[0]), "v"(lr[1]), "v"(lr[2]), "v"(lr[3]), "v"(lr[4]),
          "v"(ls[0]), "v"(ls[1]), "v"(ls[2]), "v"(ls[3]), "v"(ls[4]), "v"(b0), "v"(b1), "v"(b2), "v"(b3), "v"(b4));
}
DI void cstep3(float* t, const float* h, const float* lr, const float* ls, const float* b) {
    asm("v_fma_f32 %0, %6, %3, %12\n\tv_fma_f32 %1, %7, %4, %13\n\tv_fma_f32 %2, %8, %5, %14\n\t"
        "v_fmac_f32_dpp %0, %3, %9" QP "\n\tv_fmac_f32_dpp %1, %4, %10" QP "\n\tv_fmac_f32_dpp %2, %5, %11" QP
        : "=&v"(t[0]), "=&v"(t[1]), "=&v"(t[2])
        : "v"(h[0]), "v"(h[1]), "v"(h[2]), "v"(lr[0]), "v"(lr[1]), "v"(lr[2]), "v"(ls[0]), "v"(ls[1]), "v"(ls[2]), "v"(b[0]), "v"(b[1]), "v"(b[2]));
}
template <int o> DI void chain24(float (*t)[3], const float (*b)[5], const float* lr, const float* ls) {
    asm("v_fma_f32 %0, %18, %16, %8\n\t" "v_fma_f32 %1, %19, %17, %9\n\t" "v_fmac_f32_dpp %0, %16, %20" QP "\n\t" "v_fmac_f32_dpp %1, %17, %21" QP "\n\t"
        "v_fma_f32 %2, %18, %0, %10\n\t" "v_fma_f32 %3, %19, %1, %11\n\t" "v_fmac_f32_dpp %2, %0, %20" QP "\n\t" "v_fmac_f32_dpp %3, %1, %21" QP "\n\t"
        "v_fma_f32 %4, %18, %2, %12\n\t" "v_fma_f32 %5, %19, %3, %13\n\t" "v_fmac_f32_dpp %4, %2, %20" QP "\n\t" "v_fmac_f32_dpp %5, %3, %21" QP "\n\t"
        "v_fma_f32 %6, %18, %4, %14\n\t" "v_fma_f32 %7, %19, %5, %15\n\t" "v_fmac_f32_dpp %6, %4, %20" QP "\n\t" "v_fmac_f32_dpp %7, %5, %21" QP "\n\t"
        : "=&v"(t[1][0]), "=&v"(t[1][1]), "=&v"(t[2][0]), "=&v"(t[2][1]), "=&v"(t[3][0]), "=&v"(t[3][1]), "=&v"(t[4][0]), "=&v"(t[4][1])
        : "v"(b[0][o + 0]), "v"(b[0][o + 1]), "v"(b[1][o + 0]), "v"(b[1][o + 1]), "v"(b[2][o + 0]), "v"(b[2][o + 1]), "v"(b[3][o + 0]), "v"(b[3][o + 1]), "v"(t[0][0]), "v"(t[0][1]), "v"(lr[o + 0]), "v"(lr[o + 1]), "v"(ls[o + 0]), "v"(ls[o + 1]));
}
template <int o> DI void chain33(float (*t)[3], const float (*b)[5], const float* lr, const float* ls) {
    asm("v_fma_f32 %0, %21, %18, %9\n\t" "v_fma_f32 %1, %22, %19, %10\n\t" "v_fma_f32 %2, %23, %20, %11\n\t" "v_fmac_f32_dpp %0, %18, %24" QP "\n\t" "v_fmac_f32_dpp %1, %19, %25" QP "\n\t" "v_fmac_f32_dpp %2, %20, %26" QP "\n\t"
        "v_fma_f32 %3, %21, %0, %12\n\t" "v_fma_f32 %4, %22, %1, %13\n\t" "v_fma_f32 %5, %23, %2, %14\n\t" "v_fmac_f32_dpp %3, %0, %24" QP "\n\t" "v_fmac_f32_dpp %4, %1, %25" QP "\n\t" "v_fmac_f32_dpp %5, %2, %26" QP "\n\t"
        "v_fma_f32 %6, %21, %3, %15\n\t" "v_fma_f32 %7, %22, %4, %16\n\t" "v_fma_f32 %8, %23, %5, %17\n\t" "v_fmac_f32_dpp %6, %3, %24" QP "\n\t" "v_fmac_f32_dpp %7, %4, %25" QP "\n\t" "v_fmac_f32_dpp %8, %5, %26" QP "\n\t"
        : "=&v"(t[1][0]), "=&v"(t[1][1]), "=&v"(t[1][2]), "=&v"(t[2][0]), "=&v"(t[2][1]), "=&v"(t[2][2]), "=&v"(t[3][0]), "=&v"(t[3][1]), "=&v"(t[3][2])
        : "v"(b[0][o + 0]), "v"(b[0][o + 1]), "v"(b[0][o + 2]), "v"(b[1][o + 0]), "v"(b[1][o + 1]), "v"(b[1][o + 2]), "v"(b[2][o + 0]), "v"(b[2][o + 1]), "v"(b[2][o + 2]), "v"(t[0][0]), "v"(t[0][1]), "v"(t[0][2]), "v"(lr[o + 0]), "v"(lr[o + 1]), "v"(lr[o + 2]), "v"(ls[o + 0]), "v"(ls[o + 1]), "v"(ls[o + 2]));
}
template <int o> DI void chain32(float (*t)[3], const float (*b)[5], const float* lr, const float* ls) {
    asm("v_fma_f32 %0, %15, %12, %6\n\t" "v_fma_f32 %1, %16, %13, %7\n\t" "v_fma_f32 %2, %17, %14, %8\n\t" "v_fmac_f32_dpp %0, %12, %18" QP "\n\t" "v_fmac_f32_dpp %1, %13, %19" QP "\n\t" "v_fmac_f32_dpp %2, %14, %20" QP "\n\t"
        "v_fma_f32 %3, %15, %0, %9\n\t" "v_fma_f32 %4, %16, %1, %10\n\t" "v_fma_f32 %5, %17, %2, %11\n\t" "v_fmac_f32_dpp %3, %0, %18" QP "\n\t" "v_fmac_f32_dpp %4, %1, %19" QP "\n\t" "v_fmac_f32_dpp %5, %2, %20" QP "\n\t"
        : "=&v"(t[1][0]), "=&v"(t[1][1]), "=&v"(t[1][2]), "=&v"(t[2][0]), "=&v"(t[2][1]), "=&v"(t[2][2])
        : "v"(b[0][o + 0]), "v"(b[0][o + 1]), "v"(b[0][o + 2]), "v"(b[1][o + 0]), "v"(b[1][o + 1]), "v"(b[1][o + 2]), "v"(t[0][0]), "v"(t[0][1]), "v"(t[0][2]), "v"(lr[o + 0]), "v"(lr[o + 1]), "v"(lr[o + 2]), "v"(ls[o + 0]), "v"(ls[o + 1]), "v"(ls[o + 2]));
}
DI void pstep(f32x2& xr, f32x2& xs, f32x2 pr, f32x2 ps, f32x2 m) {
    asm("v_pk_mul_f32 %0, %2, %4 op_sel_hi:[1,0]\n\tv_pk_mul_f32 %1, %3, %4 op_sel_hi:[1,0]\n\t"
        "v_pk_fma_f32 %0, %3, %4, %0 op_sel:[0,1,0] neg_lo:[1,0,0] neg_hi:[1,0,0]\n\tv_pk_fma_f32 %1, %2, %4, %1 op_sel:[0,1,0]"
        : "=&v"(xr), "=&v"(xs) : "v"(pr), "v"(ps), "v"(m));
}
DI void padd(f32x2 (&h)[4], const f32x2 (&pr)[2], const f32x2 (&ps)[2], f32x2 c) {
    f32x2 d;
    asm("v_pk_mul_f32 %4, %6, %9 op_sel:[1,0]\n\tv_pk_fma_f32 %0, %5, %9, %0 op_sel_hi:[1,0,1]\n\tv_pk_fma_f32 %1, %6, %9, %1 op_sel_hi:[1,0,1]\n\t"
        "v_pk_fma_f32 %4, %8, %9, %4 op_sel:[1,1,0] op_sel_hi:[1,0,1] neg_hi:[1,0,0]\n\tv_pk_fma_f32 %0, %7, %9, %0 op_sel:[0,1,0]\n\tv_pk_fma_f32 %1, %8, %9, %1 op_sel:[0,1,0]\n\t"
        "v_pk_fma_f32 %2, %5, %4, %2 op_sel_hi:[1,0,1]\n\tv_pk_fma_f32 %3, %6, %4, %3 op_sel_hi:[1,0,1]\n\tv_pk_fma_f32 %2, %7, %4, %2 op_sel:[0,1,0]\n\tv_pk_fma_f32 %3, %8, %4, %3 op_sel:[0,1,0]"
        : "+v"(h[0]), "+v"(h[1]), "+v"(h[2]), "+v"(h[3]), "=&v"(d) : "v"(pr[0]), "v"(pr[1]), "v"(ps[0]), "v"(ps[1]), "v"(c));
}
DI void cstepi(float& b0, float& b1, float& b2, float& b3, float& b4, float h0, float h1, float h2, float h3, float h4, const float (&lr)[5], const float (&ls)[5]) {
    asm("v_fmac_f32 %0, %10, %5\n\tv_fmac_f32 %1, %11, %6\n\tv_fmac_f32 %2, %12, %7\n\tv_fmac_f32 %3, %13, %8\n\tv_fmac_f32 %4, %14, %9\n\t"
        "v_fmac_f32_dpp %0, %5, %15" QP "\n\tv_fmac_f32_dpp %1, %6, %16" QP "\n\tv_fmac_f32_dpp %2, %7, %17" QP "\n\tv_fmac_f32_dpp %3, %8, %18" QP "\n\tv_fmac_f32_dpp %4, %9, %19" QP
        : "+v"(b0), "+v"(b1), "+v"(b2), "+v"(b3), "+v"(b4)
        : "v"(h0), "v"(h1), "v"(h2), "v"(h3), "v"(h4), "v"(lr[0]), "v"(lr[1]), "v"(lr[2]), "v"(lr[3]), "v"(lr[4]), "v"(ls[0]), "v"(ls[1]), "v"(ls[2]), "v"(ls[3]), "v"(ls[4]));
}
#define UNDEF8(c) asm volatile("" : "=v"(c[0]), "=v"(c[1]), "=v"(c[2]), "=v"(c[3]), "=v"(c[4]), "=v"(c[5]), "=v"(c[6]), "=v"(c[7]))
#define PIN5(t) asm volatile("s_nop 1" : "+v"(t[0]), "+v"(t[1]), "+v"(t[2]), "+v"(t[3]), "+v"(t[4]))
DI unsigned cvtpk(float lo, float hi) {
    f32x2 v = {lo, hi};
    return __builtin_bit_cast(unsigned, __builtin_convertvector(v, bf16x2v));
}
DI float lo16f(unsigned u) { return __uint_as_float(u << 16); }
DI float hi16f(unsigned u) { return __uint_as_float(u & 0xffff0000u); }
DI float rbf(float v) { return lo16f(cvtpk(v, 0.f)); }
template <int CTRL> DI float dpp(float x) {
    return __int_as_float(__builtin_amdgcn_update_dpp(0, __float_as_int(x), CTRL, 0xF, 0xF, true));
}
DI float rowsum16(float x) {
    x += dpp<0x128>(x); x += dpp<0x124>(x); x += dpp<0x122>(x); x += dpp<0x121>(x);
    return x;
}
DI float xsum32(float x) { auto r = __builtin_amdgcn_permlane32_swap(__float_as_uint(x), __float_as_uint(x), false, false); return __uint_as_float(r[0]) + __uint_as_float(r[1]); }
DI void xsum2(float& a, float& d) {
    auto r = __builtin_amdgcn_permlane16_swap(__float_as_uint(a), __float_as_uint(d), false, false);
    const float s = __uint_as_float(r[0]) + __uint_as_float(r[1]);
    const float t = xsum32(s);
    auto u = __builtin_amdgcn_permlane16_swap(__float_as_uint(t), __float_as_uint(t), false, false);
    a = __uint_as_float(u[0]); d = __uint_as_float(u[1]);
}
DI float xsum2h(float a, float d) {
    auto r = __builtin_amdgcn_permlane16_swap(__float_as_uint(a), __float_as_uint(d), false, false);
    return xsum32(__uint_as_float(r[0]) + __uint_as_float(r[1]));
}
DI float hsum4(f32x4 v) { return (v.x + v.y) + (v.z + v.w); }
typedef __amdgpu_buffer_rsrc_t rsrc_t;
constexpr int SC1 = 16;
DI v4u ld128(rsrc_t rs, unsigned off) { return __builtin_amdgcn_raw_buffer_load_b128(rs, off, 0, SC1); }
DI v4u ld128s(rsrc_t rs, unsigned off, unsigned soff) { return __builtin_amdgcn_raw_buffer_load_b128(rs, off, soff, SC1); }
DI v2u ld64(rsrc_t rs, unsigned off) { return __builtin_amdgcn_raw_buffer_load_b64(rs, off, 0, SC1); }
DI void st128(v4u v, rsrc_t rs, unsigned off) { __builtin_amdgcn_raw_buffer_store_b128(v, rs, off, 0, SC1); }
DI void st64(v2u v, rsrc_t rs, unsigned off) { __builtin_amdgcn_raw_buffer_store_b64(v, rs, off, 0, SC1); }
#define LGKM0() { __builtin_amdgcn_s_waitcnt(0xC07F); asm volatile("" ::: "memory"); }
#define DRAIN() asm volatile("s_waitcnt vmcnt(0)" ::: "memory")
#define ORDER() __builtin_amdgcn_fence(__ATOMIC_ACQUIRE, "wavefront")
#define SCHED() __builtin_amdgcn_sched_barrier(0)
constexpr unsigned kSpinCap = 1u << 17;
#define WAVE_WAIT(...)                                                      \
    {                                                                       \
        unsigned spins_ = dead ? kSpinCap : 0u;                             \
        _Pragma("nounroll") for (;;) {                                      \
            asm volatile("" ::: "memory");                                  \
            bool ok;                                                        \
            __VA_ARGS__;                                                    \
            if (__builtin_amdgcn_ballot_w64(!ok) == 0ull) break;            \
            if (++spins_ > kSpinCap) { dead = true; break; }                \
            __builtin_amdgcn_s_sleep(1);                                    \
        }                                                                   \
        ORDER();                                                            \
    }
#define KPD1 1
#define KPDS 0
#define KPDS0 0
#define KPF0 11
#define KPFX 3
#define KPF1 3
constexpr int PD1 = KPD1, PDS = KPDS, PDS0 = KPDS0;
#define PL(r) "global_load_dwordx2 " r ", %5, %6 sc1\n\t"
#define PS_(r) "s_waitcnt vmcnt(1)\n\tv_cmp_ne_u64 vcc, %7, " r "\n\ts_cbranch_vccz 2f\n\t" PL(r)
template <int D0> DI void pipe_flag(v2u (&t)[4], unsigned off, const void* base, u64 tag, bool& dead) {
    unsigned n = dead ? 0u : kSpinCap;
    asm volatile("s_sleep %8\n\t" PL("%0") "s_sleep %9\n\t" PL("%1")
                 "1:\n\t" PS_("%0") PS_("%1") "s_sub_u32 %4, %4, 1\n\ts_cbranch_scc0 1b\n2:"
                 : "=&v"(t[0]), "=&v"(t[1]), "=&v"(t[2]), "=&v"(t[3]), "+s"(n) : "v"(off), "s"(base), "s"(tag), "i"(D0), "i"(KPF1) : "memory", "vcc", "scc");
    dead = __builtin_amdgcn_readfirstlane(n) == ~0u;
}
#define WAVE_RETRY(OK, ...) { if (__builtin_expect(__builtin_amdgcn_ballot_w64(!(OK)) != 0ull, 0)) WAVE_WAIT({ __VA_ARGS__; ok = OK; }) ORDER(); }
#define STAMP(i)
#define STAMP_DUMP()

#define KLB0 0
#define KLB1 6
DI void site() { asm volatile(""); }
template <int N> DI void skip() { if constexpr (N > 0) asm volatile("s_branch 1f\n\t.rept %c0\n\ts_nop 0\n\t.endr\n1:" ::"i"(N)); }
template <class F, int... I> DI void static_for_impl(std::integer_sequence<int, I...>, F&& f) { (f(std::integral_constant<int, I>{}), ...); }
template <int N, class F> DI void static_for(F&& f) { static_for_impl(std::make_integer_sequence<int, N>{}, f); }
#define IC(n) std::integral_constant<int, (n)>{}
#define LAMBDA(v) [&](auto v) __attribute__((always_inline))
#define PK2 " op_sel:[1,1,0] op_sel_hi:[1,0,1] neg_lo:[1,0,0]"
DI f32x2 cmul(f32x2 a, f32x2 b) {
    f32x2 t;
    asm("v_pk_mul_f32 %0, %1, %2 op_sel_hi:[0,1]\n\ts_nop 0\n\tv_pk_fma_f32 %0, %1, %2, %0" PK2 "\n\ts_nop 0" : "=&v"(t) : "v"(a), "v"(b));
    return t;
}
DI f32x2 cfma(f32x2 a, f32x2 b, f32x2 c) {
    f32x2 t;
    asm("v_pk_fma_f32 %0, %1, %2, %3 op_sel_hi:[0,1,1]\n\ts_nop 0\n\tv_pk_fma_f32 %0, %1, %2, %0" PK2 "\n\ts_nop 0" : "=&v"(t) : "v"(a), "v"(b), "v"(c));
    return t;
}
DI void cfma4(f32x2 (&t)[4], f32x2 a, f32x2 b0, f32x2 b1, f32x2 b2, f32x2 b3, f32x2 c0, f32x2 c1, f32x2 c2, f32x2 c3) {
    asm("v_pk_fma_f32 %0, %4, %5, %9 op_sel_hi:[0,1,1]\n\tv_pk_fma_f32 %1, %4, %6, %10 op_sel_hi:[0,1,1]\n\tv_pk_fma_f32 %2, %4, %7, %11 op_sel_hi:[0,1,1]\n\tv_pk_fma_f32 %3, %4, %8, %12 op_sel_hi:[0,1,1]\n\t"
        "v_pk_fma_f32 %0, %4, %5, %0" PK2 "\n\tv_pk_fma_f32 %1, %4, %6, %1" PK2 "\n\tv_pk_fma_f32 %2, %4, %7, %2" PK2 "\n\tv_pk_fma_f32 %3, %4, %8, %3" PK2 "\n\ts_nop 0"
        : "=&v"(t[0]), "=&v"(t[1]), "=&v"(t[2]), "=&v"(t[3]) : "v"(a), "v"(b0), "v"(b1), "v"(b2), "v"(b3), "v"(c0), "v"(c1), "v"(c2), "v"(c3));
}
DI void gacc2(f32x2& h0, f32x2& h1, f32x2 w0, f32x2 w1, v4u e0, v4u e1) {
    const v2u a0 = {e0.x, e0.y}, b0 = {e0.z, e0.w}, a1 = {e1.x, e1.y}, b1 = {e1.z, e1.w};
    asm("v_pk_fma_f32 %0, %4, %2, %0 op_sel_hi:[0,1,1]\n\tv_pk_fma_f32 %1, %6, %3, %1 op_sel_hi:[0,1,1]\n\t"
        "v_pk_fma_f32 %0, %5, %2, %0 op_sel:[0,1,0] op_sel_hi:[0,0,1] neg_lo:[1,0,0]\n\tv_pk_fma_f32 %1, %7, %3, %1 op_sel:[0,1,0] op_sel_hi:[0,0,1] neg_lo:[1,0,0]"
        : "+v"(h0), "+v"(h1) : "v"(w0), "v"(w1), "v"(a0), "v"(b0), "v"(a1), "v"(b1));
}
DI s16x4 trread(char* smem, int byteoff) {
    return __builtin_amdgcn_ds_read_tr16_b64_v4i16((LAS s16x4*)(smem + byteoff));
}

#define CV_LOAD_A(A, a, rr_, hf) { const unsigned* ap_ = (const unsigned*)(smem + ab[(a) >> 1] + (((a) & 1) ? 0 : 512) + 64 * (7 - (rr_)) + 8 * (hf)); A[a][2 * (hf)] = ap_[0]; A[a][2 * (hf) + 1] = ap_[1]; }
#define CV_LOAD_B(B, e, rr_) { B[e] = *(const v4u*)(smem + bb + 64 * (7 - (rr_)) + 512 * (e)); }
constexpr int cv_pair(int lo, int hi, int n) {
    for (int a = 0; a < 8; ++a)
        for (int e = 0; e < 8; ++e)
            if (a + e >= lo && a + e < hi && n-- == 0) return 8 * a + e;
    return -1;
}
template <int R, int PF, bool OWN, class F>
DI void conv_round(f32x4 (&ca)[8], v4u (&A)[8], const v4u (&B)[8], v4u (&An)[8], v4u (&Bn)[8], const char* smem, const int (&ab)[4], int bb, F&& fill) {
    static_for<(R == 7 ? KG0 : 36)>(LAMBDA(g_) {
        constexpr int g = g_.value, a = cv_pair(0, 8, g) / 8, e = cv_pair(0, 8, g) % 8;
        mf(ca[a + e], A[a], B[e]);
        SCHED();
        if constexpr (OWN && g < 12) { CV_LOAD_A(A, 2 + g / 2, R, g & 1) SCHED(); }
        if constexpr (g < 4 * PF && (g & 3) != 3) { if constexpr ((g & 3) == 2) CV_LOAD_B(Bn, g >> 2, R + 1) else CV_LOAD_A(An, g >> 2, R + 1, g & 1) SCHED(); }
        site();
        if constexpr (R == 7) SCHED();
        if constexpr ((g & 3) == 3 && g < 28) { fill(std::integral_constant<int, 7 * R + g / 4>{}); SCHED(); }
    });
}
template <int R0, int R1, class F, class M>
DI void conv_part(f32x4 (&ca)[8], const char* smem, const int (&ab)[4], int bb, F&& fill, M&& mid, v4u (&A)[2][8], v4u (&B)[2][8]) {
    if constexpr (R0 > 0) { CV_LOAD_A(A[0], 0, R0, 0) CV_LOAD_A(A[0], 0, R0, 1) SCHED(); }
    static_for<8>(LAMBDA(a_) { constexpr int a = a_.value; CV_LOAD_B(B[0], a, R0) SCHED(); });
    if constexpr (R0 > 0) { CV_LOAD_A(A[0], 1, R0, 0) CV_LOAD_A(A[0], 1, R0, 1) SCHED(); }
    else {
        CV_LOAD_A(A[0], 0, 0, 0) CV_LOAD_A(A[0], 0, 0, 1) SCHED();
        const v4u ad = mid();
        SCHED();
        static_for<8>(LAMBDA(e_) {
            constexpr int e = e_.value;
            mf0(ca[e], ad, B[0][e]); SCHED();
            if constexpr (e < 7) { CV_LOAD_A(A[0], e + 1, 0, 0) CV_LOAD_A(A[0], e + 1, 0, 1) SCHED(); }
        });
    }
    static_for<R1 - R0>(LAMBDA(r_) {
        constexpr int r = r_.value;
        conv_round<R0 + r, (R0 + r + 1 < R1 ? 8 : 0), (r == 0 && R0 > 0)>(ca, A[r & 1], B[r & 1], A[(r & 1) ^ 1], B[(r & 1) ^ 1], smem, ab, bb, fill);
    });
}
template <int IMM> DI void gld(v4u& dst, unsigned voff, const void* sbase) {
    asm volatile("global_load_dwordx4 %0, %1, %2 offset:%3" : "=v"(dst) : "v"(voff), "s"(sbase), "i"(IMM));
}
#define OUTP 16
#define NTM 31
template <int IMM, int BIT> DI void gldn(v4u& dst, unsigned voff, const void* sbase) {
    if constexpr ((NTM >> BIT) & 1) asm volatile("global_load_dwordx4 %0, %1, %2 offset:%3 nt" : "=v"(dst) : "v"(voff), "s"(sbase), "i"(IMM));
    else gld<IMM>(dst, voff, sbase);
}
DI void gldu16(unsigned& dst, unsigned voff, const void* sbase) { asm volatile("global_load_ushort %0, %1, %2" : "=v"(dst) : "v"(voff), "s"(sbase)); }
template <int IMM> DI void gld64(v4u& dst, const void* a) {
    if constexpr ((NTM >> 5) & 1) asm volatile("global_load_dwordx4 %0, %1, off offset:%2 nt" : "=v"(dst) : "v"(a), "i"(IMM));
    else asm volatile("global_load_dwordx4 %0, %1, off offset:%2" : "=v"(dst) : "v"(a), "i"(IMM));
}
template <int V> DI void SETM0(unsigned ldsbase) { asm volatile("s_add_u32 m0, %0, %1" ::"s"(ldsbase), "i"(V)); }
template <int GOFF> DI void GLD0(unsigned voff, const char* sbase) { asm volatile("global_load_lds_dwordx4 %0, %1 offset:%2" ::"v"(voff), "s"(sbase), "i"(GOFF) : "memory"); }
template <int GOFF, int V> DI void GLD1(unsigned voff, const char* sbase, unsigned ldsbase) {
    asm volatile("global_load_lds_dwordx4 %0, %1 offset:%3\n\ts_add_u32 m0, %2, %4" ::"v"(voff), "s"(sbase), "s"(ldsbase), "i"(GOFF), "i"(V) : "memory");
}
constexpr int dma_goff(int it) { return it < 40 ? (it >> 2) * 128 : ((it - 100) / 5) * 256; }
constexpr int dma_loff(int it) { return it < 40 ? L_XR + ((it >> 2) % 3) * 4096 + (it & 3) * 1024 : ((it - 100) % 5) * 20480 + ((it - 100) / 5) * 2048; }
#define KFD0 10
#define KNF 13
#define KNW 11
#define KW2S 17
#define KW2M 0x4A
#define KFWP 14
constexpr int kW2S = KW2S, kW2M = KW2M;
constexpr int NW2P = (20 - kW2S) * __builtin_popcount(kW2M);
constexpr int P1_PRE = WD ? 36 : 34;
constexpr unsigned WRUN = WD ? RUN : RUN / 2;
constexpr int p1_pre(int i) { return i < 5 ? 100 + i : i < 13 ? i - 5 : i < 18 ? 105 + (i - 13) : i < 22 ? 8 + (i - 18) : i < 27 ? 110 + (i - 22) : 400; }
constexpr int p1_item(int s, int g) {
    if (s == 7 && g == 0) return 324;
    if (g == 9) return s < 10 ? 115 + s : s == 11 ? 202 : s == 12 ? 203 : -1;
    if (s == KFWP && g == 1) return 208;
    if (s >= kW2S && ((kW2M >> g) & 1)) return 500 + (s - kW2S) * __builtin_popcount(kW2M) + __builtin_popcount(kW2M & ((1 << g) - 1));
    if ((s & 1) == 0 && s <= 12) return g >= 5 ? 4 * (s / 2 + 3) + (g - 5) : -1;
    if (s == 14 && g == 5) return 207;
    return -1;
}
constexpr int p1_cnt(int s, int g) {
    int n = P1_PRE;
    for (int t = 0; t <= s; ++t)
        for (int u = 0; u < 10; ++u) {
            if (t == s && u >= g) break;
            if (p1_item(t, u) >= 0 && p1_item(t, u) != 319) ++n;
        }
    return n;
}
constexpr int p1_pos(int code) {
    for (int i = 0; i < P1_PRE; ++i) if (p1_pre(i) == code) return i;
    for (int t = 0; t < 20; ++t)
        for (int u = 0; u < 10; ++u) if (p1_item(t, u) == code) return p1_cnt(t, u);
    return -1000;
}
constexpr int p1_cap(int s, int g) {
    int u = (s < 19 && g < 5) + (g == 9 && s < 19);
    if ((s & 1) && s < 19 && g >= 5 && g < 9) u += 1 + (g == 5);
    if ((s & 1) == 0 && g == 4) u += 1;
    const int it = p1_item(s, g), nt = g < 9 ? p1_item(s, g + 1) : -1;
    u += (it >= 0 && it < 500 && it != 208) + (nt >= 0 && nt < 200);
    if ((s == 18 && g == 1) || (s == 19 && g == 2) || (g == 9 && ((s & 3) == 2 || s == 17))) u += 2;
    return u < 2 ? 2 - u : 0;
}
constexpr int p1_slot(int s0, int s, int g, int gmax) {
    int n = 0;
    for (int t = s0; t <= s; ++t)
        for (int u = 0; u < gmax && !(t == s && u >= g); ++u) n += p1_cap(t, u);
    return n;
}
constexpr int p1_lim(int n) { return n > 63 ? 63 : n; }
template <int IMM> DI void gld4s(v4u& dst, unsigned voff, const void* sbase) { asm volatile("global_load_dwordx4 %0, %1, %2 offset:%3 sc1" : "=v"(dst) : "v"(voff), "s"(sbase), "i"(IMM)); }
template <int IMM> DI void gld2s(v2u& dst, unsigned voff, const void* sbase) { asm volatile("global_load_dwordx2 %0, %1, %2 offset:%3 sc1" : "=v"(dst) : "v"(voff), "s"(sbase), "i"(IMM)); }
template <class T> DI void pin(T& v) { asm volatile("" : "+v"(v)); }
DI void mul2(float& a, float& b, float x, float y, float s) { asm volatile("v_mul_f32 %0, %2, %4\n\tv_mul_f32 %1, %3, %4" : "=&v"(a), "=&v"(b) : "v"(x), "v"(y), "v"(s)); }
DI void rot1(unsigned& a, unsigned x) { asm volatile("v_alignbit_b32 %0, %1, %1, 16" : "=v"(a) : "v"(x)); }
DI void mix1(unsigned& a, unsigned x, unsigned y) { asm volatile("v_bfi_b32 %0, %3, %1, %2" : "=v"(a) : "v"(x), "v"(y), "s"(0xffffu)); }
DI void ror1(unsigned& o, unsigned prev) { asm volatile("v_mov_b32_dpp %0, %1 wave_ror:1 row_mask:0xf bank_mask:0xf" : "=v"(o) : "v"(prev)); }
DI void shr1b(unsigned& o, unsigned x) { asm volatile("v_mov_b32_dpp %0, %1 wave_shr:1 row_mask:0xf bank_mask:0xf" : "+v"(o) : "v"(x)); }
DI void xor1(float& a, unsigned x, unsigned s) { asm volatile("v_xor_b32 %0, %1, %2" : "=v"(a) : "v"(x), "v"(s)); }
template <int G> DI void bc1(float& a, unsigned x) { asm volatile("v_mov_b32_dpp %0, %1 row_newbcast:%2 row_mask:0xf bank_mask:0xf" : "=v"(a) : "v"(x), "i"(G)); }
template <int G> DI void mulb(float& o, unsigned x, float y) { asm volatile("v_mul_f32_dpp %0, %1, %2 row_newbcast:%3 row_mask:0xf bank_mask:0xf" : "=v"(o) : "v"(x), "v"(y), "i"(G)); }
template <int N> DI void lgkm() { __builtin_amdgcn_s_waitcnt(0xC07F | (N << 8)); asm volatile("" ::: "memory"); }
DI void unp2(float& a, float& b, unsigned x) { asm volatile("v_lshlrev_b32 %0, 16, %2\n\tv_and_b32 %1, 0xffff0000, %2" : "=&v"(a), "=&v"(b) : "v"(x)); }
#define GL(a) "global_load_dwordx4 a[" #a ":" #a "+3], %7, %8 sc1\n\tglobal_load_dwordx4 a[" #a "+4:" #a "+7], %7, %8 offset:256 sc1\n\t"
#define AR(d, a, o) "v_accvgpr_read_b32 " d ", a[" #a "+" #o "]\n\t"
#define GS_(a) "s_waitcnt vmcnt(4)\n\t" AR("%2", a, 1) AR("%3", a, 3) AR("%4", a, 5) AR("%5", a, 7) AR("%0", a, 0) AR("%1", a, 2)                              \
    "v_xor_b32 %2, %9, %2\n\tv_bitop3_b32 %4, %4, %9, %2 bitop3:0xbe\n\tv_bitop3_b32 %4, %3, %9, %4 bitop3:0xbe\n\tv_bitop3_b32 %4, %5, %9, %4 bitop3:0xbe\n\t" \
    AR("%2", a, 4) AR("%3", a, 6) "v_cmp_ne_u32 vcc, 0, %4\n\ts_cbranch_vccz 2f\n\t" GL(a)
template <int A> DI void gran_req(unsigned off, const void* base) {
    asm volatile("global_load_dwordx4 a[%c2:%c2+3], %0, %1 sc1\n\tglobal_load_dwordx4 a[%c2+4:%c2+7], %0, %1 offset:256 sc1" ::"v"(off), "s"(base), "i"(A));
}
template <int D0, int NP> DI void pipe_gran(float& a, float& d, unsigned off, const void* base, unsigned tag, bool& dead) {
    unsigned n = dead ? 0u : kSpinCap, y, z;
    float a1, d1;
    if constexpr (NP < 1) { __builtin_amdgcn_s_sleep(D0); gran_req<32>(off, base); __builtin_amdgcn_s_sleep(PD1); }
    if constexpr (NP < 2) { gran_req<40>(off, base); __builtin_amdgcn_s_sleep(PD1); }
    asm volatile(GL(48)
                 "1:\n\t" GS_(32) GS_(40) GS_(48) "s_sub_u32 %6, %6, 1\n\ts_cbranch_scc0 1b\n2:"
                 : "=&v"(a), "=&v"(d), "=&v"(a1), "=&v"(d1), "=&v"(y), "=&v"(z), "+s"(n) : "v"(off), "s"(base), "s"(tag) : "memory", "vcc", "scc");
    dead = __builtin_amdgcn_readfirstlane(n) == ~0u;
    a += a1; d += d1;
}
DI void fma2(float& a, float& b, float x, float y, float s, float t, float u, float v) {
    asm volatile("v_fma_f32 %0, %2, %4, %6\n\tv_fma_f32 %1, %3, %5, %7" : "=&v"(a), "=&v"(b) : "v"(x), "v"(y), "v"(s), "v"(t), "v"(u), "v"(v));
}
DI void selhi(float& a, float x) { asm volatile("v_mov_b32_dpp %0, %1 quad_perm:[0,1,2,3] row_mask:0xf bank_mask:0xc" : "+v"(a) : "v"(x)); }
DI void cvt2(unsigned& a, unsigned& b, f32x4 v) { asm volatile("v_cvt_pk_bf16_f32 %0, %2, %3\n\tv_cvt_pk_bf16_f32 %1, %4, %5" : "=&v"(a), "=&v"(b) : "v"(v.x), "v"(v.y), "v"(v.z), "v"(v.w)); }
DI void swp2n(unsigned& a, unsigned& b, unsigned& c, unsigned& d) { asm volatile("v_permlane16_swap_b32 %0, %1\n\tv_permlane16_swap_b32 %2, %3" : "+v"(a), "+v"(b), "+v"(c), "+v"(d)); }
template <int G> DI void bc2b(float& a, float& b, float x, float y) {
    asm volatile("v_mov_b32_dpp %0, %2 row_newbcast:%4 row_mask:0xf bank_mask:0xf\n\tv_mov_b32_dpp %1, %3 row_newbcast:%4 row_mask:0xf bank_mask:0xf" : "=&v"(a), "=&v"(b) : "v"(x), "v"(y), "i"(G));
}
template <class T> DI void pins(T& v) { asm volatile("" : "+s"(v)); }
template <int I> DI void gld_w2(unsigned voff, const void* sbase) {
    asm volatile("global_load_dwordx4 " WA(2) ", %0, %1 offset:%3 sc1" ::"v"(voff), "s"(sbase), "i"(W2R + 4 * I), "i"((I % 5) * 1024 - 2048));
}
DI void undef4(v4u (&x)[4]) { asm volatile("" : "=v"(x[0]), "=v"(x[1]), "=v"(x[2]), "=v"(x[3])); }
DI void wait1(v2u& v) { asm volatile("s_waitcnt vmcnt(0)" : "+v"(v)::"memory"); }
DI void ld_ur(unsigned o, const void* base) {
    asm volatile("global_load_dwordx4 a[40:40+3], %0, %1 sc1\n\tglobal_load_dwordx4 a[44:44+3], %0, %1 offset:1024 sc1\n\t"
                 "global_load_dwordx4 a[48:48+3], %0, %1 offset:2048 sc1\n\tglobal_load_dwordx4 a[52:52+3], %0, %1 offset:3072 sc1" ::"v"(o), "s"(base));
}
DI void st_ur(unsigned a) {
    asm volatile("s_waitcnt vmcnt(%1)\n\tds_write_b128 %0, a[40:40+3] offset:480\n\tds_write_b128 %0, a[44:44+3] offset:1504\n\t"
                 "ds_write_b128 %0, a[48:48+3] offset:2528\n\tds_write_b128 %0, a[52:52+3] offset:3552" ::"v"(a), "i"(2 * __builtin_popcount(kW2M)) : "memory");
}
template <int A, int IMM> DI void lda4(unsigned o, const void* base) { asm volatile("global_load_dwordx4 a[%c2:%c2+3], %0, %1 offset:%3" ::"v"(o), "s"(base), "i"(A), "i"(IMM)); }
template <int A, int IMM> DI void lda2(unsigned o, const void* base) { asm volatile("global_load_dwordx2 a[%c2:%c2+1], %0, %1 offset:%3" ::"v"(o), "s"(base), "i"(A), "i"(IMM)); }
template <int A> DI void rda(unsigned& v) { asm volatile("v_accvgpr_read_b32 %0, a[%c1+0]" : "=v"(v) : "i"(A)); }
DI void poll1(unsigned a, const void* base) { asm volatile("global_load_dwordx2 a[36:36+1], %0, %1 sc1" ::"v"(a), "s"(base)); }
DI void poll2(unsigned a, unsigned b, const void* base) { asm volatile("global_load_dwordx2 a[36:36+1], %0, %2 sc1\n\tglobal_load_dwordx2 a[36+2:36+3], %1, %2 sc1" ::"v"(a), "v"(b), "s"(base)); }
DI void touch_(unsigned a, const void* base) { asm volatile("global_load_dword a[32+1], %0, %1" ::"v"(a), "s"(base)); }
template <int N> DI void wait_st(v2u& a, v4u& b) { asm volatile("s_waitcnt vmcnt(%2)" : "+v"(a), "+v"(b) : "i"(N) : "memory"); }
DI void undef3(v4u (&x)[3]) { asm volatile("" : "=v"(x[0]), "=v"(x[1]), "=v"(x[2])); }
template <int IMM> DI void gld4t(v4u& dst, unsigned voff, const void* sbase) { asm volatile("global_load_dwordx4 %0, %1, %2 offset:%3 sc1" : "+v"(dst) : "v"(voff), "s"(sbase), "i"(IMM)); }
template <int N> DI void vm_wait() { asm volatile("s_waitcnt vmcnt(%0)" ::"i"(N) : "memory"); }
DI float dot2a(unsigned a, unsigned b, float c) {
    return __builtin_amdgcn_fdot2_f32_bf16(__builtin_bit_cast(bf16x2v, a), __builtin_bit_cast(bf16x2v, b), c, false);
}
DI void disc(unsigned l0, unsigned l1, unsigned l2, f32x2& lam, f32x2& co) {
    const float dt = rbf(__builtin_amdgcn_exp2f(lo16f(l2) * 1.44269504088896f));
    const float lre = lo16f(l0), lim = lo16f(l1);
    const float e_ = __builtin_amdgcn_exp2f(dt * lre * 1.44269504088896f);
    const float rv = __builtin_amdgcn_fractf(dt * lim * 0.159154943091895f);
    lam = f32x2{e_ * __builtin_amdgcn_cosf(rv), e_ * __builtin_amdgcn_sinf(rv)};
    const float nr = lam.x - 1.0f, ni = lam.y, id = __builtin_amdgcn_rcpf(lre * lre + lim * lim);
    co = f32x2{(nr * lre + ni * lim) * id, (ni * lre - nr * lim) * id};
}
DI f32x4 unpk4(v2u u) { return f32x4{lo16f(u.x), hi16f(u.x), lo16f(u.y), hi16f(u.y)}; }
#define D2 "v_dot2_f32_bf16 "
#define CP "v_cvt_pk_bf16_f32 "
DI void d2x4(unsigned* o, unsigned a0, unsigned a1, unsigned a2, unsigned a3, unsigned b0, unsigned b1, unsigned b2, unsigned b3) {
    asm volatile(D2 "%0, %4, %8, 0\n\t" D2 "%1, %5, %9, 0\n\t" D2 "%2, %6, %10, 0\n\t" D2 "%3, %7, %11, 0"
                 : "=&v"(o[0]), "=&v"(o[1]), "=&v"(o[2]), "=&v"(o[3]) : "v"(a0), "v"(a1), "v"(a2), "v"(a3), "v"(b0), "v"(b1), "v"(b2), "v"(b3));
}

__global__ __launch_bounds__(256, 1) __attribute__((aligned(4096))) void k_main(Params p) {
    __shared__ __attribute__((aligned(16))) char smem[LDS_BYTES];
    p.ws += (0u - (unsigned)(size_t)p.ws) & 0x1FFFFFu;
    const rsrc_t rs = __builtin_amdgcn_make_buffer_rsrc((void*)p.ws, 0, WS_BYTES, 0x00020000);
    const int w0 = __builtin_amdgcn_readfirstlane(threadIdx.x >> 6), b0 = blockIdx.x;
    unsigned pol, sdo;
    const char* wl;
    rsrc_t rl;
    const rsrc_t ro = __builtin_amdgcn_make_buffer_rsrc((void*)p.out, 0, S_ * D_ * 4, 0x00020000);
    bool dead = false;
    asm volatile("" ::: "a255");
    float SW[5];
    f32x4 xin[5];
    v2u gt[5], dsk[5];
    f32x4 acc[2][5];
    f32x4 rr[2];
    float tt[2][5][4];
    float lr[5], ls[5], lqr[5], lqs[5];
    f32x2 l8, l32, l128;
    bool kearly;
    unsigned sdq;
    int sgn_, ab[4], bb, zpa, uwa, spa, tbw, tbr, cwa, spl, dst[3];
    unsigned sgo, sgs, glo, gso, fgo, fgo1;
    int srca;
    unsigned adm, dso, w2o, wro, fwo, xso, fho1, fho, hso, eo, es, ho[4];
    bool cv0;
    int mt1_;
    auto addrs = [&](auto layer_, int lane, int w, int b) __attribute__((always_inline)) {
        constexpr int layer = layer_.value;
        const int c = lane & 15, q = lane >> 4;
        TILES()
        static_assert(l_sg(0, 0) == SC + 0xA900 && l_sg(0, 1) == SC + 0xB400 && l_sg(0, 2) == SC + 0xCE00 && l_sg(0, 3) == SC + 0xDC00, "");
        sgn_ = (layer ? l_sg(1, 0) + w * INBOX : SC + (int)((0xDC00CE00B400A900ull >> (16 * w)) & 0xffff)) + q * 320 + c * 4;
        const int pl = lane < 40 ? lane : 39, tbb = l_tb(layer, 0) + w * (layer ? INBOX : 5120);
        spl = SC + pl * 8;
        eo = OFF_EG + (unsigned)((layer * 2 + (nt1 >> 3)) * 16 * RUN + (nt1 & 7) * 640 + pl * 16);
        es = eo + (unsigned)mt1 * RUN;
        spa = l_rs(layer, 0) + (2 * (mt2 & 1) + (w >> 1)) * (layer ? INBOX : 256) + (2 * (w & 1) + (c >> 3)) * 64 + (c & 7) * 4;
        tbw = tbb + c * 64 + ((q ^ ((c >> 1) & 3)) * 16);
        tbr = tbb + (2 * (c >> 2) + (q & 1)) * 64 + (((2 * (q >> 1) + ((c >> 1) & 1)) ^ (c >> 2)) * 16) + (c & 1) * 8;
        hso = OFF_HF + (unsigned)((layer * 4 + ((mt1 >> 1) & 3)) * PG + ((8 * (mt1 & 1) + 2 * w + (q >> 1)) * 8 + (nt1 >> 1)) * RUN + (10 * (nt1 & 1) + 5 * (q & 1)) * 256 + c * 16);
        cwa = L_CW + c * 4;
        fho1 = lane ? kNone : OFF_FH + (unsigned)((2 * (mt1 & 7) + (w >> 1)) * RUN + (layer * 32 + nt1 * 2 + (w & 1)) * 128);
        fho = OFF_FH + (unsigned)((mt2 & 15) * RUN + (layer * 4 + w) * 1024 + (lane >> 3) * 128);
        pin(sgn_); pin(spl); pin(eo); pin(es); pin(spa); pin(tbw); pin(tbr); pin(hso); pin(cwa); pin(fho1); pin(fho);
        const unsigned hb_ = OFF_HF + (unsigned)((layer * 4 + ((mt1 >> 1) & 3)) * PG + (32 * (mt2 & 3) + 2 * w) * RUN);
#pragma unroll
        for (int m = 0; m < 4; ++m) { ho[m] = hb_ + (unsigned)(((m + 1 + w) & 3) * 8 * RUN + lane * 16); pin(ho[m]); }
#pragma unroll
        for (int m = 0; m < 3; ++m) { dst[m] = SC + ((w + 1 + m) & 3) * INBOX + m * 5120 + lane * 16; pin(dst[m]); }
        mt1_ = mt1;
        sgo = OFF_SG + (unsigned)((layer * 16 + (mt2 & 15)) * RUN + w * 2048 + c * 16);
        sgs = q < 2 ? sgo + (unsigned)(nt2 * 256 + q * 8) : kNone;
        sgo += (unsigned)(q * 512);
        pin(sgo); pin(sgs);
        if constexpr (layer == 0) {
            srca = SC + w * INBOX + lane * 16;
            const unsigned gb_ = OFF_GT + (unsigned)((nt2 >> 2) * PG + (nt2 & 3) * 32 * RUN);
            const int cl = i32 + 32 * w;
            glo = gb_ + (unsigned)((lane >> 3) * RUN + ((lane >> 1) & 3) * 2048 + (64 * (mt2 & 15) + 16 * w) * 2 + (lane & 1) * 16);
            gso = cv0 ? gb_ + (unsigned)((cl >> 2) * RUN + (cl & 3) * 2048 + (q & 1) * 512 + c * 32 + (q >> 1) * 16) : kNone;
            fgo = gb_ + (unsigned)(20 * RUN + (lane >> 1) * 128);
            fgo1 = w == 0 ? gb_ + (unsigned)(20 * RUN + i32 * 128 + (lane >> 5) * 8192) : kNone;
            pin(srca); pin(glo); pin(gso); pin(fgo); pin(fgo1);
            const int r0 = SC + (w < 3 ? w : 0) * REGSZ, y0 = 2031 + 8 * q - c, l34 = lane < 33 ? lane : 33;
#pragma unroll
            for (int i = 0; i < 4; ++i) { ab[i] = r0 + ((y0 & 1) ? CV_O : CV_E) + ((y0 + 1) >> 1) * 4 - 448 - 512 - 1024 * i; pin(ab[i]); }
            bb = r0 + 32 * c + 16 * q;
            zpa = w < 3 ? r0 + l34 * 16 + (l34 >= 30 ? CV_E + 4096 - 480 : 0) + (l34 >= 32 ? CV_O + 4100 - 32 - CV_E - 4096 : 0) : SC + 47360 + lane * 16;
            uwa = r0 + lane * 16;
            xso = (unsigned)((64 * mt2 + 16 * w + c) * D_ + 80 * nt2 + 16 * (q & 1) + 8 * (q >> 1)) * 2u;
            dso = (unsigned)(160 * nt2 + 8 * q);
            adm = (unsigned)((c & 1) * 16 + (q == 2 + (c >> 3) ? 256 << ((c & 7) >> 1) : 0));
            pin(adm);
            pin(bb); pin(zpa); pin(uwa); pin(xso); pin(dso);
        }
        SCHED();
    };
    unsigned tag0, tag1;
#define FLAG(off) { if (lane == 0) st64(v2u{tag0, tag1}, rs, off); }
#define ISTAG(f) (((f.x ^ tag0) | (f.y ^ tag1)) == 0u)
#define KW_REQ() { if (cv) ld_ur(wro, p.ws + OFF_KVT); static_for<NW2P>(LAMBDA(i_) { gld_w2<i_.value>(w2o, (WD ? wl : p.ws) + 2048 + (i_.value / 5) * WRUN); }); }
#define FW_OK() { if (__builtin_expect(!kearly, 0)) { WAVE_WAIT({ const v2u fw_ = ld64(rs, fwo); ok = ISTAG(fw_); }) KW_REQ() } }
#define TT_GAP(n) { constexpr int mb_ = (n) / 10, nb_ = ((n) % 10) / 2, e_ = ((n) & 1) * 2; \
        mul2(tt[mb_][nb_][e_], tt[mb_][nb_][e_ + 1], rm_[4 * mb_ + e_], rm_[4 * mb_ + e_ + 1], SW[nb_]); }
    {
        const int tid = threadIdx.x, lane = tid & 63, w = w0, b = b0;
        const int c = lane & 15, q = lane >> 4;
        TILES()
        STAMP(0)
        const unsigned lds0 = (unsigned)(size_t)(LAS char*)smem;
        v2u nonce;
        {
            u64 pc;
            int len, len2;
            asm volatile("s_getpc_b64 %0\nkpf_begin:\n\ts_mov_b32 %1, kpf_end-kpf_begin\n\ts_mov_b32 %2, kpf_end2-kpf_begin" : "=s"(pc), "=s"(len), "=s"(len2));
            len = len > len2 ? len : len2;
            const int o0 = (((j & 15) * 4 + w) + 64 * (lane >> 3)) * 128;
            touch_((unsigned)(o0 < len ? o0 : 0), (const void*)pc);
        }
        const bool cv = w + (i32 >> 4) < 3;
        const int chn = 80 * nt2 + i32 + 32 * (cv ? w : 0);
        const bool kw = w - (i32 >> 4) < 2;
        const int kp = 16 * nt2 + mt1, ku = 5 * kp + (i32 < 16 ? w & 1 : 2 + (w < 3 ? w : 0)), kc = ku >> 6, kr = ku & 63;
        const int lc = cv ? lane : 8;
        int kwi = 2 + (i32 >> 4) - w;
        v4u ax[15], sa[5], cr[2], wr[4];
        unsigned uL[3];
        const int cu_ = (b < 200 ? b : 0) * 4 + w, cdb = cu_ / 20, ccg = cu_ % 20;
        {
            int ca_ = 32 * ccg, cb_ = 40 * nt1 + 10 * w - 32;
            asm volatile("" : "+s"(ca_), "+s"(cb_), "+s"(kwi));
            unsigned cpo = (unsigned)((lane < 32 ? ca_ : cb_) + (lane < 41 ? lane : 41)) * 2u;
            unsigned ksh = (unsigned)(64 * kc + 8 * (lane & 7)) * 2u, ko = (unsigned)(32 * kr + 4 * (lane >> 3)) * 1280u + ksh, kh = ko - 3840u;
            ksh *= 4u;
            pin(cpo); pin(ksh); pin(ko); pin(kh);
            SCHED();
            gldu16(uL[0], cpo, p.Lre); gldu16(uL[1], cpo, p.Lim); gldu16(uL[2], cpo, p.logdt);
            const v4u rk = {(unsigned)(u64)p.k, (unsigned)((u64)p.k >> 32) & 0xffffu, S_ * D_ * 2u, 0x00020000u};
#define L4(d, v, s_, o, f) "global_load_dwordx4 %" #d ", %" #v ", %" #s_ " offset:" #o f "\n\t"
#define B4(d, o) "buffer_load_dwordx4 %" #d ", %17, %21, 0 offen offset:" #o " nt\n\t"
            asm volatile("s_cmp_lt_i32 %22, 1\n\ts_cbranch_scc1 Lrole2\n\t"
                         L4(3, 15, 18, 0, " nt") L4(4, 15, 18, 1280, " nt") L4(5, 15, 18, 2560, " nt") L4(6, 15, 18, 3840, " nt")
                         L4(7, 15, 20, 0, " nt") L4(8, 15, 20, 1280, " nt") L4(9, 15, 20, 2560, " nt") L4(10, 15, 20, 3840, " nt")
                         B4(0, 0) B4(1, 1280) B4(2, 2560) L4(11, 16, 19, 0, "") L4(12, 16, 19, 16, "") L4(13, 16, 19, 32, "") L4(14, 16, 19, 48, "")
                         "\nLrole2:"
                         : "=&v"(ax[0]), "=&v"(ax[1]), "=&v"(ax[2]), "=&v"(ax[3]), "=&v"(ax[4]), "=&v"(ax[5]), "=&v"(ax[6]), "=&v"(ax[7]), "=&v"(ax[8]), "=&v"(ax[9]),
                           "=&v"(ax[10]), "=&v"(ax[11]), "=&v"(ax[12]), "=&v"(ax[13]), "=&v"(ax[14])
                         : "v"(ko), "v"(ksh), "v"(kh), "s"(p.k), "s"(p.shk), "s"(p.v), "s"(rk), "s"(kwi) : "scc");
            SCHED();
        }
        const char* ssrc;
        {
            u64 pa = (u64)(p.x0 + (128 * mt1 + 8 * nt1 + 2 * w) * D_), pb = (u64)(((mt1 & 1) ? p.Bim : p.Bre) + (40 * nt1 + (mt1 >> 1)) * D_);
            asm volatile("" : "+s"(pa), "+s"(pb));
            const int nbx = 2 * w + q - 2, r = q < 2 ? q : 8 * (nbx < 5 ? nbx : 4);
            ssrc = (const char*)(q < 2 ? pa : pb) + (unsigned)(r * 1280 + c * 16);
            pin(ssrc);
        }
        SCHED();
        STAMP(22)
        const int row8 = lane >> 3;
        const unsigned wvo = (unsigned)(40 * nt1 + row8) * 1280u + (unsigned)(((lane & 7) ^ ((w & 1) | ((row8 >> 1) << 1))) * 16 + (w >> 1) * 128);
        const char* const wbase = (const char*)((w & 1) ? p.Bim : p.Bre);
        const unsigned w1l = lds0 + (unsigned)(((w & 1) * 10 + (w >> 1)) * 1024);
        const unsigned xvo0 = (unsigned)(8 * (row8 >> 1) + 2 * (row8 & 1)) * 1280u + (unsigned)(((lane & 7) ^ ((row8 >> 1) << 1)) * 16);
        const unsigned xvo1 = (xvo0 ^ 16u) + 1280u;
        const char* const xbase = (const char*)p.x0 + (size_t)(128 * mt1 + 32 * w) * 1280;
        const unsigned xl = lds0 + (unsigned)(w * REGSZ);
        unsigned xv[4] = {xvo0, xvo1, xvo0 + 5120u, xvo1 + 5120u}, wv[5];
#pragma unroll
        for (int i = 0; i < 5; ++i) { wv[i] = wvo + i * 10240u; pin(wv[i]); if (i < 4) pin(xv[i]); }
#define DMA_M0(it) SETM0<dma_loff(it) - dma_goff(it)>((it) < 40 ? xl : w1l)
#define DMA_LD1(it, nx_) GLD1<dma_goff(it), dma_loff(nx_) - dma_goff(nx_)>((it) < 40 ? xv[(it) & 3] : wv[(it) < 40 ? 0 : ((it) - 100) % 5], (it) < 40 ? xbase : wbase, (nx_) < 40 ? xl : w1l)
#define DMA_LD(it) GLD0<dma_goff(it)>((it) < 40 ? xv[(it) & 3] : wv[(it) < 40 ? 0 : ((it) - 100) % 5], (it) < 40 ? xbase : wbase)
        const int swz = (c & 1) | ((c >> 2) << 1);
        int w1lane0 = (c & 1) * 10240 + (c >> 1) * 128 + ((q ^ swz) * 16);
        int w1lane1 = (c & 1) * 10240 + (c >> 1) * 128 + (((4 + q) ^ swz) * 16);
        int xlane0 = L_XR + w * REGSZ + (c & 1) * 1024 + (c >> 1) * 128 + ((q ^ swz) * 16);
        int xlane1 = L_XR + w * REGSZ + (c & 1) * 1024 + (c >> 1) * 128 + (((4 + q) ^ swz) * 16);
        unsigned cvo = (unsigned)((16 * cdb + c) * D_ + 32 * ccg + 8 * q) * 2u, for0, for1;
        {
            const int G = 4 * ccg + q, ch = 10 * (G / 5) + G % 5, ci = ch + 5;
            const unsigned fo = OFF_W2F + (unsigned)((cdb / 10) * PG + ((cdb / 5) & 1) * 64 * RUN + (cdb % 5) * 1024 + c * 16);
            for0 = fo + (unsigned)((ch >> 2) * WRUN + (ch & 3) * 256); for1 = fo + (unsigned)((ci >> 2) * WRUN + (ci & 3) * 256);
        }
        unsigned sto = OFF_ST + (unsigned)((mt1 * 16 + 4 * w + q) * 256 + c * 8), swo = (lane & 3) == 3 ? OFF_CV + (unsigned)((lane >> 2) * 128) : OFF_ST + (unsigned)(((lane >> 2) * 16 + nt1) * 256 + 128 + (lane & 3) * 16);
        unsigned kvo = OFF_KVT + (unsigned)((64 * kc + 8 * (lane >> 3) + ((lane >> 2) & 1)) * 4096 + (32 * kr + 8 * (lane & 3)) * 2);
        int kbw = L_KB + w * 1024 + (lane & 7) * 128 + (((lane >> 3) * 8) ^ ((lane & 7) * 16));
        int kbr = L_KB + w * 1024 + (lane >> 3) * 128 + (((lane & 7) * 16) ^ ((lane >> 3) * 16));
        const unsigned dum = kNone;
        if (!kw) kvo = dum;
        if (b >= 200) { for0 = dum; for1 = dum; cvo = (unsigned)((b * 4 + w) * 128); }
        wro = (unsigned)(chn * 4096 + lc * 16);
        cv0 = cv;
        pin(kvo); pin(kbw); pin(kbr);
        int w1hi0 = w1lane0 + 61440, w1hi1 = w1lane1 + 61440;
        pin(w1hi0); pin(w1hi1);
        pin(w1lane0); pin(w1lane1); pin(xlane0); pin(xlane1); pin(cvo); pin(for0); pin(for1); pin(sto); pin(swo); pin(wro);
        SCHED();
        auto pre = LAMBDA(i_) {
            constexpr int it = p1_pre(i_.value);
            SCHED();
            DMA_M0(it);
            asm volatile("s_nop 0");
            DMA_LD(it);
            SCHED();
        };
        {
            int f0 = 2 * ((chn & ~63) / 5), f1 = 2 * ((chn | 63) / 5) + 1, nk = cv ? 28 : 0;
            asm volatile("" : "+s"(f0), "+s"(f1), "+s"(nk));
            const int l10 = lane - 32 < 0 || lane - 32 > 9 ? 0 : lane - 32;
            fwo = lane < nk ? OFF_FK + (unsigned)(f0 + lane < f1 ? f0 + lane : f1) * 128u : OFF_FW + (unsigned)(25 * nt2 + w + 5 * (l10 >> 1) + (l10 & 1)) * 128u;
        }
        w2o = OFF_W2F + (unsigned)((nt2 >> 1) * PG + (nt2 & 1) * 64 * RUN + 10 * w * WRUN + lane * 16);
        int ime = SC + (w < 3 ? w * REGSZ + CV_E : 43264) + 1008 - 16 * lane, imo = SC + (w < 3 ? w * REGSZ + CV_O + 4 : 47360) + 1008 - 16 * lane, im3 = w < 3 ? ime + 3072 : imo;
        int tca = L_TAB + (c >> 1) * 136, tcb = tca + 8 + 32 * w + 8 * q, tcl = L_TAB + (lane < 40 ? lane : 39) * 136, coa = L_CO + w * REGSZ + q * 64, rsa = l_rs(0, 0) + w * 256 + lane * 4;
        unsigned sgn = (c & 1) ? 0u : 0x80000000u;
        unsigned fko1 = w ? kNone : lane < 32 ? OFF_FK + (unsigned)((2 * kp + (i32 >> 4)) * 128) : OFF_FW + (unsigned)(b * 128);
        pin(fwo); pin(w2o); pin(fko1); pin(ime); pin(imo); pin(im3); pin(tca); pin(tcb); pin(tcl); pin(coa); pin(rsa); pin(sgn);
        SCHED();
        auto cw_ = [&]() __attribute__((always_inline)) {
            gldn<0, 2>(cr[0], cvo, p.Cre); gldn<0, 2>(cr[1], cvo, p.Cim);
            static_for<4>(LAMBDA(i) { gldn<i.value * 1024, 1>(wr[i.value], wro, p.ssmk); });
        };
        STAMP(33)
        gld2s<0>(nonce, 0u, p.ws + OFF_NONCE);
        asm volatile("s_waitcnt vmcnt(16)\n\ts_cmp_gt_i32 %3, 0\n\ts_cbranch_scc1 Lrole3\n\ts_waitcnt vmcnt(1)\nLrole3:" : "+v"(uL[0]), "+v"(uL[1]), "+v"(uL[2]) : "s"(kwi) : "memory", "scc");
        {
            f32x2 lam, co, lp[16];
            disc(uL[0], uL[1], uL[2], lam, co);
            if (lane < 32) *(f32x2*)(smem + L_CO + w * REGSZ + lane * 8) = 2.0f * co;
            lp[0] = cmul(lam, lam); lp[0] = cmul(lp[0], lp[0]); lp[1] = cmul(lp[0], lp[0]);
            lp[0] = f32x2{1.f, 0.f};
            lp[2] = cmul(lp[1], lp[1]); lp[3] = cmul(lp[2], lp[1]); lp[4] = cmul(lp[2], lp[2]);
            const f32x2 z2 = {0.f, 0.f};
            f32x2 t4[4];
            cfma4(t4, lp[4], lp[1], lp[2], lp[3], lp[4], z2, z2, z2, z2);
            lp[5] = t4[0]; lp[6] = t4[1]; lp[7] = t4[2]; lp[8] = t4[3];
            cfma4(t4, lp[8], lp[1], lp[2], lp[3], lp[4], z2, z2, z2, z2);
            lp[9] = t4[0]; lp[10] = t4[1]; lp[11] = t4[2]; lp[12] = t4[3];
            cfma4(t4, lp[8], lp[5], lp[6], lp[7], lp[7], z2, z2, z2, z2);
            lp[13] = t4[0]; lp[14] = t4[1]; lp[15] = t4[2];
            if (lane >= 32 && lane < 42) {
                f32x2* tl = (f32x2*)(smem + L_TAB + (10 * w + lane - 32) * 136);
                tl[0] = lam;
#pragma unroll
                for (int i = 0; i < 16; ++i) tl[1 + i] = lp[i];
            }
        }
        SCHED();
        STAMP(35)
        static_for<5>(LAMBDA(i) { gld64<256 * i.value>(sa[i.value], ssrc); });
        cw_();
        addrs(std::integral_constant<int, 0>{}, lane, w, b);
        v4u kvt[4];
        unsigned one2 = 0x3F803F80u, kq[12];
        float kf_[8];
        pin(one2);
        auto kvf = LAMBDA(n_) {
            constexpr int n = n_.value, h = n / 5, u = n % 5;
            if constexpr (u == 0) {
#pragma unroll
                for (int r = 0; r < 6; ++r) { kq[r] = __builtin_amdgcn_perm(ax[r][h], ax[r + 1][h], 0x05040100u); kq[6 + r] = __builtin_amdgcn_perm(ax[r][h], ax[r + 1][h], 0x07060302u); }
                if constexpr (h > 0) kvt[h - 1] = *(const v4u*)(smem + kbr);
            } else if constexpr (u == 1) {
                unsigned t[8];
                d2x4(t, kq[0], kq[1], kq[2], kq[3], ax[11 + h][1], ax[11 + h][1], ax[11 + h][1], ax[11 + h][1]);
                d2x4(t + 4, kq[6], kq[7], kq[8], kq[9], ax[11 + h][3], ax[11 + h][3], ax[11 + h][3], ax[11 + h][3]);
#pragma unroll
                for (int r = 0; r < 8; ++r) kf_[r] = __uint_as_float(t[r]);
            } else if constexpr (u == 2) {
#pragma unroll
                for (int r = 0; r < 8; ++r) kf_[r] = dot2a(kq[r + 2 * (r >> 2) + 2], ax[11 + h][2 * (r >> 2)], kf_[r]);
            } else if constexpr (u == 3) {
#pragma unroll
                for (int r = 0; r < 4; ++r) { kq[r] = ax[7 + r][h] << 16; kq[4 + r] = ax[7 + r][h] & 0xffff0000u; pin(kq[r]); pin(kq[4 + r]); }
            } else {
#pragma unroll
                for (int r = 0; r < 8; ++r) kf_[r] *= __uint_as_float(kq[r]);
                *(v2u*)(smem + kbw) = v2u{cvtpk(kf_[0], kf_[1]), cvtpk(kf_[2], kf_[3])};
                *(v2u*)(smem + (kbw ^ 64)) = v2u{cvtpk(kf_[4], kf_[5]), cvtpk(kf_[6], kf_[7])};
                if constexpr (h == 3) kvt[3] = *(const v4u*)(smem + kbr);
            }
            SCHED();
        };
        asm volatile("s_waitcnt vmcnt(%15)" : "+v"(ax[0]), "+v"(ax[1]), "+v"(ax[2]), "+v"(ax[3]), "+v"(ax[4]), "+v"(ax[5]), "+v"(ax[6]), "+v"(ax[7]), "+v"(ax[8]), "+v"(ax[9]),
                     "+v"(ax[10]), "+v"(ax[11]), "+v"(ax[12]), "+v"(ax[13]), "+v"(ax[14]) : "i"(11) : "memory");
        pin(nonce);
        STAMP(30)
        unsigned tch = 0u;
        constexpr int NF = KNF;
        static_assert(NF < 27, "");
        if (kw) {
            {
                static_for<20>(LAMBDA(n_) {
                    constexpr int n = n_.value, DN = 20 - KFD0;
                    site(); SCHED();
                    kvf(n_);
                    if constexpr (n >= KFD0) static_for<(n - KFD0 + 1) * NF / (DN ? DN : 1) - (n - KFD0) * NF / (DN ? DN : 1)>(LAMBDA(i_) { pre(std::integral_constant<int, (n - KFD0) * NF / (DN ? DN : 1) + i_.value>{}); });
                });
                if constexpr (KFD0 == 20) static_for<NF>(pre);
            }
        } else {
            if (w == 3) {
            const int l = lane, l3 = l & 3;
            unsigned a = OFF_SG + (unsigned)((((l >> 3) & 1) * 16 + (mt2 & 15)) * RUN + ((l >> 1) & 3) * 2048 + nt2 * 256 + (l & 1) * 128);
            unsigned a1 = OFF_PS + (unsigned)((mt1 & 7) * RUN + (((mt2 & 1) * 4 + l3) * 8 + nt2) * 128);
            unsigned a2 = OFF_FX + (unsigned)((mt1 & 7) * RUN + ((2 * (mt2 & 1) + (l3 >> 1)) * 16 + nt2 * 2 + (l3 & 1)) * 128);
            unsigned a3 = OFF_FH + (unsigned)((2 * (mt1 & 7) + (l3 >> 1)) * RUN + (((l >> 2) & 1) * 32 + nt1 * 2 + (l3 & 1)) * 128);
            unsigned a4 = OFF_EG + (unsigned)(((((l >> 3) & 1) * 2 + (nt1 >> 3)) * 16 + mt1) * RUN + (nt1 & 7) * 640 + (l & 7) * 128 + (l & 16) * 512);
            unsigned a5 = OFF_GT + (unsigned)((nt2 >> 2) * PG + ((nt2 & 3) * 32 + 20) * RUN + i32 * 128 + (l & 16) * 512);
            unsigned a6 = OFF_HF - MID + (unsigned)((((l >> 3) & 1) * 4 + ((mt1 >> 1) & 3)) * PG + j * 4 * RUN);
            unsigned a7 = OFF_X2F - MID + (unsigned)(((mt1 >> 2) & 1) * PG + ((xq & 1) * 64 + j * 2) * RUN);
            pin(a); pin(a1); pin(a2); pin(a3); pin(a4); pin(a5); pin(a6); pin(a7);
            a4 = (l & 7) == 5 ? a5 : a4; a4 = (l & 7) == 6 ? a6 : a4; a4 = (l & 7) == 7 ? a7 : a4;
            a = l >= 16 ? a1 : a; a = l >= 20 ? a2 : a; a = l >= 24 ? a3 : a; a = l >= 32 ? a4 : a;
            tch = a;
        }
            vm_wait<KNW>();
            static_for<NF>(pre);
            if (w == 2 && b < 16) {
                unsigned v0, v1, a0, a1, a2, a3, t0, t1;
                asm volatile("s_waitcnt lgkmcnt(0)\n\ts_load_dword %2, %8, 0x0 glc\n\ts_load_dword %3, %8, 0x2000 glc\n\ts_load_dword %4, %8, 0x1000 glc\n\ts_load_dword %5, %8, 0x3000 glc\n\ts_waitcnt lgkmcnt(0)\n\t"
                             "s_not_b32 %6, %2\n\ts_not_b32 %7, %3\n\ts_load_dword %6, %8, 0x0 glc\n\ts_load_dword %7, %8, 0x2000 glc\n\ts_waitcnt lgkmcnt(1)\n\t"
                             "s_cmp_eq_u32 %7, %3\n\ts_cselect_b32 %0, 1, 0\n\ts_waitcnt lgkmcnt(0)\n\t"
                             "s_not_b32 %6, %4\n\ts_not_b32 %7, %5\n\ts_load_dword %7, %8, 0x3000 glc\n\ts_load_dword %6, %8, 0x1000 glc\n\ts_waitcnt lgkmcnt(1)\n\t"
                             "s_cmp_eq_u32 %6, %4\n\ts_cselect_b32 %1, 0, 1\n\ts_waitcnt lgkmcnt(0)"
                             : "=&s"(v0), "=&s"(v1), "=&s"(a0), "=&s"(a1), "=&s"(a2), "=&s"(a3), "=&s"(t0), "=&s"(t1) : "s"(p.ws + (OFF_W2F - MID + (unsigned)(40 + b) * RUN)) : "memory", "scc");
                const unsigned sd = (unsigned)(b >> 2) & 1u;
                st128(v4u{v0 ^ sd, nonce.x + 1u, v1 ^ sd, nonce.x + 1u}, rs, lane ? kNone : OFF_CV + (unsigned)(b * 128));
            }
            skip<SKP>();
            undef4(kvt);
        }
        tag0 = __builtin_amdgcn_readfirstlane(nonce.x) + 1u; tag1 = __builtin_amdgcn_readfirstlane(nonce.y) + (tag0 == 0u ? 1u : 0u);
        SCHED();
        STAMP(34)
        v4u orr, oi;
        v2u gr;
        unsigned gro;
        {
            asm volatile("s_waitcnt vmcnt(%7)" : "+v"(cr[0]), "+v"(cr[1]), "+v"(sa[0]), "+v"(sa[1]), "+v"(sa[2]), "+v"(sa[3]), "+v"(sa[4]) : "i"(4 + NF) : "memory");
#pragma unroll
            for (int e = 0; e < 4; ++e) {
                const f32x4 co = *(const f32x4*)(smem + coa + e * 16);
                const f32x2 o0 = lo16f(cr[0][e]) * f32x2{co.x, -co.y} - lo16f(cr[1][e]) * f32x2{co.y, co.x};
                const f32x2 o1 = hi16f(cr[0][e]) * f32x2{co.z, -co.w} - hi16f(cr[1][e]) * f32x2{co.w, co.z};
                orr[e] = cvtpk(o0.x, o1.x); oi[e] = cvtpk(o0.y, o1.y);
            }
            pin(orr); pin(oi);
            SCHED();
        }
        static_for<(27 - NF) / 3>(LAMBDA(i_) { pre(std::integral_constant<int, NF + i_.value>{}); });
        {
            float s1[2] = {0.f, 0.f}, s2[2] = {0.f, 0.f};
#pragma unroll
            for (int i = 0; i < 5; ++i)
#pragma unroll
                for (int e = 0; e < 4; ++e) { s1[e & 1] = dot2a(sa[i][e], one2, s1[e & 1]); s2[e & 1] = dot2a(sa[i][e], sa[i][e], s2[e & 1]); }
            const float a1 = rowsum16(s1[0] + s1[1]), a2 = rowsum16(s2[0] + s2[1]);
            const float mean = a1 * (1.0f / D_);
            const float var = fmaxf(a2 * (1.0f / D_) - mean * mean, 0.f);
            const float ri = __builtin_amdgcn_rcpf(rbf(__builtin_amdgcn_sqrtf(rbf(rbf(var) + 1e-5f))));
            gr = v2u{__float_as_uint(q >= 2 ? a1 : c ? -ri * rbf(mean) : ri), tag0};
            gro = c < 2 ? OFF_ST + (unsigned)((mt1 * 16 + nt1) * 256 + ((q < 2 ? 8 * c : 14) + 2 * w + q) * 8) : kNone;
            pin(gr); pin(gro);
        }
        SCHED();
        STAMP(59)
        {
            static_for<27 - NF - (27 - NF) / 3>(LAMBDA(i_) { pre(std::integral_constant<int, NF + (27 - NF) / 3 + i_.value>{}); });
#pragma unroll
            for (int i = 0; i < 4; ++i) st128(kvt[i], rs, kvo + i * 8192);
            st128(orr, rs, for0); st128(oi, rs, for1);
            if (WD) __builtin_amdgcn_raw_buffer_store_b128(orr, rs, for0, 8192, SC1);
            if (WD) __builtin_amdgcn_raw_buffer_store_b128(oi, rs, for1, 8192, SC1);
        }
        st64(gr, rs, gro);
        SCHED();
        STAMP(23)
        asm volatile("s_waitcnt vmcnt(%4) lgkmcnt(0)" : "+v"(wr[0]), "+v"(wr[1]), "+v"(wr[2]), "+v"(wr[3]) : "i"(P1_PRE - 9) : "memory");
        __builtin_amdgcn_s_barrier();
        asm volatile("" ::: "memory");
        STAMP(24)
        v4u wf[2][5], af[2][2][2];
#pragma unroll
        for (int nb = 0; nb < 5; ++nb) wf[0][nb] = *(const v4u*)(smem + w1lane0 + nb * 20480 + (0) * 2048);
#pragma unroll
        for (int i = 0; i < 4; ++i) af[0][i >> 1][i & 1] = *(const v4u*)(smem + ((i >> 1) ? xlane1 : xlane0) + (i & 1) * 2048);
        SCHED();
        const char *kvtb = p.ws + OFF_KVT, *w2b0;
        u64 vb0, vb1;
        pins(kvtb);
        v2u sg, tc[10], fw, tg2 = {tag0, tag1};
        pin(tg2);
        v4u sw0;
        unsigned ie[4][4], io[4][4], iw, zq[4];
        float rr_[8];
        auto kstep = LAMBDA(s_) {
            constexpr int s = s_.value, ph = s >> 2, s4 = s & 3, cu = s & 1, nx = cu ^ 1, jj = s >> 1, pp = jj & 1;
            if constexpr (s4 == 3 && ph < 4) {
                constexpr int pw = p1_pos(104 + 5 * (ph + 1)), px = p1_pos(4 * (jj + 1) + 3), pm = pw > px ? pw : px;
                constexpr int vm = p1_lim(p1_cnt(s, 0) - 1 - pm);
                asm volatile("" ::: "memory");
                __builtin_amdgcn_s_waitcnt((vm & 15) | ((vm >> 4) << 14) | 0x70);
                __builtin_amdgcn_s_barrier();
                asm volatile("" ::: "memory");
                STAMP(25 + ph)
                SCHED();
            }
            if constexpr (s == 15) {
                wait_st<p1_cnt(s, 0) - 1 - p1_pos(203)>(sg, sw0);
                WAVE_RETRY((((sg.y ^ tag0) | (sw0.y ^ tag0) | (sw0.w ^ tag0)) == 0u), sg = ld64(rs, sto); sw0 = ld128(rs, swo))
                STAMP(7)
            }
            if constexpr (s == 16) {
                const u64 m = 0x8888888888888888ull;
                pol = __builtin_popcountll(vb0 & m) + __builtin_popcountll(vb1 & m) > 16 ? 8192u : 0u;
                sdo = pol ^ (unsigned)((b0 & 4) * 2048);
                wl = p.ws + sdo; w2b0 = (WD ? wl : p.ws) + 2048;
                pins(pol); pins(sdo); pins(wl); pins(w2b0);
                rl = __builtin_amdgcn_make_buffer_rsrc((void*)wl, 0, WS_BYTES, 0x00020000);
                static_assert(kW2S > 16, "");
            }
            if constexpr (s == kW2S) { vm_wait<p1_cnt(s, 0) - 1 - p1_pos(208)>(); SCHED(); }
            if constexpr (s == 18) {
                vm_wait<p1_cnt(s, 0) - 1 - (p1_pos(207) > p1_pos(208) ? p1_pos(207) : p1_pos(208))>();
                pin(fw); pin(sdq);
                kearly = __builtin_amdgcn_ballot_w64(!ISTAG(fw)) == 0ull;
                ORDER();
                if (kearly && cv) ld_ur(wro, kvtb);
                SCHED();
            }
            if constexpr (s4 != 3 || ph == 4) lgkm<(cu == 0 && s > 0 ? 3 : s == 17 ? 4 : s == 19 ? 2 : 0)>();
            SCHED();
            static_for<10>(LAMBDA(g_) {
                constexpr int g = g_.value, mb = g / 5, k = g % 5, it = p1_item(s, g), nt_ = g < 9 ? p1_item(s, g + 1) : -1;
                constexpr bool d0 = it >= 0 && it < 200, d1 = nt_ >= 0 && nt_ < 200;
                if constexpr (s == 0) mf0(acc[mb][k], af[pp][cu][mb], wf[cu][k]); else mf(acc[mb][k], af[pp][cu][mb], wf[cu][k]);
                SCHED();
                if constexpr (s < 19) {
                    if constexpr (g < 5) wf[nx][g] = *(const v4u*)(smem + (g < 3 ? (nx ? w1lane1 : w1lane0) : (nx ? w1hi1 : w1hi0)) + (g < 3 ? g : g - 3) * 20480 + ((s + 1) >> 1) * 1024);
                    if constexpr (cu == 1 && g == 5) vm_wait<p1_lim(p1_cnt(s, 5) - 1 - p1_pos(4 * (jj + 1) + 3))>();
                    if constexpr (cu == 1 && g >= 5 && g < 9)
                        af[pp ^ 1][(g - 5) >> 1][(g - 5) & 1] = *(const v4u*)(smem + (((g - 5) >> 1) ? xlane1 : xlane0) + ((jj + 1) % 3) * 4096 + ((g - 5) & 1) * 2048);
                }
                if constexpr (cu == 0 && g == 4 && s > 0) lgkm<(s == 8 || s == 10 || s == 14 ? 8 : 5)>();
                static_for<p1_cap(s, g)>(LAMBDA(e_) {
                    constexpr int na = p1_slot(0, s, g, 10) + e_.value, i = (na / 10) & 3, u = na % 10;
                    static_assert(p1_slot(0, 8, 0, 10) >= 40, "");
                    if constexpr (na < 40) {
                        if constexpr (u == 0) { if constexpr (i > 0) ror1(iw, wr[i - 1].w); else { iw = 0u; pin(iw); } }
                        else if constexpr (u < 5) rot1(ie[i][u - 1], wr[i][4 - u]);
                        else if constexpr (u == 5) shr1b(iw, wr[i].w);
                        else if constexpr (u < 9) mix1(io[i][u - 6], wr[i][9 - u], wr[i][8 - u]);
                        else mix1(io[i][3], wr[i].x, iw);
                    }
                    constexpr int nb = p1_slot(8, s, g, 3) + e_.value;
                    if constexpr (s >= 8 && s < 12 && g < 3) {
                        if constexpr (nb < 10) tc[nb] = *(const v2u*)(smem + (nb < 5 ? tca : tcb) + (nb % 5) * 1088);
                        else if constexpr (nb == 10) { const f32x2* tp = (const f32x2*)(smem + tcl); l8 = tp[2]; l32 = tp[5]; }
                        else if constexpr (nb == 11) l128 = *(const f32x2*)(smem + tcl + 72);
                        static_assert(p1_slot(8, 12, 0, 3) == 12, "");
                    }
                    constexpr int nc = p1_slot(12, s, g, 10) + e_.value;
                    if constexpr (s >= 12 && s < 14 && nc < 10) {
                        if constexpr (nc < 5) { lr[nc] = __uint_as_float(tc[nc].x); xor1(ls[nc], tc[nc].y, sgn); }
                        else { lqr[nc - 5] = __uint_as_float(tc[nc].x); xor1(lqs[nc - 5], tc[nc].y, sgn); }
                        static_assert(p1_slot(12, 14, 0, 10) >= 10, "");
                    }
                    if constexpr (s == 15 && g < 5) SW[g] = __int_as_float(__builtin_amdgcn_ds_bpermute(16 * c + 4 * (g >> 1), (g & 1) ? sw0.z : sw0.x));
                    constexpr bool img = (s == 16 && g >= 5 && g < 9 && e_.value == 0) || (s == 18 && (g == 5 || g == 6) && e_.value == 0);
                    constexpr int nd = p1_slot(15, s, g, 10) + e_.value - 5 - (s > 16 ? 4 : s == 16 && g >= 5 ? (g < 9 ? g - 4 : 4) : 0) - (s > 18 ? 2 : s == 18 && g >= 5 ? (g == 5 ? 1 : 2) : 0);
                    if constexpr (s >= 15 && nd >= 0 && !img) {
                        if constexpr (nd < 4) bc1<nd>(rr_[nd], sg.x);
                        else if constexpr (nd < 44) mulb<8 + (nd - 4) % 8>(tt[(nd - 4) % 8 / 4][(nd - 4) / 8][(nd - 4) % 4], sg.x, SW[(nd - 4) / 8]);
                        else if constexpr (nd < 48) bc1<nd - 40>(rr_[nd - 40], sg.x);
                        static_assert(p1_slot(15, 16, 0, 10) <= 9 && p1_slot(15, 20, 0, 10) >= 59, "");
                    }
                });
                if constexpr (s == 15 && g == 6) { vb0 = __builtin_amdgcn_ballot_w64(sw0.x == 1u); pins(vb0); }
                if constexpr (s == 15 && g == 7) { vb1 = __builtin_amdgcn_ballot_w64(sw0.z == 1u); pins(vb1); }
                if constexpr (s == 14 && g < 3) *(v4u*)(smem + ime + 1024 * g) = v4u{ie[3 - g][0], ie[3 - g][1], ie[3 - g][2], ie[3 - g][3]};
                if constexpr (s == 16 && g >= 5 && g < 9) *(v4u*)(smem + imo + 1024 * (8 - g)) = v4u{io[g - 5][0], io[g - 5][1], io[g - 5][2], io[g - 5][3]};
                if constexpr (s == 18 && g == 5) *(v4u*)(smem + im3) = v4u{ie[0][0], ie[0][1], ie[0][2], ie[0][3]};
                if constexpr (s == 18 && g == 6) *(unsigned*)(smem + rsa) = sg.x;
                if constexpr (s == 14 && (g == 6 || g == 8)) { zq[g - 6] = 0u; zq[g - 5] = 0u; pin(zq[g - 6]); pin(zq[g - 5]); }
                if constexpr (s == 19 && g == 0) *(v4u*)(smem + zpa) = v4u{zq[0], zq[1], zq[2], zq[3]};
                if constexpr (s == 18 && g == 1) touch_(tch ^ sdo, p.ws);
                if constexpr (s == 19 && g == 2) { if (__builtin_expect(!kearly, 0)) poll1(fwo, p.ws); }
                if constexpr (it >= 0) {
                    if constexpr (d0 && d1) DMA_LD1(it, d1 ? nt_ : 0);
                    else if constexpr (d0) DMA_LD(it);
                    else if constexpr (it == 202) gld2s<0>(sg, sto, p.ws);
                    else if constexpr (it == 203) gld4s<0>(sw0, swo, p.ws);
                    else if constexpr (it == 207) gldu16(sdq, (unsigned)chn * 2u, p.ssmD);
                    else if constexpr (it == 208) gld2s<0>(fw, fwo, p.ws);
                    else if constexpr (it >= 500) gld_w2<it % 50>(w2o, w2b0 + ((it % 50) / 5) * WRUN);
                    else { static_assert(p1_pos(19) >= P1_PRE, ""); st64(tg2, rs, fko1); }
                }
                if constexpr (d1 && !d0) DMA_M0(d1 ? nt_ : 0);
                site();
                SCHED();
            });
        };
        static_for<20>(kstep);
        SCHED();
        rr[0] = f32x4{rr_[0], rr_[1], rr_[2], rr_[3]}; rr[1] = f32x4{rr_[4], rr_[5], rr_[6], rr_[7]};
        ACC_FENCE10(acc[0], acc[1]);
        STAMP(1)
    }

    auto layer_body = LAMBDA(layer_) {
        constexpr int layer = layer_.value;
        const int sga = sgn_;
        const bool cv = layer == 0 && cv0;
        v2u fh;
        unsigned ks[10];
        f32x4 ca[8], a2[4][5];
        float rsr, rsm;
        int otw, otr, ots;
        v4u cA[2][8], cB[2][8];
        constexpr int R2 = kCR1, LP = (7 - R2) & 1;
        static_assert(kFHP >= 7 - KG0 / 4 + 1, "");
        constexpr bool TB = (SBT >> layer) & 1;
        float bv[8][5];
        f32x2 pr[5][2], ps[5][2];
        auto powers = [&]() __attribute__((always_inline)) {
            if constexpr (TB) {
#pragma unroll
                for (int nb = 0; nb < 5; ++nb) {
                    const f32x2 m = {lr[nb] * lr[nb] - ls[nb] * ls[nb], 2.0f * lr[nb] * ls[nb]};
                    pr[nb][0] = f32x2{lr[nb], m.x}; ps[nb][0] = f32x2{ls[nb], m.y};
#pragma unroll
                    for (int j = 1; j < 2; ++j) pstep(pr[nb][j], ps[nb][j], pr[nb][j - 1], ps[nb][j - 1], m);
#pragma unroll
                    for (int j = 0; j < 2; ++j) { pin(pr[nb][j]); pin(ps[nb][j]); }
                }
                SCHED();
            }
        };
        {
            float a0[5], a1[5];
            static_for<2>(LAMBDA(m_) {
                constexpr int mb = m_.value;
#pragma unroll
                for (int nb = 0; nb < 5; ++nb) {
                    const f32x4 v = __builtin_elementwise_fma(acc[mb][nb], rr[mb], f32x4{tt[mb][nb][0], tt[mb][nb][1], tt[mb][nb][2], tt[mb][nb][3]});
#pragma unroll
                    for (int i = 0; i < 4; ++i) bv[4 * mb + i][nb] = v[i];
                }
                SCHED();
                if constexpr (TB) {
#pragma unroll
                    for (int i = mb ? 4 : 1; i < 4 + 4 * mb; ++i) cstepi(bv[i][0], bv[i][1], bv[i][2], bv[i][3], bv[i][4], bv[i - 1][0], bv[i - 1][1], bv[i - 1][2], bv[i - 1][3], bv[i - 1][4], lr, ls);
#pragma unroll
                    for (int nb = 0; nb < 5; ++nb) a0[nb] = bv[7][nb];
                }
                if constexpr (mb == 0 && !TB) cstep(a0, bv[0], lr, ls, bv[1][0], bv[1][1], bv[1][2], bv[1][3], bv[1][4]);
#pragma unroll
                for (int i8 = mb ? 4 : 2; i8 < (TB ? 0 : 4 + 4 * mb); i8 += 2) {
                    cstep(a1, a0, lr, ls, bv[i8][0], bv[i8][1], bv[i8][2], bv[i8][3], bv[i8][4]);
                    cstep(a0, a1, lr, ls, bv[i8 + 1][0], bv[i8 + 1][1], bv[i8 + 1][2], bv[i8 + 1][3], bv[i8 + 1][4]);
                }
                SCHED();
            });
#pragma unroll
            for (int nb = 0; nb < 5; ++nb) *(float*)(smem + sga + nb * 64) = a0[nb];
        }
        SCHED();
        const int w_ = w0;
        {
        const int w = w_, mt1 = mt1_;
        if (layer == 0 && __builtin_expect(!kearly, 0)) {
            unsigned f0_, f1_;
            asm volatile("s_waitcnt vmcnt(0)\n\t" AR("%0", 36, 0) AR("%1", 36, 1) : "=v"(f0_), "=v"(f1_)::"memory");
            kearly = __builtin_amdgcn_ballot_w64(((f0_ ^ tag0) | (f1_ ^ tag1)) != 0u) == 0ull;
            ORDER();
            if (kearly) KW_REQ()
        }
        asm volatile("s_waitcnt lgkmcnt(0)" ::: "memory");
        __builtin_amdgcn_s_barrier();
        asm volatile("" ::: "memory");
        STAMP(layer ? 14 : 2)
#pragma unroll
        for (int s = 0; s < 10; ++s) ks[s] = kso(s);
        constexpr int NX = 58;
        constexpr int NW2A = NW2P + 7 * kCR1 + kXA, NW3 = NW2P + kN3E + kN3;
        auto w2ld = LAMBDA(i_) {
            constexpr int i = i_.value;
            gld_w2<i>(w2o, (WD ? wl : p.ws) + 2048 + (i / 5) * WRUN);
        };
        auto w2a = LAMBDA(i_) {
            constexpr int k = i_.value;
            if constexpr (k < kXA) { w2ld(std::integral_constant<int, NW2P + 2 * k>{}); w2ld(std::integral_constant<int, NW2P + 2 * k + 1>{}); }
            else w2ld(std::integral_constant<int, NW2P + kXA + k>{});
        };
        auto w2x = LAMBDA(i_) {
            constexpr int i = i_.value < NX - 50 ? 50 + i_.value : NW2A + i_.value - (NX - 50) < 50 ? NW2A + i_.value - (NX - 50) : 99;
            if constexpr (i < 50) w2ld(std::integral_constant<int, i>{});
            else if constexpr (i < 52) lda4<40 + 4 * (i - 50), (i - 50) * 64>(xso, p.x0);
            else if constexpr (i == 52) lda4<48, 128>(xso - 2u * (unsigned)(bb & 16), p.x0);
            else if constexpr (i < NX) lda2<20 + 2 * (i - 53), 32 * (i - 53)>(dso, p.Dskip);
        };
        constexpr int NB = NX - NW2A, NB1 = KB1 < NB ? KB1 : NB, KPL = 7 * (8 - R2) - kFHP, NFH = NB - NB1 - (KPL - kFHK) > 0 ? NB - NB1 - (KPL - kFHK) : 0;
        auto w2b = LAMBDA(i_) {
            constexpr int k = i_.value - 7 * R2;
            if constexpr (k < NB1) w2x(std::integral_constant<int, k>{});
            if constexpr (k > kFHK) w2x(std::integral_constant<int, NB1 + k - kFHK - 1>{});
            if constexpr (k == kFHK) { vm_wait<NB1>(); st64(v2u{tag0, tag1}, rl, fho1); STAMP(39) }
            if constexpr (k == kFHK - 1) { STAMP(38) }
            if constexpr (k == KPL) poll1(fho, wl);
            static_assert(NB1 <= kFHK + 1 && NB1 + 7 * (7 - R2) + KG0 / 4 - kFHK - 1 >= NB && NW2A <= 50 && NW3 <= NW2A && NFH <= NB - NB1, "");
        };
        auto partb = [&]() __attribute__((always_inline)) {
            const float* sp = (const float*)(smem + spa);
            rsr = sp[0]; rsm = sp[8];
            if (layer == 1) { pin(rsr); pin(rsm); }
            if (layer == 1) {
                ACC_TIE5(dsk);
#pragma unroll
                for (int nb = 0; nb < 5; ++nb) { a2[3][nb] = xin[nb] + (xin[nb] * rsr + rsm) * unpk4(dsk[nb]); pin(a2[3][nb]); }
            }
        };
        constexpr int LBW = layer ? 2 : 3;
        if (w == 3) {
            site(); SCHED();
            f32x2 sv[16];
#pragma unroll
            for (int i = 0; i < 16; ++i) sv[i] = *(const f32x2*)(smem + spl + (l_sg(layer, i >> 2) - SC + (i & 3) * 320));
            f32x2 u[4], v[4], x[4];
            cfma4(u, l8, sv[0], sv[4], sv[8], sv[12], sv[1], sv[5], sv[9], sv[13]);
            cfma4(v, l8, u[0], u[1], u[2], u[3], sv[2], sv[6], sv[10], sv[14]);
            cfma4(x, l8, v[0], v[1], v[2], v[3], sv[3], sv[7], sv[11], sv[15]);
            f32x2 P[4][4];
            P[0][0] = f32x2{0.f, 0.f}; P[0][1] = x[0]; P[0][2] = cfma(l32, x[0], x[1]); P[0][3] = cfma(l32, P[0][2], x[2]);
            const f32x2 E = cfma(l32, P[0][3], x[3]);
            __builtin_amdgcn_raw_buffer_store_b128(v4u{__float_as_uint(E.x), tag0, __float_as_uint(E.y), tag0}, rs, es, sdo ^ 8192u, SC1);
            st128(v4u{__float_as_uint(E.x), tag0, __float_as_uint(E.y), tag0}, rl, es);
            SCHED();
            if (layer == 0) { FW_OK() static_for<kN3E>(LAMBDA(i_) { w2ld(std::integral_constant<int, NW2P + i_.value>{}); }); }
#pragma unroll
            for (int kk = 1; kk < 4; ++kk) cfma4(P[kk], l8, P[kk - 1][0], P[kk - 1][1], P[kk - 1][2], P[kk - 1][3], sv[kk - 1], sv[kk + 3], sv[kk + 7], sv[kk + 11]);
#pragma unroll
            for (int i = 0; i < 16; ++i) *(f32x2*)(smem + spl + (l_sg(layer, i >> 2) - SC + (i & 3) * 320)) = P[i & 3][i >> 2];
            if (layer == 0) partb();
            if (layer == 1) powers();
        }
        if (w == LBW) {
            site(); SCHED();
            f32x2 wg[17], t4[4];
            unsigned ea[15], tg[15];
            unsigned m14 = (unsigned)mt1;
            pins(m14);
            m14 *= RUN;
            const f32x2 z2 = {0.f, 0.f};
            wg[1] = cmul(l128, l128); wg[2] = cmul(wg[1], wg[1]); wg[3] = cmul(wg[2], wg[1]); wg[4] = cmul(wg[2], wg[2]);
#pragma unroll
            for (int i = 4; i < 16; i += 4) {
                cfma4(t4, wg[i], wg[1], wg[2], wg[3], wg[4], z2, z2, z2, z2);
#pragma unroll
                for (int e = 0; e < 4; ++e) wg[i + 1 + e] = t4[e];
            }
#pragma unroll
            for (int i = 0; i < 15; ++i) {
                const int ch = mt1 - 1 - i;
                if (i) pin(wg[i]);
                ea[i] = ch < 0 ? kNone : m14 - (unsigned)(i + 1) * RUN; tg[i] = ch < 0 ? 0u : tag0;
                pins(ea[i]); pins(tg[i]);
            }
            powers();
            f32x2 H0, H1;
            skip<(layer ? SK1 : SK0)>();
            __builtin_amdgcn_s_sleep(layer ? KLB1 : KLB0);
            unsigned spins = dead ? kSpinCap : 0u;
            bool miss;
#pragma nounroll
            do {
                asm volatile("" ::: "memory");
                v4u eg[15];
#pragma unroll
                for (int i = 0; i < 15; ++i) eg[i] = ld128s(rl, eo, ea[i]);
                SCHED();
                unsigned x = __builtin_amdgcn_bitop3_b32(eg[0].y, eg[0].w, tg[0], 0x7E);
                H0 = f32x2{__uint_as_float(eg[0].x), __uint_as_float(eg[0].z)}; H1 = f32x2{0.f, 0.f};
#pragma unroll
                for (int i = 1; i < 15; i += 2) {
                    x |= __builtin_amdgcn_bitop3_b32(eg[i].y, eg[i].w, tg[i], 0x7E) | __builtin_amdgcn_bitop3_b32(eg[i + 1].y, eg[i + 1].w, tg[i + 1], 0x7E);
                    pin(x);
                    gacc2(H0, H1, wg[i], wg[i + 1], eg[i], eg[i + 1]);
                    SCHED();
                }
                miss = __builtin_amdgcn_ballot_w64(x != 0u) != 0ull;
            } while (miss && ++spins <= kSpinCap);
            dead = dead || miss;
            ORDER();
            *(f32x2*)(smem + spl + (L_CW - SC)) = H0 + H1;
            STAMP(layer ? 36 : 37)
            if (layer == 0) {
                static_for<NW3 - NW2P - kN3E>(LAMBDA(i_) { w2ld(std::integral_constant<int, NW2P + kN3E + i_.value>{}); });
            }
        }
        int w2_ = w;
        pins(w2_);
        if (layer == 0) UNDEF8(ca);
        if (layer == 0 && w2_ != LBW) {
            if (cv) {
                site(); SCHED();
                FW_OK()
                st_ur((unsigned)(size_t)(LAS char*)smem + (unsigned)uwa);
                STAMP(32)
                conv_part<0, kCR1>(ca, smem, ab, bb, w2a, [&]() __attribute__((always_inline)) {
                    v4u ad;
                    const unsigned dv = sdq << (adm & 16u);
#pragma unroll
                    for (int i = 0; i < 4; ++i) ad[i] = dv & (unsigned)__builtin_amdgcn_sbfe((int)adm, 8 + i, 1);
                    asm volatile("s_nop 1" : "+v"(ad));
                    return ad;
                }, cA, cB);
                partb();
                powers();
            } else {
                FW_OK()
                static_for<NW2A - NW2P>(LAMBDA(i_) { w2ld(std::integral_constant<int, NW2P + i_.value>{}); __builtin_amdgcn_s_sleep(1); });
                partb();
                powers();
            }
        }
        if (layer == 1 && w < LBW) { site(); SCHED(); powers(); }
        asm volatile("s_waitcnt lgkmcnt(0)" ::: "memory");
        __builtin_amdgcn_s_barrier();
        asm volatile("" ::: "memory");
        STAMP(layer ? 15 : 4)
        {
            float cw[5], ex[5];
#pragma unroll
            for (int nb = 0; nb < 5; ++nb) { cw[nb] = *(const float*)(smem + cwa + nb * 64); ex[nb] = *(const float*)(smem + sga + nb * 64); }
            v2u ta[5], td[5];
            if constexpr (TB) {
                cstepi(ex[0], ex[1], ex[2], ex[3], ex[4], cw[0], cw[1], cw[2], cw[3], cw[4], lqr, lqs);
                f32x2 cs[5];
                PIN5(ex);
#pragma unroll
                for (int nb = 0; nb < 5; ++nb) { cs[nb] = f32x2{ex[nb], dpp<0xB1>(ex[nb])}; pin(cs[nb]); }
                SCHED();
                static_for<5>(LAMBDA(n_) {
                    constexpr int nb = n_.value;
                    f32x2 h[4] = {{bv[0][nb], bv[1][nb]}, {bv[2][nb], bv[3][nb]}, {bv[4][nb], bv[5][nb]}, {bv[6][nb], bv[7][nb]}};
                    padd(h, pr[nb], ps[nb], cs[nb]);
                    *(v4u*)(smem + tbw + nb * 1024) = v4u{cvtpk(h[0].x, h[0].y), cvtpk(h[1].x, h[1].y), cvtpk(h[2].x, h[2].y), cvtpk(h[3].x, h[3].y)};
                    ta[nb] = __builtin_bit_cast(v2u, trread(smem, tbr + nb * 1024)); td[nb] = __builtin_bit_cast(v2u, trread(smem, tbr + nb * 1024 + 512));
                    SCHED();
                    if constexpr (nb >= LAG) { st128(v4u{ta[nb - LAG].x, ta[nb - LAG].y, td[nb - LAG].x, td[nb - LAG].y}, rl, hso + (nb - LAG) * 256); SCHED(); }
                });
            } else
            static_for<2>(LAMBDA(h_) {
                constexpr int n0 = 2 * h_.value;
                float hv[9][3];
                cstep3(hv[0], cw + n0, lqr + n0, lqs + n0, ex + n0);
                if constexpr (n0 == 0) {
                    chain24<0>(hv, bv, lr, ls);
                    chain24<0>(hv + 4, bv + 4, lr, ls);
                } else {
                    chain33<2>(hv, bv, lr, ls);
                    SCHED();
#pragma unroll
                    for (int rd = 0; rd < 2; ++rd) st128(v4u{ta[rd].x, ta[rd].y, td[rd].x, td[rd].y}, rl, hso + rd * 256);
                    SCHED();
                    chain33<2>(hv + 3, bv + 3, lr, ls);
                    chain32<2>(hv + 6, bv + 6, lr, ls);
                }
#pragma unroll
                for (int e = 0; e < (n0 ? 3 : 2); ++e) {
                    v4u o;
                    o.x = cvtpk(hv[1][e], hv[2][e]); o.y = cvtpk(hv[3][e], hv[4][e]);
                    o.z = cvtpk(hv[5][e], hv[6][e]); o.w = cvtpk(hv[7][e], hv[8][e]);
                    *(v4u*)(smem + tbw + (n0 + e) * 1024) = o;
                }
#pragma unroll
                for (int rd = n0; rd < (n0 ? 5 : 2); ++rd) { ta[rd] = __builtin_bit_cast(v2u, trread(smem, tbr + rd * 1024)); td[rd] = __builtin_bit_cast(v2u, trread(smem, tbr + rd * 1024 + 512)); }
                SCHED();
            });
#pragma unroll
            for (int rd = TB ? 5 - LAG : 2; rd < 5; ++rd) st128(v4u{ta[rd].x, ta[rd].y, td[rd].x, td[rd].y}, rl, hso + rd * 256);
            SCHED();
        }
#pragma unroll
        for (int s = 0; s < 10; ++s) pins(ks[s]);
        if constexpr (layer == 1) {
            partb();
            SCHED();
            if constexpr (OTR) {
                const int ln = (srca >> 4) & 63, od = (b0 >> 3) & 1, r0 = srca - 16 * ln;
                otw = (ln & 15) * 336 + (ln >> 4) * 16; otr = (ln & 12) * 336 + ((ln >> 4) + od) * 64 + (ln & 3) * 16; ots = otw + (od ? 0 : 256);
                ACC_TIE5(gt);
#pragma unroll
                for (int nb = 0; nb < 5; ++nb) *(v2u*)(smem + r0 + 10240 + otw / 2 + nb * 32) = gt[nb];
#pragma unroll
                for (int nb = 0; nb < 5; ++nb) gt[nb] = *(const v2u*)(smem + r0 + 10240 + (nb < 4 ? otr / 2 + nb * 168 : ots / 2));
                otw += r0; otr += r0; ots += r0;
                pin(otw); pin(otr); pin(ots);
                ACC_TIE5(gt);
                static_assert((SC & 1023) == 0 && (INBOX & 1023) == 0 && L_CW + 320 <= SC + 2 * INBOX + 10240, "");
                SCHED();
            }
            DRAIN();
            st64(v2u{tag0, tag1}, rl, fho1);
        }
        STAMP(layer ? 16 : 5)
        if (cv) {
            if constexpr (layer == 0) site();
            SCHED();
            conv_part<kCR1, 8>(ca, smem, ab, bb, w2b, []() {}, cA, cB);
        } else if (layer == 0) {
            static_for<NB1>(LAMBDA(i_) { w2x(i_); __builtin_amdgcn_s_sleep(1); });
            vm_wait<NB1>();
            st64(v2u{tag0, tag1}, rl, fho1);
            static_for<NB - NB1 - NFH>(LAMBDA(i_) { w2x(std::integral_constant<int, NB1 + i_.value>{}); __builtin_amdgcn_s_sleep(1); });
            if (w == 3) static_for<NW2A - NW3>(LAMBDA(i_) { w2ld(std::integral_constant<int, NW3 + i_.value>{}); __builtin_amdgcn_s_sleep(1); });
            skip<SK5>();
            poll1(fho, wl);
            static_for<NFH>(LAMBDA(i_) { w2x(std::integral_constant<int, NB - NFH + i_.value>{}); __builtin_amdgcn_s_sleep(1); });
            static_assert(NFH <= NB - (NX - 50), "");
            UNDEF8(cA[LP]); UNDEF8(cB[LP]); UNDEF8(ca);
        }
        if (layer == 0) { unsigned f0_, f1_; asm volatile("s_waitcnt vmcnt(%2)\n\t" AR("%0", 36, 0) AR("%1", 36, 1) : "=v"(f0_), "=v"(f1_) : "i"(NFH) : "memory"); fh = v2u{f0_, f1_}; }
        STAMP(layer ? 29 : 6)
        }
        unsigned g[8][2];

            v2u pt[4];
            if (layer == 0) WAVE_RETRY(ISTAG(fh), fh = ld64(rl, fho))
            else { skip<SK2>(); pipe_flag<KPF0>(pt, fho, wl, (u64)tag0 | ((u64)tag1 << 32), dead); ORDER(); }
            STAMP(layer ? 17 : 31)
            site(); SCHED();
            constexpr int RING2 = layer ? KR2 : KRT;
            v4u ar[RING2];
            constexpr int NRT = layer ? RING2 : (36 - KG0) / KRS + 2 < KRT ? (36 - KG0) / KRS + 2 : KRT;
            unsigned xs_[3][4];
            auto gate = LAMBDA(n_) {
                constexpr int n = n_.value;
                if constexpr (n >= 100) swp2n(g[2 * (n - 100)][0], g[2 * (n - 100) + 1][0], g[2 * (n - 100)][1], g[2 * (n - 100) + 1][1]);
                else if constexpr (n >= 0) cvt2(g[n][0], g[n][1], ca[n]);
            };
            if constexpr (layer == 0) {
#pragma unroll
                for (int i = 0; i < 2; ++i) { ar[i] = ld128s(rl, ho[0], ks[i]); SCHED(); }
                static_for<36 - KG0>(LAMBDA(n_) {
                    constexpr int n = n_.value, g = KG0 + n;
                    constexpr int a = g < 8 ? 0 : g < 15 ? 1 : g < 21 ? 2 : g < 26 ? 3 : g < 30 ? 4 : g < 33 ? 5 : g < 35 ? 6 : 7;
                    constexpr int e = g - (a * 8 - a * (a - 1) / 2), i = 2 + n / KRS;
                    mf(ca[a + e], cA[LP][a], cB[LP][e]);
                    SCHED();
                    if constexpr (n % KRS == 0 && i < NRT) ar[i] = ld128s(rl, ho[i / 10], ks[i % 10]);
                    if constexpr (n < 12) rda<40 + n>(xs_[n >> 2][n & 3]);
                    constexpr int gt_[12] = {0, 1, 2, -1, 100, -1, 3, -1, -1, 101, 4, -1};
                    if constexpr (g >= 24) gate(IC(gt_[g < 24 ? 0 : g - 24]));
                    site();
                    SCHED();
                });
                static_assert(KG0 <= 15 && NRT >= 12, "");
            } else {
#pragma unroll
                for (int i = 0; i < RING2; ++i) { ar[i] = ld128s(rl, ho[i / 10], ks[i % 10]); SCHED(); }
            }
        v4u gl[3];
        v2u f0;
        bool gearly = false;
        {
            const char* src = smem + srca;
            const char *glb1 = wl + 8 * RUN, *glb2 = wl + 12 * RUN;
            float zx[4], zd[4], zt[4], zz[5][4];
            if constexpr (STAMPS == 1) { STAMP(layer ? 50 : 40) }
            static_for<40>(LAMBDA(i_) {
                constexpr int i = i_.value, m = i / 10, s = i % 10;
                static_for<5>(LAMBDA(k_) {
                    constexpr int kk = k_.value, n = 2 * (i - 3) + kk / 2 - 3, nb = n / 8, u = n % 8, pv = i - 1 + RING2;
                    if constexpr (s == 0 && m < 3) mfa0<kk>(a2[m][kk], ar[i % RING2]); else mfa<5 * s + kk>(a2[m][kk], ar[i % RING2]);
                    SCHED();
                    constexpr int gu_[12] = {-1, 5, -1, -1, 102, 6, -1, -1, 7, -1, 103, -1};
                    if constexpr (layer == 0 && 5 * i + kk < 12) gate(IC(gu_[(5 * i + kk) % 12]));
                    if constexpr (layer == 0 && i == 25 && kk == 1) { pins(glb1); pins(glb2); }
                    if constexpr (layer == 0 && i >= 3 && i < 26 && (kk & 1)) {
                        if constexpr (n < 0) swp2n(xs_[n + 3][0], xs_[n + 3][2], xs_[n + 3][1], xs_[n + 3][3]);
                        else if constexpr (nb < 5) {
                            if constexpr (u < 2) unp2(zx[2 * u], zx[2 * u + 1], xs_[nb >> 1][2 * (nb & 1) + u]);
                            else if constexpr (u < 4) unp2(zd[2 * u - 4], zd[2 * u - 3], dsk[nb][u - 2]);
                            else if constexpr (u < 6) fma2(zt[2 * u - 8], zt[2 * u - 7], zx[2 * u - 8], zx[2 * u - 7], rsr, rsr, rsm, rsm);
                            else fma2(zz[nb][2 * u - 12], zz[nb][2 * u - 11], zt[2 * u - 12], zt[2 * u - 11], zd[2 * u - 12], zd[2 * u - 11], zx[2 * u - 12], zx[2 * u - 11]);
                        }
                    }
                    if constexpr (i == 0 && kk == 0 && layer == 1) { pin(pt[0]); pin(pt[1]); pin(pt[2]); pin(pt[3]); }
                    if constexpr (layer == 0 && i < 2 && kk > 0 && 12 + 4 * i + kk - 1 >= NRT && 12 + 4 * i + kk - 1 < RING2) ar[12 + 4 * i + kk - 1] = ld128s(rl, ho[1], ks[2 + 4 * i + kk - 1]);
                    if constexpr (layer == 0 && i == kGSI && kk > 0) __builtin_amdgcn_raw_buffer_store_b128(v4u{g[2 * kk - 2][0], g[2 * kk - 2][1], g[2 * kk - 1][0], g[2 * kk - 1][1]}, rs, gso + ((kk - 1) & 1) * 1024, kk < 3 ? pol : pol ^ 8192u, SC1);
                    if constexpr (layer == 0 && i > 2 && i <= 7 && kk == (i == kGSI ? 0 : 2)) {
                        { unsigned d0_, d1_; rda<20 + 2 * (i - 3)>(d0_); rda<21 + 2 * (i - 3)>(d1_); dsk[i - 3] = v2u{d0_, d1_}; }
                    }
                    if constexpr (kk == 0 && i > 0 && pv < 40) ar[pv % RING2] = ld128s(rl, ho[pv / 10], ks[pv % 10]);
                    constexpr int s1 = s - 1 - (KMB && layer == 0 && m == 1 ? kFGI - 10 : 0);
                    if constexpr (kk == 2 && m >= 1 && s1 >= 0 && s1 < 5) *(f32x4*)(smem + dst[m - 1] + s1 * 1024) = a2[m - 1][s1];
                    site();
                    if constexpr (i >= 37) a2[i - 37][kk] = *(const f32x4*)(src + (i - 37) * 5120 + kk * 1024);
                    SCHED();
                });
                if constexpr (i == 10 && (layer || !KMB)) { asm volatile("s_waitcnt lgkmcnt(0)" ::: "memory"); __builtin_amdgcn_s_barrier(); asm volatile("" ::: "memory"); }
                if constexpr (i == kFGI && layer == 0) {
                    static_assert(kFGI >= 10 && kFGI <= 14, "");
                    asm volatile("s_waitcnt vmcnt(%0) lgkmcnt(0)" ::"i"(kFGI - kGSI) : "memory");
                    __builtin_amdgcn_s_barrier(); asm volatile("" ::: "memory");
                    st64(v2u{tag0, tag1}, rs, fgo1);
                }
                if constexpr (i == 26 && layer == 0) {
#pragma unroll
                    for (int nb = 0; nb < 5; ++nb) { a2[3][nb] = f32x4{zz[nb][0], zz[nb][1], zz[nb][2], zz[nb][3]}; pin(a2[3][nb]); }
                }
                if constexpr (i == kGPI && layer == 0) gld2s<0>(f0, fgo, wl);
                if constexpr (i == kGCI && layer == 0) {
                    wait1(f0);
                    static_for<39 - i>(LAMBDA(r_) { pin(ar[(i + 1 + r_.value) % RING2]); });
                    gearly = __builtin_amdgcn_ballot_w64(!ISTAG(f0)) == 0ull;
                    ORDER();
                    undef3(gl);
                    if (__builtin_expect(gearly, 1)) { gld4t<0>(gl[0], glo, wl); gld4t<0>(gl[1], glo, glb1); gld4t<0>(gl[2], glo, glb2); }
                    else if (G2ND) poll2(fgo, fgo, wl);
                }
                if constexpr (i == 36) {
                    asm volatile("s_waitcnt lgkmcnt(0)" ::: "memory");
                    __builtin_amdgcn_s_barrier(); asm volatile("" ::: "memory");
                }
                if constexpr (STAMPS == 1 && (i == 0 || i == 9 || i == 10 || i == 15 || i == 25 || i == 35 || i == 36)) { STAMP((layer ? 51 : 41) + (i == 0 ? 0 : i == 9 ? 1 : i == 10 ? 2 : i == 15 ? 3 : i == 25 ? 4 : i == 35 ? 5 : 6)) }
                SCHED();
            });
            STAMP(layer ? 18 : 8)
        }
#pragma unroll
        for (int nb = 0; nb < 5; ++nb) a2[0][nb] = (a2[0][nb] + a2[1][nb]) + a2[2][nb];
        asm volatile("s_nop 1" : "+v"(a2[3][0]), "+v"(a2[3][1]), "+v"(a2[3][2]), "+v"(a2[3][3]), "+v"(a2[3][4]),
                     "+v"(a2[0][0]), "+v"(a2[0][1]), "+v"(a2[0][2]), "+v"(a2[0][3]), "+v"(a2[0][4]));
        float ty;
        {
            f32x4 s1, s2;
#pragma unroll
            for (int nb = 0; nb < 5; ++nb) {
                const f32x4 y = a2[3][nb] + a2[0][nb];
                xin[nb] = y;
                if (nb == 0) { s1 = y; s2 = y * y; } else { s1 += y; s2 += y * y; }
            }
            ty = xsum2h(hsum4(s1), hsum4(s2));
            st64(v2u{__float_as_uint(ty), tag0}, rl, sgs);
        }
        SCHED();
        STAMP(layer ? 19 : 9)
        if constexpr (layer == 1 && OTR) {
#pragma unroll
            for (int nb = 0; nb < 5; ++nb) *(f32x4*)(smem + otw + nb * 64) = xin[nb];
#pragma unroll
            for (int nb = 0; nb < 5; ++nb) xin[nb] = *(const f32x4*)(smem + (nb < 4 ? otr + nb * 336 : ots));
            SCHED();
        }
        int lane_o, b_o = b0, w_o = w0;
        asm volatile("v_mbcnt_lo_u32_b32 %0, -1, 0\n\tv_mbcnt_hi_u32_b32 %0, -1, %0" : "=v"(lane_o));
        asm volatile("" : "+s"(b_o), "+s"(w_o));
        const int lane = lane_o, b = b_o, w = w_o;
        const int c = lane & 15, q = lane >> 4;
        TILES()
        if (layer == 0 && __builtin_expect(!gearly, 0)) {
            if (G2ND) {
                unsigned f0_, f1_;
                asm volatile("s_waitcnt vmcnt(1)\n\t" AR("%0", 36, 0) AR("%1", 36, 1) : "=v"(f0_), "=v"(f1_)::"memory");
                f0 = v2u{f0_, f1_};
            }
            STAMP(63)
            WAVE_RETRY(ISTAG(f0), f0 = ld64(rl, fgo))
            gld4t<0>(gl[0], glo, wl); gld4t<0>(gl[1], glo, wl + 8 * RUN); gld4t<0>(gl[2], glo, wl + 12 * RUN);
            asm volatile("s_waitcnt vmcnt(0)" : "+v"(gl[0]), "+v"(gl[1]), "+v"(gl[2])::"memory");
        }
        if (layer == 0) {
            asm volatile("s_waitcnt vmcnt(1)" : "+v"(gl[0]), "+v"(gl[1]), "+v"(gl[2])::"memory");
            STAMP(60)
            char* tb = smem + SC + w * INBOX;
            *(v4u*)(tb + lane * 16) = gl[0]; *(v4u*)(tb + 1024 + lane * 16) = gl[1]; *(v4u*)(tb + 1536 + lane * 16) = gl[2];
#pragma unroll
            for (int nb = 0; nb < 5; ++nb) gt[nb] = __builtin_bit_cast(v2u, trread(tb, (16 * nb + 4 * q + (c >> 2)) * 32 + (c & 3) * 8));
        } else ACC_TIE5(gt);
        SCHED();
        constexpr int PR = layer ? PRM1 : PRM0, NP = (PR & 1) + (PR >> 1);
        if constexpr (PR & 1) gran_req<32>(sgo, wl);
        SCHED();
        f32x4 gf[5], yg[5];
#pragma unroll
        for (int nb = 0; nb < 5; ++nb) { gf[nb] = unpk4(gt[nb]); yg[nb] = xin[nb] * gf[nb]; pin(gf[nb]); pin(yg[nb]); }
        SCHED();
        if constexpr (PR & 2) gran_req<(PR & 1 ? 40 : 32)>(sgo, wl);
        SCHED();
        unsigned xs = OFF_X2F + (unsigned)(((mt2 >> 3) & 1) * PG + ((2 * (mt2 & 7) + (w >> 1)) * 8 + ((c >> 2) & 1) * 4 + (nt2 >> 1)) * RUN + (10 * (nt2 & 1) + 2 * (q & 1) + (q >> 1)) * 256 + (8 * (w & 1) + 4 * (c >> 3) + (c & 3)) * 16);
        unsigned pso = q < 2 ? OFF_PS + (unsigned)((mt1 & 7) * RUN + (((mt2 & 1) * 4 + w) * 8 + nt2) * 128 + c * 8 + q * 4) : kNone;
        unsigned fxo1 = lane ? kNone : OFF_FX + (unsigned)((mt1 & 7) * RUN + ((2 * (mt2 & 1) + (w >> 1)) * 16 + nt2 * 2 + (w & 1)) * 128);
        unsigned oo = (unsigned)(((64 * mt2 + 16 * w + c) * D_ + 80 * nt2 + 4 * q) * 4);
        unsigned o4 = (unsigned)(((64 * mt2 + 16 * w + (c & 12)) * D_ + 80 * nt2 + 16 * (q + (nt2 & 1)) + 4 * (c & 3)) * 4);
        if (OTR) oo += (nt2 & 1) ? 0u : 256u;
        unsigned xs2 = (q & 1) ? kNone : xs + 2048u;
        if (layer == 0) { pin(xs); pin(xs2); pin(pso); pin(fxo1); } else { pin(oo); if (OTR) pin(o4); }
        SCHED();
        {
            float a, d;
            STAMP(layer ? 62 : 61)
            if constexpr (layer == 0) site();
            pipe_gran<(layer ? PDS : PDS0), NP>(a, d, sgo, wl, tag0, dead);
            STAMP(layer ? 20 : 10)
            xsum2(a, d);
            const float me = a * (1.0f / D_);
            const float ri = __builtin_amdgcn_rsqf(fmaxf(d * (1.0f / D_) - me * me, 0.f) + 1e-5f), mr = -me * ri;
            if constexpr (layer == 1 && OTR) {
                static_for<5>(LAMBDA(j_) {
                    constexpr int jj = (j_.value + 4) % 5;
                    const float rj = jj < 4 ? dpp<0x55 * (jj & 3)>(ri) : ri, mj = jj < 4 ? dpp<0x55 * (jj & 3)>(mr) : mr;
                    const f32x4 o = yg[jj] * rj + (gf[jj] * mj + xin[jj]);
                    __builtin_amdgcn_raw_buffer_store_b128(__builtin_bit_cast(v4u, o), ro, jj < 4 ? o4 : oo, jj < 4 ? jj * 2560 : 0, OUTP);
                });
            } else if constexpr (layer == 0 && XI) {
#pragma unroll
                for (int i = 0; i < 3; ++i) {
                    v2u pk[2];
#pragma unroll
                    for (int e = 0; e < (i < 2 ? 2 : 1); ++e) {
                        const f32x4 x = yg[2 * i + e] * ri + (gf[2 * i + e] * mr + xin[2 * i + e]);
                        xin[2 * i + e] = x;
                        pk[e] = v2u{cvtpk(x.x, x.y), cvtpk(x.z, x.w)};
                    }
                    const v2u pa = pk[0], pb = pk[i < 2];
                    const auto r0 = __builtin_amdgcn_permlane16_swap(pa.x, pb.x, false, false);
                    const auto r1 = __builtin_amdgcn_permlane16_swap(pa.y, pb.y, false, false);
                    st128(v4u{r0[0], r1[0], r0[1], r1[1]}, rl, i < 2 ? xs + i * 1024 : xs2);
                    SCHED();
                }
            } else {
#pragma unroll
                for (int nb = 0; nb < 5; ++nb) xin[nb] = yg[nb] * ri + (gf[nb] * mr + xin[nb]);
            }
        }
        if constexpr (layer == 0) {
            v2u pk[5];
#pragma unroll
            for (int nb = 0; nb < 5; ++nb) { const f32x4 x = xin[nb]; pk[nb] = v2u{cvtpk(x.x, x.y), cvtpk(x.z, x.w)}; }
#pragma unroll
            for (int i = 0; i < (XI ? 0 : 3); ++i) {
                const v2u pa = pk[2 * i], pb = pk[i < 2 ? 2 * i + 1 : 4];
                const auto r0 = __builtin_amdgcn_permlane16_swap(pa.x, pb.x, false, false);
                const auto r1 = __builtin_amdgcn_permlane16_swap(pa.y, pb.y, false, false);
                if (i < 2 || (q & 1) == 0) st128(v4u{r0[0], r1[0], r0[1], r1[1]}, rl, xs + i * 1024);
            }
            {
                f32x4 s1, s2;
#pragma unroll
                for (int nb = 0; nb < 5; ++nb) { const f32x4 x = xin[nb]; if (nb == 0) { s1 = x; s2 = x * x; } else { s1 += x; s2 += x * x; } }
                __builtin_amdgcn_raw_buffer_store_b32(__float_as_uint(xsum2h(hsum4(s1), hsum4(s2))), rl, pso, 0, SC1);
            }
            SCHED();
            int w1lane0 = (c & 1) * 10240 + (c >> 1) * 128 + ((q ^ ((c & 1) | ((c >> 2) << 1))) * 16);
            int w1lane1 = (c & 1) * 10240 + (c >> 1) * 128 + (((4 + q) ^ ((c & 1) | ((c >> 2) << 1))) * 16);
            unsigned fxo = OFF_FX + (unsigned)((mt1 & 7) * RUN + w * 2048 + (lane >> 2) * 128);
            unsigned xo = OFF_X2F + (unsigned)(((mt1 >> 2) & 1) * PG + (4 * (mt1 & 3) + w) * 8 * RUN + lane * 16), xo1 = xo + 4 * RUN;
            const int row = 8 * q + (c & 7);
            unsigned po = OFF_PS + (unsigned)((mt1 & 7) * RUN + ((2 * w + (row >> 4)) * 8 + 4 * (c >> 3)) * 128 + (row & 15) * 8);
            int w1hi0 = w1lane0 + 61440, w1hi1 = w1lane1 + 61440;
            int rsw = l_rs(1, 0) + w * INBOX + lane * 4;
            if (GW) pin(rsw);
            pin(w1lane0); pin(w1lane1); pin(w1hi0); pin(w1hi1); pin(fxo); pin(xo); pin(xo1); pin(po);
            v4u wf[2][5];
#pragma unroll
            for (int nb = 0; nb < 5; ++nb) wf[0][nb] = *(const v4u*)(smem + w1lane0 + nb * 20480);
            v2u tg2 = {tag0, tag1};
            pin(tg2);
            SCHED();
            DRAIN();
            st64(tg2, rl, fxo1);
            if (b == 255 && w == 0) FLAG(OFF_NONCE)
            STAMP(11)
            addrs(std::integral_constant<int, 1>{}, lane, w, b);
            v2u pt[4];
            skip<SKX>();
            pipe_flag<KPFX>(pt, fxo, wl, (u64)tag0 | ((u64)tag1 << 32), dead);
            ORDER();
            STAMP(12)
            v4u ar[RING1][2];
#pragma unroll
            for (int s = 0; s < RING1; ++s)
#pragma unroll
                for (int mb = 0; mb < 2; ++mb) { ar[s][mb] = ld128s(rl, mb ? xo1 : xo, kso(s)); SCHED(); }
            v2u ps[4];
#pragma unroll
            for (int i = 0; i < 4; ++i) ps[i] = ld64(rl, po + i * 128);
            SCHED();
            float t1, t2, rinv, mri, rsel, rr_[8], rm_[8];
            static_for<20>(LAMBDA(s_) {
                constexpr int s = s_.value, cu = s & 1, nx = cu ^ 1;
                LGKM0() SCHED();
                static_for<10>(LAMBDA(g_) {
                    constexpr int g = g_.value, mb = g / 5, k = g % 5;
                    if constexpr (s == 0) mf0(acc[mb][k], ar[0][mb], wf[0][k]); else mf(acc[mb][k], ar[s % RING1][mb], wf[cu][k]);
                    SCHED();
                    if constexpr (s == 0 && g == 0) { pin(pt[0]); pin(pt[1]); pin(pt[2]); pin(pt[3]); }
                    if constexpr (s < 19 && g < 3) wf[nx][g] = *(const v4u*)(smem + (nx ? w1lane1 : w1lane0) + g * 20480 + ((s + 1) >> 1) * 1024);
                    if constexpr (s < 19 && (g == 3 || g == 4)) wf[nx][g] = *(const v4u*)(smem + (nx ? w1hi1 : w1hi0) + (g - 3) * 20480 + ((s + 1) >> 1) * 1024);
                    if constexpr (g == 5 && s + RING1 < 20) ar[s % RING1][0] = ld128s(rl, xo, kso(s + RING1));
                    if constexpr (g == 2 && s > 0 && s - 1 + RING1 < 20) ar[(s - 1) % RING1][1] = ld128s(rl, xo1, kso(s - 1 + RING1));
                    constexpr int z = GW ? s + 3 : s;
                    if constexpr (z == 8 && g == 8) { t1 = __uint_as_float(ps[0].x) + __uint_as_float(ps[1].x); pin(t1); t2 = __uint_as_float(ps[0].y) + __uint_as_float(ps[1].y); pin(t2); }
                    if constexpr (z == 9 && g == 6) { t1 += __uint_as_float(ps[2].x); pin(t1); t2 += __uint_as_float(ps[2].y); pin(t2); }
                    if constexpr (z == 9 && g == 7) { t1 += __uint_as_float(ps[3].x); pin(t1); t2 += __uint_as_float(ps[3].y); pin(t2); }
                    if constexpr (z == 10 && g == 6) { t1 += dpp<0x128>(t1); t2 += dpp<0x128>(t2); pin(t1); pin(t2); }
                    if constexpr (z == 10 && g == 7) { t1 *= (1.0f / D_); pin(t1); t2 *= (1.0f / D_); pin(t2); }
                    if constexpr (z == 10 && g == 8) { t2 = fmaxf(t2 - t1 * t1, 0.f); pin(t2); }
                    if constexpr (z == 11 && g == 6) { rinv = __builtin_amdgcn_rsqf(t2 + 1e-5f); pin(rinv); }
                    if constexpr (z == 11 && g == 8) { mri = -t1 * rinv; pin(mri); }
                    if constexpr (!GW && s == 12 && g == 4) *(float*)(smem + l_rs(1, 0) + w * INBOX + lane * 4) = c < 8 ? rinv : mri;
                    if constexpr (GW && s == 19 && g == 1) selhi(rsel, mri);
                    if constexpr (GW && s == 19 && g == 0) { rsel = rinv; pin(rsel); }
                    if constexpr (GW && s == 19 && g == 2) *(float*)(smem + rsw) = rsel;
                    if constexpr (z >= 12 && z < 15 && g >= 6 && g < 9 && 3 * (z - 12) + g - 6 < 8) bc2b<3 * (z - 12) + g - 6>(rr_[3 * (z - 12) + g - 6], rm_[3 * (z - 12) + g - 6], rinv, mri);
                    if constexpr (!GW && s >= 15 && s < 19 && g >= 5) TT_GAP(5 * (s - 15) + g - 5)
                    if constexpr (GW && s >= 12 && s < 19 && g >= 6 && g < 9 && 3 * (s - 12) + g - 6 < 20) TT_GAP(3 * (s - 12) + g - 6)
                    site();
                    SCHED();
                });
            });
            rr[0] = f32x4{rr_[0], rr_[1], rr_[2], rr_[3]}; rr[1] = f32x4{rr_[4], rr_[5], rr_[6], rr_[7]};
            asm volatile("kpf_end2:");
            ACC_FENCE10(acc[0], acc[1]);
            STAMP(13)
        } else {
#pragma unroll
            for (int nb = 0; nb < (OTR ? 0 : 5); ++nb) __builtin_amdgcn_raw_buffer_store_b128(__builtin_bit_cast(v4u, xin[nb]), ro, oo + 64 * nb, 0, OUTP);
            STAMP(21)
            STAMP_DUMP()
            asm volatile("kpf_end:");
        }
    };
    layer_body(std::integral_constant<int, 0>{});
    layer_body(std::integral_constant<int, 1>{});
}

extern "C" void kernel_launch(void* const* d_in, const int* in_sizes, int n_in,
                              void* d_out, int out_size, void* d_ws, size_t ws_size,
                              hipStream_t stream) {
    Params p;
    p.x0 = (const u16*)d_in[0];
    p.Lre = (const u16*)d_in[1];
    p.Lim = (const u16*)d_in[2];
    p.Bre = (const u16*)d_in[3];
    p.Bim = (const u16*)d_in[4];
    p.Cre = (const u16*)d_in[5];
    p.Cim = (const u16*)d_in[6];
    p.logdt = (const u16*)d_in[7];
    p.Dskip = (const u16*)d_in[8];
    p.k = (const u16*)d_in[10];
    p.v = (const u16*)d_in[11];
    p.shk = (const u16*)d_in[12];
    p.ssmk = (const u16*)d_in[13];
    p.ssmD = (const u16*)d_in[14];
    p.ws = (char*)d_ws;
    p.out = (float*)d_out;
    k_main<<<256, 256, 0, stream>>>(p);
}
```

```cpp
#include <hip/hip_runtime.h>
#include <stdint.h>
#include <utility>

#define STAMPS 0

typedef unsigned short u16;
typedef unsigned long long u64;
typedef unsigned v4u __attribute__((ext_vector_type(4)));
typedef unsigned v2u __attribute__((ext_vector_type(2)));
typedef float f32x4 __attribute__((ext_vector_type(4)));
typedef float f32x2 __attribute__((ext_vector_type(2)));
typedef short s16x4 __attribute__((ext_vector_type(4)));
typedef __bf16 bf16x2v __attribute__((ext_vector_type(2)));
#define LAS __attribute__((address_space(3)))
#define DI __device__ __forceinline__

constexpr int S_ = 2048, D_ = 640;
#define KG0 12
#define KRT 12
#define KRS 2
#define KB1 4
#define SBT 2
#define LAG 1
#define SKP 0
#define SK0 0
#define SK5 850
#define SKX 0
#define SK1 0
#define SK2 60
#define PRM0 2
#define PRM1 1
#define KR1 5
#define KR2 10
constexpr int RING1 = KR1;
#define KCR1 3
constexpr int kCR1 = KCR1;
#define KXA 10
#define KN3E 17
#define KN3 6
constexpr int kXA = KXA;
constexpr int kN3E = KN3E, kN3 = KN3;
#define KFHK 4
#define KFHP 12
constexpr int kFHK = KFHK, kFHP = KFHP;
#define KGPI 27
#define KGCI 33
#define KFGI 11
#define KGSI 2
constexpr int kGSI = KGSI, kFGI = KFGI, kGPI = KGPI, kGCI = KGCI;

constexpr unsigned OFF_NONCE = 0;
constexpr unsigned OFF_CV = 1024;
constexpr unsigned OFF_ST = 4096;
constexpr unsigned OFF_FW = OFF_ST + 65536;
constexpr unsigned OFF_FK = OFF_FW + 131072;
constexpr unsigned OFF_KVT = OFF_FK + 81920;
constexpr unsigned RUN = 16384, PG = 4194304, MID = 1536;
constexpr unsigned OFF_SG = 8 * PG;
constexpr unsigned OFF_FH = OFF_SG + 32 * RUN;
constexpr unsigned OFF_FX = OFF_FH + 16 * RUN;
constexpr unsigned OFF_PS = OFF_FX + 8 * RUN;
constexpr unsigned OFF_EG = OFF_PS + 8 * RUN + MID;
constexpr unsigned OFF_X2F = 9 * PG + MID;
constexpr unsigned OFF_HF = 11 * PG + MID;
constexpr unsigned OFF_W2F = 19 * PG + MID;
constexpr unsigned OFF_GT = 23 * PG;
constexpr unsigned WS_BYTES = 25 * PG;
constexpr unsigned kso(int s) { return (unsigned)(s / 5) * RUN + (unsigned)(s % 5) * 1024u; }
constexpr unsigned kNone = 0x40000000u;
constexpr unsigned OFF_DBG = 0x1800000;
#define MAP 0
#define TILES() const int xq = b & 7, j = b >> 3, mt2 = MAP ? 8 * (xq >> 1) + (j >> 2) : 4 * xq + (j >> 3), nt2 = MAP ? 4 * (xq & 1) + (j & 3) : j & 7, mt1 = mt2 >> 1, \
    nt1 = MAP ? 8 * (xq & 1) + (j & 7) : j & 15, i32 = 16 * (mt2 & 1) + mt1;

constexpr int SC = 102400;
constexpr int REGSZ = 13056;
constexpr int L_XR = SC;
constexpr int L_CO = SC + 12288;
constexpr int L_KB = SC + 51712;
constexpr int L_TAB = SC + 55808;
constexpr int L_CW = SC + 39168;
constexpr int INBOX = 15360;
constexpr int l_rs(int l, int w) { return l ? SC + w * INBOX : L_KB + w * 256; }
constexpr int l_sg(int l, int w) { return l ? SC + w * INBOX + 256 : SC + (w == 0 ? 43264 : w == 1 ? 46080 : w == 2 ? 52736 : 56320); }
constexpr int l_tb(int l, int w) { return l ? SC + w * INBOX + 2048 : SC + 40960 + w * 5120; }
constexpr int LDS_BYTES = 163840;
constexpr int CV_E = 4608;
constexpr int CV_O = CV_E + (1056 + 15) * 4;

struct Params {
    const u16 *x0, *Lre, *Lim, *Bre, *Bim, *Cre, *Cim, *logdt, *Dskip, *k, *v, *shk, *ssmk, *ssmD;
    char* ws;
    float* out;
};

#define MF "v_mfma_f32_16x16x32_bf16 "
DI void mf(f32x4& c, v4u a, v4u b) { asm(MF "%0, %1, %2, %0" : "+v"(c) : "v"(a), "v"(b)); }
DI void mf0(f32x4& c, v4u a, v4u b) { asm(MF "%0, %1, %2, 0" : "=&v"(c) : "v"(a), "v"(b)); }
constexpr int W2R = 56;
#define WA(n) "a[%c" #n ":%c" #n "+3]"
template <int R> DI void mfa(f32x4& c, v4u a) { asm(MF "%0, " WA(2) ", %1, %0" : "+v"(c) : "v"(a), "i"(W2R + 4 * R)); }
template <int R> DI void mfa0(f32x4& c, v4u a) { asm(MF "%0, " WA(2) ", %1, 0" : "=&v"(c) : "v"(a), "i"(W2R + 4 * R)); }
#define ACC_FENCE10(c, d) asm volatile("" : "+v"(c[0]), "+v"(c[1]), "+v"(c[2]), "+v"(c[3]), "+v"(c[4]), "+v"(d[0]), "+v"(d[1]), "+v"(d[2]), "+v"(d[3]), "+v"(d[4]))
#define ACC_TIE5(c) asm volatile("" : "+v"(c[0]), "+v"(c[1]), "+v"(c[2]), "+v"(c[3]), "+v"(c[4]))
#define QP " quad_perm:[1,0,3,2] row_mask:0xf bank_mask:0xf"
DI void cstep(float (&t)[5], const float (&h)[5], const float (&lr)[5], const float (&ls)[5], float b0, float b1, float b2, float b3, float b4) {
    asm("v_fma_f32 %0, %10, %5, %20\n\tv_fma_f32 %1, %11, %6, %21\n\tv_fma_f32 %2, %12, %7, %22\n\tv_fma_f32 %3, %13, %8, %23\n\tv_fma_f32 %4, %14, %9, %24\n\t"
        "v_fmac_f32_dpp %0, %5, %15" QP "\n\tv_fmac_f32_dpp %1, %6, %16" QP "\n\tv_fmac_f32_dpp %2, %7, %17" QP "\n\tv_fmac_f32_dpp %3, %8, %18" QP "\n\tv_fmac_f32_dpp %4, %9, %19" QP
        : "=&v"(t[0]), "=&v"(t[1]), "=&v"(t[2]), "=&v"(t[3]), "=&v"(t[4])
        : "v"(h[0]), "v"(h[1]), "v"(h[2]), "v"(h[3]), "v"(h[4]), "v"(lr[0]), "v"(lr[1]), "v"(lr[2]), "v"(lr[3]), "v"(lr[4]),
          "v"(ls[0]), "v"(ls[1]), "v"(ls[2]), "v"(ls[3]), "v"(ls[4]), "v"(b0), "v"(b1), "v"(b2), "v"(b3), "v"(b4));
}
DI void cstep3(float* t, const float* h, const float* lr, const float* ls, const float* b) {
    asm("v_fma_f32 %0, %6, %3, %12\n\tv_fma_f32 %1, %7, %4, %13\n\tv_fma_f32 %2, %8, %5, %14\n\t"
        "v_fmac_f32_dpp %0, %3, %9" QP "\n\tv_fmac_f32_dpp %1, %4, %10" QP "\n\tv_fmac_f32_dpp %2, %5, %11" QP
        : "=&v"(t[0]), "=&v"(t[1]), "=&v"(t[2])
        : "v"(h[0]), "v"(h[1]), "v"(h[2]), "v"(lr[0]), "v"(lr[1]), "v"(lr[2]), "v"(ls[0]), "v"(ls[1]), "v"(ls[2]), "v"(b[0]), "v"(b[1]), "v"(b[2]));
}
template <int o> DI void chain24(float (*t)[3], const float (*b)[5], const float* lr, const float* ls) {
    asm("v_fma_f32 %0, %18, %16, %8\n\t" "v_fma_f32 %1, %19, %17, %9\n\t" "v_fmac_f32_dpp %0, %16, %20" QP "\n\t" "v_fmac_f32_dpp %1, %17, %21" QP "\n\t"
        "v_fma_f32 %2, %18, %0, %10\n\t" "v_fma_f32 %3, %19, %1, %11\n\t" "v_fmac_f32_dpp %2, %0, %20" QP "\n\t" "v_fmac_f32_dpp %3, %1, %21" QP "\n\t"
        "v_fma_f32 %4, %18, %2, %12\n\t" "v_fma_f32 %5, %19, %3, %13\n\t" "v_fmac_f32_dpp %4, %2, %20" QP "\n\t" "v_fmac_f32_dpp %5, %3, %21" QP "\n\t"
        "v_fma_f32 %6, %18, %4, %14\n\t" "v_fma_f32 %7, %19, %5, %15\n\t" "v_fmac_f32_dpp %6, %4, %20" QP "\n\t" "v_fmac_f32_dpp %7, %5, %21" QP "\n\t"
        : "=&v"(t[1][0]), "=&v"(t[1][1]), "=&v"(t[2][0]), "=&v"(t[2][1]), "=&v"(t[3][0]), "=&v"(t[3][1]), "=&v"(t[4][0]), "=&v"(t[4][1])
        : "v"(b[0][o + 0]), "v"(b[0][o + 1]), "v"(b[1][o + 0]), "v"(b[1][o + 1]), "v"(b[2][o + 0]), "v"(b[2][o + 1]), "v"(b[3][o + 0]), "v"(b[3][o + 1]), "v"(t[0][0]), "v"(t[0][1]), "v"(lr[o + 0]), "v"(lr[o + 1]), "v"(ls[o + 0]), "v"(ls[o + 1]));
}
template <int o> DI void chain33(float (*t)[3], const float (*b)[5], const float* lr, const float* ls) {
    asm("v_fma_f32 %0, %21, %18, %9\n\t" "v_fma_f32 %1, %22, %19, %10\n\t" "v_fma_f32 %2, %23, %20, %11\n\t" "v_fmac_f32_dpp %0, %18, %24" QP "\n\t" "v_fmac_f32_dpp %1, %19, %25" QP "\n\t" "v_fmac_f32_dpp %2, %20, %26" QP "\n\t"
        "v_fma_f32 %3, %21, %0, %12\n\t" "v_fma_f32 %4, %22, %1, %13\n\t" "v_fma_f32 %5, %23, %2, %14\n\t" "v_fmac_f32_dpp %3, %0, %24" QP "\n\t" "v_fmac_f32_dpp %4, %1, %25" QP "\n\t" "v_fmac_f32_dpp %5, %2, %26" QP "\n\t"
        "v_fma_f32 %6, %21, %3, %15\n\t" "v_fma_f32 %7, %22, %4, %16\n\t" "v_fma_f32 %8, %23, %5, %17\n\t" "v_fmac_f32_dpp %6, %3, %24" QP "\n\t" "v_fmac_f32_dpp %7, %4, %25" QP "\n\t" "v_fmac_f32_dpp %8, %5, %26" QP "\n\t"
        : "=&v"(t[1][0]), "=&v"(t[1][1]), "=&v"(t[1][2]), "=&v"(t[2][0]), "=&v"(t[2][1]), "=&v"(t[2][2]), "=&v"(t[3][0]), "=&v"(t[3][1]), "=&v"(t[3][2])
        : "v"(b[0][o + 0]), "v"(b[0][o + 1]), "v"(b[0][o + 2]), "v"(b[1][o + 0]), "v"(b[1][o + 1]), "v"(b[1][o + 2]), "v"(b[2][o + 0]), "v"(b[2][o + 1]), "v"(b[2][o + 2]), "v"(t[0][0]), "v"(t[0][1]), "v"(t[0][2]), "v"(lr[o + 0]), "v"(lr[o + 1]), "v"(lr[o + 2]), "v"(ls[o + 0]), "v"(ls[o + 1]), "v"(ls[o + 2]));
}
template <int o> DI void chain32(float (*t)[3], const float (*b)[5], const float* lr, const float* ls) {
    asm("v_fma_f32 %0, %15, %12, %6\n\t" "v_fma_f32 %1, %16, %13, %7\n\t" "v_fma_f32 %2, %17, %14, %8\n\t" "v_fmac_f32_dpp %0, %12, %18" QP "\n\t" "v_fmac_f32_dpp %1, %13, %19" QP "\n\t" "v_fmac_f32_dpp %2, %14, %20" QP "\n\t"
        "v_fma_f32 %3, %15, %0, %9\n\t" "v_fma_f32 %4, %16, %1, %10\n\t" "v_fma_f32 %5, %17, %2, %11\n\t" "v_fmac_f32_dpp %3, %0, %18" QP "\n\t" "v_fmac_f32_dpp %4, %1, %19" QP "\n\t" "v_fmac_f32_dpp %5, %2, %20" QP "\n\t"
        : "=&v"(t[1][0]), "=&v"(t[1][1]), "=&v"(t[1][2]), "=&v"(t[2][0]), "=&v"(t[2][1]), "=&v"(t[2][2])
        : "v"(b[0][o + 0]), "v"(b[0][o + 1]), "v"(b[0][o + 2]), "v"(b[1][o + 0]), "v"(b[1][o + 1]), "v"(b[1][o + 2]), "v"(t[0][0]), "v"(t[0][1]), "v"(t[0][2]), "v"(lr[o + 0]), "v"(lr[o + 1]), "v"(lr[o + 2]), "v"(ls[o + 0]), "v"(ls[o + 1]), "v"(ls[o + 2]));
}
DI void pstep(f32x2& xr, f32x2& xs, f32x2 pr, f32x2 ps, f32x2 m) {
    asm("v_pk_mul_f32 %0, %2, %4 op_sel_hi:[1,0]\n\tv_pk_mul_f32 %1, %3, %4 op_sel_hi:[1,0]\n\t"
        "v_pk_fma_f32 %0, %3, %4, %0 op_sel:[0,1,0] neg_lo:[1,0,0] neg_hi:[1,0,0]\n\tv_pk_fma_f32 %1, %2, %4, %1 op_sel:[0,1,0]"
        : "=&v"(xr), "=&v"(xs) : "v"(pr), "v"(ps), "v"(m));
}
DI void padd(f32x2 (&h)[4], const f32x2 (&pr)[2], const f32x2 (&ps)[2], f32x2 c) {
    f32x2 d;
    asm("v_pk_mul_f32 %4, %6, %9 op_sel:[1,0]\n\tv_pk_fma_f32 %0, %5, %9, %0 op_sel_hi:[1,0,1]\n\tv_pk_fma_f32 %1, %6, %9, %1 op_sel_hi:[1,0,1]\n\t"
        "v_pk_fma_f32 %4, %8, %9, %4 op_sel:[1,1,0] op_sel_hi:[1,0,1] neg_hi:[1,0,0]\n\tv_pk_fma_f32 %0, %7, %9, %0 op_sel:[0,1,0]\n\tv_pk_fma_f32 %1, %8, %9, %1 op_sel:[0,1,0]\n\t"
        "v_pk_fma_f32 %2, %5, %4, %2 op_sel_hi:[1,0,1]\n\tv_pk_fma_f32 %3, %6, %4, %3 op_sel_hi:[1,0,1]\n\tv_pk_fma_f32 %2, %7, %4, %2 op_sel:[0,1,0]\n\tv_pk_fma_f32 %3, %8, %4, %3 op_sel:[0,1,0]"
        : "+v"(h[0]), "+v"(h[1]), "+v"(h[2]), "+v"(h[3]), "=&v"(d) : "v"(pr[0]), "v"(pr[1]), "v"(ps[0]), "v"(ps[1]), "v"(c));
}
DI void cstepi(float& b0, float& b1, float& b2, float& b3, float& b4, float h0, float h1, float h2, float h3, float h4, const float (&lr)[5], const float (&ls)[5]) {
    asm("v_fmac_f32 %0, %10, %5\n\tv_fmac_f32 %1, %11, %6\n\tv_fmac_f32 %2, %12, %7\n\tv_fmac_f32 %3, %13, %8\n\tv_fmac_f32 %4, %14, %9\n\t"
        "v_fmac_f32_dpp %0, %5, %15" QP "\n\tv_fmac_f32_dpp %1, %6, %16" QP "\n\tv_fmac_f32_dpp %2, %7, %17" QP "\n\tv_fmac_f32_dpp %3, %8, %18" QP "\n\tv_fmac_f32_dpp %4, %9, %19" QP
        : "+v"(b0), "+v"(b1), "+v"(b2), "+v"(b3), "+v"(b4)
        : "v"(h0), "v"(h1), "v"(h2), "v"(h3), "v"(h4), "v"(lr[0]), "v"(lr[1]), "v"(lr[2]), "v"(lr[3]), "v"(lr[4]), "v"(ls[0]), "v"(ls[1]), "v"(ls[2]), "v"(ls[3]), "v"(ls[4]));
}
#define UNDEF8(c) asm volatile("" : "=v"(c[0]), "=v"(c[1]), "=v"(c[2]), "=v"(c[3]), "=v"(c[4]), "=v"(c[5]), "=v"(c[6]), "=v"(c[7]))
#define PIN5(t) asm volatile("s_nop 1" : "+v"(t[0]), "+v"(t[1]), "+v"(t[2]), "+v"(t[3]), "+v"(t[4]))
DI unsigned cvtpk(float lo, float hi) {
    f32x2 v = {lo, hi};
    return __builtin_bit_cast(unsigned, __builtin_convertvector(v, bf16x2v));
}
DI float lo16f(unsigned u) { return __uint_as_float(u << 16); }
DI float hi16f(unsigned u) { return __uint_as_float(u & 0xffff0000u); }
DI float rbf(float v) { return lo16f(cvtpk(v, 0.f)); }
template <int CTRL> DI float dpp(float x) {
    return __int_as_float(__builtin_amdgcn_update_dpp(0, __float_as_int(x), CTRL, 0xF, 0xF, true));
}
DI float rowsum16(float x) {
    x += dpp<0x128>(x); x += dpp<0x124>(x); x += dpp<0x122>(x); x += dpp<0x121>(x);
    return x;
}
DI float xsum32(float x) { auto r = __builtin_amdgcn_permlane32_swap(__float_as_uint(x), __float_as_uint(x), false, false); return __uint_as_float(r[0]) + __uint_as_float(r[1]); }
DI void xsum2(float& a, float& d) {
    auto r = __builtin_amdgcn_permlane16_swap(__float_as_uint(a), __float_as_uint(d), false, false);
    const float s = __uint_as_float(r[0]) + __uint_as_float(r[1]);
    const float t = xsum32(s);
    auto u = __builtin_amdgcn_permlane16_swap(__float_as_uint(t), __float_as_uint(t), false, false);
    a = __uint_as_float(u[0]); d = __uint_as_float(u[1]);
}
DI float xsum2h(float a, float d) {
    auto r = __builtin_amdgcn_permlane16_swap(__float_as_uint(a), __float_as_uint(d), false, false);
    return xsum32(__uint_as_float(r[0]) + __uint_as_float(r[1]));
}
DI float hsum4(f32x4 v) { return (v.x + v.y) + (v.z + v.w); }
typedef __amdgpu_buffer_rsrc_t rsrc_t;
constexpr int SC1 = 16;
DI v4u ld128(rsrc_t rs, unsigned off) { return __builtin_amdgcn_raw_buffer_load_b128(rs, off, 0, SC1); }
DI v4u ld128s(rsrc_t rs, unsigned off, unsigned soff) { return __builtin_amdgcn_raw_buffer_load_b128(rs, off, soff, SC1); }
DI v2u ld64(rsrc_t rs, unsigned off) { return __builtin_amdgcn_raw_buffer_load_b64(rs, off, 0, SC1); }
DI void st128(v4u v, rsrc_t rs, unsigned off) { __builtin_amdgcn_raw_buffer_store_b128(v, rs, off, 0, SC1); }
DI void st64(v2u v, rsrc_t rs, unsigned off) { __builtin_amdgcn_raw_buffer_store_b64(v, rs, off, 0, SC1); }
#define LGKM0() { __builtin_amdgcn_s_waitcnt(0xC07F); asm volatile("" ::: "memory"); }
#define DRAIN() asm volatile("s_waitcnt vmcnt(0)" ::: "memory")
#define ORDER() __builtin_amdgcn_fence(__ATOMIC_ACQUIRE, "wavefront")
#define SCHED() __builtin_amdgcn_sched_barrier(0)
constexpr unsigned kSpinCap = 1u << 17;
#define WAVE_WAIT(...)                                                      \
    {                                                                       \
        unsigned spins_ = dead ? kSpinCap : 0u;                             \
        _Pragma("nounroll") for (;;) {                                      \
            asm volatile("" ::: "memory");                                  \
            bool ok;                                                        \
            __VA_ARGS__;                                                    \
            if (__builtin_amdgcn_ballot_w64(!ok) == 0ull) break;            \
            if (++spins_ > kSpinCap) { dead = true; break; }                \
            __builtin_amdgcn_s_sleep(1);                                    \
        }                                                                   \
        ORDER();                                                            \
    }
#define KPD1 1
#define KPDS 0
#define KPDS0 0
#define KPF0 11
#define KPFX 3
#define KPF1 3
constexpr int PD1 = KPD1, PDS = KPDS, PDS0 = KPDS0;
#define PL(r) "global_load_dwordx2 " r ", %5, %6 sc1\n\t"
#define PS_(r) "s_waitcnt vmcnt(1)\n\tv_cmp_ne_u64 vcc, %7, " r "\n\ts_cbranch_vccz 2f\n\t" PL(r)
template <int D0> DI void pipe_flag(v2u (&t)[4], unsigned off, const void* base, u64 tag, bool& dead) {
    unsigned n = dead ? 0u : kSpinCap;
    asm volatile("s_sleep %8\n\t" PL("%0") "s_sleep %9\n\t" PL("%1")
                 "1:\n\t" PS_("%0") PS_("%1") "s_sub_u32 %4, %4, 1\n\ts_cbranch_scc0 1b\n2:"
                 : "=&v"(t[0]), "=&v"(t[1]), "=&v"(t[2]), "=&v"(t[3]), "+s"(n) : "v"(off), "s"(base), "s"(tag), "i"(D0), "i"(KPF1) : "memory", "vcc", "scc");
    dead = __builtin_amdgcn_readfirstlane(n) == ~0u;
}
#define WAVE_RETRY(OK, ...) { if (__builtin_expect(__builtin_amdgcn_ballot_w64(!(OK)) != 0ull, 0)) WAVE_WAIT({ __VA_ARGS__; ok = OK; }) ORDER(); }
#define STAMP(i)
#define STAMP_DUMP()

#define KLB0 0
#define KLB1 6
DI void site() { asm volatile(""); }
template <int N> DI void skip() { if constexpr (N > 0) asm volatile("s_branch 1f\n\t.rept %c0\n\ts_nop 0\n\t.endr\n1:" ::"i"(N)); }
template <class F, int... I> DI void static_for_impl(std::integer_sequence<int, I...>, F&& f) { (f(std::integral_constant<int, I>{}), ...); }
template <int N, class F> DI void static_for(F&& f) { static_for_impl(std::make_integer_sequence<int, N>{}, f); }
#define IC(n) std::integral_constant<int, (n)>{}
#define LAMBDA(v) [&](auto v) __attribute__((always_inline))
#define PK2 " op_sel:[1,1,0] op_sel_hi:[1,0,1] neg_lo:[1,0,0]"
DI f32x2 cmul(f32x2 a, f32x2 b) {
    f32x2 t;
    asm("v_pk_mul_f32 %0, %1, %2 op_sel_hi:[0,1]\n\ts_nop 0\n\tv_pk_fma_f32 %0, %1, %2, %0" PK2 "\n\ts_nop 0" : "=&v"(t) : "v"(a), "v"(b));
    return t;
}
DI f32x2 cfma(f32x2 a, f32x2 b, f32x2 c) {
    f32x2 t;
    asm("v_pk_fma_f32 %0, %1, %2, %3 op_sel_hi:[0,1,1]\n\ts_nop 0\n\tv_pk_fma_f32 %0, %1, %2, %0" PK2 "\n\ts_nop 0" : "=&v"(t) : "v"(a), "v"(b), "v"(c));
    return t;
}
DI void cfma4(f32x2 (&t)[4], f32x2 a, f32x2 b0, f32x2 b1, f32x2 b2, f32x2 b3, f32x2 c0, f32x2 c1, f32x2 c2, f32x2 c3) {
    asm("v_pk_fma_f32 %0, %4, %5, %9 op_sel_hi:[0,1,1]\n\tv_pk_fma_f32 %1, %4, %6, %10 op_sel_hi:[0,1,1]\n\tv_pk_fma_f32 %2, %4, %7, %11 op_sel_hi:[0,1,1]\n\tv_pk_fma_f32 %3, %4, %8, %12 op_sel_hi:[0,1,1]\n\t"
        "v_pk_fma_f32 %0, %4, %5, %0" PK2 "\n\tv_pk_fma_f32 %1, %4, %6, %1" PK2 "\n\tv_pk_fma_f32 %2, %4, %7, %2" PK2 "\n\tv_pk_fma_f32 %3, %4, %8, %3" PK2 "\n\ts_nop 0"
        : "=&v"(t[0]), "=&v"(t[1]), "=&v"(t[2]), "=&v"(t[3]) : "v"(a), "v"(b0), "v"(b1), "v"(b2), "v"(b3), "v"(c0), "v"(c1), "v"(c2), "v"(c3));
}
DI void gacc2(f32x2& h0, f32x2& h1, f32x2 w0, f32x2 w1, v4u e0, v4u e1) {
    const v2u a0 = {e0.x, e0.y}, b0 = {e0.z, e0.w}, a1 = {e1.x, e1.y}, b1 = {e1.z, e1.w};
    asm("v_pk_fma_f32 %0, %4, %2, %0 op_sel_hi:[0,1,1]\n\tv_pk_fma_f32 %1, %6, %3, %1 op_sel_hi:[0,1,1]\n\t"
        "v_pk_fma_f32 %0, %5, %2, %0 op_sel:[0,1,0] op_sel_hi:[0,0,1] neg_lo:[1,0,0]\n\tv_pk_fma_f32 %1, %7, %3, %1 op_sel:[0,1,0] op_sel_hi:[0,0,1] neg_lo:[1,0,0]"
        : "+v"(h0), "+v"(h1) : "v"(w0), "v"(w1), "v"(a0), "v"(b0), "v"(a1), "v"(b1));
}
DI s16x4 trread(char* smem, int byteoff) {
    return __builtin_amdgcn_ds_read_tr16_b64_v4i16((LAS s16x4*)(smem + byteoff));
}

#define CV_LOAD_A(A, a, rr_, hf) { const unsigned* ap_ = (const unsigned*)(smem + ab[(a) >> 1] + (((a) & 1) ? 0 : 512) + 64 * (7 - (rr_)) + 8 * (hf)); A[a][2 * (hf)] = ap_[0]; A[a][2 * (hf) + 1] = ap_[1]; }
#define CV_LOAD_B(B, e, rr_) { B[e] = *(const v4u*)(smem + bb + 64 * (7 - (rr_)) + 512 * (e)); }
constexpr int cv_pair(int lo, int hi, int n) {
    for (int a = 0; a < 8; ++a)
        for (int e = 0; e < 8; ++e)
            if (a + e >= lo && a + e < hi && n-- == 0) return 8 * a + e;
    return -1;
}
template <int R, int PF, bool OWN, class F>
DI void conv_round(f32x4 (&ca)[8], v4u (&A)[8], const v4u (&B)[8], v4u (&An)[8], v4u (&Bn)[8], const char* smem, const int (&ab)[4], int bb, F&& fill) {
    static_for<(R == 7 ? KG0 : 36)>(LAMBDA(g_) {
        constexpr int g = g_.value, a = cv_pair(0, 8, g) / 8, e = cv_pair(0, 8, g) % 8;
        mf(ca[a + e], A[a], B[e]);
        SCHED();
        if constexpr (OWN && g < 12) { CV_LOAD_A(A, 2 + g / 2, R, g & 1) SCHED(); }
        if constexpr (g < 4 * PF && (g & 3) != 3) { if constexpr ((g & 3) == 2) CV_LOAD_B(Bn, g >> 2, R + 1) else CV_LOAD_A(An, g >> 2, R + 1, g & 1) SCHED(); }
        site();
        if constexpr (R == 7) SCHED();
        if constexpr ((g & 3) == 3 && g < 28) { fill(std::integral_constant<int, 7 * R + g / 4>{}); SCHED(); }
    });
}
template <int R0, int R1, class F, class M>
DI void conv_part(f32x4 (&ca)[8], const char* smem, const int (&ab)[4], int bb, F&& fill, M&& mid, v4u (&A)[2][8], v4u (&B)[2][8]) {
    if constexpr (R0 > 0) { CV_LOAD_A(A[0], 0, R0, 0) CV_LOAD_A(A[0], 0, R0, 1) SCHED(); }
    static_for<8>(LAMBDA(a_) { constexpr int a = a_.value; CV_LOAD_B(B[0], a, R0) SCHED(); });
    if constexpr (R0 > 0) { CV_LOAD_A(A[0], 1, R0, 0) CV_LOAD_A(A[0], 1, R0, 1) SCHED(); }
    else {
        CV_LOAD_A(A[0], 0, 0, 0) CV_LOAD_A(A[0], 0, 0, 1) SCHED();
        const v4u ad = mid();
        SCHED();
        static_for<8>(LAMBDA(e_) {
            constexpr int e = e_.value;
            mf0(ca[e], ad, B[0][e]); SCHED();
            if constexpr (e < 7) { CV_LOAD_A(A[0], e + 1, 0, 0) CV_LOAD_A(A[0], e + 1, 0, 1) SCHED(); }
        });
    }
    static_for<R1 - R0>(LAMBDA(r_) {
        constexpr int r = r_.value;
        conv_round<R0 + r, (R0 + r + 1 < R1 ? 8 : 0), (r == 0 && R0 > 0)>(ca, A[r & 1], B[r & 1], A[(r & 1) ^ 1], B[(r & 1) ^ 1], smem, ab, bb, fill);
    });
}
template <int IMM> DI void gld(v4u& dst, unsigned voff, const void* sbase) {
    asm volatile("global_load_dwordx4 %0, %1, %2 offset:%3" : "=v"(dst) : "v"(voff), "s"(sbase), "i"(IMM));
}
#define OUTP 16
#define NTM 31
template <int IMM, int BIT> DI void gldn(v4u& dst, unsigned voff, const void* sbase) {
    if constexpr ((NTM >> BIT) & 1) asm volatile("global_load_dwordx4 %0, %1, %2 offset:%3 nt" : "=v"(dst) : "v"(voff), "s"(sbase), "i"(IMM));
    else gld<IMM>(dst, voff, sbase);
}
DI void gldu16(unsigned& dst, unsigned voff, const void* sbase) { asm volatile("global_load_ushort %0, %1, %2" : "=v"(dst) : "v"(voff), "s"(sbase)); }
template <int IMM> DI void gld64(v4u& dst, const void* a) {
    if constexpr ((NTM >> 5) & 1) asm volatile("global_load_dwordx4 %0, %1, off offset:%2 nt" : "=v"(dst) : "v"(a), "i"(IMM));
    else asm volatile("global_load_dwordx4 %0, %1, off offset:%2" : "=v"(dst) : "v"(a), "i"(IMM));
}
template <int V> DI void SETM0(unsigned ldsbase) { asm volatile("s_add_u32 m0, %0, %1" ::"s"(ldsbase), "i"(V)); }
template <int GOFF> DI void GLD0(unsigned voff, const char* sbase) { asm volatile("global_load_lds_dwordx4 %0, %1 offset:%2" ::"v"(voff), "s"(sbase), "i"(GOFF) : "memory"); }
template <int GOFF, int V> DI void GLD1(unsigned voff, const char* sbase, unsigned ldsbase) {
    asm volatile("global_load_lds_dwordx4 %0, %1 offset:%3\n\ts_add_u32 m0, %2, %4" ::"v"(voff), "s"(sbase), "s"(ldsbase), "i"(GOFF), "i"(V) : "memory");
}
constexpr int dma_goff(int it) { return it < 40 ? (it >> 2) * 128 : ((it - 100) / 5) * 256; }
constexpr int dma_loff(int it) { return it < 40 ? L_XR + ((it >> 2) % 3) * 4096 + (it & 3) * 1024 : ((it - 100) % 5) * 20480 + ((it - 100) / 5) * 2048; }
#define KFD0 10
#define KNF 13
#define KNW 11
#define KW2S 17
#define KW2M 0x4A
#define KFWP 14
constexpr int kW2S = KW2S, kW2M = KW2M;
constexpr int NW2P = (20 - kW2S) * __builtin_popcount(kW2M);
constexpr int P1_PRE = 34;
constexpr unsigned WRUN = RUN / 2;
constexpr int p1_pre(int i) { return i < 5 ? 100 + i : i < 13 ? i - 5 : i < 18 ? 105 + (i - 13) : i < 22 ? 8 + (i - 18) : i < 27 ? 110 + (i - 22) : 400; }
constexpr int p1_item(int s, int g) {
    if (s == 7 && g == 0) return 324;
    if (g == 9) return s < 10 ? 115 + s : s == 11 ? 202 : s == 12 ? 203 : -1;
    if (s == KFWP && g == 1) return 208;
    if (s >= kW2S && ((kW2M >> g) & 1)) return 500 + (s - kW2S) * __builtin_popcount(kW2M) + __builtin_popcount(kW2M & ((1 << g) - 1));
    if ((s & 1) == 0 && s <= 12) return g >= 5 ? 4 * (s / 2 + 3) + (g - 5) : -1;
    if (s == 14 && g == 5) return 207;
    return -1;
}
constexpr int p1_cnt(int s, int g) {
    int n = P1_PRE;
    for (int t = 0; t <= s; ++t)
        for (int u = 0; u < 10; ++u) {
            if (t == s && u >= g) break;
            if (p1_item(t, u) >= 0 && p1_item(t, u) != 319) ++n;
        }
    return n;
}
constexpr int p1_pos(int code) {
    for (int i = 0; i < P1_PRE; ++i) if (p1_pre(i) == code) return i;
    for (int t = 0; t < 20; ++t)
        for (int u = 0; u < 10; ++u) if (p1_item(t, u) == code) return p1_cnt(t, u);
    return -1000;
}
constexpr int p1_cap(int s, int g) {
    int u = (s < 19 && g < 5) + (g == 9 && s < 19);
    if ((s & 1) && s < 19 && g >= 5 && g < 9) u += 1 + (g == 5);
    if ((s & 1) == 0 && g == 4) u += 1;
    const int it = p1_item(s, g), nt = g < 9 ? p1_item(s, g + 1) : -1;
    u += (it >= 0 && it < 500 && it != 208) + (nt >= 0 && nt < 200);
    if ((s == 18 && g == 1) || (s == 19 && g == 2) || (g == 9 && ((s & 3) == 2 || s == 17))) u += 2;
    return u < 2 ? 2 - u : 0;
}
constexpr int p1_slot(int s0, int s, int g, int gmax) {
    int n = 0;
    for (int t = s0; t <= s; ++t)
        for (int u = 0; u < gmax && !(t == s && u >= g); ++u) n += p1_cap(t, u);
    return n;
}
constexpr int p1_lim(int n) { return n > 63 ? 63 : n; }
template <int IMM> DI void gld4s(v4u& dst, unsigned voff, const void* sbase) { asm volatile("global_load_dwordx4 %0, %1, %2 offset:%3 sc1" : "=v"(dst) : "v"(voff), "s"(sbase), "i"(IMM)); }
template <int IMM> DI void gld2s(v2u& dst, unsigned voff, const void* sbase) { asm volatile("global_load_dwordx2 %0, %1, %2 offset:%3 sc1" : "=v"(dst) : "v"(voff), "s"(sbase), "i"(IMM)); }
template <class T> DI void pin(T& v) { asm volatile("" : "+v"(v)); }
DI void mul2(float& a, float& b, float x, float y, float s) { asm volatile("v_mul_f32 %0, %2, %4\n\tv_mul_f32 %1, %3, %4" : "=&v"(a), "=&v"(b) : "v"(x), "v"(y), "v"(s)); }
DI void rot1(unsigned& a, unsigned x) { asm volatile("v_alignbit_b32 %0, %1, %1, 16" : "=v"(a) : "v"(x)); }
DI void mix1(unsigned& a, unsigned x, unsigned y) { asm volatile("v_bfi_b32 %0, %3, %1, %2" : "=v"(a) : "v"(x), "v"(y), "s"(0xffffu)); }
DI void ror1(unsigned& o, unsigned prev) { asm volatile("v_mov_b32_dpp %0, %1 wave_ror:1 row_mask:0xf bank_mask:0xf" : "=v"(o) : "v"(prev)); }
DI void shr1b(unsigned& o, unsigned x) { asm volatile("v_mov_b32_dpp %0, %1 wave_shr:1 row_mask:0xf bank_mask:0xf" : "+v"(o) : "v"(x)); }
DI void xor1(float& a, unsigned x, unsigned s) { asm volatile("v_xor_b32 %0, %1, %2" : "=v"(a) : "v"(x), "v"(s)); }
template <int G> DI void bc1(float& a, unsigned x) { asm volatile("v_mov_b32_dpp %0, %1 row_newbcast:%2 row_mask:0xf bank_mask:0xf" : "=v"(a) : "v"(x), "i"(G)); }
template <int G> DI void mulb(float& o, unsigned x, float y) { asm volatile("v_mul_f32_dpp %0, %1, %2 row_newbcast:%3 row_mask:0xf bank_mask:0xf" : "=v"(o) : "v"(x), "v"(y), "i"(G)); }
template <int N> DI void lgkm() { __builtin_amdgcn_s_waitcnt(0xC07F | (N << 8)); asm volatile("" ::: "memory"); }
DI void unp2(float& a, float& b, unsigned x) { asm volatile("v_lshlrev_b32 %0, 16, %2\n\tv_and_b32 %1, 0xffff0000, %2" : "=&v"(a), "=&v"(b) : "v"(x)); }
#define GL(a) "global_load_dwordx4 a[" #a ":" #a "+3], %7, %8 sc1\n\tglobal_load_dwordx4 a[" #a "+4:" #a "+7], %7, %8 offset:256 sc1\n\t"
#define AR(d, a, o) "v_accvgpr_read_b32 " d ", a[" #a "+" #o "]\n\t"
#define GS_(a) "s_waitcnt vmcnt(4)\n\t" AR("%2", a, 1) AR("%3", a, 3) AR("%4", a, 5) AR("%5", a, 7) AR("%0", a, 0) AR("%1", a, 2)                              \
    "v_xor_b32 %2, %9, %2\n\tv_bitop3_b32 %4, %4, %9, %2 bitop3:0xbe\n\tv_bitop3_b32 %4, %3, %9, %4 bitop3:0xbe\n\tv_bitop3_b32 %4, %5, %9, %4 bitop3:0xbe\n\t" \
    AR("%2", a, 4) AR("%3", a, 6) "v_cmp_ne_u32 vcc, 0, %4\n\ts_cbranch_vccz 2f\n\t" GL(a)
template <int A> DI void gran_req(unsigned off, const void* base) {
    asm volatile("global_load_dwordx4 a[%c2:%c2+3], %0, %1 sc1\n\tglobal_load_dwordx4 a[%c2+4:%c2+7], %0, %1 offset:256 sc1" ::"v"(off), "s"(base), "i"(A));
}
template <int D0, int NP> DI void pipe_gran(float& a, float& d, unsigned off, const void* base, unsigned tag, bool& dead) {
    unsigned n = dead ? 0u : kSpinCap, y, z;
    float a1, d1;
    if constexpr (NP < 1) { __builtin_amdgcn_s_sleep(D0); gran_req<32>(off, base); __builtin_amdgcn_s_sleep(PD1); }
    if constexpr (NP < 2) { gran_req<40>(off, base); __builtin_amdgcn_s_sleep(PD1); }
    asm volatile(GL(48)
                 "1:\n\t" GS_(32) GS_(40) GS_(48) "s_sub_u32 %6, %6, 1\n\ts_cbranch_scc0 1b\n2:"
                 : "=&v"(a), "=&v"(d), "=&v"(a1), "=&v"(d1), "=&v"(y), "=&v"(z), "+s"(n) : "v"(off), "s"(base), "s"(tag) : "memory", "vcc", "scc");
    dead = __builtin_amdgcn_readfirstlane(n) == ~0u;
    a += a1; d += d1;
}
DI void fma2(float& a, float& b, float x, float y, float s, float t, float u, float v) {
    asm volatile("v_fma_f32 %0, %2, %4, %6\n\tv_fma_f32 %1, %3, %5, %7" : "=&v"(a), "=&v"(b) : "v"(x), "v"(y), "v"(s), "v"(t), "v"(u), "v"(v));
}
DI void cvt2(unsigned& a, unsigned& b, f32x4 v) { asm volatile("v_cvt_pk_bf16_f32 %0, %2, %3\n\tv_cvt_pk_bf16_f32 %1, %4, %5" : "=&v"(a), "=&v"(b) : "v"(v.x), "v"(v.y), "v"(v.z), "v"(v.w)); }
DI void swp2n(unsigned& a, unsigned& b, unsigned& c, unsigned& d) { asm volatile("v_permlane16_swap_b32 %0, %1\n\tv_permlane16_swap_b32 %2, %3" : "+v"(a), "+v"(b), "+v"(c), "+v"(d)); }
template <int G> DI void bc2b(float& a, float& b, float x, float y) {
    asm volatile("v_mov_b32_dpp %0, %2 row_newbcast:%4 row_mask:0xf bank_mask:0xf\n\tv_mov_b32_dpp %1, %3 row_newbcast:%4 row_mask:0xf bank_mask:0xf" : "=&v"(a), "=&v"(b) : "v"(x), "v"(y), "i"(G));
}
template <class T> DI void pins(T& v) { asm volatile("" : "+s"(v)); }
template <int I> DI void gld_w2(unsigned voff, const void* sbase) {
    asm volatile("global_load_dwordx4 " WA(2) ", %0, %1 offset:%3 sc1" ::"v"(voff), "s"(sbase), "i"(W2R + 4 * I), "i"((I % 5) * 1024 - 2048));
}
DI void undef4(v4u (&x)[4]) { asm volatile("" : "=v"(x[0]), "=v"(x[1]), "=v"(x[2]), "=v"(x[3])); }
DI void wait1(v2u& v) { asm volatile("s_waitcnt vmcnt(0)" : "+v"(v)::"memory"); }
DI void ld_ur(unsigned o, const void* base) {
    asm volatile("global_load_dwordx4 a[40:40+3], %0, %1 sc1\n\tglobal_load_dwordx4 a[44:44+3], %0, %1 offset:1024 sc1\n\t"
                 "global_load_dwordx4 a[48:48+3], %0, %1 offset:2048 sc1\n\tglobal_load_dwordx4 a[52:52+3], %0, %1 offset:3072 sc1" ::"v"(o), "s"(base));
}
DI void st_ur(unsigned a) {
    asm volatile("s_waitcnt vmcnt(%1)\n\tds_write_b128 %0, a[40:40+3] offset:480\n\tds_write_b128 %0, a[44:44+3] offset:1504\n\t"
                 "ds_write_b128 %0, a[48:48+3] offset:2528\n\tds_write_b128 %0, a[52:52+3] offset:3552" ::"v"(a), "i"(2 * __builtin_popcount(kW2M)) : "memory");
}
template <int A, int IMM> DI void lda4(unsigned o, const void* base) { asm volatile("global_load_dwordx4 a[%c2:%c2+3], %0, %1 offset:%3" ::"v"(o), "s"(base), "i"(A), "i"(IMM)); }
template <int A, int IMM> DI void lda2(unsigned o, const void* base) { asm volatile("global_load_dwordx2 a[%c2:%c2+1], %0, %1 offset:%3" ::"v"(o), "s"(base), "i"(A), "i"(IMM)); }
template <int A> DI void rda(unsigned& v) { asm volatile("v_accvgpr_read_b32 %0, a[%c1+0]" : "=v"(v) : "i"(A)); }
DI void poll1(unsigned a, const void* base) { asm volatile("global_load_dwordx2 a[36:36+1], %0, %1 sc1" ::"v"(a), "s"(base)); }
DI void poll2(unsigned a, unsigned b, const void* base) { asm volatile("global_load_dwordx2 a[36:36+1], %0, %2 sc1\n\tglobal_load_dwordx2 a[36+2:36+3], %1, %2 sc1" ::"v"(a), "v"(b), "s"(base)); }
DI void touch_(unsigned a, const void* base) { asm volatile("global_load_dword a[32+1], %0, %1" ::"v"(a), "s"(base)); }
template <int N> DI void wait_st(v2u& a, v4u& b) { asm volatile("s_waitcnt vmcnt(%2)" : "+v"(a), "+v"(b) : "i"(N) : "memory"); }
DI void undef3(v4u (&x)[3]) { asm volatile("" : "=v"(x[0]), "=v"(x[1]), "=v"(x[2])); }
template <int IMM> DI void gld4t(v4u& dst, unsigned voff, const void* sbase) { asm volatile("global_load_dwordx4 %0, %1, %2 offset:%3 sc1" : "+v"(dst) : "v"(voff), "s"(sbase), "i"(IMM)); }
template <int N> DI void vm_wait() { asm volatile("s_waitcnt vmcnt(%0)" ::"i"(N) : "memory"); }
DI float dot2a(unsigned a, unsigned b, float c) {
    return __builtin_amdgcn_fdot2_f32_bf16(__builtin_bit_cast(bf16x2v, a), __builtin_bit_cast(bf16x2v, b), c, false);
}
DI void disc(unsigned l0, unsigned l1, unsigned l2, f32x2& lam, f32x2& co) {
    const float dt = rbf(__builtin_amdgcn_exp2f(lo16f(l2) * 1.44269504088896f));
    const float lre = lo16f(l0), lim = lo16f(l1);
    const float e_ = __builtin_amdgcn_exp2f(dt * lre * 1.44269504088896f);
    const float rv = __builtin_amdgcn_fractf(dt * lim * 0.159154943091895f);
    lam = f32x2{e_ * __builtin_amdgcn_cosf(rv), e_ * __builtin_amdgcn_sinf(rv)};
    const float nr = lam.x - 1.0f, ni = lam.y, id = __builtin_amdgcn_rcpf(lre * lre + lim * lim);
    co = f32x2{(nr * lre + ni * lim) * id, (ni * lre - nr * lim) * id};
}
DI f32x4 unpk4(v2u u) { return f32x4{lo16f(u.x), hi16f(u.x), lo16f(u.y), hi16f(u.y)}; }
#define D2 "v_dot2_f32_bf16 "
#define CP "v_cvt_pk_bf16_f32 "
DI void d2x4(unsigned* o, unsigned a0, unsigned a1, unsigned a2, unsigned a3, unsigned b0, unsigned b1, unsigned b2, unsigned b3) {
    asm volatile(D2 "%0, %4, %8, 0\n\t" D2 "%1, %5, %9, 0\n\t" D2 "%2, %6, %10, 0\n\t" D2 "%3, %7, %11, 0"
                 : "=&v"(o[0]), "=&v"(o[1]), "=&v"(o[2]), "=&v"(o[3]) : "v"(a0), "v"(a1), "v"(a2), "v"(a3), "v"(b0), "v"(b1), "v"(b2), "v"(b3));
}

__global__ __launch_bounds__(256, 1) __attribute__((aligned(4096))) void k_main(Params p) {
    __shared__ __attribute__((aligned(16))) char smem[LDS_BYTES];
    p.ws += (0u - (unsigned)(size_t)p.ws) & 0x1FFFFFu;
    const rsrc_t rs = __builtin_amdgcn_make_buffer_rsrc((void*)p.ws, 0, WS_BYTES, 0x00020000);
    const int w0 = __builtin_amdgcn_readfirstlane(threadIdx.x >> 6), b0 = blockIdx.x;
    unsigned pol, sdo;
    const char* wl;
    rsrc_t rl;
    const rsrc_t ro = __builtin_amdgcn_make_buffer_rsrc((void*)p.out, 0, S_ * D_ * 4, 0x00020000);
    bool dead = false;
    asm volatile("" ::: "a255");
    float SW[5];
    f32x4 xin[5];
    v2u gt[5], dsk[5];
    f32x4 acc[2][5];
    f32x4 rr[2];
    float tt[2][5][4];
    float lr[5], ls[5], lqr[5], lqs[5];
    f32x2 l8, l32, l128;
    bool kearly;
    unsigned sdq;
    int sgn_, ab[4], bb, zpa, uwa, spa, tbw, tbr, cwa, spl, dst[3];
    unsigned sgo, sgs, glo, gso, fgo, fgo1;
    int srca;
    unsigned adm, dso, w2o, wro, fwo, xso, fho1, fho, hso, eo, es, ho[4];
    bool cv0;
    int mt1_;
    auto addrs = [&](auto layer_, int lane, int w, int b) __attribute__((always_inline)) {
        constexpr int layer = layer_.value;
        const int c = lane & 15, q = lane >> 4;
        TILES()
        static_assert(l_sg(0, 0) == SC + 0xA900 && l_sg(0, 1) == SC + 0xB400 && l_sg(0, 2) == SC + 0xCE00 && l_sg(0, 3) == SC + 0xDC00, "");
        sgn_ = (layer ? l_sg(1, 0) + w * INBOX : SC + (int)((0xDC00CE00B400A900ull >> (16 * w)) & 0xffff)) + q * 320 + c * 4;
        const int pl = lane < 40 ? lane : 39, tbb = l_tb(layer, 0) + w * (layer ? INBOX : 5120);
        spl = SC + pl * 8;
        eo = OFF_EG + (unsigned)((layer * 2 + (nt1 >> 3)) * 16 * RUN + (nt1 & 7) * 640 + pl * 16);
        es = eo + (unsigned)mt1 * RUN;
        spa = l_rs(layer, 0) + (2 * (mt2 & 1) + (w >> 1)) * (layer ? INBOX : 256) + (2 * (w & 1) + (c >> 3)) * 64 + (c & 7) * 4;
        tbw = tbb + c * 64 + ((q ^ ((c >> 1) & 3)) * 16);
        tbr = tbb + (2 * (c >> 2) + (q & 1)) * 64 + (((2 * (q >> 1) + ((c >> 1) & 1)) ^ (c >> 2)) * 16) + (c & 1) * 8;
        hso = OFF_HF + (unsigned)((layer * 4 + ((mt1 >> 1) & 3)) * PG + ((8 * (mt1 & 1) + 2 * w + (q >> 1)) * 8 + (nt1 >> 1)) * RUN + (10 * (nt1 & 1) + 5 * (q & 1)) * 256 + c * 16);
        cwa = L_CW + c * 4;
        fho1 = lane ? kNone : OFF_FH + (unsigned)((2 * (mt1 & 7) + (w >> 1)) * RUN + (layer * 32 + nt1 * 2 + (w & 1)) * 128);
        fho = OFF_FH + (unsigned)((mt2 & 15) * RUN + (layer * 4 + w) * 1024 + (lane >> 3) * 128);
        pin(sgn_); pin(spl); pin(eo); pin(es); pin(spa); pin(tbw); pin(tbr); pin(hso); pin(cwa); pin(fho1); pin(fho);
        const unsigned hb_ = OFF_HF + (unsigned)((layer * 4 + ((mt1 >> 1) & 3)) * PG + (32 * (mt2 & 3) + 2 * w) * RUN);
#pragma unroll
        for (int m = 0; m < 4; ++m) { ho[m] = hb_ + (unsigned)(((m + 1 + w) & 3) * 8 * RUN + lane * 16); pin(ho[m]); }
#pragma unroll
        for (int m = 0; m < 3; ++m) { dst[m] = SC + ((w + 1 + m) & 3) * INBOX + m * 5120 + lane * 16; pin(dst[m]); }
        mt1_ = mt1;
        sgo = OFF_SG + (unsigned)((layer * 16 + (mt2 & 15)) * RUN + w * 2048 + c * 16);
        sgs = q < 2 ? sgo + (unsigned)(nt2 * 256 + q * 8) : kNone;
        sgo += (unsigned)(q * 512);
        pin(sgo); pin(sgs);
        if constexpr (layer == 0) {
            srca = SC + w * INBOX + lane * 16;
            const unsigned gb_ = OFF_GT + (unsigned)((nt2 >> 2) * PG + (nt2 & 3) * 32 * RUN);
            const int cl = i32 + 32 * w;
            glo = gb_ + (unsigned)((lane >> 3) * RUN + ((lane >> 1) & 3) * 2048 + (64 * (mt2 & 15) + 16 * w) * 2 + (lane & 1) * 16);
            gso = cv0 ? gb_ + (unsigned)((cl >> 2) * RUN + (cl & 3) * 2048 + (q & 1) * 512 + c * 32 + (q >> 1) * 16) : kNone;
            fgo = gb_ + (unsigned)(20 * RUN + (lane >> 1) * 128);
            fgo1 = w == 0 ? gb_ + (unsigned)(20 * RUN + i32 * 128 + (lane >> 5) * 8192) : kNone;
            pin(srca); pin(glo); pin(gso); pin(fgo); pin(fgo1);
            const int r0 = SC + (w < 3 ? w : 0) * REGSZ, y0 = 2031 + 8 * q - c, l34 = lane < 33 ? lane : 33;
#pragma unroll
            for (int i = 0; i < 4; ++i) { ab[i] = r0 + ((y0 & 1) ? CV_O : CV_E) + ((y0 + 1) >> 1) * 4 - 448 - 512 - 1024 * i; pin(ab[i]); }
            bb = r0 + 32 * c + 16 * q;
            zpa = w < 3 ? r0 + l34 * 16 + (l34 >= 30 ? CV_E + 4096 - 480 : 0) + (l34 >= 32 ? CV_O + 4100 - 32 - CV_E - 4096 : 0) : SC + 47360 + lane * 16;
            uwa = r0 + lane * 16;
            xso = (unsigned)((64 * mt2 + 16 * w + c) * D_ + 80 * nt2 + 16 * (q & 1) + 8 * (q >> 1)) * 2u;
            dso = (unsigned)(160 * nt2 + 8 * q);
            adm = (unsigned)((c & 1) * 16 + (q == 2 + (c >> 3) ? 256 << ((c & 7) >> 1) : 0));
            pin(adm);
            pin(bb); pin(zpa); pin(uwa); pin(xso); pin(dso);
        }
        SCHED();
    };
    unsigned tag0, tag1;
#define FLAG(off) { if (lane == 0) st64(v2u{tag0, tag1}, rs, off); }
#define ISTAG(f) (((f.x ^ tag0) | (f.y ^ tag1)) == 0u)
#define KW_REQ() { if (cv) ld_ur(wro, p.ws + OFF_KVT); static_for<NW2P>(LAMBDA(i_) { gld_w2<i_.value>(w2o, p.ws + 2048 + (i_.value / 5) * WRUN); }); }
#define FW_OK() { if (__builtin_expect(!kearly, 0)) { WAVE_WAIT({ const v2u fw_ = ld64(rs, fwo); ok = ISTAG(fw_); }) KW_REQ() } }
#define TT_GAP(n) { constexpr int mb_ = (n) / 10, nb_ = ((n) % 10) / 2, e_ = ((n) & 1) * 2; \
        mul2(tt[mb_][nb_][e_], tt[mb_][nb_][e_ + 1], rm_[4 * mb_ + e_], rm_[4 * mb_ + e_ + 1], SW[nb_]); }
    {
        const int tid = threadIdx.x, lane = tid & 63, w = w0, b = b0;
        const int c = lane & 15, q = lane >> 4;
        TILES()
        STAMP(0)
        const unsigned lds0 = (unsigned)(size_t)(LAS char*)smem;
        v2u nonce;
        {
            u64 pc;
            int len, len2;
            asm volatile("s_getpc_b64 %0\nkpf_begin:\n\ts_mov_b32 %1, kpf_end-kpf_begin\n\ts_mov_b32 %2, kpf_end2-kpf_begin" : "=s"(pc), "=s"(len), "=s"(len2));
            len = len > len2 ? len : len2;
            const int o0 = (((j & 15) * 4 + w) + 64 * (lane >> 3)) * 128;
            touch_((unsigned)(o0 < len ? o0 : 0), (const void*)pc);
        }
        const bool cv = w + (i32 >> 4) < 3;
        const int chn = 80 * nt2 + i32 + 32 * (cv ? w : 0);
        const bool kw = w - (i32 >> 4) < 2;
        const int kp = 16 * nt2 + mt1, ku = 5 * kp + (i32 < 16 ? w & 1 : 2 + (w < 3 ? w : 0)), kc = ku >> 6, kr = ku & 63;
        const int lc = cv ? lane : 8;
        int kwi = 2 + (i32 >> 4) - w;
        v4u ax[15], sa[5], cr[2], wr[4];
        unsigned uL[3];
        const int cu_ = (b < 200 ? b : 0) * 4 + w, cdb = cu_ / 20, ccg = cu_ % 20;
        {
            int ca_ = 32 * ccg, cb_ = 40 * nt1 + 10 * w - 32;
            asm volatile("" : "+s"(ca_), "+s"(cb_), "+s"(kwi));
            unsigned cpo = (unsigned)((lane < 32 ? ca_ : cb_) + (lane < 41 ? lane : 41)) * 2u;
            unsigned ksh = (unsigned)(64 * kc + 8 * (lane & 7)) * 2u, ko = (unsigned)(32 * kr + 4 * (lane >> 3)) * 1280u + ksh, kh = ko - 3840u;
            ksh *= 4u;
            pin(cpo); pin(ksh); pin(ko); pin(kh);
            SCHED();
            gldu16(uL[0], cpo, p.Lre); gldu16(uL[1], cpo, p.Lim); gldu16(uL[2], cpo, p.logdt);
            const v4u rk = {(unsigned)(u64)p.k, (unsigned)((u64)p.k >> 32) & 0xffffu, S_ * D_ * 2u, 0x00020000u};
#define L4(d, v, s_, o, f) "global_load_dwordx4 %" #d ", %" #v ", %" #s_ " offset:" #o f "\n\t"
#define B4(d, o) "buffer_load_dwordx4 %" #d ", %17, %21, 0 offen offset:" #o " nt\n\t"
            asm volatile("s_cmp_lt_i32 %22, 1\n\ts_cbranch_scc1 Lrole2\n\t"
                         L4(3, 15, 18, 0, " nt") L4(4, 15, 18, 1280, " nt") L4(5, 15, 18, 2560, " nt") L4(6, 15, 18, 3840, " nt")
                         L4(7, 15, 20, 0, " nt") L4(8, 15, 20, 1280, " nt") L4(9, 15, 20, 2560, " nt") L4(10, 15, 20, 3840, " nt")
                         B4(0, 0) B4(1, 1280) B4(2, 2560) L4(11, 16, 19, 0, "") L4(12, 16, 19, 16, "") L4(13, 16, 19, 32, "") L4(14, 16, 19, 48, "")
                         "\nLrole2:"
                         : "=&v"(ax[0]), "=&v"(ax[1]), "=&v"(ax[2]), "=&v"(ax[3]), "=&v"(ax[4]), "=&v"(ax[5]), "=&v"(ax[6]), "=&v"(ax[7]), "=&v"(ax[8]), "=&v"(ax[9]),
                           "=&v"(ax[10]), "=&v"(ax[11]), "=&v"(ax[12]), "=&v"(ax[13]), "=&v"(ax[14])
                         : "v"(ko), "v"(ksh), "v"(kh), "s"(p.k), "s"(p.shk), "s"(p.v), "s"(rk), "s"(kwi) : "scc");
            SCHED();
        }
        const char* ssrc;
        {
            u64 pa = (u64)(p.x0 + (128 * mt1 + 8 * nt1 + 2 * w) * D_), pb = (u64)(((mt1 & 1) ? p.Bim : p.Bre) + (40 * nt1 + (mt1 >> 1)) * D_);
            asm volatile("" : "+s"(pa), "+s"(pb));
            const int nbx = 2 * w + q - 2, r = q < 2 ? q : 8 * (nbx < 5 ? nbx : 4);
            ssrc = (const char*)(q < 2 ? pa : pb) + (unsigned)(r * 1280 + c * 16);
            pin(ssrc);
        }
        SCHED();
        STAMP(22)
        const int row8 = lane >> 3;
        const unsigned wvo = (unsigned)(40 * nt1 + row8) * 1280u + (unsigned)(((lane & 7) ^ ((w & 1) | ((row8 >> 1) << 1))) * 16 + (w >> 1) * 128);
        const char* const wbase = (const char*)((w & 1) ? p.Bim : p.Bre);
        const unsigned w1l = lds0 + (unsigned)(((w & 1) * 10 + (w >> 1)) * 1024);
        const unsigned xvo0 = (unsigned)(8 * (row8 >> 1) + 2 * (row8 & 1)) * 1280u + (unsigned)(((lane & 7) ^ ((row8 >> 1) << 1)) * 16);
        const unsigned xvo1 = (xvo0 ^ 16u) + 1280u;
        const char* const xbase = (const char*)p.x0 + (size_t)(128 * mt1 + 32 * w) * 1280;
        const unsigned xl = lds0 + (unsigned)(w * REGSZ);
        unsigned xv[4] = {xvo0, xvo1, xvo0 + 5120u, xvo1 + 5120u}, wv[5];
#pragma unroll
        for (int i = 0; i < 5; ++i) { wv[i] = wvo + i * 10240u; pin(wv[i]); if (i < 4) pin(xv[i]); }
#define DMA_M0(it) SETM0<dma_loff(it) - dma_goff(it)>((it) < 40 ? xl : w1l)
#define DMA_LD1(it, nx_) GLD1<dma_goff(it), dma_loff(nx_) - dma_goff(nx_)>((it) < 40 ? xv[(it) & 3] : wv[(it) < 40 ? 0 : ((it) - 100) % 5], (it) < 40 ? xbase : wbase, (nx_) < 40 ? xl : w1l)
#define DMA_LD(it) GLD0<dma_goff(it)>((it) < 40 ? xv[(it) & 3] : wv[(it) < 40 ? 0 : ((it) - 100) % 5], (it) < 40 ? xbase : wbase)
        const int swz = (c & 1) | ((c >> 2) << 1);
        int w1lane0 = (c & 1) * 10240 + (c >> 1) * 128 + ((q ^ swz) * 16);
        int w1lane1 = (c & 1) * 10240 + (c >> 1) * 128 + (((4 + q) ^ swz) * 16);
        int xlane0 = L_XR + w * REGSZ + (c & 1) * 1024 + (c >> 1) * 128 + ((q ^ swz) * 16);
        int xlane1 = L_XR + w * REGSZ + (c & 1) * 1024 + (c >> 1) * 128 + (((4 + q) ^ swz) * 16);
        unsigned cvo = (unsigned)((16 * cdb + c) * D_ + 32 * ccg + 8 * q) * 2u, for0, for1;
        {
            const int G = 4 * ccg + q, ch = 10 * (G / 5) + G % 5, ci = ch + 5;
            const unsigned fo = OFF_W2F + (unsigned)((cdb / 10) * PG + ((cdb / 5) & 1) * 64 * RUN + (cdb % 5) * 1024 + c * 16);
            for0 = fo + (unsigned)((ch >> 2) * WRUN + (ch & 3) * 256); for1 = fo + (unsigned)((ci >> 2) * WRUN + (ci & 3) * 256);
        }
        unsigned sto = OFF_ST + (unsigned)((mt1 * 16 + 4 * w + q) * 256 + c * 8), swo = (lane & 3) == 3 ? OFF_CV + (unsigned)((lane >> 2) * 128) : OFF_ST + (unsigned)(((lane >> 2) * 16 + nt1) * 256 + 128 + (lane & 3) * 16);
        unsigned kvo = OFF_KVT + (unsigned)((64 * kc + 8 * (lane >> 3) + ((lane >> 2) & 1)) * 4096 + (32 * kr + 8 * (lane & 3)) * 2);
        int kbw = L_KB + w * 1024 + (lane & 7) * 128 + (((lane >> 3) * 8) ^ ((lane & 7) * 16));
        int kbr = L_KB + w * 1024 + (lane >> 3) * 128 + (((lane & 7) * 16) ^ ((lane >> 3) * 16));
        const unsigned dum = kNone;
        if (!kw) kvo = dum;
        if (b >= 200) { for0 = dum; for1 = dum; cvo = (unsigned)((b * 4 + w) * 128); }
        wro = (unsigned)(chn * 4096 + lc * 16);
        cv0 = cv;
        pin(kvo); pin(kbw); pin(kbr);
        int w1hi0 = w1lane0 + 61440, w1hi1 = w1lane1 + 61440;
        pin(w1hi0); pin(w1hi1);
        pin(w1lane0); pin(w1lane1); pin(xlane0); pin(xlane1); pin(cvo); pin(for0); pin(for1); pin(sto); pin(swo); pin(wro);
        SCHED();
        auto pre = LAMBDA(i_) {
            constexpr int it = p1_pre(i_.value);
            SCHED();
            DMA_M0(it);
            asm volatile("s_nop 0");
            DMA_LD(it);
            SCHED();
        };
        {
            int f0 = 2 * ((chn & ~63) / 5), f1 = 2 * ((chn | 63) / 5) + 1, nk = cv ? 28 : 0;
            asm volatile("" : "+s"(f0), "+s"(f1), "+s"(nk));
            const int l10 = lane - 32 < 0 || lane - 32 > 9 ? 0 : lane - 32;
            fwo = lane < nk ? OFF_FK + (unsigned)(f0 + lane < f1 ? f0 + lane : f1) * 128u : OFF_FW + (unsigned)(25 * nt2 + w + 5 * (l10 >> 1) + (l10 & 1)) * 128u;
        }
        w2o = OFF_W2F + (unsigned)((nt2 >> 1) * PG + (nt2 & 1) * 64 * RUN + 10 * w * WRUN + lane * 16);
        int ime = SC + (w < 3 ? w * REGSZ + CV_E : 43264) + 1008 - 16 * lane, imo = SC + (w < 3 ? w * REGSZ + CV_O + 4 : 47360) + 1008 - 16 * lane, im3 = w < 3 ? ime + 3072 : imo;
        int tca = L_TAB + (c >> 1) * 136, tcb = tca + 8 + 32 * w + 8 * q, tcl = L_TAB + (lane < 40 ? lane : 39) * 136, coa = L_CO + w * REGSZ + q * 64, rsa = l_rs(0, 0) + w * 256 + lane * 4;
        unsigned sgn = (c & 1) ? 0u : 0x80000000u;
        unsigned fko1 = w ? kNone : lane < 32 ? OFF_FK + (unsigned)((2 * kp + (i32 >> 4)) * 128) : OFF_FW + (unsigned)(b * 128);
        pin(fwo); pin(w2o); pin(fko1); pin(ime); pin(imo); pin(im3); pin(tca); pin(tcb); pin(tcl); pin(coa); pin(rsa); pin(sgn);
        SCHED();
        auto cw_ = [&]() __attribute__((always_inline)) {
            gldn<0, 2>(cr[0], cvo, p.Cre); gldn<0, 2>(cr[1], cvo, p.Cim);
            static_for<4>(LAMBDA(i) { gldn<i.value * 1024, 1>(wr[i.value], wro, p.ssmk); });
        };
        STAMP(33)
        gld2s<0>(nonce, 0u, p.ws + OFF_NONCE);
        asm volatile("s_waitcnt vmcnt(16)\n\ts_cmp_gt_i32 %3, 0\n\ts_cbranch_scc1 Lrole3\n\ts_waitcnt vmcnt(1)\nLrole3:" : "+v"(uL[0]), "+v"(uL[1]), "+v"(uL[2]) : "s"(kwi) : "memory", "scc");
        {
            f32x2 lam, co, lp[16];
            disc(uL[0], uL[1], uL[2], lam, co);
            if (lane < 32) *(f32x2*)(smem + L_CO + w * REGSZ + lane * 8) = 2.0f * co;
            lp[0] = cmul(lam, lam); lp[0] = cmul(lp[0], lp[0]); lp[1] = cmul(lp[0], lp[0]);
            lp[0] = f32x2{1.f, 0.f};
            lp[2] = cmul(lp[1], lp[1]); lp[3] = cmul(lp[2], lp[1]); lp[4] = cmul(lp[2], lp[2]);
            const f32x2 z2 = {0.f, 0.f};
            f32x2 t4[4];
            cfma4(t4, lp[4], lp[1], lp[2], lp[3], lp[4], z2, z2, z2, z2);
            lp[5] = t4[0]; lp[6] = t4[1]; lp[7] = t4[2]; lp[8] = t4[3];
            cfma4(t4, lp[8], lp[1], lp[2], lp[3], lp[4], z2, z2, z2, z2);
            lp[9] = t4[0]; lp[10] = t4[1]; lp[11] = t4[2]; lp[12] = t4[3];
            cfma4(t4, lp[8], lp[5], lp[6], lp[7], lp[7], z2, z2, z2, z2);
            lp[13] = t4[0]; lp[14] = t4[1]; lp[15] = t4[2];
            if (lane >= 32 && lane < 42) {
                f32x2* tl = (f32x2*)(smem + L_TAB + (10 * w + lane - 32) * 136);
                tl[0] = lam;
#pragma unroll
                for (int i = 0; i < 16; ++i) tl[1 + i] = lp[i];
            }
        }
        SCHED();
        STAMP(35)
        static_for<5>(LAMBDA(i) { gld64<256 * i.value>(sa[i.value], ssrc); });
        cw_();
        addrs(std::integral_constant<int, 0>{}, lane, w, b);
        v4u kvt[4];
        unsigned one2 = 0x3F803F80u, kq[12];
        float kf_[8];
        pin(one2);
        auto kvf = LAMBDA(n_) {
            constexpr int n = n_.value, h = n / 5, u = n % 5;
            if constexpr (u == 0) {
#pragma unroll
                for (int r = 0; r < 6; ++r) { kq[r] = __builtin_amdgcn_perm(ax[r][h], ax[r + 1][h], 0x05040100u); kq[6 + r] = __builtin_amdgcn_perm(ax[r][h], ax[r + 1][h], 0x07060302u); }
                if constexpr (h > 0) kvt[h - 1] = *(const v4u*)(smem + kbr);
            } else if constexpr (u == 1) {
                unsigned t[8];
                d2x4(t, kq[0], kq[1], kq[2], kq[3], ax[11 + h][1], ax[11 + h][1], ax[11 + h][1], ax[11 + h][1]);
                d2x4(t + 4, kq[6], kq[7], kq[8], kq[9], ax[11 + h][3], ax[11 + h][3], ax[11 + h][3], ax[11 + h][3]);
#pragma unroll
                for (int r = 0; r < 8; ++r) kf_[r] = __uint_as_float(t[r]);
            } else if constexpr (u == 2) {
#pragma unroll
                for (int r = 0; r < 8; ++r) kf_[r] = dot2a(kq[r + 2 * (r >> 2) + 2], ax[11 + h][2 * (r >> 2)], kf_[r]);
            } else if constexpr (u == 3) {
#pragma unroll
                for (int r = 0; r < 4; ++r) { kq[r] = ax[7 + r][h] << 16; kq[4 + r] = ax[7 + r][h] & 0xffff0000u; pin(kq[r]); pin(kq[4 + r]); }
            } else {
#pragma unroll
                for (int r = 0; r < 8; ++r) kf_[r] *= __uint_as_float(kq[r]);
                *(v2u*)(smem + kbw) = v2u{cvtpk(kf_[0], kf_[1]), cvtpk(kf_[2], kf_[3])};
                *(v2u*)(smem + (kbw ^ 64)) = v2u{cvtpk(kf_[4], kf_[5]), cvtpk(kf_[6], kf_[7])};
                if constexpr (h == 3) kvt[3] = *(const v4u*)(smem + kbr);
            }
            SCHED();
        };
        asm volatile("s_waitcnt vmcnt(%15)" : "+v"(ax[0]), "+v"(ax[1]), "+v"(ax[2]), "+v"(ax[3]), "+v"(ax[4]), "+v"(ax[5]), "+v"(ax[6]), "+v"(ax[7]), "+v"(ax[8]), "+v"(ax[9]),
                     "+v"(ax[10]), "+v"(ax[11]), "+v"(ax[12]), "+v"(ax[13]), "+v"(ax[14]) : "i"(11) : "memory");
        pin(nonce);
        STAMP(30)
        unsigned tch = 0u;
        constexpr int NF = KNF;
        static_assert(NF < 27, "");
        if (kw) {
            {
                static_for<20>(LAMBDA(n_) {
                    constexpr int n = n_.value, DN = 20 - KFD0;
                    site(); SCHED();
                    kvf(n_);
                    if constexpr (n >= KFD0) static_for<(n - KFD0 + 1) * NF / (DN ? DN : 1) - (n - KFD0) * NF / (DN ? DN : 1)>(LAMBDA(i_) { pre(std::integral_constant<int, (n - KFD0) * NF / (DN ? DN : 1) + i_.value>{}); });
                });
                if constexpr (KFD0 == 20) static_for<NF>(pre);
            }
        } else {
            if (w == 3) {
            const int l = lane, l3 = l & 3;
            unsigned a = OFF_SG + (unsigned)((((l >> 3) & 1) * 16 + (mt2 & 15)) * RUN + ((l >> 1) & 3) * 2048 + nt2 * 256 + (l & 1) * 128);
            unsigned a1 = OFF_PS + (unsigned)((mt1 & 7) * RUN + (((mt2 & 1) * 4 + l3) * 8 + nt2) * 128);
            unsigned a2 = OFF_FX + (unsigned)((mt1 & 7) * RUN + ((2 * (mt2 & 1) + (l3 >> 1)) * 16 + nt2 * 2 + (l3 & 1)) * 128);
            unsigned a3 = OFF_FH + (unsigned)((2 * (mt1 & 7) + (l3 >> 1)) * RUN + (((l >> 2) & 1) * 32 + nt1 * 2 + (l3 & 1)) * 128);
            unsigned a4 = OFF_EG + (unsigned)(((((l >> 3) & 1) * 2 + (nt1 >> 3)) * 16 + mt1) * RUN + (nt1 & 7) * 640 + (l & 7) * 128 + (l & 16) * 512);
            unsigned a5 = OFF_GT + (unsigned)((nt2 >> 2) * PG + ((nt2 & 3) * 32 + 20) * RUN + i32 * 128 + (l & 16) * 512);
            unsigned a6 = OFF_HF - MID + (unsigned)((((l >> 3) & 1) * 4 + ((mt1 >> 1) & 3)) * PG + j * 4 * RUN);
            unsigned a7 = OFF_X2F - MID + (unsigned)(((mt1 >> 2) & 1) * PG + ((xq & 1) * 64 + j * 2) * RUN);
            pin(a); pin(a1); pin(a2); pin(a3); pin(a4); pin(a5); pin(a6); pin(a7);
            a4 = (l & 7) == 5 ? a5 : a4; a4 = (l & 7) == 6 ? a6 : a4; a4 = (l & 7) == 7 ? a7 : a4;
            a = l >= 16 ? a1 : a; a = l >= 20 ? a2 : a; a = l >= 24 ? a3 : a; a = l >= 32 ? a4 : a;
            tch = a;
        }
            vm_wait<KNW>();
            static_for<NF>(pre);
            if (w == 2 && b < 16) {
                unsigned v0, v1, a0, a1, a2, a3, t0, t1;
                asm volatile("s_waitcnt lgkmcnt(0)\n\ts_load_dword %2, %8, 0x0 glc\n\ts_load_dword %3, %8, 0x2000 glc\n\ts_load_dword %4, %8, 0x1000 glc\n\ts_load_dword %5, %8, 0x3000 glc\n\ts_waitcnt lgkmcnt(0)\n\t"
                             "s_not_b32 %6, %2\n\ts_not_b32 %7, %3\n\ts_load_dword %6, %8, 0x0 glc\n\ts_load_dword %7, %8, 0x2000 glc\n\ts_waitcnt lgkmcnt(1)\n\t"
                             "s_cmp_eq_u32 %7, %3\n\ts_cselect_b32 %0, 1, 0\n\ts_waitcnt lgkmcnt(0)\n\t"
                             "s_not_b32 %6, %4\n\ts_not_b32 %7, %5\n\ts_load_dword %7, %8, 0x3000 glc\n\ts_load_dword %6, %8, 0x1000 glc\n\ts_waitcnt lgkmcnt(1)\n\t"
                             "s_cmp_eq_u32 %6, %4\n\ts_cselect_b32 %1, 0, 1\n\ts_waitcnt lgkmcnt(0)"
                             : "=&s"(v0), "=&s"(v1), "=&s"(a0), "=&s"(a1), "=&s"(a2), "=&s"(a3), "=&s"(t0), "=&s"(t1) : "s"(p.ws + (OFF_W2F - MID + (unsigned)(40 + b) * RUN)) : "memory", "scc");
                const unsigned sd = (unsigned)(b >> 2) & 1u;
                st128(v4u{v0 ^ sd, nonce.x + 1u, v1 ^ sd, nonce.x + 1u}, rs, lane ? kNone : OFF_CV + (unsigned)(b * 128));
            }
            skip<SKP>();
            undef4(kvt);
        }
        tag0 = __builtin_amdgcn_readfirstlane(nonce.x) + 1u; tag1 = __builtin_amdgcn_readfirstlane(nonce.y) + (tag0 == 0u ? 1u : 0u);
        SCHED();
        STAMP(34)
        v4u orr, oi;
        v2u gr;
        unsigned gro;
        {
            asm volatile("s_waitcnt vmcnt(%7)" : "+v"(cr[0]), "+v"(cr[1]), "+v"(sa[0]), "+v"(sa[1]), "+v"(sa[2]), "+v"(sa[3]), "+v"(sa[4]) : "i"(4 + NF) : "memory");
#pragma unroll
            for (int e = 0; e < 4; ++e) {
                const f32x4 co = *(const f32x4*)(smem + coa + e * 16);
                const f32x2 o0 = lo16f(cr[0][e]) * f32x2{co.x, -co.y} - lo16f(cr[1][e]) * f32x2{co.y, co.x};
                const f32x2 o1 = hi16f(cr[0][e]) * f32x2{co.z, -co.w} - hi16f(cr[1][e]) * f32x2{co.w, co.z};
                orr[e] = cvtpk(o0.x, o1.x); oi[e] = cvtpk(o0.y, o1.y);
            }
            pin(orr); pin(oi);
            SCHED();
        }
        static_for<(27 - NF) / 3>(LAMBDA(i_) { pre(std::integral_constant<int, NF + i_.value>{}); });
        {
            float s1[2] = {0.f, 0.f}, s2[2] = {0.f, 0.f};
#pragma unroll
            for (int i = 0; i < 5; ++i)
#pragma unroll
                for (int e = 0; e < 4; ++e) { s1[e & 1] = dot2a(sa[i][e], one2, s1[e & 1]); s2[e & 1] = dot2a(sa[i][e], sa[i][e], s2[e & 1]); }
            const float a1 = rowsum16(s1[0] + s1[1]), a2 = rowsum16(s2[0] + s2[1]);
            const float mean = a1 * (1.0f / D_);
            const float var = fmaxf(a2 * (1.0f / D_) - mean * mean, 0.f);
            const float ri = __builtin_amdgcn_rcpf(rbf(__builtin_amdgcn_sqrtf(rbf(rbf(var) + 1e-5f))));
            gr = v2u{__float_as_uint(q >= 2 ? a1 : c ? -ri * rbf(mean) : ri), tag0};
            gro = c < 2 ? OFF_ST + (unsigned)((mt1 * 16 + nt1) * 256 + ((q < 2 ? 8 * c : 14) + 2 * w + q) * 8) : kNone;
            pin(gr); pin(gro);
        }
        SCHED();
        STAMP(59)
        {
            static_for<27 - NF - (27 - NF) / 3>(LAMBDA(i_) { pre(std::integral_constant<int, NF + (27 - NF) / 3 + i_.value>{}); });
#pragma unroll
            for (int i = 0; i < 4; ++i) st128(kvt[i], rs, kvo + i * 8192);
            st128(orr, rs, for0); st128(oi, rs, for1);
        }
        st64(gr, rs, gro);
        SCHED();
        STAMP(23)
        asm volatile("s_waitcnt vmcnt(%4) lgkmcnt(0)" : "+v"(wr[0]), "+v"(wr[1]), "+v"(wr[2]), "+v"(wr[3]) : "i"(P1_PRE - 9) : "memory");
        __builtin_amdgcn_s_barrier();
        asm volatile("" ::: "memory");
        STAMP(24)
        v4u wf[2][5], af[2][2][2];
#pragma unroll
        for (int nb = 0; nb < 5; ++nb) wf[0][nb] = *(const v4u*)(smem + w1lane0 + nb * 20480 + (0) * 2048);
#pragma unroll
        for (int i = 0; i < 4; ++i) af[0][i >> 1][i & 1] = *(const v4u*)(smem + ((i >> 1) ? xlane1 : xlane0) + (i & 1) * 2048);
        SCHED();
        const char *kvtb = p.ws + OFF_KVT, *w2b0;
        u64 vb0, vb1;
        pins(kvtb);
        v2u sg, tc[10], fw, tg2 = {tag0, tag1};
        pin(tg2);
        v4u sw0;
        unsigned ie[4][4], io[4][4], iw, zq[4];
        float rr_[8];
        auto kstep = LAMBDA(s_) {
            constexpr int s = s_.value, ph = s >> 2, s4 = s & 3, cu = s & 1, nx = cu ^ 1, jj = s >> 1, pp = jj & 1;
            if constexpr (s4 == 3 && ph < 4) {
                constexpr int pw = p1_pos(104 + 5 * (ph + 1)), px = p1_pos(4 * (jj + 1) + 3), pm = pw > px ? pw : px;
                constexpr int vm = p1_lim(p1_cnt(s, 0) - 1 - pm);
                asm volatile("" ::: "memory");
                __builtin_amdgcn_s_waitcnt((vm & 15) | ((vm >> 4) << 14) | 0x70);
                __builtin_amdgcn_s_barrier();
                asm volatile("" ::: "memory");
                STAMP(25 + ph)
                SCHED();
            }
            if constexpr (s == 15) {
                wait_st<p1_cnt(s, 0) - 1 - p1_pos(203)>(sg, sw0);
                WAVE_RETRY((((sg.y ^ tag0) | (sw0.y ^ tag0) | (sw0.w ^ tag0)) == 0u), sg = ld64(rs, sto); sw0 = ld128(rs, swo))
                STAMP(7)
            }
            if constexpr (s == 16) {
                const u64 m = 0x8888888888888888ull;
                pol = __builtin_popcountll(vb0 & m) + __builtin_popcountll(vb1 & m) > 16 ? 8192u : 0u;
                sdo = pol ^ (unsigned)((b0 & 4) * 2048);
                wl = p.ws + sdo; w2b0 = p.ws + 2048;
                pins(pol); pins(sdo); pins(wl); pins(w2b0);
                rl = __builtin_amdgcn_make_buffer_rsrc((void*)wl, 0, WS_BYTES, 0x00020000);
                static_assert(kW2S > 16, "");
            }
            if constexpr (s == kW2S) { vm_wait<p1_cnt(s, 0) - 1 - p1_pos(208)>(); SCHED(); }
            if constexpr (s == 18) {
                vm_wait<p1_cnt(s, 0) - 1 - (p1_pos(207) > p1_pos(208) ? p1_pos(207) : p1_pos(208))>();
                pin(fw); pin(sdq);
                kearly = __builtin_amdgcn_ballot_w64(!ISTAG(fw)) == 0ull;
                ORDER();
                if (kearly && cv) ld_ur(wro, kvtb);
                SCHED();
            }
            if constexpr (s4 != 3 || ph == 4) lgkm<(cu == 0 && s > 0 ? 3 : s == 17 ? 4 : s == 19 ? 2 : 0)>();
            SCHED();
            static_for<10>(LAMBDA(g_) {
                constexpr int g = g_.value, mb = g / 5, k = g % 5, it = p1_item(s, g), nt_ = g < 9 ? p1_item(s, g + 1) : -1;
                constexpr bool d0 = it >= 0 && it < 200, d1 = nt_ >= 0 && nt_ < 200;
                if constexpr (s == 0) mf0(acc[mb][k], af[pp][cu][mb], wf[cu][k]); else mf(acc[mb][k], af[pp][cu][mb], wf[cu][k]);
                SCHED();
                if constexpr (s < 19) {
                    if constexpr (g < 5) wf[nx][g] = *(const v4u*)(smem + (g < 3 ? (nx ? w1lane1 : w1lane0) : (nx ? w1hi1 : w1hi0)) + (g < 3 ? g : g - 3) * 20480 + ((s + 1) >> 1) * 1024);
                    if constexpr (cu == 1 && g == 5) vm_wait<p1_lim(p1_cnt(s, 5) - 1 - p1_pos(4 * (jj + 1) + 3))>();
                    if constexpr (cu == 1 && g >= 5 && g < 9)
                        af[pp ^ 1][(g - 5) >> 1][(g - 5) & 1] = *(const v4u*)(smem + (((g - 5) >> 1) ? xlane1 : xlane0) + ((jj + 1) % 3) * 4096 + ((g - 5) & 1) * 2048);
                }
                if constexpr (cu == 0 && g == 4 && s > 0) lgkm<(s == 8 || s == 10 || s == 14 ? 8 : 5)>();
                static_for<p1_cap(s, g)>(LAMBDA(e_) {
                    constexpr int na = p1_slot(0, s, g, 10) + e_.value, i = (na / 10) & 3, u = na % 10;
                    static_assert(p1_slot(0, 8, 0, 10) >= 40, "");
                    if constexpr (na < 40) {
                        if constexpr (u == 0) { if constexpr (i > 0) ror1(iw, wr[i - 1].w); else { iw = 0u; pin(iw); } }
                        else if constexpr (u < 5) rot1(ie[i][u - 1], wr[i][4 - u]);
                        else if constexpr (u == 5) shr1b(iw, wr[i].w);
                        else if constexpr (u < 9) mix1(io[i][u - 6], wr[i][9 - u], wr[i][8 - u]);
                        else mix1(io[i][3], wr[i].x, iw);
                    }
                    constexpr int nb = p1_slot(8, s, g, 3) + e_.value;
                    if constexpr (s >= 8 && s < 12 && g < 3) {
                        if constexpr (nb < 10) tc[nb] = *(const v2u*)(smem + (nb < 5 ? tca : tcb) + (nb % 5) * 1088);
                        else if constexpr (nb == 10) { const f32x2* tp = (const f32x2*)(smem + tcl); l8 = tp[2]; l32 = tp[5]; }
                        else if constexpr (nb == 11) l128 = *(const f32x2*)(smem + tcl + 72);
                        static_assert(p1_slot(8, 12, 0, 3) == 12, "");
                    }
                    constexpr int nc = p1_slot(12, s, g, 10) + e_.value;
                    if constexpr (s >= 12 && s < 14 && nc < 10) {
                        if constexpr (nc < 5) { lr[nc] = __uint_as_float(tc[nc].x); xor1(ls[nc], tc[nc].y, sgn); }
                        else { lqr[nc - 5] = __uint_as_float(tc[nc].x); xor1(lqs[nc - 5], tc[nc].y, sgn); }
                        static_assert(p1_slot(12, 14, 0, 10) >= 10, "");
                    }
                    if constexpr (s == 15 && g < 5) SW[g] = __int_as_float(__builtin_amdgcn_ds_bpermute(16 * c + 4 * (g >> 1), (g & 1) ? sw0.z : sw0.x));
                    constexpr bool img = (s == 16 && g >= 5 && g < 9 && e_.value == 0) || (s == 18 && (g == 5 || g == 6) && e_.value == 0);
                    constexpr int nd = p1_slot(15, s, g, 10) + e_.value - 5 - (s > 16 ? 4 : s == 16 && g >= 5 ? (g < 9 ? g - 4 : 4) : 0) - (s > 18 ? 2 : s == 18 && g >= 5 ? (g == 5 ? 1 : 2) : 0);
                    if constexpr (s >= 15 && nd >= 0 && !img) {
                        if constexpr (nd < 4) bc1<nd>(rr_[nd], sg.x);
                        else if constexpr (nd < 44) mulb<8 + (nd - 4) % 8>(tt[(nd - 4) % 8 / 4][(nd - 4) / 8][(nd - 4) % 4], sg.x, SW[(nd - 4) / 8]);
                        else if constexpr (nd < 48) bc1<nd - 40>(rr_[nd - 40], sg.x);
                        static_assert(p1_slot(15, 16, 0, 10) <= 9 && p1_slot(15, 20, 0, 10) >= 59, "");
                    }
                });
                if constexpr (s == 15 && g == 6) { vb0 = __builtin_amdgcn_ballot_w64(sw0.x == 1u); pins(vb0); }
                if constexpr (s == 15 && g == 7) { vb1 = __builtin_amdgcn_ballot_w64(sw0.z == 1u); pins(vb1); }
                if constexpr (s == 14 && g < 3) *(v4u*)(smem + ime + 1024 * g) = v4u{ie[3 - g][0], ie[3 - g][1], ie[3 - g][2], ie[3 - g][3]};
                if constexpr (s == 16 && g >= 5 && g < 9) *(v4u*)(smem + imo + 1024 * (8 - g)) = v4u{io[g - 5][0], io[g - 5][1], io[g - 5][2], io[g - 5][3]};
                if constexpr (s == 18 && g == 5) *(v4u*)(smem + im3) = v4u{ie[0][0], ie[0][1], ie[0][2], ie[0][3]};
                if constexpr (s == 18 && g == 6) *(unsigned*)(smem + rsa) = sg.x;
                if constexpr (s == 14 && (g == 6 || g == 8)) { zq[g - 6] = 0u; zq[g - 5] = 0u; pin(zq[g - 6]); pin(zq[g - 5]); }
                if constexpr (s == 19 && g == 0) *(v4u*)(smem + zpa) = v4u{zq[0], zq[1], zq[2], zq[3]};
                if constexpr (s == 18 && g == 1) touch_(tch ^ sdo, p.ws);
                if constexpr (s == 19 && g == 2) { if (__builtin_expect(!kearly, 0)) poll1(fwo, p.ws); }
                if constexpr (it >= 0) {
                    if constexpr (d0 && d1) DMA_LD1(it, d1 ? nt_ : 0);
                    else if constexpr (d0) DMA_LD(it);
                    else if constexpr (it == 202) gld2s<0>(sg, sto, p.ws);
                    else if constexpr (it == 203) gld4s<0>(sw0, swo, p.ws);
                    else if constexpr (it == 207) gldu16(sdq, (unsigned)chn * 2u, p.ssmD);
                    else if constexpr (it == 208) gld2s<0>(fw, fwo, p.ws);
                    else if constexpr (it >= 500) gld_w2<it % 50>(w2o, w2b0 + ((it % 50) / 5) * WRUN);
                    else { static_assert(p1_pos(19) >= P1_PRE, ""); st64(tg2, rs, fko1); }
                }
                if constexpr (d1 && !d0) DMA_M0(d1 ? nt_ : 0);
                site();
                SCHED();
            });
        };
        static_for<20>(kstep);
        SCHED();
        rr[0] = f32x4{rr_[0], rr_[1], rr_[2], rr_[3]}; rr[1] = f32x4{rr_[4], rr_[5], rr_[6], rr_[7]};
        ACC_FENCE10(acc[0], acc[1]);
        STAMP(1)
    }

    auto layer_body = LAMBDA(layer_) {
        constexpr int layer = layer_.value;
        const int sga = sgn_;
        const bool cv = layer == 0 && cv0;
        v2u fh;
        unsigned ks[10];
        f32x4 ca[8], a2[4][5];
        float rsr, rsm;
        int otw, otr, ots;
        v4u cA[2][8], cB[2][8];
        constexpr int R2 = kCR1, LP = (7 - R2) & 1;
        static_assert(kFHP >= 7 - KG0 / 4 + 1, "");
        constexpr bool TB = (SBT >> layer) & 1;
        float bv[8][5];
        f32x2 pr[5][2], ps[5][2];
        auto powers = [&]() __attribute__((always_inline)) {
            if constexpr (TB) {
#pragma unroll
                for (int nb = 0; nb < 5; ++nb) {
                    const f32x2 m = {lr[nb] * lr[nb] - ls[nb] * ls[nb], 2.0f * lr[nb] * ls[nb]};
                    pr[nb][0] = f32x2{lr[nb], m.x}; ps[nb][0] = f32x2{ls[nb], m.y};
#pragma unroll
                    for (int j = 1; j < 2; ++j) pstep(pr[nb][j], ps[nb][j], pr[nb][j - 1], ps[nb][j - 1], m);
#pragma unroll
                    for (int j = 0; j < 2; ++j) { pin(pr[nb][j]); pin(ps[nb][j]); }
                }
                SCHED();
            }
        };
        {
            float a0[5], a1[5];
            static_for<2>(LAMBDA(m_) {
                constexpr int mb = m_.value;
#pragma unroll
                for (int nb = 0; nb < 5; ++nb) {
                    const f32x4 v = __builtin_elementwise_fma(acc[mb][nb], rr[mb], f32x4{tt[mb][nb][0], tt[mb][nb][1], tt[mb][nb][2], tt[mb][nb][3]});
#pragma unroll
                    for (int i = 0; i < 4; ++i) bv[4 * mb + i][nb] = v[i];
                }
                SCHED();
                if constexpr (TB) {
#pragma unroll
                    for (int i = mb ? 4 : 1; i < 4 + 4 * mb; ++i) cstepi(bv[i][0], bv[i][1], bv[i][2], bv[i][3], bv[i][4], bv[i - 1][0], bv[i - 1][1], bv[i - 1][2], bv[i - 1][3], bv[i - 1][4], lr, ls);
#pragma unroll
                    for (int nb = 0; nb < 5; ++nb) a0[nb] = bv[7][nb];
                }
                if constexpr (mb == 0 && !TB) cstep(a0, bv[0], lr, ls, bv[1][0], bv[1][1], bv[1][2], bv[1][3], bv[1][4]);
#pragma unroll
                for (int i8 = mb ? 4 : 2; i8 < (TB ? 0 : 4 + 4 * mb); i8 += 2) {
                    cstep(a1, a0, lr, ls, bv[i8][0], bv[i8][1], bv[i8][2], bv[i8][3], bv[i8][4]);
                    cstep(a0, a1, lr, ls, bv[i8 + 1][0], bv[i8 + 1][1], bv[i8 + 1][2], bv[i8 + 1][3], bv[i8 + 1][4]);
                }
                SCHED();
            });
#pragma unroll
            for (int nb = 0; nb < 5; ++nb) *(float*)(smem + sga + nb * 64) = a0[nb];
        }
        SCHED();
        const int w_ = w0;
        {
        const int w = w_, mt1 = mt1_;
        if (layer == 0 && __builtin_expect(!kearly, 0)) {
            unsigned f0_, f1_;
            asm volatile("s_waitcnt vmcnt(0)\n\t" AR("%0", 36, 0) AR("%1", 36, 1) : "=v"(f0_), "=v"(f1_)::"memory");
            kearly = __builtin_amdgcn_ballot_w64(((f0_ ^ tag0) | (f1_ ^ tag1)) != 0u) == 0ull;
            ORDER();
            if (kearly) KW_REQ()
        }
        asm volatile("s_waitcnt lgkmcnt(0)" ::: "memory");
        __builtin_amdgcn_s_barrier();
        asm volatile("" ::: "memory");
        STAMP(layer ? 14 : 2)
#pragma unroll
        for (int s = 0; s < 10; ++s) ks[s] = kso(s);
        constexpr int NX = 58;
        constexpr int NW2A = NW2P + 7 * kCR1 + kXA, NW3 = NW2P + kN3E + kN3;
        auto w2ld = LAMBDA(i_) {
            constexpr int i = i_.value;
            gld_w2<i>(w2o, p.ws + 2048 + (i / 5) * WRUN);
        };
        auto w2a = LAMBDA(i_) {
            constexpr int k = i_.value;
            if constexpr (k < kXA) { w2ld(std::integral_constant<int, NW2P + 2 * k>{}); w2ld(std::integral_constant<int, NW2P + 2 * k + 1>{}); }
            else w2ld(std::integral_constant<int, NW2P + kXA + k>{});
        };
        auto w2x = LAMBDA(i_) {
            constexpr int i = i_.value < NX - 50 ? 50 + i_.value : NW2A + i_.value - (NX - 50) < 50 ? NW2A + i_.value - (NX - 50) : 99;
            if constexpr (i < 50) w2ld(std::integral_constant<int, i>{});
            else if constexpr (i < 52) lda4<40 + 4 * (i - 50), (i - 50) * 64>(xso, p.x0);
            else if constexpr (i == 52) lda4<48, 128>(xso - 2u * (unsigned)(bb & 16), p.x0);
            else if constexpr (i < NX) lda2<20 + 2 * (i - 53), 32 * (i - 53)>(dso, p.Dskip);
        };
        constexpr int NB = NX - NW2A, NB1 = KB1 < NB ? KB1 : NB, KPL = 7 * (8 - R2) - kFHP, NFH = NB - NB1 - (KPL - kFHK) > 0 ? NB - NB1 - (KPL - kFHK) : 0;
        auto w2b = LAMBDA(i_) {
            constexpr int k = i_.value - 7 * R2;
            if constexpr (k < NB1) w2x(std::integral_constant<int, k>{});
            if constexpr (k > kFHK) w2x(std::integral_constant<int, NB1 + k - kFHK - 1>{});
            if constexpr (k == kFHK) { vm_wait<NB1>(); st64(v2u{tag0, tag1}, rl, fho1); STAMP(39) }
            if constexpr (k == kFHK - 1) { STAMP(38) }
            if constexpr (k == KPL) poll1(fho, wl);
            static_assert(NB1 <= kFHK + 1 && NB1 + 7 * (7 - R2) + KG0 / 4 - kFHK - 1 >= NB && NW2A <= 50 && NW3 <= NW2A && NFH <= NB - NB1, "");
        };
        auto partb = [&]() __attribute__((always_inline)) {
            const float* sp = (const float*)(smem + spa);
            rsr = sp[0]; rsm = sp[8];
            if (layer == 1) { pin(rsr); pin(rsm); }
            if (layer == 1) {
                ACC_TIE5(dsk);
#pragma unroll
                for (int nb = 0; nb < 5; ++nb) { a2[3][nb] = xin[nb] + (xin[nb] * rsr + rsm) * unpk4(dsk[nb]); pin(a2[3][nb]); }
            }
        };
        constexpr int LBW = layer ? 2 : 3;
        if (w == 3) {
            site(); SCHED();
            f32x2 sv[16];
#pragma unroll
            for (int i = 0; i < 16; ++i) sv[i] = *(const f32x2*)(smem + spl + (l_sg(layer, i >> 2) - SC + (i & 3) * 320));
            f32x2 u[4], v[4], x[4];
            cfma4(u, l8, sv[0], sv[4], sv[8], sv[12], sv[1], sv[5], sv[9], sv[13]);
            cfma4(v, l8, u[0], u[1], u[2], u[3], sv[2], sv[6], sv[10], sv[14]);
            cfma4(x, l8, v[0], v[1], v[2], v[3], sv[3], sv[7], sv[11], sv[15]);
            f32x2 P[4][4];
            P[0][0] = f32x2{0.f, 0.f}; P[0][1] = x[0]; P[0][2] = cfma(l32, x[0], x[1]); P[0][3] = cfma(l32, P[0][2], x[2]);
            const f32x2 E = cfma(l32, P[0][3], x[3]);
            __builtin_amdgcn_raw_buffer_store_b128(v4u{__float_as_uint(E.x), tag0, __float_as_uint(E.y), tag0}, rs, es, sdo ^ 8192u, SC1);
            st128(v4u{__float_as_uint(E.x), tag0, __float_as_uint(E.y), tag0}, rl, es);
            SCHED();
            if (layer == 0) { FW_OK() static_for<kN3E>(LAMBDA(i_) { w2ld(std::integral_constant<int, NW2P + i_.value>{}); }); }
#pragma unroll
            for (int kk = 1; kk < 4; ++kk) cfma4(P[kk], l8, P[kk - 1][0], P[kk - 1][1], P[kk - 1][2], P[kk - 1][3], sv[kk - 1], sv[kk + 3], sv[kk + 7], sv[kk + 11]);
#pragma unroll
            for (int i = 0; i < 16; ++i) *(f32x2*)(smem + spl + (l_sg(layer, i >> 2) - SC + (i & 3) * 320)) = P[i & 3][i >> 2];
            if (layer == 0) partb();
            if (layer == 1) powers();
        }
        if (w == LBW) {
            site(); SCHED();
            f32x2 wg[17], t4[4];
            unsigned ea[15], tg[15];
            unsigned m14 = (unsigned)mt1;
            pins(m14);
            m14 *= RUN;
            const f32x2 z2 = {0.f, 0.f};
            wg[1] = cmul(l128, l128); wg[2] = cmul(wg[1], wg[1]); wg[3] = cmul(wg[2], wg[1]); wg[4] = cmul(wg[2], wg[2]);
#pragma unroll
            for (int i = 4; i < 16; i += 4) {
                cfma4(t4, wg[i], wg[1], wg[2], wg[3], wg[4], z2, z2, z2, z2);
#pragma unroll
                for (int e = 0; e < 4; ++e) wg[i + 1 + e] = t4[e];
            }
#pragma unroll
            for (int i = 0; i < 15; ++i) {
                const int ch = mt1 - 1 - i;
                if (i) pin(wg[i]);
                ea[i] = ch < 0 ? kNone : m14 - (unsigned)(i + 1) * RUN; tg[i] = ch < 0 ? 0u : tag0;
                pins(ea[i]); pins(tg[i]);
            }
            powers();
            f32x2 H0, H1;
            skip<(layer ? SK1 : SK0)>();
            __builtin_amdgcn_s_sleep(layer ? KLB1 : KLB0);
            unsigned spins = dead ? kSpinCap : 0u;
            bool miss;
#pragma nounroll
            do {
                asm volatile("" ::: "memory");
                v4u eg[15];
#pragma unroll
                for (int i = 0; i < 15; ++i) eg[i] = ld128s(rl, eo, ea[i]);
                SCHED();
                unsigned x = __builtin_amdgcn_bitop3_b32(eg[0].y, eg[0].w, tg[0], 0x7E);
                H0 = f32x2{__uint_as_float(eg[0].x), __uint_as_float(eg[0].z)}; H1 = f32x2{0.f, 0.f};
#pragma unroll
                for (int i = 1; i < 15; i += 2) {
                    x |= __builtin_amdgcn_bitop3_b32(eg[i].y, eg[i].w, tg[i], 0x7E) | __builtin_amdgcn_bitop3_b32(eg[i + 1].y, eg[i + 1].w, tg[i + 1], 0x7E);
                    pin(x);
                    gacc2(H0, H1, wg[i], wg[i + 1], eg[i], eg[i + 1]);
                    SCHED();
                }
                miss = __builtin_amdgcn_ballot_w64(x != 0u) != 0ull;
            } while (miss && ++spins <= kSpinCap);
            dead = dead || miss;
            ORDER();
            *(f32x2*)(smem + spl + (L_CW - SC)) = H0 + H1;
            STAMP(layer ? 36 : 37)
            if (layer == 0) {
                static_for<NW3 - NW2P - kN3E>(LAMBDA(i_) { w2ld(std::integral_constant<int, NW2P + kN3E + i_.value>{}); });
            }
        }
        int w2_ = w;
        pins(w2_);
        if (layer == 0) UNDEF8(ca);
        if (layer == 0 && w2_ != LBW) {
            if (cv) {
                site(); SCHED();
                FW_OK()
                st_ur((unsigned)(size_t)(LAS char*)smem + (unsigned)uwa);
                STAMP(32)
                conv_part<0, kCR1>(ca, smem, ab, bb, w2a, [&]() __attribute__((always_inline)) {
                    v4u ad;
                    const unsigned dv = sdq << (adm & 16u);
#pragma unroll
                    for (int i = 0; i < 4; ++i) ad[i] = dv & (unsigned)__builtin_amdgcn_sbfe((int)adm, 8 + i, 1);
                    asm volatile("s_nop 1" : "+v"(ad));
                    return ad;
                }, cA, cB);
                partb();
                powers();
            } else {
                FW_OK()
                static_for<NW2A - NW2P>(LAMBDA(i_) { w2ld(std::integral_constant<int, NW2P + i_.value>{}); __builtin_amdgcn_s_sleep(1); });
                partb();
                powers();
            }
        }
        if (layer == 1 && w < LBW) { site(); SCHED(); powers(); }
        asm volatile("s_waitcnt lgkmcnt(0)" ::: "memory");
        __builtin_amdgcn_s_barrier();
        asm volatile("" ::: "memory");
        STAMP(layer ? 15 : 4)
        {
            float cw[5], ex[5];
#pragma unroll
            for (int nb = 0; nb < 5; ++nb) { cw[nb] = *(const float*)(smem + cwa + nb * 64); ex[nb] = *(const float*)(smem + sga + nb * 64); }
            v2u ta[5], td[5];
            if constexpr (TB) {
                cstepi(ex[0], ex[1], ex[2], ex[3], ex[4], cw[0], cw[1], cw[2], cw[3], cw[4], lqr, lqs);
                f32x2 cs[5];
                PIN5(ex);
#pragma unroll
                for (int nb = 0; nb < 5; ++nb) { cs[nb] = f32x2{ex[nb], dpp<0xB1>(ex[nb])}; pin(cs[nb]); }
                SCHED();
                static_for<5>(LAMBDA(n_) {
                    constexpr int nb = n_.value;
                    f32x2 h[4] = {{bv[0][nb], bv[1][nb]}, {bv[2][nb], bv[3][nb]}, {bv[4][nb], bv[5][nb]}, {bv[6][nb], bv[7][nb]}};
                    padd(h, pr[nb], ps[nb], cs[nb]);
                    *(v4u*)(smem + tbw + nb * 1024) = v4u{cvtpk(h[0].x, h[0].y), cvtpk(h[1].x, h[1].y), cvtpk(h[2].x, h[2].y), cvtpk(h[3].x, h[3].y)};
                    ta[nb] = __builtin_bit_cast(v2u, trread(smem, tbr + nb * 1024)); td[nb] = __builtin_bit_cast(v2u, trread(smem, tbr + nb * 1024 + 512));
                    SCHED();
                    if constexpr (nb >= LAG) { st128(v4u{ta[nb - LAG].x, ta[nb - LAG].y, td[nb - LAG].x, td[nb - LAG].y}, rl, hso + (nb - LAG) * 256); SCHED(); }
                });
            } else
            static_for<2>(LAMBDA(h_) {
                constexpr int n0 = 2 * h_.value;
                float hv[9][3];
                cstep3(hv[0], cw + n0, lqr + n0, lqs + n0, ex + n0);
                if constexpr (n0 == 0) {
                    chain24<0>(hv, bv, lr, ls);
                    chain24<0>(hv + 4, bv + 4, lr, ls);
                } else {
                    chain33<2>(hv, bv, lr, ls);
                    SCHED();
#pragma unroll
                    for (int rd = 0; rd < 2; ++rd) st128(v4u{ta[rd].x, ta[rd].y, td[rd].x, td[rd].y}, rl, hso + rd * 256);
                    SCHED();
                    chain33<2>(hv + 3, bv + 3, lr, ls);
                    chain32<2>(hv + 6, bv + 6, lr, ls);
                }
#pragma unroll
                for (int e = 0; e < (n0 ? 3 : 2); ++e) {
                    v4u o;
                    o.x = cvtpk(hv[1][e], hv[2][e]); o.y = cvtpk(hv[3][e], hv[4][e]);
                    o.z = cvtpk(hv[5][e], hv[6][e]); o.w = cvtpk(hv[7][e], hv[8][e]);
                    *(v4u*)(smem + tbw + (n0 + e) * 1024) = o;
                }
#pragma unroll
                for (int rd = n0; rd < (n0 ? 5 : 2); ++rd) { ta[rd] = __builtin_bit_cast(v2u, trread(smem, tbr + rd * 1024)); td[rd] = __builtin_bit_cast(v2u, trread(smem, tbr + rd * 1024 + 512)); }
                SCHED();
            });
#pragma unroll
            for (int rd = TB ? 5 - LAG : 2; rd < 5; ++rd) st128(v4u{ta[rd].x, ta[rd].y, td[rd].x, td[rd].y}, rl, hso + rd * 256);
            SCHED();
        }
#pragma unroll
        for (int s = 0; s < 10; ++s) pins(ks[s]);
        if constexpr (layer == 1) {
            partb();
            SCHED();
            {
                const int ln = (srca >> 4) & 63, od = (b0 >> 3) & 1, r0 = srca - 16 * ln;
                otw = (ln & 15) * 336 + (ln >> 4) * 16; otr = (ln & 12) * 336 + ((ln >> 4) + od) * 64 + (ln & 3) * 16; ots = otw + (od ? 0 : 256);
                ACC_TIE5(gt);
#pragma unroll
                for (int nb = 0; nb < 5; ++nb) *(v2u*)(smem + r0 + 10240 + otw / 2 + nb * 32) = gt[nb];
#pragma unroll
                for (int nb = 0; nb < 5; ++nb) gt[nb] = *(const v2u*)(smem + r0 + 10240 + (nb < 4 ? otr / 2 + nb * 168 : ots / 2));
                otw += r0; otr += r0; ots += r0;
                pin(otw); pin(otr); pin(ots);
                ACC_TIE5(gt);
                static_assert((SC & 1023) == 0 && (INBOX & 1023) == 0 && L_CW + 320 <= SC + 2 * INBOX + 10240, "");
                SCHED();
            }
            DRAIN();
            st64(v2u{tag0, tag1}, rl, fho1);
        }
        STAMP(layer ? 16 : 5)
        if (cv) {
            if constexpr (layer == 0) site();
            SCHED();
            conv_part<kCR1, 8>(ca, smem, ab, bb, w2b, []() {}, cA, cB);
        } else if (layer == 0) {
            static_for<NB1>(LAMBDA(i_) { w2x(i_); __builtin_amdgcn_s_sleep(1); });
            vm_wait<NB1>();
            st64(v2u{tag0, tag1}, rl, fho1);
            static_for<NB - NB1 - NFH>(LAMBDA(i_) { w2x(std::integral_constant<int, NB1 + i_.value>{}); __builtin_amdgcn_s_sleep(1); });
            if (w == 3) static_for<NW2A - NW3>(LAMBDA(i_) { w2ld(std::integral_constant<int, NW3 + i_.value>{}); __builtin_amdgcn_s_sleep(1); });
            skip<SK5>();
            poll1(fho, wl);
            static_for<NFH>(LAMBDA(i_) { w2x(std::integral_constant<int, NB - NFH + i_.value>{}); __builtin_amdgcn_s_sleep(1); });
            static_assert(NFH <= NB - (NX - 50), "");
            UNDEF8(cA[LP]); UNDEF8(cB[LP]); UNDEF8(ca);
        }
        if (layer == 0) { unsigned f0_, f1_; asm volatile("s_waitcnt vmcnt(%2)\n\t" AR("%0", 36, 0) AR("%1", 36, 1) : "=v"(f0_), "=v"(f1_) : "i"(NFH) : "memory"); fh = v2u{f0_, f1_}; }
        STAMP(layer ? 29 : 6)
        }
        unsigned g[8][2];

            v2u pt[4];
            if (layer == 0) WAVE_RETRY(ISTAG(fh), fh = ld64(rl, fho))
            else { skip<SK2>(); pipe_flag<KPF0>(pt, fho, wl, (u64)tag0 | ((u64)tag1 << 32), dead); ORDER(); }
            STAMP(layer ? 17 : 31)
            site(); SCHED();
            constexpr int RING2 = layer ? KR2 : KRT;
            v4u ar[RING2];
            constexpr int NRT = layer ? RING2 : (36 - KG0) / KRS + 2 < KRT ? (36 - KG0) / KRS + 2 : KRT;
            unsigned xs_[3][4];
            auto gate = LAMBDA(n_) {
                constexpr int n = n_.value;
                if constexpr (n >= 100) swp2n(g[2 * (n - 100)][0], g[2 * (n - 100) + 1][0], g[2 * (n - 100)][1], g[2 * (n - 100) + 1][1]);
                else if constexpr (n >= 0) cvt2(g[n][0], g[n][1], ca[n]);
            };
            if constexpr (layer == 0) {
#pragma unroll
                for (int i = 0; i < 2; ++i) { ar[i] = ld128s(rl, ho[0], ks[i]); SCHED(); }
                static_for<36 - KG0>(LAMBDA(n_) {
                    constexpr int n = n_.value, g = KG0 + n;
                    constexpr int a = g < 8 ? 0 : g < 15 ? 1 : g < 21 ? 2 : g < 26 ? 3 : g < 30 ? 4 : g < 33 ? 5 : g < 35 ? 6 : 7;
                    constexpr int e = g - (a * 8 - a * (a - 1) / 2), i = 2 + n / KRS;
                    mf(ca[a + e], cA[LP][a], cB[LP][e]);
                    SCHED();
                    if constexpr (n % KRS == 0 && i < NRT) ar[i] = ld128s(rl, ho[i / 10], ks[i % 10]);
                    if constexpr (n < 12) rda<40 + n>(xs_[n >> 2][n & 3]);
                    constexpr int gt_[12] = {0, 1, 2, -1, 100, -1, 3, -1, -1, 101, 4, -1};
                    if constexpr (g >= 24) gate(IC(gt_[g < 24 ? 0 : g - 24]));
                    site();
                    SCHED();
                });
                static_assert(KG0 <= 15 && NRT >= 12, "");
            } else {
#pragma unroll
                for (int i = 0; i < RING2; ++i) { ar[i] = ld128s(rl, ho[i / 10], ks[i % 10]); SCHED(); }
            }
        v4u gl[3];
        v2u f0;
        bool gearly = false;
        {
            const char* src = smem + srca;
            const char *glb1 = wl + 8 * RUN, *glb2 = wl + 12 * RUN;
            float zx[4], zd[4], zt[4], zz[5][4];
            if constexpr (STAMPS == 1) { STAMP(layer ? 50 : 40) }
            static_for<40>(LAMBDA(i_) {
                constexpr int i = i_.value, m = i / 10, s = i % 10;
                static_for<5>(LAMBDA(k_) {
                    constexpr int kk = k_.value, n = 2 * (i - 3) + kk / 2 - 3, nb = n / 8, u = n % 8, pv = i - 1 + RING2;
                    if constexpr (s == 0 && m < 3) mfa0<kk>(a2[m][kk], ar[i % RING2]); else mfa<5 * s + kk>(a2[m][kk], ar[i % RING2]);
                    SCHED();
                    constexpr int gu_[12] = {-1, 5, -1, -1, 102, 6, -1, -1, 7, -1, 103, -1};
                    if constexpr (layer == 0 && 5 * i + kk < 12) gate(IC(gu_[(5 * i + kk) % 12]));
                    if constexpr (layer == 0 && i == 25 && kk == 1) { pins(glb1); pins(glb2); }
                    if constexpr (layer == 0 && i >= 3 && i < 26 && (kk & 1)) {
                        if constexpr (n < 0) swp2n(xs_[n + 3][0], xs_[n + 3][2], xs_[n + 3][1], xs_[n + 3][3]);
                        else if constexpr (nb < 5) {
                            if constexpr (u < 2) unp2(zx[2 * u], zx[2 * u + 1], xs_[nb >> 1][2 * (nb & 1) + u]);
                            else if constexpr (u < 4) unp2(zd[2 * u - 4], zd[2 * u - 3], dsk[nb][u - 2]);
                            else if constexpr (u < 6) fma2(zt[2 * u - 8], zt[2 * u - 7], zx[2 * u - 8], zx[2 * u - 7], rsr, rsr, rsm, rsm);
                            else fma2(zz[nb][2 * u - 12], zz[nb][2 * u - 11], zt[2 * u - 12], zt[2 * u - 11], zd[2 * u - 12], zd[2 * u - 11], zx[2 * u - 12], zx[2 * u - 11]);
                        }
                    }
                    if constexpr (i == 0 && kk == 0 && layer == 1) { pin(pt[0]); pin(pt[1]); pin(pt[2]); pin(pt[3]); }
                    if constexpr (layer == 0 && i < 2 && kk > 0 && 12 + 4 * i + kk - 1 >= NRT && 12 + 4 * i + kk - 1 < RING2) ar[12 + 4 * i + kk - 1] = ld128s(rl, ho[1], ks[2 + 4 * i + kk - 1]);
                    if constexpr (layer == 0 && i == kGSI && kk > 0) __builtin_amdgcn_raw_buffer_store_b128(v4u{g[2 * kk - 2][0], g[2 * kk - 2][1], g[2 * kk - 1][0], g[2 * kk - 1][1]}, rs, gso + ((kk - 1) & 1) * 1024, kk < 3 ? pol : pol ^ 8192u, SC1);
                    if constexpr (layer == 0 && i > 2 && i <= 7 && kk == (i == kGSI ? 0 : 2)) {
                        { unsigned d0_, d1_; rda<20 + 2 * (i - 3)>(d0_); rda<21 + 2 * (i - 3)>(d1_); dsk[i - 3] = v2u{d0_, d1_}; }
                    }
                    if constexpr (kk == 0 && i > 0 && pv < 40) ar[pv % RING2] = ld128s(rl, ho[pv / 10], ks[pv % 10]);
                    constexpr int s1 = s - 1 - (layer == 0 && m == 1 ? kFGI - 10 : 0);
                    if constexpr (kk == 2 && m >= 1 && s1 >= 0 && s1 < 5) *(f32x4*)(smem + dst[m - 1] + s1 * 1024) = a2[m - 1][s1];
                    site();
                    if constexpr (i >= 37) a2[i - 37][kk] = *(const f32x4*)(src + (i - 37) * 5120 + kk * 1024);
                    SCHED();
                });
                if constexpr (i == 10 && layer) { asm volatile("s_waitcnt lgkmcnt(0)" ::: "memory"); __builtin_amdgcn_s_barrier(); asm volatile("" ::: "memory"); }
                if constexpr (i == kFGI && layer == 0) {
                    static_assert(kFGI >= 10 && kFGI <= 14, "");
                    asm volatile("s_waitcnt vmcnt(%0) lgkmcnt(0)" ::"i"(kFGI - kGSI) : "memory");
                    __builtin_amdgcn_s_barrier(); asm volatile("" ::: "memory");
                    st64(v2u{tag0, tag1}, rs, fgo1);
                }
                if constexpr (i == 26 && layer == 0) {
#pragma unroll
                    for (int nb = 0; nb < 5; ++nb) { a2[3][nb] = f32x4{zz[nb][0], zz[nb][1], zz[nb][2], zz[nb][3]}; pin(a2[3][nb]); }
                }
                if constexpr (i == kGPI && layer == 0) gld2s<0>(f0, fgo, wl);
                if constexpr (i == kGCI && layer == 0) {
                    wait1(f0);
                    static_for<39 - i>(LAMBDA(r_) { pin(ar[(i + 1 + r_.value) % RING2]); });
                    gearly = __builtin_amdgcn_ballot_w64(!ISTAG(f0)) == 0ull;
                    ORDER();
                    undef3(gl);
                    if (__builtin_expect(gearly, 1)) { gld4t<0>(gl[0], glo, wl); gld4t<0>(gl[1], glo, glb1); gld4t<0>(gl[2], glo, glb2); }
                    else poll2(fgo, fgo, wl);
                }
                if constexpr (i == 36) {
                    asm volatile("s_waitcnt lgkmcnt(0)" ::: "memory");
                    __builtin_amdgcn_s_barrier(); asm volatile("" ::: "memory");
                }
                if constexpr (STAMPS == 1 && (i == 0 || i == 9 || i == 10 || i == 15 || i == 25 || i == 35 || i == 36)) { STAMP((layer ? 51 : 41) + (i == 0 ? 0 : i == 9 ? 1 : i == 10 ? 2 : i == 15 ? 3 : i == 25 ? 4 : i == 35 ? 5 : 6)) }
                SCHED();
            });
            STAMP(layer ? 18 : 8)
        }
#pragma unroll
        for (int nb = 0; nb < 5; ++nb) a2[0][nb] = (a2[0][nb] + a2[1][nb]) + a2[2][nb];
        asm volatile("s_nop 1" : "+v"(a2[3][0]), "+v"(a2[3][1]), "+v"(a2[3][2]), "+v"(a2[3][3]), "+v"(a2[3][4]),
                     "+v"(a2[0][0]), "+v"(a2[0][1]), "+v"(a2[0][2]), "+v"(a2[0][3]), "+v"(a2[0][4]));
        float ty;
        {
            f32x4 s1, s2;
#pragma unroll
            for (int nb = 0; nb < 5; ++nb) {
                const f32x4 y = a2[3][nb] + a2[0][nb];
                xin[nb] = y;
                if (nb == 0) { s1 = y; s2 = y * y; } else { s1 += y; s2 += y * y; }
            }
            ty = xsum2h(hsum4(s1), hsum4(s2));
            st64(v2u{__float_as_uint(ty), tag0}, rl, sgs);
        }
        SCHED();
        STAMP(layer ? 19 : 9)
        if constexpr (layer == 1) {
#pragma unroll
            for (int nb = 0; nb < 5; ++nb) *(f32x4*)(smem + otw + nb * 64) = xin[nb];
#pragma unroll
            for (int nb = 0; nb < 5; ++nb) xin[nb] = *(const f32x4*)(smem + (nb < 4 ? otr + nb * 336 : ots));
            SCHED();
        }
        int lane_o, b_o = b0, w_o = w0;
        asm volatile("v_mbcnt_lo_u32_b32 %0, -1, 0\n\tv_mbcnt_hi_u32_b32 %0, -1, %0" : "=v"(lane_o));
        asm volatile("" : "+s"(b_o), "+s"(w_o));
        const int lane = lane_o, b = b_o, w = w_o;
        const int c = lane & 15, q = lane >> 4;
        TILES()
        if (layer == 0 && __builtin_expect(!gearly, 0)) {
            {
                unsigned f0_, f1_;
                asm volatile("s_waitcnt vmcnt(1)\n\t" AR("%0", 36, 0) AR("%1", 36, 1) : "=v"(f0_), "=v"(f1_)::"memory");
                f0 = v2u{f0_, f1_};
            }
            STAMP(63)
            WAVE_RETRY(ISTAG(f0), f0 = ld64(rl, fgo))
            gld4t<0>(gl[0], glo, wl); gld4t<0>(gl[1], glo, wl + 8 * RUN); gld4t<0>(gl[2], glo, wl + 12 * RUN);
            asm volatile("s_waitcnt vmcnt(0)" : "+v"(gl[0]), "+v"(gl[1]), "+v"(gl[2])::"memory");
        }
        if (layer == 0) {
            asm volatile("s_waitcnt vmcnt(1)" : "+v"(gl[0]), "+v"(gl[1]), "+v"(gl[2])::"memory");
            STAMP(60)
            char* tb = smem + SC + w * INBOX;
            *(v4u*)(tb + lane * 16) = gl[0]; *(v4u*)(tb + 1024 + lane * 16) = gl[1]; *(v4u*)(tb + 1536 + lane * 16) = gl[2];
#pragma unroll
            for (int nb = 0; nb < 5; ++nb) gt[nb] = __builtin_bit_cast(v2u, trread(tb, (16 * nb + 4 * q + (c >> 2)) * 32 + (c & 3) * 8));
        } else ACC_TIE5(gt);
        SCHED();
        constexpr int PR = layer ? PRM1 : PRM0, NP = (PR & 1) + (PR >> 1);
        if constexpr (PR & 1) gran_req<32>(sgo, wl);
        SCHED();
        f32x4 gf[5], yg[5];
#pragma unroll
        for (int nb = 0; nb < 5; ++nb) { gf[nb] = unpk4(gt[nb]); yg[nb] = xin[nb] * gf[nb]; pin(gf[nb]); pin(yg[nb]); }
        SCHED();
        if constexpr (PR & 2) gran_req<(PR & 1 ? 40 : 32)>(sgo, wl);
        SCHED();
        unsigned xs = OFF_X2F + (unsigned)(((mt2 >> 3) & 1) * PG + ((2 * (mt2 & 7) + (w >> 1)) * 8 + ((c >> 2) & 1) * 4 + (nt2 >> 1)) * RUN + (10 * (nt2 & 1) + 2 * (q & 1) + (q >> 1)) * 256 + (8 * (w & 1) + 4 * (c >> 3) + (c & 3)) * 16);
        unsigned pso = q < 2 ? OFF_PS + (unsigned)((mt1 & 7) * RUN + (((mt2 & 1) * 4 + w) * 8 + nt2) * 128 + c * 8 + q * 4) : kNone;
        unsigned fxo1 = lane ? kNone : OFF_FX + (unsigned)((mt1 & 7) * RUN + ((2 * (mt2 & 1) + (w >> 1)) * 16 + nt2 * 2 + (w & 1)) * 128);
        unsigned oo = (unsigned)(((64 * mt2 + 16 * w + c) * D_ + 80 * nt2 + 4 * q) * 4);
        unsigned o4 = (unsigned)(((64 * mt2 + 16 * w + (c & 12)) * D_ + 80 * nt2 + 16 * (q + (nt2 & 1)) + 4 * (c & 3)) * 4);
        oo += (nt2 & 1) ? 0u : 256u;
        unsigned xs2 = (q & 1) ? kNone : xs + 2048u;
        if (layer == 0) { pin(xs); pin(xs2); pin(pso); pin(fxo1); } else { pin(oo); pin(o4); }
        SCHED();
        {
            float a, d;
            STAMP(layer ? 62 : 61)
            if constexpr (layer == 0) site();
            pipe_gran<(layer ? PDS : PDS0), NP>(a, d, sgo, wl, tag0, dead);
            STAMP(layer ? 20 : 10)
            xsum2(a, d);
            const float me = a * (1.0f / D_);
            const float ri = __builtin_amdgcn_rsqf(fmaxf(d * (1.0f / D_) - me * me, 0.f) + 1e-5f), mr = -me * ri;
            if constexpr (layer == 1) {
                static_for<5>(LAMBDA(j_) {
                    constexpr int jj = (j_.value + 4) % 5;
                    const float rj = jj < 4 ? dpp<0x55 * (jj & 3)>(ri) : ri, mj = jj < 4 ? dpp<0x55 * (jj & 3)>(mr) : mr;
                    const f32x4 o = yg[jj] * rj + (gf[jj] * mj + xin[jj]);
                    __builtin_amdgcn_raw_buffer_store_b128(__builtin_bit_cast(v4u, o), ro, jj < 4 ? o4 : oo, jj < 4 ? jj * 2560 : 0, OUTP);
                });
            } else {
#pragma unroll
                for (int i = 0; i < 3; ++i) {
                    v2u pk[2];
#pragma unroll
                    for (int e = 0; e < (i < 2 ? 2 : 1); ++e) {
                        const f32x4 x = yg[2 * i + e] * ri + (gf[2 * i + e] * mr + xin[2 * i + e]);
                        xin[2 * i + e] = x;
                        pk[e] = v2u{cvtpk(x.x, x.y), cvtpk(x.z, x.w)};
                    }
                    const v2u pa = pk[0], pb = pk[i < 2];
                    const auto r0 = __builtin_amdgcn_permlane16_swap(pa.x, pb.x, false, false);
                    const auto r1 = __builtin_amdgcn_permlane16_swap(pa.y, pb.y, false, false);
                    st128(v4u{r0[0], r1[0], r0[1], r1[1]}, rl, i < 2 ? xs + i * 1024 : xs2);
                    SCHED();
                }
            }
        }
        if constexpr (layer == 0) {
            {
                f32x4 s1, s2;
#pragma unroll
                for (int nb = 0; nb < 5; ++nb) { const f32x4 x = xin[nb]; if (nb == 0) { s1 = x; s2 = x * x; } else { s1 += x; s2 += x * x; } }
                __builtin_amdgcn_raw_buffer_store_b32(__float_as_uint(xsum2h(hsum4(s1), hsum4(s2))), rl, pso, 0, SC1);
            }
            SCHED();
            int w1lane0 = (c & 1) * 10240 + (c >> 1) * 128 + ((q ^ ((c & 1) | ((c >> 2) << 1))) * 16);
            int w1lane1 = (c & 1) * 10240 + (c >> 1) * 128 + (((4 + q) ^ ((c & 1) | ((c >> 2) << 1))) * 16);
            unsigned fxo = OFF_FX + (unsigned)((mt1 & 7) * RUN + w * 2048 + (lane >> 2) * 128);
            unsigned xo = OFF_X2F + (unsigned)(((mt1 >> 2) & 1) * PG + (4 * (mt1 & 3) + w) * 8 * RUN + lane * 16), xo1 = xo + 4 * RUN;
            const int row = 8 * q + (c & 7);
            unsigned po = OFF_PS + (unsigned)((mt1 & 7) * RUN + ((2 * w + (row >> 4)) * 8 + 4 * (c >> 3)) * 128 + (row & 15) * 8);
            int w1hi0 = w1lane0 + 61440, w1hi1 = w1lane1 + 61440;
            pin(w1lane0); pin(w1lane1); pin(w1hi0); pin(w1hi1); pin(fxo); pin(xo); pin(xo1); pin(po);
            v4u wf[2][5];
#pragma unroll
            for (int nb = 0; nb < 5; ++nb) wf[0][nb] = *(const v4u*)(smem + w1lane0 + nb * 20480);
            v2u tg2 = {tag0, tag1};
            pin(tg2);
            SCHED();
            DRAIN();
            st64(tg2, rl, fxo1);
            if (b == 255 && w == 0) FLAG(OFF_NONCE)
            STAMP(11)
            addrs(std::integral_constant<int, 1>{}, lane, w, b);
            v2u pt[4];
            skip<SKX>();
            pipe_flag<KPFX>(pt, fxo, wl, (u64)tag0 | ((u64)tag1 << 32), dead);
            ORDER();
            STAMP(12)
            v4u ar[RING1][2];
#pragma unroll
            for (int s = 0; s < RING1; ++s)
#pragma unroll
                for (int mb = 0; mb < 2; ++mb) { ar[s][mb] = ld128s(rl, mb ? xo1 : xo, kso(s)); SCHED(); }
            v2u ps[4];
#pragma unroll
            for (int i = 0; i < 4; ++i) ps[i] = ld64(rl, po + i * 128);
            SCHED();
            float t1, t2, rinv, mri, rr_[8], rm_[8];
            static_for<20>(LAMBDA(s_) {
                constexpr int s = s_.value, cu = s & 1, nx = cu ^ 1;
                LGKM0() SCHED();
                static_for<10>(LAMBDA(g_) {
                    constexpr int g = g_.value, mb = g / 5, k = g % 5;
                    if constexpr (s == 0) mf0(acc[mb][k], ar[0][mb], wf[0][k]); else mf(acc[mb][k], ar[s % RING1][mb], wf[cu][k]);
                    SCHED();
                    if constexpr (s == 0 && g == 0) { pin(pt[0]); pin(pt[1]); pin(pt[2]); pin(pt[3]); }
                    if constexpr (s < 19 && g < 3) wf[nx][g] = *(const v4u*)(smem + (nx ? w1lane1 : w1lane0) + g * 20480 + ((s + 1) >> 1) * 1024);
                    if constexpr (s < 19 && (g == 3 || g == 4)) wf[nx][g] = *(const v4u*)(smem + (nx ? w1hi1 : w1hi0) + (g - 3) * 20480 + ((s + 1) >> 1) * 1024);
                    if constexpr (g == 5 && s + RING1 < 20) ar[s % RING1][0] = ld128s(rl, xo, kso(s + RING1));
                    if constexpr (g == 2 && s > 0 && s - 1 + RING1 < 20) ar[(s - 1) % RING1][1] = ld128s(rl, xo1, kso(s - 1 + RING1));
                    if constexpr (s == 8 && g == 8) { t1 = __uint_as_float(ps[0].x) + __uint_as_float(ps[1].x); pin(t1); t2 = __uint_as_float(ps[0].y) + __uint_as_float(ps[1].y); pin(t2); }
                    if constexpr (s == 9 && g == 6) { t1 += __uint_as_float(ps[2].x); pin(t1); t2 += __uint_as_float(ps[2].y); pin(t2); }
                    if constexpr (s == 9 && g == 7) { t1 += __uint_as_float(ps[3].x); pin(t1); t2 += __uint_as_float(ps[3].y); pin(t2); }
                    if constexpr (s == 10 && g == 6) { t1 += dpp<0x128>(t1); t2 += dpp<0x128>(t2); pin(t1); pin(t2); }
                    if constexpr (s == 10 && g == 7) { t1 *= (1.0f / D_); pin(t1); t2 *= (1.0f / D_); pin(t2); }
                    if constexpr (s == 10 && g == 8) { t2 = fmaxf(t2 - t1 * t1, 0.f); pin(t2); }
                    if constexpr (s == 11 && g == 6) { rinv = __builtin_amdgcn_rsqf(t2 + 1e-5f); pin(rinv); }
                    if constexpr (s == 11 && g == 8) { mri = -t1 * rinv; pin(mri); }
                    if constexpr (s == 12 && g == 4) *(float*)(smem + l_rs(1, 0) + w * INBOX + lane * 4) = c < 8 ? rinv : mri;
                    if constexpr (s >= 12 && s < 15 && g >= 6 && g < 9 && 3 * (s - 12) + g - 6 < 8) bc2b<3 * (s - 12) + g - 6>(rr_[3 * (s - 12) + g - 6], rm_[3 * (s - 12) + g - 6], rinv, mri);
                    if constexpr (s >= 15 && s < 19 && g >= 5) TT_GAP(5 * (s - 15) + g - 5)
                    site();
                    SCHED();
                });
            });
            rr[0] = f32x4{rr_[0], rr_[1], rr_[2], rr_[3]}; rr[1] = f32x4{rr_[4], rr_[5], rr_[6], rr_[7]};
            asm volatile("kpf_end2:");
            ACC_FENCE10(acc[0], acc[1]);
            STAMP(13)
        } else {
            STAMP(21)
            STAMP_DUMP()
            asm volatile("kpf_end:");
        }
    };
    layer_body(std::integral_constant<int, 0>{});
    layer_body(std::integral_constant<int, 1>{});
}

extern "C" void kernel_launch(void* const* d_in, const int* in_sizes, int n_in,
                              void* d_out, int out_size, void* d_ws, size_t ws_size,
                              hipStream_t stream) {
    Params p;
    p.x0 = (const u16*)d_in[0];
    p.Lre = (const u16*)d_in[1];
    p.Lim = (const u16*)d_in[2];
    p.Bre = (const u16*)d_in[3];
    p.Bim = (const u16*)d_in[4];
    p.Cre = (const u16*)d_in[5];
    p.Cim = (const u16*)d_in[6];
    p.logdt = (const u16*)d_in[7];
    p.Dskip = (const u16*)d_in[8];
    p.k = (const u16*)d_in[10];
    p.v = (const u16*)d_in[11];
    p.shk = (const u16*)d_in[12];
    p.ssmk = (const u16*)d_in[13];
    p.ssmD = (const u16*)d_in[14];
    p.ws = (char*)d_ws;
    p.out = (float*)d_out;
    k_main<<<256, 256, 0, stream>>>(p);
}
```
